```python
import torch

def _softmax(x, axis=-1):
    return torch.softmax(x, dim=axis)
def _gelu(x):
    return torch.nn.functional.gelu(x, approximate="tanh")
def _silu(x):
    return torch.nn.functional.silu(x)
def _sigmoid(x):
    return torch.sigmoid(x)
def _layernorm(x, axis=-1, eps=1e-5):
    m = x.mean(dim=axis, keepdim=True)
    v = x.var(dim=axis, keepdim=True, unbiased=False)
    return (x - m) / torch.sqrt(v + eps)
def _scatter_add(tgt, idx, src, axis=0):
    return tgt.clone().index_add_(axis, idx, src)
def _segment_sum(src, seg, n):
    out = torch.zeros((n,) + src.shape[1:], dtype=src.dtype)
    return out.index_add_(0, seg, src)
def _pad(x, pw):
    flat = tuple(v for pair in reversed(pw) for v in pair)
    return torch.nn.functional.pad(x, flat)

kernel_name = 'concat_heads__mha_block_cached__gqa_block_cached__near_ridge'

def setup_inputs():

    _w = (lambda a: (lambda t: t.to(torch.bfloat16) if t.is_floating_point() else t)(torch.as_tensor(a)))
    def _A_setup():
        B, Sq, D = 8, 304, 512
        H, Sk, Dh = 32, 1024, 128
        return {
            "x": _w((lambda *s: torch.randn(*s, dtype=torch.bfloat16) * 0.8)(B, Sq, D)),
            "k_cache": _w((lambda *s: torch.randn(*s, dtype=torch.bfloat16) * 0.8)(B, H, Sk, Dh)),
            "v_cache": _w((lambda *s: torch.randn(*s, dtype=torch.bfloat16) * 0.05)(B, H, Sk, Dh)),
            "wq": _w((lambda *s: torch.randn(*s, dtype=torch.bfloat16) * 0.8)(D, H * Dh) * (D ** -0.5)),
            "wo": _w((lambda *s: torch.randn(*s, dtype=torch.bfloat16) * 0.05)(H * Dh, D) * ((H * Dh) ** -0.5)),
        }
    _A_in = _A_setup()

    def _B_setup():
        B, Sq, D = 8, 304, 512
        Hq, Hkv, Sk, Dh = 32, 32, 1024, 128
        assert Hq % Hkv == 0, (Hq, Hkv)
        return {
            "x": _w((lambda *s: torch.randn(*s, dtype=torch.bfloat16) * 0.8)(B, Sq, D)),
            "k_cache": _w((lambda *s: torch.randn(*s, dtype=torch.bfloat16) * 0.8)(B, Hkv, Sk, Dh)),
            "v_cache": _w((lambda *s: torch.randn(*s, dtype=torch.bfloat16) * 0.05)(B, Hkv, Sk, Dh)),
            "wq": _w((lambda *s: torch.randn(*s, dtype=torch.bfloat16) * 0.8)(D, Hq * Dh) * (D ** -0.5)),
            "wo": _w((lambda *s: torch.randn(*s, dtype=torch.bfloat16) * 0.05)(Hq * Dh, D) * ((Hq * Dh) ** -0.5)),
        }
    _B_in = _B_setup()

    return {
        "A_x": _A_in["x"],
        "A_k_cache": _A_in["k_cache"],
        "A_v_cache": _A_in["v_cache"],
        "A_wq": _A_in["wq"],
        "A_wo": _A_in["wo"],
        "B_x": _B_in["x"],
        "B_k_cache": _B_in["k_cache"],
        "B_v_cache": _B_in["v_cache"],
        "B_wq": _B_in["wq"],
        "B_wo": _B_in["wo"],
    }

def reference(A_x, A_k_cache, A_v_cache, A_wq, A_wo, B_x, B_k_cache, B_v_cache, B_wq, B_wo):
    def _A_ref(x, k_cache, v_cache, wq, wo):
        B, Sq, D = x.shape
        H, Dh = 32, 128
        q = (lambda x, *d: x.permute(*d))((x @ wq).reshape(B, Sq, H, Dh), 0, 2, 1, 3)
        scores = torch.einsum("bhqd,bhkd->bhqk", q, k_cache) * (Dh ** -0.5)
        p = _softmax(scores, axis=-1)
        o = torch.einsum("bhqk,bhkd->bhqd", p, v_cache)
        return (lambda x, *d: x.permute(*d))(o, 0, 2, 1, 3).reshape(B, Sq, H * Dh) @ wo

    def _B_ref(x, k_cache, v_cache, wq, wo):
        B, Sq, D = x.shape
        Hq, Dh = 32, 128
        Hkv = k_cache.shape[1]
        rep = Hq // Hkv
        q = (lambda x, *d: x.permute(*d))((x @ wq).reshape(B, Sq, Hq, Dh), 0, 2, 1, 3)
        k = (lambda x, n, axis: torch.repeat_interleave(x, n, dim=axis))(k_cache, rep, axis=1)
        v = (lambda x, n, axis: torch.repeat_interleave(x, n, dim=axis))(v_cache, rep, axis=1)
        scores = torch.einsum("bhqd,bhkd->bhqk", q, k) * (Dh ** -0.5)
        p = _softmax(scores, axis=-1)
        o = torch.einsum("bhqk,bhkd->bhqd", p, v)
        return (lambda x, *d: x.permute(*d))(o, 0, 2, 1, 3).reshape(B, Sq, Hq * Dh) @ wo

    a = _A_ref(A_x, A_k_cache, A_v_cache, A_wq, A_wo)
    b = _B_ref(B_x, B_k_cache, B_v_cache, B_wq, B_wo)
    return torch.concatenate([a, b], axis=-1)
```

```cpp
#include <hip/hip_runtime.h>
#include <hip/hip_bf16.h>

typedef unsigned short u16;
constexpr int    K_LDS = 163840;
constexpr size_t WS_HS = (size_t)1024 * 131072;
constexpr size_t WS_ST = WS_HS + 262144;
constexpr size_t WS_USE = WS_ST + 3 * 1048576;

__global__ __launch_bounds__(256, 1) void fused_attn(
    const u16* x0, const u16* k0, const u16* v0, const u16* wq0, const u16* wo0,
    const u16* x1, const u16* k1, const u16* v1, const u16* wq1, const u16* wo1,
    char* ws, u16* out) {
  const unsigned w = __builtin_amdgcn_readfirstlane(threadIdx.x >> 6);
  asm volatile(
R"A(.macro Wl n
s_waitcnt lgkmcnt(\n)
.endm
.macro Ta d,p,o
ds_read_b64_tr_b16 a[\d:\d+1], v\p offset:\o
.endm
.macro Ra d,p,o
ds_read_b128 a[\d:\d+3], v\p offset:\o
.endm
.macro Zvaa d,a,b
v_mfma_f32_16x16x32_bf16 v[\d:\d+3], a[\a:\a+3], a[\b:\b+3], 0
.endm
.macro Mvaa d,a,b
v_mfma_f32_16x16x32_bf16 v[\d:\d+3], a[\a:\a+3], a[\b:\b+3], v[\d:\d+3]
.endm
.macro Zaaa d,a,b
v_mfma_f32_16x16x32_bf16 a[\d:\d+3], a[\a:\a+3], a[\b:\b+3], 0
.endm
.macro Maaa d,a,b
v_mfma_f32_16x16x32_bf16 a[\d:\d+3], a[\a:\a+3], a[\b:\b+3], a[\d:\d+3]
.endm
.macro Tv d,p,o
ds_read_b64_tr_b16 v[\d:\d+1], v\p offset:\o
.endm
.macro Zava d,a,b
v_mfma_f32_16x16x32_bf16 a[\d:\d+3], v[\a:\a+3], a[\b:\b+3], 0
.endm
.macro Mava d,a,b
v_mfma_f32_16x16x32_bf16 a[\d:\d+3], v[\a:\a+3], a[\b:\b+3], a[\d:\d+3]
.endm
.macro Mvva d,a,b
v_mfma_f32_16x16x32_bf16 v[\d:\d+3], v[\a:\a+3], a[\b:\b+3], v[\d:\d+3]
.endm
.macro Pk d,a,b
v_cvt_pk_bf16_f32 v\d, v\a, v\b
.endm
.macro Ar d,a
v_accvgpr_read_b32 v\d, a\a
.endm
.macro Zvav d,a,b
v_mfma_f32_16x16x32_bf16 v[\d:\d+3], a[\a:\a+3], v[\b:\b+3], 0
.endm
.macro Mvav d,a,b
v_mfma_f32_16x16x32_bf16 v[\d:\d+3], a[\a:\a+3], v[\b:\b+3], v[\d:\d+3]
.endm
.macro Cvav d,a,b,c
v_mfma_f32_16x16x32_bf16 v[\d:\d+3], a[\a:\a+3], v[\b:\b+3], v[\c:\c+3]
.endm
.macro Ex d
v_exp_f32 v\d, v\d
.endm
.macro Zaav d,a,b
v_mfma_f32_16x16x32_bf16 a[\d:\d+3], a[\a:\a+3], v[\b:\b+3], 0
.endm
.macro Maav d,a,b
v_mfma_f32_16x16x32_bf16 a[\d:\d+3], a[\a:\a+3], v[\b:\b+3], a[\d:\d+3]
.endm
.macro Ad d,a
v_add_f32 v\d, v\d, v\a
.endm
.macro Cvaa d,a,b,c
v_mfma_f32_16x16x32_bf16 v[\d:\d+3], a[\a:\a+3], a[\b:\b+3], v[\c:\c+3]
.endm
.macro Mavv d,a,b
v_mfma_f32_16x16x32_bf16 a[\d:\d+3], v[\a:\a+3], v[\b:\b+3], a[\d:\d+3]
.endm
s_bfe_u32 s28, %[wg], 0x10002
s_mul_i32 s29, s28, 40
s_load_dwordx8 s[8:15], %[ka], s29
s_add_u32 s29, s29, 32
s_load_dwordx2 s[16:17], %[ka], s29
s_load_dwordx4 s[20:23], %[ka], 80
v_mbcnt_lo_u32_b32 v245, -1, 0
v_mbcnt_hi_u32_b32 v245, -1, v245
s_lshr_b32 s26, %[wg], 3
s_and_b32 s24, %[wg], 1
s_lshl_b32 s24, s24, 3
s_and_b32 s29, %[wg], 2
s_lshl_b32 s29, s29, 1
s_or_b32 s24, s24, s29
s_and_b32 s29, s26, 3
s_or_b32 s24, s24, s29
s_lshr_b32 s27, s26, 2
s_lshl_b32 s25, s28, 3
s_add_u32 s25, s25, s27
s_lshl_b32 s26, s26, 2
s_add_u32 s26, s26, %[w]
s_lshl_b32 s26, s26, 7
s_mov_b32 s76, %[w]
s_mul_i32 s78, s76, 4104
v_bfe_u32 v204, v245, 2, 2
v_and_b32 v205, 3, v245
v_lshrrev_b32 v206, 4, v245
v_lshlrev_b32 v207, 6, v206
v_lshl_add_u32 v207, v205, 4, v207
v_lshl_add_u32 v201, v204, 13, v207
s_lshl_b32 s56, s76, 16
v_add_u32 v208, s56, v201
v_add_u32 v209, 31744, v208
v_add_u32 v210, 260096, v208
v_add_u32 v211, 291840, v208
s_mul_i32 s77, s76, 4128
v_lshl_add_u32 v202, v204, 10, v207
s_mul_i32 s56, s76, 81920
v_add_u32 v212, s56, v202
v_add_u32 v213, 3072, v212
v_add_u32 v214, 6112, v212
v_add_u32 v215, 9184, v212
v_lshrrev_b32 v203, 1, v204
v_lshlrev_b32 v203, 5, v203
v_lshl_add_u32 v203, v205, 6, v203
v_lshl_add_u32 v203, v206, 4, v203
v_lshl_add_u32 v240, v204, 12, v203
v_lshl_add_u32 v216, v204, 10, v203
v_add_u32 v216, s77, v216
v_add_u32 v217, 82560, v216
v_mul_u32_u24 v243, 4104, v206
v_lshl_add_u32 v243, v204, 6, v243
v_lshl_add_u32 v243, v205, 4, v243
v_add_u32 v244, 115584, v243
v_add_u32 v243, 49536, v243
s_lshl_b32 s29, s24, 9
s_mul_i32 s30, s27, 311296
Wl 0
s_add_u32 s68, s14, s29
s_addc_u32 s69, s15, 0
s_mov_b32 s70, 4186624
s_mov_b32 s71, 0x20000
s_add_u32 m0, s78, 49536
s_mov_b32 s79, 0
buffer_load_dwordx4 v208, s[68:71], s79 offen offset:0 lds
buffer_load_dwordx4 v209, s[68:71], s79 offen offset:1024 lds
buffer_load_dwordx4 v210, s[68:71], s79 offen offset:2048 lds
buffer_load_dwordx4 v211, s[68:71], s79 offen offset:3072 lds
s_add_u32 m0, s78, 115584
s_mov_b32 s79, 524288
buffer_load_dwordx4 v208, s[68:71], s79 offen offset:0 lds
buffer_load_dwordx4 v209, s[68:71], s79 offen offset:1024 lds
buffer_load_dwordx4 v210, s[68:71], s79 offen offset:2048 lds
buffer_load_dwordx4 v211, s[68:71], s79 offen offset:3072 lds
s_add_u32 s64, s8, s30
s_addc_u32 s65, s9, 0
s_mov_b32 s66, 311296
s_mov_b32 s67, 0x20000
s_add_u32 m0, s77, 0
s_mov_b32 s79, 0
buffer_load_dwordx4 v212, s[64:67], s79 offen offset:0 lds
buffer_load_dwordx4 v213, s[64:67], s79 offen offset:1024 lds
buffer_load_dwordx4 v214, s[64:67], s79 offen offset:2080 lds
buffer_load_dwordx4 v215, s[64:67], s79 offen offset:3104 lds
s_add_u32 m0, s77, 16512
s_mov_b32 s79, 16384
buffer_load_dwordx4 v212, s[64:67], s79 offen offset:0 lds
buffer_load_dwordx4 v213, s[64:67], s79 offen offset:1024 lds
buffer_load_dwordx4 v214, s[64:67], s79 offen offset:2080 lds
buffer_load_dwordx4 v215, s[64:67], s79 offen offset:3104 lds
s_getpc_b64 s[88:89]
PC0_%=:
s_mov_b32 s90, KEND_%=-PC0_%=
s_mov_b32 s91, 0x20000
v_lshlrev_b32 v219, 14, v245
buffer_load_dword v220, v219, s[88:91], s26 offen
s_mov_b64 s[8:9], s[88:89]
s_lshl_b32 s14, s76, 12
s_add_u32 s14, s14, PG0_%=-PC0_%=+4096
s_add_u32 s14, s14, s8
s_addc_u32 s15, s9, 0
s_mov_b64 s[18:19], s[12:13]
s_mov_b64 exec, 0
s_swappc_b64 s[12:13], s[14:15]
s_add_u32 s14, s14, 4096
s_addc_u32 s15, s15, 0
s_add_u32 m0, s77, 82560
s_mov_b32 s79, 32768
buffer_load_dwordx4 v212, s[64:67], s79 offen offset:0 lds
buffer_load_dwordx4 v213, s[64:67], s79 offen offset:1024 lds
buffer_load_dwordx4 v214, s[64:67], s79 offen offset:2080 lds
buffer_load_dwordx4 v215, s[64:67], s79 offen offset:3104 lds
s_add_u32 m0, s77, 33024
s_mov_b32 s79, 49152
buffer_load_dwordx4 v212, s[64:67], s79 offen offset:0 lds
buffer_load_dwordx4 v213, s[64:67], s79 offen offset:1024 lds
buffer_load_dwordx4 v214, s[64:67], s79 offen offset:2080 lds
buffer_load_dwordx4 v215, s[64:67], s79 offen offset:3104 lds
s_cmp_eq_u32 s76, 3
s_cbranch_scc0 WA_%=
s_waitcnt vmcnt(9)
s_barrier
Ta 0,243,0
Ta 2,243,1024
Ta 4,243,2048
Ta 6,243,3072
Ta 8,244,0
Ta 10,244,1024
Ta 12,244,2048
Ta 14,244,3072
Ra 160,216,0
Ra 164,216,256
Ra 168,216,512
Ra 172,216,768
Ra 176,216,16512
Ra 180,216,16768
Ra 184,216,17024
Ra 188,216,17280
Wl 4
Ta 16,243,8
Ta 18,243,1032
Ta 20,243,2056
Ta 22,243,3080
Ta 24,244,8
Ta 26,244,1032
Ta 28,244,2056
Ta 30,244,3080
Zvaa 0,0,160
s_add_u32 m0, s78, 66048
s_mov_b32 s79, 256
Ta 32,243,256
Mvaa 0,4,164
s_add_u32 s20, s20, 0x1fffff
s_addc_u32 s21, s21, 0
Ta 34,243,1280
buffer_load_dwordx4 v208, s[68:71], s79 offen offset:0 lds
Mvaa 0,8,168
s_and_b32 s20, s20, 0xffe00000
s_lshl_b32 s53, s25, 4
Ta 36,243,2304
Mvaa 0,12,172
Wl 11
s_add_u32 s56, s53, s24
Ta 38,243,3328
Zvaa 4,12,188
s_lshl_b32 s56, s56, 2
Ta 40,244,256
buffer_load_dwordx4 v209, s[68:71], s79 offen offset:1024 lds
Mvaa 4,8,184
s_add_u32 s52, s56, s76
s_lshl_b32 s56, s52, 8
Ta 42,244,1280
Mvaa 4,4,180
s_add_u32 s56, s56, 134217728
s_mov_b64 s[50:51], s[20:21]
Ta 44,244,2304
Mvaa 4,0,176
Wl 7
Ta 46,244,3328
buffer_load_dwordx4 v210, s[68:71], s79 offen offset:2048 lds
Zvaa 20,16,160
s_add_u32 s50, s50, s56
s_addc_u32 s51, s51, 0
Ta 48,243,264
Mvaa 20,20,164
s_lshl_b32 s56, s52, 17
s_mov_b64 s[96:97], s[20:21]
Ta 50,243,1288
Mvaa 20,24,168
s_add_u32 s96, s96, s56
s_addc_u32 s97, s97, 0
Ta 52,243,2312
buffer_load_dwordx4 v211, s[68:71], s79 offen offset:3072 lds
Mvaa 20,28,172
s_mov_b32 s54, 0
s_load_dword s55, s[96:97], 103424
Ta 54,243,3336
v_lshlrev_b32 v200, 2, v245
Zvaa 24,28,188
s_add_u32 m0, s78, 132096
s_mov_b32 s79, 524544
Ta 56,244,264
Mvaa 24,24,184
s_lshl_b32 s29, s27, 4
Ta 58,244,1288
buffer_load_dwordx4 v208, s[68:71], s79 offen offset:0 lds
Mvaa 24,20,180
s_add_u32 s29, s29, s24
s_lshl_b32 s29, s29, 19
Ta 60,244,2312
Mvaa 24,16,176
Wl 7
Ta 62,244,3336
Zvaa 40,32,160
s_add_u32 s36, s10, s29
s_addc_u32 s37, s11, 0
Ta 64,243,512
buffer_load_dwordx4 v209, s[68:71], s79 offen offset:1024 lds
Mvaa 40,36,164
s_mov_b32 s38, 525312
s_mov_b32 s39, 0x20000
Ta 66,243,1536
Mvaa 40,40,168
s_add_u32 s40, s18, s29
s_addc_u32 s41, s19, 0
Ta 68,243,2560
Mvaa 40,44,172
s_mov_b32 s42, 525312
Ta 70,243,3584
buffer_load_dwordx4 v210, s[68:71], s79 offen offset:2048 lds
Zvaa 44,44,188
s_mov_b32 s43, 0x20000
Ta 72,244,512
Mvaa 44,40,184
s_sub_u32 s36, s36, 1024
s_subb_u32 s37, s37, 0
Ta 74,244,1536
Mvaa 44,36,180
s_sub_u32 s40, s40, 1024
s_subb_u32 s41, s41, 0
Ta 76,244,2560
buffer_load_dwordx4 v211, s[68:71], s79 offen offset:3072 lds
Mvaa 44,32,176
Wl 7
s_mov_b64 s[10:11], s[90:91]
Ta 78,244,3584
Zvaa 60,48,160
s_and_b32 s29, s24, 3
s_lshl_b32 s29, s29, 8
Ta 80,243,520
Mvaa 60,52,164
s_add_u32 s72, s16, s29
s_addc_u32 s73, s17, 0
Ta 82,243,1544
global_load_dword v201, v200, s[50:51] sc1
Mvaa 60,56,168
s_mov_b32 s74, 4193536
s_mov_b32 s75, 0x20000
Ta 84,243,2568
Mvaa 60,60,172
s_lshr_b32 s44, s24, 2
s_and_b32 s46, s24, 3
Ta 86,243,3592
Zvaa 64,60,188
s_and_b32 s52, s52, 511
s_lshl_b32 s52, s52, 13
Ta 88,244,520
Mvaa 64,56,184
s_lshl_b32 s56, s46, 8
s_sub_u32 s52, s52, s56
Ta 90,244,1544
Mvaa 64,52,180
s_lshl_b32 s56, s25, 6
s_add_u32 s56, s56, s76
Ta 92,244,2568
Mvaa 64,48,176
Wl 7
s_lshl_b32 s56, s56, 8
Ta 94,244,3592
Zvaa 80,64,160
s_add_u32 s56, s56, 134217728
s_mov_b64 s[92:93], s[20:21]
Ta 96,243,768
Mvaa 80,68,164
s_add_u32 s92, s92, s56
s_addc_u32 s93, s93, 0
Ta 98,243,1792
Mvaa 80,72,168
s_lshl_b32 s57, s46, 2
s_add_u32 s56, s56, s57
Ta 100,243,2816
Mvaa 80,76,172
s_add_u32 s56, s56, 16
s_mov_b64 s[94:95], s[20:21]
Ta 102,243,3840
Zvaa 84,76,188
s_add_u32 s94, s94, s56
s_addc_u32 s95, s95, 0
Ta 104,244,768
Mvaa 84,72,184
s_lshl_b32 s34, 0x100000, s46
s_or_b32 s34, s34, 0xf0
Ta 106,244,1792
Mvaa 84,68,180
s_lshl_b32 s35, 0x100010, s46
s_mul_i32 s45, s76, 4128
Ta 108,244,2816
Mvaa 84,64,176
Wl 7
s_lshl_b32 s49, s24, 18
Ta 110,244,3840
Zvaa 100,80,160
s_lshl_b32 s56, s76, 14
s_add_u32 s49, s49, s56
Ta 112,243,776
Mvaa 100,84,164
s_lshl_b32 s59, s44, 12
s_lshl_b32 s53, s25, 6
Ta 114,243,1800
Mvaa 100,88,168
s_lshl_b32 s56, s44, 4
s_add_u32 s56, s56, s76
Ta 116,243,2824
Mvaa 100,92,172
s_add_u32 s56, s56, s53
s_lshl_b32 s56, s56, 17
Ta 118,243,3848
Zvaa 104,92,188
Ta 120,244,776
v_and_b32 v254, 7, v245
v_mul_u32_u24 v254, 384, v254
Mvaa 104,88,184
Ta 122,244,1800
v_and_b32 v255, 8, v245
)A"
R"A(v_lshl_add_u32 v254, v255, 10, v254
Mvaa 104,84,180
Ta 124,244,2824
v_add_u32 v254, 119808, v254
Mvaa 104,80,176
Wl 7
s_mov_b64 s[84:85], s[20:21]
Ta 126,244,3848
Zvaa 120,96,160
s_add_u32 s84, s84, s56
s_addc_u32 s85, s85, 0
s_mov_b32 s98, 73728
Mvaa 120,100,164
s_mov_b32 s99, 0x20000
s_mov_b32 s86, 5898240
s_mov_b32 s87, 0x20000
Mvaa 120,104,168
s_mul_i32 s30, s27, 304
s_mul_i32 s56, s76, 80
s_add_u32 s30, s30, s56
Mvaa 120,108,172
s_lshl_b32 s30, s30, 11
s_lshl_b32 s56, s28, 10
s_add_u32 s30, s30, s56
Zvaa 124,108,188
s_waitcnt vmcnt(9)
s_add_u32 s30, s30, s29
Ra 192,217,0
Mvaa 124,104,184
s_lshl_b32 s56, s44, 6
s_add_u32 s30, s30, s56
Ra 196,217,256
Mvaa 124,100,180
s_add_u32 s88, s22, s30
s_addc_u32 s89, s23, 0
Ra 200,217,512
Mvaa 124,96,176
Wl 3
s_mov_b32 s90, 0
Ra 204,217,768
Zvaa 140,112,160
s_mov_b32 s91, 0x20000
Ra 208,216,33024
Mvaa 140,116,164
s_cmp_eq_u32 s76, 3
s_cselect_b32 s29, 0x40000000, 0
s_mov_b32 s56, 161856
s_cselect_b32 s90, 129088, s56
Ra 212,216,33280
Mvaa 140,120,168
s_mov_b32 s82, 0x3e0293ee
s_mov_b32 s83, 0x3e0293ee
Ra 216,216,33536
Mvaa 140,124,172
s_lshl_b32 s80, s76, 12
s_lshr_b32 s56, s76, 1
Ra 220,216,33792
Zvaa 144,124,188
s_lshl_b32 s56, s56, 5
s_add_u32 s80, s80, s56
global_store_dword v254, v245, s[96:97] sc1
v_lshl_add_u32 v203, v204, 15, v207
Mvaa 144,120,184
v_lshrrev_b32 v202, 1, v204
v_lshl_add_u32 v203, v202, 9, v203
Mvaa 144,116,180
v_xor_b32 v248, 0, v203
v_add_u32 v248, 1024, v248
Mvaa 144,112,176
Wl 4
v_xor_b32 v249, 512, v203
v_add_u32 v249, 1024, v249
Zvaa 8,0,192
s_add_u32 m0, s77, 0
s_mov_b32 s79, 256
v_xor_b32 v250, 16384, v203
v_add_u32 v250, 1024, v250
Mvaa 8,4,196
buffer_load_dwordx4 v212, s[64:67], s79 offen offset:0 lds
v_xor_b32 v251, 16896, v203
Mvaa 8,8,200
v_add_u32 v251, 1024, v251
Mvaa 8,12,204
Wl 0
s_and_b32 s56, s76, 1
buffer_load_dwordx4 v213, s[64:67], s79 offen offset:1024 lds
Zvaa 12,12,220
s_lshl_b32 s56, s56, 9
s_lshr_b32 s57, s76, 1
s_lshl_b32 s57, s57, 14
Mvaa 12,8,216
s_add_u32 s56, s56, s57
buffer_load_dwordx4 v214, s[64:67], s79 offen offset:2080 lds
v_xor_b32 v242, s56, v203
Mvaa 12,4,212
v_add_u32 v241, 256, v242
v_add_u32 v242, 1024, v242
Mvaa 12,0,208
buffer_load_dwordx4 v215, s[64:67], s79 offen offset:3104 lds
v_lshlrev_b32 v219, 4, v245
Zvaa 28,16,192
s_lshr_b32 s26, s26, 9
s_lshl_b32 s26, s26, 7
s_mov_b32 s98, 57344
Mvaa 28,20,196
Mvaa 28,24,200
Mvaa 28,28,204
Zvaa 32,28,220
Mvaa 32,24,216
Mvaa 32,20,212
Mvaa 32,16,208
Zvaa 48,32,192
s_add_u32 m0, s77, 16512
s_mov_b32 s79, 16640
Mvaa 48,36,196
buffer_load_dwordx4 v212, s[64:67], s79 offen offset:0 lds
Mvaa 48,40,200
Mvaa 48,44,204
buffer_load_dwordx4 v213, s[64:67], s79 offen offset:1024 lds
Zvaa 52,44,220
Mvaa 52,40,216
buffer_load_dwordx4 v214, s[64:67], s79 offen offset:2080 lds
Mvaa 52,36,212
Mvaa 52,32,208
buffer_load_dwordx4 v215, s[64:67], s79 offen offset:3104 lds
Zvaa 68,48,192
Mvaa 68,52,196
Mvaa 68,56,200
Mvaa 68,60,204
Zvaa 72,60,220
Mvaa 72,56,216
Mvaa 72,52,212
Mvaa 72,48,208
Zvaa 88,64,192
s_add_u32 m0, s77, 82560
s_mov_b32 s79, 33024
Mvaa 88,68,196
buffer_load_dwordx4 v212, s[64:67], s79 offen offset:0 lds
Mvaa 88,72,200
Mvaa 88,76,204
buffer_load_dwordx4 v213, s[64:67], s79 offen offset:1024 lds
Zvaa 92,76,220
Mvaa 92,72,216
buffer_load_dwordx4 v214, s[64:67], s79 offen offset:2080 lds
Mvaa 92,68,212
Mvaa 92,64,208
buffer_load_dwordx4 v215, s[64:67], s79 offen offset:3104 lds
Zvaa 108,80,192
Mvaa 108,84,196
Mvaa 108,88,200
Mvaa 108,92,204
Zvaa 112,92,220
Mvaa 112,88,216
Mvaa 112,84,212
Mvaa 112,80,208
Zvaa 128,96,192
s_waitcnt vmcnt(13)
s_barrier
s_add_u32 m0, s78, 49536
s_mov_b32 s79, 1048576
Ta 240,243,16512
Mvaa 128,100,196
Ta 242,243,17536
buffer_load_dwordx4 v208, s[68:71], s79 offen offset:0 lds
Mvaa 128,104,200
Ta 244,243,18560
Mvaa 128,108,204
Ta 246,243,19584
buffer_load_dwordx4 v209, s[68:71], s79 offen offset:1024 lds
Zvaa 132,108,220
Ta 248,244,16512
Mvaa 132,104,216
Ta 250,244,17536
buffer_load_dwordx4 v210, s[68:71], s79 offen offset:2048 lds
Mvaa 132,100,212
Ta 252,244,18560
Mvaa 132,96,208
Ta 254,244,19584
buffer_load_dwordx4 v211, s[68:71], s79 offen offset:3072 lds
Zvaa 148,112,192
s_add_u32 m0, s78, 115584
s_mov_b32 s79, 1572864
Mvaa 148,116,196
buffer_load_dwordx4 v208, s[68:71], s79 offen offset:0 lds
Mvaa 148,120,200
Mvaa 148,124,204
buffer_load_dwordx4 v209, s[68:71], s79 offen offset:1024 lds
Zvaa 152,124,220
Mvaa 152,120,216
buffer_load_dwordx4 v210, s[68:71], s79 offen offset:2048 lds
Mvaa 152,116,212
Mvaa 152,112,208
Wl 0
buffer_load_dwordx4 v211, s[68:71], s79 offen offset:3072 lds
v_readfirstlane_b32 s81, v201
v_and_b32 v200, 0xc0, v200
v_and_b32 v201, 0x7fffffff, v201
NONCEB_%=:
s_add_u32 s81, s81, 1
s_bitset0_b32 s81, 31
v_cmp_eq_u32 vcc, s81, v201
s_nop 0
s_cmp_lg_u64 vcc, 0
s_cbranch_scc1 NONCEB_%=
v_mov_b32 v201, s81
global_store_dword v200, v201, s[50:51] sc1
Zaaa 0,240,160
Maaa 0,244,164
s_add_u32 m0, s77, 33024
Tv 224,243,16520
Maaa 0,248,168
s_mov_b32 s79, 49408
Tv 226,243,17544
Maaa 0,252,172
Tv 228,243,18568
buffer_load_dwordx4 v212, s[64:67], s79 offen offset:0 lds
Zaaa 4,252,188
Tv 230,243,19592
Maaa 4,248,184
Tv 232,244,16520
Maaa 4,244,180
Tv 234,244,17544
Maaa 4,240,176
Tv 236,244,18568
buffer_load_dwordx4 v213, s[64:67], s79 offen offset:1024 lds
Zaaa 8,240,192
Tv 238,244,19592
Maaa 8,244,196
Maaa 8,248,200
Maaa 8,252,204
buffer_load_dwordx4 v214, s[64:67], s79 offen offset:2080 lds
Zaaa 12,252,220
Maaa 12,248,216
Maaa 12,244,212
Maaa 12,240,208
Wl 0
buffer_load_dwordx4 v215, s[64:67], s79 offen offset:3104 lds
Zava 20,224,160
Mava 20,228,164
Ta 240,243,16768
Mava 20,232,168
Ta 242,243,17792
Mava 20,236,172
Ta 244,243,18816
Zava 24,236,188
Ta 246,243,19840
Mava 24,232,184
Ta 248,244,16768
Mava 24,228,180
Ta 250,244,17792
Mava 24,224,176
Ta 252,244,18816
Zava 28,224,192
Ta 254,244,19840
Mava 28,228,196
Mava 28,232,200
Mava 28,236,204
Zava 32,236,220
Mava 32,232,216
Mava 32,228,212
Mava 32,224,208
Wl 0
Zaaa 40,240,160
Maaa 40,244,164
Tv 224,243,16776
Maaa 40,248,168
Tv 226,243,17800
Maaa 40,252,172
Tv 228,243,18824
Zaaa 44,252,188
Tv 230,243,19848
Maaa 44,248,184
Tv 232,244,16776
Maaa 44,244,180
Tv 234,244,17800
Maaa 44,240,176
Tv 236,244,18824
Zaaa 48,240,192
Tv 238,244,19848
Maaa 48,244,196
Maaa 48,248,200
buffer_load_dword v220, v219, s[72:75], s52 offen
Maaa 48,252,204
s_add_u32 s52, s52, 1024
Zaaa 52,252,220
Maaa 52,248,216
Maaa 52,244,212
Maaa 52,240,208
Wl 0
Zava 60,224,160
Mava 60,228,164
Ta 240,243,17024
Mava 60,232,168
Ta 242,243,18048
Mava 60,236,172
Ta 244,243,19072
Zava 64,236,188
Ta 246,243,20096
Mava 64,232,184
Ta 248,244,17024
Mava 64,228,180
Ta 250,244,18048
Mava 64,224,176
Ta 252,244,19072
Zava 68,224,192
Ta 254,244,20096
Mava 68,228,196
Mava 68,232,200
Mava 68,236,204
Zava 72,236,220
Mava 72,232,216
Mava 72,228,212
Mava 72,224,208
Wl 0
Zaaa 80,240,160
Maaa 80,244,164
Tv 224,243,17032
Maaa 80,248,168
Tv 226,243,18056
Maaa 80,252,172
Tv 228,243,19080
Zaaa 84,252,188
Tv 230,243,20104
Maaa 84,248,184
Tv 232,244,17032
Maaa 84,244,180
Tv 234,244,18056
Maaa 84,240,176
Tv 236,244,19080
Zaaa 88,240,192
Tv 238,244,20104
Maaa 88,244,196
Maaa 88,248,200
Maaa 88,252,204
Zaaa 92,252,220
Maaa 92,248,216
Maaa 92,244,212
Maaa 92,240,208
Wl 0
Zava 100,224,160
Mava 100,228,164
Ta 240,243,17280
Mava 100,232,168
Ta 242,243,18304
Mava 100,236,172
Ta 244,243,19328
Zava 104,236,188
Ta 246,243,20352
Mava 104,232,184
Ta 248,244,17280
Mava 104,228,180
Ta 250,244,18304
Mava 104,224,176
Ta 252,244,19328
Zava 108,224,192
Ta 254,244,20352
Mava 108,228,196
Mava 108,232,200
Mava 108,236,204
Zava 112,236,220
Mava 112,232,216
Mava 112,228,212
Mava 112,224,208
Wl 0
Zaaa 120,240,160
Maaa 120,244,164
Tv 224,243,17288
Maaa 120,248,168
Tv 226,243,18312
Maaa 120,252,172
Tv 228,243,19336
Zaaa 124,252,188
Tv 230,243,20360
Maaa 124,248,184
Tv 232,244,17288
Maaa 124,244,180
Tv 234,244,18312
Maaa 124,240,176
Tv 236,244,19336
Zaaa 128,240,192
Tv 238,244,20360
Maaa 128,244,196
Maaa 128,248,200
buffer_load_dword v220, v219, s[72:75], s52 offen
Maaa 128,252,204
s_add_u32 s52, s52, 1024
Zaaa 132,252,220
Maaa 132,248,216
Maaa 132,244,212
Maaa 132,240,208
Wl 0
Zava 140,224,160
s_mov_b64 exec, 0
s_swappc_b64 s[12:13], s[14:15]
s_add_u32 s14, s14, 4096
s_addc_u32 s15, s15, 0
s_waitcnt vmcnt(2)
s_barrier
Mava 140,228,164
s_add_u32 m0, s78, 66048
)A"
R"A(Ta 240,243,0
Mava 140,232,168
s_mov_b32 s79, 1048832
Ta 242,243,1024
Mava 140,236,172
Ta 244,243,2048
buffer_load_dwordx4 v208, s[68:71], s79 offen offset:0 lds
Zava 144,236,188
Ta 246,243,3072
Ra 160,216,0
Mava 144,232,184
Ta 248,244,0
Ra 164,216,256
Mava 144,228,180
Ta 250,244,1024
Ra 168,216,512
Mava 144,224,176
Ta 252,244,2048
Ra 172,216,768
buffer_load_dwordx4 v209, s[68:71], s79 offen offset:1024 lds
Zava 148,224,192
Ta 254,244,3072
Ra 176,216,16512
Mava 148,228,196
Ra 180,216,16768
Mava 148,232,200
Ra 184,216,17024
Mava 148,236,204
Ra 188,216,17280
buffer_load_dwordx4 v210, s[68:71], s79 offen offset:2048 lds
Zava 152,236,220
Ra 192,217,0
Mava 152,232,216
Ra 196,217,256
Mava 152,228,212
Ra 200,217,512
Mava 152,224,208
Wl 7
Ra 204,217,768
buffer_load_dwordx4 v211, s[68:71], s79 offen offset:3072 lds
Mvaa 0,240,160
Ra 208,216,33024
Mvaa 0,244,164
s_add_u32 m0, s78, 132096
Tv 224,243,8
Ra 212,216,33280
Mvaa 0,248,168
s_mov_b32 s79, 1573120
Tv 226,243,1032
Ra 216,216,33536
Mvaa 0,252,172
Wl 9
Tv 228,243,2056
Ra 220,216,33792
buffer_load_dwordx4 v208, s[68:71], s79 offen offset:0 lds
Mvaa 4,252,188
Tv 230,243,3080
Mvaa 4,248,184
Tv 232,244,8
Mvaa 4,244,180
Tv 234,244,1032
Mvaa 4,240,176
Wl 10
Tv 236,244,2056
buffer_load_dwordx4 v209, s[68:71], s79 offen offset:1024 lds
Mvaa 8,240,192
Tv 238,244,3080
Mvaa 8,244,196
Mvaa 8,248,200
Mvaa 8,252,204
Wl 5
buffer_load_dwordx4 v210, s[68:71], s79 offen offset:2048 lds
Mvaa 12,252,220
Mvaa 12,248,216
Mvaa 12,244,212
Mvaa 12,240,208
Wl 0
buffer_load_dwordx4 v211, s[68:71], s79 offen offset:3072 lds
Mvva 20,224,160
Mvva 20,228,164
s_add_u32 m0, s77, 0
Ta 240,243,256
Mvva 20,232,168
s_mov_b32 s79, 512
Ta 242,243,1280
Mvva 20,236,172
s_mov_b32 s55, s82
s_load_dword s55, s[96:97], 120576
Ta 244,243,2304
buffer_load_dwordx4 v212, s[64:67], s79 offen offset:0 lds
Mvva 24,236,188
s_cmp_eq_u32 s55, s82
s_addc_u32 s54, s54, 0
Ta 246,243,3328
Mvva 24,232,184
Ta 248,244,256
Mvva 24,228,180
s_cmp_eq_u32 s55, s82
s_addc_u32 s54, s54, 0
Ta 250,244,1280
Mvva 24,224,176
Ta 252,244,2304
buffer_load_dwordx4 v213, s[64:67], s79 offen offset:1024 lds
Mvva 28,224,192
s_cmp_eq_u32 s55, s82
s_addc_u32 s54, s54, 0
Ta 254,244,3328
Mvva 28,228,196
ds_read_b32 v254, v243
Mvva 28,232,200
s_cmp_eq_u32 s55, s82
s_addc_u32 s54, s54, 0
Mvva 28,236,204
buffer_load_dwordx4 v214, s[64:67], s79 offen offset:2080 lds
Mvva 32,236,220
s_cmp_eq_u32 s55, s82
s_addc_u32 s54, s54, 0
Mvva 32,232,216
Mvva 32,228,212
s_cmp_eq_u32 s55, s82
s_addc_u32 s54, s54, 0
Mvva 32,224,208
Wl 1
buffer_load_dwordx4 v215, s[64:67], s79 offen offset:3104 lds
Mvaa 40,240,160
s_cmp_eq_u32 s55, s82
s_addc_u32 s54, s54, 0
Mvaa 40,244,164
Tv 224,243,264
Mvaa 40,248,168
s_cmp_eq_u32 s55, s82
s_addc_u32 s54, s54, 0
Tv 226,243,1288
Mvaa 40,252,172
Tv 228,243,2312
Mvaa 44,252,188
s_cmp_eq_u32 s55, s82
s_addc_u32 s54, s54, 0
Tv 230,243,3336
Mvaa 44,248,184
Tv 232,244,264
Mvaa 44,244,180
s_cmp_eq_u32 s55, s82
s_addc_u32 s54, s54, 0
Tv 234,244,1288
Mvaa 44,240,176
Tv 236,244,2312
Mvaa 48,240,192
s_cmp_eq_u32 s55, s82
s_addc_u32 s54, s54, 0
Tv 238,244,3336
Mvaa 48,244,196
ds_read_b32 v254, v243
Mvaa 48,248,200
s_cmp_eq_u32 s55, s82
s_addc_u32 s54, s54, 0
Mvaa 48,252,204
Mvaa 52,252,220
s_cmp_eq_u32 s55, s82
s_addc_u32 s54, s54, 0
Mvaa 52,248,216
Mvaa 52,244,212
s_cmp_eq_u32 s55, s82
s_addc_u32 s54, s54, 0
Mvaa 52,240,208
Wl 1
Mvva 60,224,160
s_cmp_eq_u32 s55, s82
s_addc_u32 s54, s54, 0
Mvva 60,228,164
s_add_u32 m0, s77, 16512
Ta 240,243,512
Mvva 60,232,168
s_mov_b32 s79, 16896
s_cmp_eq_u32 s55, s82
s_addc_u32 s54, s54, 0
Ta 242,243,1536
Mvva 60,236,172
Ta 244,243,2560
buffer_load_dwordx4 v212, s[64:67], s79 offen offset:0 lds
Mvva 64,236,188
s_cmp_eq_u32 s55, s82
s_addc_u32 s54, s54, 0
Ta 246,243,3584
Mvva 64,232,184
Ta 248,244,512
Mvva 64,228,180
s_cmp_eq_u32 s55, s82
s_addc_u32 s54, s54, 0
Ta 250,244,1536
Mvva 64,224,176
Ta 252,244,2560
buffer_load_dwordx4 v213, s[64:67], s79 offen offset:1024 lds
Mvva 68,224,192
s_cmp_eq_u32 s55, s82
s_addc_u32 s54, s54, 0
Ta 254,244,3584
Mvva 68,228,196
ds_read_b32 v254, v243
Mvva 68,232,200
s_cmp_eq_u32 s55, s82
s_addc_u32 s54, s54, 0
Mvva 68,236,204
buffer_load_dwordx4 v214, s[64:67], s79 offen offset:2080 lds
Mvva 72,236,220
s_cmp_eq_u32 s55, s82
s_addc_u32 s54, s54, 0
Mvva 72,232,216
Mvva 72,228,212
s_cmp_eq_u32 s55, s82
s_addc_u32 s54, s54, 0
Mvva 72,224,208
Wl 1
buffer_load_dwordx4 v215, s[64:67], s79 offen offset:3104 lds
Mvaa 80,240,160
s_cmp_eq_u32 s55, s82
s_addc_u32 s54, s54, 0
Mvaa 80,244,164
Tv 224,243,520
Mvaa 80,248,168
s_cmp_eq_u32 s55, s82
s_addc_u32 s54, s54, 0
Tv 226,243,1544
Mvaa 80,252,172
Tv 228,243,2568
Mvaa 84,252,188
s_cmp_eq_u32 s55, s82
s_addc_u32 s54, s54, 0
Tv 230,243,3592
Mvaa 84,248,184
Tv 232,244,520
Mvaa 84,244,180
s_cmp_eq_u32 s55, s82
s_addc_u32 s54, s54, 0
Tv 234,244,1544
Mvaa 84,240,176
Tv 236,244,2568
Mvaa 88,240,192
s_cmp_eq_u32 s55, s82
s_addc_u32 s54, s54, 0
Tv 238,244,3592
Mvaa 88,244,196
ds_read_b32 v254, v243
Mvaa 88,248,200
s_cmp_eq_u32 s55, s82
s_addc_u32 s54, s54, 0
Mvaa 88,252,204
Mvaa 92,252,220
s_cmp_eq_u32 s55, s82
s_addc_u32 s54, s54, 0
Mvaa 92,248,216
Mvaa 92,244,212
s_cmp_eq_u32 s55, s82
s_addc_u32 s54, s54, 0
Mvaa 92,240,208
Wl 1
Mvva 100,224,160
s_cmp_eq_u32 s55, s82
s_addc_u32 s54, s54, 0
Mvva 100,228,164
s_add_u32 m0, s77, 82560
Ta 240,243,768
Mvva 100,232,168
s_mov_b32 s79, 33280
s_cmp_eq_u32 s55, s82
s_addc_u32 s54, s54, 0
Ta 242,243,1792
Mvva 100,236,172
Ta 244,243,2816
buffer_load_dwordx4 v212, s[64:67], s79 offen offset:0 lds
Mvva 104,236,188
s_cmp_eq_u32 s55, s82
s_addc_u32 s54, s54, 0
Ta 246,243,3840
Mvva 104,232,184
Ta 248,244,768
Mvva 104,228,180
s_cmp_eq_u32 s55, s82
s_addc_u32 s54, s54, 0
Ta 250,244,1792
Mvva 104,224,176
Ta 252,244,2816
buffer_load_dwordx4 v213, s[64:67], s79 offen offset:1024 lds
Mvva 108,224,192
s_cmp_eq_u32 s55, s82
s_addc_u32 s54, s54, 0
Ta 254,244,3840
Mvva 108,228,196
ds_read_b32 v254, v243
Mvva 108,232,200
s_cmp_eq_u32 s55, s82
s_addc_u32 s54, s54, 0
Mvva 108,236,204
buffer_load_dwordx4 v214, s[64:67], s79 offen offset:2080 lds
Mvva 112,236,220
s_cmp_eq_u32 s55, s82
s_addc_u32 s54, s54, 0
Mvva 112,232,216
Mvva 112,228,212
s_cmp_eq_u32 s55, s82
s_addc_u32 s54, s54, 0
Mvva 112,224,208
Wl 1
buffer_load_dwordx4 v215, s[64:67], s79 offen offset:3104 lds
Mvaa 120,240,160
s_cmp_eq_u32 s55, s82
s_addc_u32 s54, s54, 0
Mvaa 120,244,164
Tv 224,243,776
Mvaa 120,248,168
s_cmp_eq_u32 s55, s82
s_addc_u32 s54, s54, 0
Tv 226,243,1800
Mvaa 120,252,172
Tv 228,243,2824
Mvaa 124,252,188
s_cmp_eq_u32 s55, s82
s_addc_u32 s54, s54, 0
Tv 230,243,3848
Mvaa 124,248,184
Tv 232,244,776
Mvaa 124,244,180
s_cmp_eq_u32 s55, s82
s_addc_u32 s54, s54, 0
Tv 234,244,1800
Mvaa 124,240,176
Tv 236,244,2824
Mvaa 128,240,192
s_cmp_eq_u32 s55, s82
s_addc_u32 s54, s54, 0
Tv 238,244,3848
Mvaa 128,244,196
Mvaa 128,248,200
s_cmp_eq_u32 s55, s82
s_addc_u32 s54, s54, 0
buffer_load_dword v220, v219, s[72:75], s52 offen
Mvaa 128,252,204
s_add_u32 s52, s52, 1024
Mvaa 132,252,220
s_cmp_eq_u32 s55, s82
s_addc_u32 s54, s54, 0
Mvaa 132,248,216
Mvaa 132,244,212
s_cmp_eq_u32 s55, s82
s_addc_u32 s54, s54, 0
Mvaa 132,240,208
Wl 0
Mvva 140,224,160
s_mov_b64 exec, 0
s_swappc_b64 s[12:13], s[14:15]
s_add_u32 s14, s14, 4096
s_addc_u32 s15, s15, 0
s_waitcnt vmcnt(13)
s_barrier
Mvva 140,228,164
s_add_u32 m0, s78, 49536
Ta 240,243,16512
Mvva 140,232,168
s_mov_b32 s79, 2097152
Ta 242,243,17536
Mvva 140,236,172
Ta 244,243,18560
buffer_load_dwordx4 v208, s[68:71], s79 offen offset:0 lds
Mvva 144,236,188
Ta 246,243,19584
Mvva 144,232,184
Ta 248,244,16512
Mvva 144,228,180
Ta 250,244,17536
Mvva 144,224,176
Ta 252,244,18560
buffer_load_dwordx4 v209, s[68:71], s79 offen offset:1024 lds
Mvva 148,224,192
Ta 254,244,19584
Mvva 148,228,196
Mvva 148,232,200
Mvva 148,236,204
buffer_load_dwordx4 v210, s[68:71], s79 offen offset:2048 lds
Mvva 152,236,220
Mvva 152,232,216
Mvva 152,228,212
Mvva 152,224,208
Wl 0
buffer_load_dwordx4 v211, s[68:71], s79 offen offset:3072 lds
Maaa 0,240,160
Maaa 0,244,164
s_add_u32 m0, s78, 115584
Tv 224,243,16520
Maaa 0,248,168
s_mov_b32 s79, 2621440
Tv 226,243,17544
Maaa 0,252,172
Tv 228,243,18568
buffer_load_dwordx4 v208, s[68:71], s79 offen offset:0 lds
Maaa 4,252,188
Tv 230,243,19592
Maaa 4,248,184
Tv 232,244,16520
Maaa 4,244,180
Tv 234,244,17544
)A"
R"A(Maaa 4,240,176
Tv 236,244,18568
buffer_load_dwordx4 v209, s[68:71], s79 offen offset:1024 lds
Maaa 8,240,192
Tv 238,244,19592
Maaa 8,244,196
Maaa 8,248,200
Maaa 8,252,204
buffer_load_dwordx4 v210, s[68:71], s79 offen offset:2048 lds
Maaa 12,252,220
Maaa 12,248,216
Maaa 12,244,212
Maaa 12,240,208
Wl 0
buffer_load_dwordx4 v211, s[68:71], s79 offen offset:3072 lds
Mava 20,224,160
Mava 20,228,164
s_add_u32 m0, s77, 33024
Ta 240,243,16768
Mava 20,232,168
s_mov_b32 s79, 49664
Ta 242,243,17792
Mava 20,236,172
s_mov_b32 s55, s82
s_load_dword s55, s[96:97], 129152
Ta 244,243,18816
buffer_load_dwordx4 v212, s[64:67], s79 offen offset:0 lds
Mava 24,236,188
s_cmp_eq_u32 s55, s82
s_subb_u32 s54, s54, 0
Ta 246,243,19840
Mava 24,232,184
Ta 248,244,16768
Mava 24,228,180
s_cmp_eq_u32 s55, s82
s_subb_u32 s54, s54, 0
Ta 250,244,17792
Mava 24,224,176
Ta 252,244,18816
buffer_load_dwordx4 v213, s[64:67], s79 offen offset:1024 lds
Mava 28,224,192
s_cmp_eq_u32 s55, s82
s_subb_u32 s54, s54, 0
Ta 254,244,19840
Mava 28,228,196
ds_read_b32 v254, v243
Mava 28,232,200
s_cmp_eq_u32 s55, s82
s_subb_u32 s54, s54, 0
Mava 28,236,204
buffer_load_dwordx4 v214, s[64:67], s79 offen offset:2080 lds
Mava 32,236,220
s_cmp_eq_u32 s55, s82
s_subb_u32 s54, s54, 0
Mava 32,232,216
Mava 32,228,212
s_cmp_eq_u32 s55, s82
s_subb_u32 s54, s54, 0
Mava 32,224,208
Wl 1
buffer_load_dwordx4 v215, s[64:67], s79 offen offset:3104 lds
Maaa 40,240,160
s_cmp_eq_u32 s55, s82
s_subb_u32 s54, s54, 0
Maaa 40,244,164
Tv 224,243,16776
Maaa 40,248,168
s_cmp_eq_u32 s55, s82
s_subb_u32 s54, s54, 0
Tv 226,243,17800
Maaa 40,252,172
Tv 228,243,18824
Maaa 44,252,188
s_cmp_eq_u32 s55, s82
s_subb_u32 s54, s54, 0
Tv 230,243,19848
Maaa 44,248,184
Tv 232,244,16776
Maaa 44,244,180
s_cmp_eq_u32 s55, s82
s_subb_u32 s54, s54, 0
Tv 234,244,17800
Maaa 44,240,176
Tv 236,244,18824
Maaa 48,240,192
s_cmp_eq_u32 s55, s82
s_subb_u32 s54, s54, 0
Tv 238,244,19848
Maaa 48,244,196
ds_read_b32 v254, v243
Maaa 48,248,200
s_cmp_eq_u32 s55, s82
s_subb_u32 s54, s54, 0
buffer_load_dword v220, v219, s[72:75], s52 offen
Maaa 48,252,204
s_add_u32 s52, s52, 1024
Maaa 52,252,220
s_cmp_eq_u32 s55, s82
s_subb_u32 s54, s54, 0
Maaa 52,248,216
Maaa 52,244,212
s_cmp_eq_u32 s55, s82
s_subb_u32 s54, s54, 0
Maaa 52,240,208
Wl 1
Mava 60,224,160
s_cmp_eq_u32 s55, s82
s_subb_u32 s54, s54, 0
Mava 60,228,164
Ta 240,243,17024
Mava 60,232,168
s_cmp_eq_u32 s55, s82
s_subb_u32 s54, s54, 0
Ta 242,243,18048
Mava 60,236,172
Ta 244,243,19072
Mava 64,236,188
s_cmp_eq_u32 s55, s82
s_subb_u32 s54, s54, 0
Ta 246,243,20096
Mava 64,232,184
Ta 248,244,17024
Mava 64,228,180
s_cmp_eq_u32 s55, s82
s_subb_u32 s54, s54, 0
Ta 250,244,18048
Mava 64,224,176
Ta 252,244,19072
Mava 68,224,192
s_cmp_eq_u32 s55, s82
s_subb_u32 s54, s54, 0
Ta 254,244,20096
Mava 68,228,196
ds_read_b32 v254, v243
Mava 68,232,200
s_cmp_eq_u32 s55, s82
s_subb_u32 s54, s54, 0
Mava 68,236,204
Mava 72,236,220
s_cmp_eq_u32 s55, s82
s_subb_u32 s54, s54, 0
Mava 72,232,216
Mava 72,228,212
s_cmp_eq_u32 s55, s82
s_subb_u32 s54, s54, 0
Mava 72,224,208
Wl 1
Maaa 80,240,160
s_cmp_eq_u32 s55, s82
s_subb_u32 s54, s54, 0
Maaa 80,244,164
Tv 224,243,17032
Maaa 80,248,168
s_cmp_eq_u32 s55, s82
s_subb_u32 s54, s54, 0
Tv 226,243,18056
Maaa 80,252,172
Tv 228,243,19080
Maaa 84,252,188
s_cmp_eq_u32 s55, s82
s_subb_u32 s54, s54, 0
Tv 230,243,20104
Maaa 84,248,184
Tv 232,244,17032
Maaa 84,244,180
s_cmp_eq_u32 s55, s82
s_subb_u32 s54, s54, 0
Tv 234,244,18056
Maaa 84,240,176
Tv 236,244,19080
Maaa 88,240,192
s_cmp_eq_u32 s55, s82
s_subb_u32 s54, s54, 0
Tv 238,244,20104
Maaa 88,244,196
ds_read_b32 v254, v243
Maaa 88,248,200
s_cmp_eq_u32 s55, s82
s_subb_u32 s54, s54, 0
Maaa 88,252,204
Maaa 92,252,220
s_cmp_eq_u32 s55, s82
s_subb_u32 s54, s54, 0
Maaa 92,248,216
Maaa 92,244,212
s_cmp_eq_u32 s55, s82
s_subb_u32 s54, s54, 0
Maaa 92,240,208
Wl 1
Mava 100,224,160
s_cmp_eq_u32 s55, s82
s_subb_u32 s54, s54, 0
Mava 100,228,164
Ta 240,243,17280
Mava 100,232,168
s_cmp_eq_u32 s55, s82
s_subb_u32 s54, s54, 0
Ta 242,243,18304
Mava 100,236,172
Ta 244,243,19328
Mava 104,236,188
s_cmp_eq_u32 s55, s82
s_subb_u32 s54, s54, 0
Ta 246,243,20352
Mava 104,232,184
Ta 248,244,17280
Mava 104,228,180
s_cmp_eq_u32 s55, s82
s_subb_u32 s54, s54, 0
Ta 250,244,18304
Mava 104,224,176
Ta 252,244,19328
Mava 108,224,192
s_cmp_eq_u32 s55, s82
s_subb_u32 s54, s54, 0
Ta 254,244,20352
Mava 108,228,196
ds_read_b32 v254, v243
Mava 108,232,200
s_cmp_eq_u32 s55, s82
s_subb_u32 s54, s54, 0
Mava 108,236,204
Mava 112,236,220
s_cmp_eq_u32 s55, s82
s_subb_u32 s54, s54, 0
Mava 112,232,216
Mava 112,228,212
s_cmp_eq_u32 s55, s82
s_subb_u32 s54, s54, 0
Mava 112,224,208
Wl 1
Maaa 120,240,160
s_cmp_eq_u32 s55, s82
s_subb_u32 s54, s54, 0
Maaa 120,244,164
Tv 224,243,17288
Maaa 120,248,168
s_cmp_eq_u32 s55, s82
s_subb_u32 s54, s54, 0
Tv 226,243,18312
Maaa 120,252,172
Tv 228,243,19336
Maaa 124,252,188
s_cmp_eq_u32 s55, s82
s_subb_u32 s54, s54, 0
Tv 230,243,20360
Maaa 124,248,184
Tv 232,244,17288
Maaa 124,244,180
s_cmp_eq_u32 s55, s82
s_subb_u32 s54, s54, 0
Tv 234,244,18312
Maaa 124,240,176
Tv 236,244,19336
Maaa 128,240,192
s_cmp_eq_u32 s55, s82
s_subb_u32 s54, s54, 0
Tv 238,244,20360
Maaa 128,244,196
Maaa 128,248,200
s_cmp_eq_u32 s55, s82
s_subb_u32 s54, s54, 0
buffer_load_dword v220, v219, s[72:75], s52 offen
Maaa 128,252,204
s_add_u32 s52, s52, 1024
Maaa 132,252,220
s_cmp_eq_u32 s55, s82
s_subb_u32 s54, s54, 0
Maaa 132,248,216
Maaa 132,244,212
s_cmp_eq_u32 s55, s82
s_subb_u32 s54, s54, 0
Maaa 132,240,208
Wl 0
Mava 140,224,160
s_mov_b64 exec, 0
s_swappc_b64 s[12:13], s[14:15]
s_add_u32 s14, s14, 4096
s_addc_u32 s15, s15, 0
s_waitcnt vmcnt(2)
s_barrier
Mava 140,228,164
s_add_u32 m0, s78, 66048
Ta 240,243,0
Mava 140,232,168
s_mov_b32 s79, 2097408
Ta 242,243,1024
Mava 140,236,172
Ta 244,243,2048
buffer_load_dwordx4 v208, s[68:71], s79 offen offset:0 lds
Mava 144,236,188
Ta 246,243,3072
Ra 160,216,0
Mava 144,232,184
Ta 248,244,0
Ra 164,216,256
Mava 144,228,180
Ta 250,244,1024
Ra 168,216,512
Mava 144,224,176
Ta 252,244,2048
Ra 172,216,768
buffer_load_dwordx4 v209, s[68:71], s79 offen offset:1024 lds
Mava 148,224,192
Ta 254,244,3072
Ra 176,216,16512
Mava 148,228,196
Ra 180,216,16768
Mava 148,232,200
Ra 184,216,17024
Mava 148,236,204
Ra 188,216,17280
buffer_load_dwordx4 v210, s[68:71], s79 offen offset:2048 lds
Mava 152,236,220
Ra 192,217,0
Mava 152,232,216
Ra 196,217,256
Mava 152,228,212
Ra 200,217,512
Mava 152,224,208
Wl 7
Ra 204,217,768
buffer_load_dwordx4 v211, s[68:71], s79 offen offset:3072 lds
Mvaa 0,240,160
Ra 208,216,33024
Mvaa 0,244,164
s_add_u32 m0, s78, 132096
Tv 224,243,8
Ra 212,216,33280
Mvaa 0,248,168
s_mov_b32 s79, 2621696
Tv 226,243,1032
Ra 216,216,33536
Mvaa 0,252,172
Wl 9
Tv 228,243,2056
Ra 220,216,33792
buffer_load_dwordx4 v208, s[68:71], s79 offen offset:0 lds
Mvaa 4,252,188
Tv 230,243,3080
Mvaa 4,248,184
Tv 232,244,8
Mvaa 4,244,180
Tv 234,244,1032
Mvaa 4,240,176
Wl 10
Tv 236,244,2056
buffer_load_dwordx4 v209, s[68:71], s79 offen offset:1024 lds
Mvaa 8,240,192
Tv 238,244,3080
Mvaa 8,244,196
Mvaa 8,248,200
Mvaa 8,252,204
Wl 5
buffer_load_dwordx4 v210, s[68:71], s79 offen offset:2048 lds
Mvaa 12,252,220
Mvaa 12,248,216
Mvaa 12,244,212
Mvaa 12,240,208
Wl 0
buffer_load_dwordx4 v211, s[68:71], s79 offen offset:3072 lds
Mvva 20,224,160
Mvva 20,228,164
s_add_u32 m0, s77, 0
Ta 240,243,256
Mvva 20,232,168
s_mov_b32 s79, 768
Ta 242,243,1280
Mvva 20,236,172
s_mov_b32 s55, s82
s_load_dword s55, s[96:97], 129536
Ta 244,243,2304
buffer_load_dwordx4 v212, s[64:67], s79 offen offset:0 lds
Mvva 24,236,188
s_cmp_eq_u32 s55, s82
s_subb_u32 s54, s54, 0
Ta 246,243,3328
Mvva 24,232,184
Ta 248,244,256
Mvva 24,228,180
s_cmp_eq_u32 s55, s82
s_subb_u32 s54, s54, 0
Ta 250,244,1280
Mvva 24,224,176
Ta 252,244,2304
buffer_load_dwordx4 v213, s[64:67], s79 offen offset:1024 lds
Mvva 28,224,192
s_cmp_eq_u32 s55, s82
s_subb_u32 s54, s54, 0
Ta 254,244,3328
Mvva 28,228,196
ds_read_b32 v254, v243
Mvva 28,232,200
s_cmp_eq_u32 s55, s82
s_subb_u32 s54, s54, 0
Mvva 28,236,204
buffer_load_dwordx4 v214, s[64:67], s79 offen offset:2080 lds
Mvva 32,236,220
s_cmp_eq_u32 s55, s82
s_subb_u32 s54, s54, 0
)A"
R"A(Mvva 32,232,216
Mvva 32,228,212
s_cmp_eq_u32 s55, s82
s_subb_u32 s54, s54, 0
Mvva 32,224,208
Wl 1
buffer_load_dwordx4 v215, s[64:67], s79 offen offset:3104 lds
Mvaa 40,240,160
s_cmp_eq_u32 s55, s82
s_subb_u32 s54, s54, 0
Mvaa 40,244,164
Tv 224,243,264
Mvaa 40,248,168
s_cmp_eq_u32 s55, s82
s_subb_u32 s54, s54, 0
Tv 226,243,1288
Mvaa 40,252,172
Tv 228,243,2312
Mvaa 44,252,188
s_cmp_eq_u32 s55, s82
s_subb_u32 s54, s54, 0
Tv 230,243,3336
Mvaa 44,248,184
Tv 232,244,264
Mvaa 44,244,180
s_cmp_eq_u32 s55, s82
s_subb_u32 s54, s54, 0
Tv 234,244,1288
Mvaa 44,240,176
Tv 236,244,2312
Mvaa 48,240,192
s_cmp_eq_u32 s55, s82
s_subb_u32 s54, s54, 0
Tv 238,244,3336
Mvaa 48,244,196
ds_read_b32 v254, v243
Mvaa 48,248,200
s_cmp_eq_u32 s55, s82
s_subb_u32 s54, s54, 0
Mvaa 48,252,204
Mvaa 52,252,220
s_cmp_eq_u32 s55, s82
s_subb_u32 s54, s54, 0
Mvaa 52,248,216
Mvaa 52,244,212
s_cmp_eq_u32 s55, s82
s_subb_u32 s54, s54, 0
Mvaa 52,240,208
Wl 1
Mvva 60,224,160
s_cmp_eq_u32 s55, s82
s_subb_u32 s54, s54, 0
Mvva 60,228,164
s_add_u32 m0, s77, 16512
Ta 240,243,512
Mvva 60,232,168
s_mov_b32 s79, 17152
s_cmp_eq_u32 s55, s82
s_subb_u32 s54, s54, 0
Ta 242,243,1536
Mvva 60,236,172
Ta 244,243,2560
buffer_load_dwordx4 v212, s[64:67], s79 offen offset:0 lds
Mvva 64,236,188
s_cmp_eq_u32 s55, s82
s_subb_u32 s54, s54, 0
Ta 246,243,3584
Mvva 64,232,184
Ta 248,244,512
Mvva 64,228,180
s_cmp_eq_u32 s55, s82
s_subb_u32 s54, s54, 0
Ta 250,244,1536
Mvva 64,224,176
Ta 252,244,2560
buffer_load_dwordx4 v213, s[64:67], s79 offen offset:1024 lds
Mvva 68,224,192
s_cmp_eq_u32 s55, s82
s_subb_u32 s54, s54, 0
Ta 254,244,3584
Mvva 68,228,196
ds_read_b32 v254, v243
Mvva 68,232,200
s_cmp_eq_u32 s55, s82
s_subb_u32 s54, s54, 0
Mvva 68,236,204
buffer_load_dwordx4 v214, s[64:67], s79 offen offset:2080 lds
Mvva 72,236,220
s_cmp_eq_u32 s55, s82
s_subb_u32 s54, s54, 0
Mvva 72,232,216
Mvva 72,228,212
s_cmp_eq_u32 s55, s82
s_subb_u32 s54, s54, 0
Mvva 72,224,208
Wl 1
buffer_load_dwordx4 v215, s[64:67], s79 offen offset:3104 lds
Mvaa 80,240,160
s_cmp_eq_u32 s55, s82
s_subb_u32 s54, s54, 0
Mvaa 80,244,164
Tv 224,243,520
Mvaa 80,248,168
s_cmp_eq_u32 s55, s82
s_subb_u32 s54, s54, 0
Tv 226,243,1544
Mvaa 80,252,172
Tv 228,243,2568
Mvaa 84,252,188
s_cmp_eq_u32 s55, s82
s_subb_u32 s54, s54, 0
Tv 230,243,3592
Mvaa 84,248,184
Tv 232,244,520
Mvaa 84,244,180
s_cmp_eq_u32 s55, s82
s_subb_u32 s54, s54, 0
Tv 234,244,1544
Mvaa 84,240,176
Tv 236,244,2568
Mvaa 88,240,192
s_cmp_eq_u32 s55, s82
s_subb_u32 s54, s54, 0
Tv 238,244,3592
Mvaa 88,244,196
ds_read_b32 v254, v243
Mvaa 88,248,200
s_cmp_eq_u32 s55, s82
s_subb_u32 s54, s54, 0
Mvaa 88,252,204
Mvaa 92,252,220
s_cmp_eq_u32 s55, s82
s_subb_u32 s54, s54, 0
Mvaa 92,248,216
Mvaa 92,244,212
s_cmp_eq_u32 s55, s82
s_subb_u32 s54, s54, 0
Mvaa 92,240,208
Wl 1
Mvva 100,224,160
s_cmp_eq_u32 s55, s82
s_subb_u32 s54, s54, 0
Mvva 100,228,164
s_add_u32 m0, s77, 82560
Ta 240,243,768
Mvva 100,232,168
s_mov_b32 s79, 33536
s_cmp_eq_u32 s55, s82
s_subb_u32 s54, s54, 0
Ta 242,243,1792
Mvva 100,236,172
Ta 244,243,2816
buffer_load_dwordx4 v212, s[64:67], s79 offen offset:0 lds
Mvva 104,236,188
s_cmp_eq_u32 s55, s82
s_subb_u32 s54, s54, 0
Ta 246,243,3840
Mvva 104,232,184
Ta 248,244,768
Mvva 104,228,180
s_cmp_eq_u32 s55, s82
s_subb_u32 s54, s54, 0
Ta 250,244,1792
Mvva 104,224,176
Ta 252,244,2816
buffer_load_dwordx4 v213, s[64:67], s79 offen offset:1024 lds
Mvva 108,224,192
s_cmp_eq_u32 s55, s82
s_subb_u32 s54, s54, 0
Ta 254,244,3840
Mvva 108,228,196
ds_read_b32 v254, v243
Mvva 108,232,200
s_cmp_eq_u32 s55, s82
s_subb_u32 s54, s54, 0
Mvva 108,236,204
buffer_load_dwordx4 v214, s[64:67], s79 offen offset:2080 lds
Mvva 112,236,220
s_cmp_eq_u32 s55, s82
s_subb_u32 s54, s54, 0
Mvva 112,232,216
Mvva 112,228,212
s_cmp_eq_u32 s55, s82
s_subb_u32 s54, s54, 0
Mvva 112,224,208
Wl 1
buffer_load_dwordx4 v215, s[64:67], s79 offen offset:3104 lds
Mvaa 120,240,160
s_cmp_eq_u32 s55, s82
s_subb_u32 s54, s54, 0
Mvaa 120,244,164
Tv 224,243,776
Mvaa 120,248,168
s_cmp_eq_u32 s55, s82
s_subb_u32 s54, s54, 0
Tv 226,243,1800
Mvaa 120,252,172
Tv 228,243,2824
Mvaa 124,252,188
s_cmp_eq_u32 s55, s82
s_subb_u32 s54, s54, 0
Tv 230,243,3848
Mvaa 124,248,184
Tv 232,244,776
Mvaa 124,244,180
s_cmp_eq_u32 s55, s82
s_subb_u32 s54, s54, 0
Tv 234,244,1800
Mvaa 124,240,176
Tv 236,244,2824
Mvaa 128,240,192
s_cmp_eq_u32 s55, s82
s_subb_u32 s54, s54, 0
Tv 238,244,3848
Mvaa 128,244,196
Mvaa 128,248,200
s_cmp_eq_u32 s55, s82
s_subb_u32 s54, s54, 0
buffer_load_dword v220, v219, s[72:75], s52 offen
Mvaa 128,252,204
s_add_u32 s52, s52, 1024
Mvaa 132,252,220
s_cmp_eq_u32 s55, s82
s_subb_u32 s54, s54, 0
Mvaa 132,248,216
Mvaa 132,244,212
s_cmp_eq_u32 s55, s82
s_subb_u32 s54, s54, 0
Mvaa 132,240,208
Wl 0
Mvva 140,224,160
s_mov_b64 exec, 0
s_swappc_b64 s[12:13], s[14:15]
s_add_u32 s14, s14, 4096
s_addc_u32 s15, s15, 0
s_waitcnt vmcnt(13)
s_barrier
Mvva 140,228,164
s_add_u32 m0, s78, 49536
Ta 240,243,16512
Mvva 140,232,168
s_mov_b32 s79, 3145728
Ta 242,243,17536
Mvva 140,236,172
Ta 244,243,18560
buffer_load_dwordx4 v208, s[68:71], s79 offen offset:0 lds
Mvva 144,236,188
Ta 246,243,19584
Mvva 144,232,184
Ta 248,244,16512
Mvva 144,228,180
Ta 250,244,17536
Mvva 144,224,176
Ta 252,244,18560
buffer_load_dwordx4 v209, s[68:71], s79 offen offset:1024 lds
Mvva 148,224,192
Ta 254,244,19584
Mvva 148,228,196
Mvva 148,232,200
Mvva 148,236,204
buffer_load_dwordx4 v210, s[68:71], s79 offen offset:2048 lds
Mvva 152,236,220
Mvva 152,232,216
Mvva 152,228,212
Mvva 152,224,208
Wl 0
buffer_load_dwordx4 v211, s[68:71], s79 offen offset:3072 lds
Maaa 0,240,160
Maaa 0,244,164
s_add_u32 m0, s78, 115584
Tv 224,243,16520
Maaa 0,248,168
s_mov_b32 s79, 3670016
Tv 226,243,17544
Maaa 0,252,172
Tv 228,243,18568
buffer_load_dwordx4 v208, s[68:71], s79 offen offset:0 lds
Maaa 4,252,188
Tv 230,243,19592
Maaa 4,248,184
Tv 232,244,16520
Maaa 4,244,180
Tv 234,244,17544
Maaa 4,240,176
Tv 236,244,18568
buffer_load_dwordx4 v209, s[68:71], s79 offen offset:1024 lds
Maaa 8,240,192
Tv 238,244,19592
Maaa 8,244,196
Maaa 8,248,200
Maaa 8,252,204
buffer_load_dwordx4 v210, s[68:71], s79 offen offset:2048 lds
Maaa 12,252,220
Maaa 12,248,216
Maaa 12,244,212
Maaa 12,240,208
Wl 0
buffer_load_dwordx4 v211, s[68:71], s79 offen offset:3072 lds
Mava 20,224,160
Mava 20,228,164
s_add_u32 m0, s77, 33024
Ta 240,243,16768
Mava 20,232,168
s_mov_b32 s79, 49920
Ta 242,243,17792
Mava 20,236,172
s_mov_b32 s55, s82
s_load_dword s55, s[96:97], 121728
Ta 244,243,18816
buffer_load_dwordx4 v212, s[64:67], s79 offen offset:0 lds
Mava 24,236,188
s_cmp_eq_u32 s55, s82
s_addc_u32 s54, s54, 0
Ta 246,243,19840
Mava 24,232,184
Ta 248,244,16768
Mava 24,228,180
s_cmp_eq_u32 s55, s82
s_addc_u32 s54, s54, 0
Ta 250,244,17792
Mava 24,224,176
Ta 252,244,18816
buffer_load_dwordx4 v213, s[64:67], s79 offen offset:1024 lds
Mava 28,224,192
s_cmp_eq_u32 s55, s82
s_addc_u32 s54, s54, 0
Ta 254,244,19840
Mava 28,228,196
ds_read_b32 v254, v243
Mava 28,232,200
s_cmp_eq_u32 s55, s82
s_addc_u32 s54, s54, 0
Mava 28,236,204
buffer_load_dwordx4 v214, s[64:67], s79 offen offset:2080 lds
Mava 32,236,220
s_cmp_eq_u32 s55, s82
s_addc_u32 s54, s54, 0
Mava 32,232,216
Mava 32,228,212
s_cmp_eq_u32 s55, s82
s_addc_u32 s54, s54, 0
Mava 32,224,208
Wl 1
buffer_load_dwordx4 v215, s[64:67], s79 offen offset:3104 lds
Maaa 40,240,160
s_cmp_eq_u32 s55, s82
s_addc_u32 s54, s54, 0
Maaa 40,244,164
Tv 224,243,16776
Maaa 40,248,168
s_cmp_eq_u32 s55, s82
s_addc_u32 s54, s54, 0
Tv 226,243,17800
Maaa 40,252,172
Tv 228,243,18824
Maaa 44,252,188
s_cmp_eq_u32 s55, s82
s_addc_u32 s54, s54, 0
Tv 230,243,19848
Maaa 44,248,184
Tv 232,244,16776
Maaa 44,244,180
s_cmp_eq_u32 s55, s82
s_addc_u32 s54, s54, 0
Tv 234,244,17800
Maaa 44,240,176
Tv 236,244,18824
Maaa 48,240,192
s_cmp_eq_u32 s55, s82
s_addc_u32 s54, s54, 0
Tv 238,244,19848
Maaa 48,244,196
ds_read_b32 v254, v243
Maaa 48,248,200
s_cmp_eq_u32 s55, s82
s_addc_u32 s54, s54, 0
buffer_load_dword v220, v219, s[72:75], s52 offen
Maaa 48,252,204
s_add_u32 s52, s52, 1024
Maaa 52,252,220
s_cmp_eq_u32 s55, s82
s_addc_u32 s54, s54, 0
Maaa 52,248,216
Maaa 52,244,212
s_cmp_eq_u32 s55, s82
s_addc_u32 s54, s54, 0
Maaa 52,240,208
Wl 1
Mava 60,224,160
s_cmp_eq_u32 s55, s82
s_addc_u32 s54, s54, 0
Mava 60,228,164
Ta 240,243,17024
Mava 60,232,168
s_cmp_eq_u32 s55, s82
s_addc_u32 s54, s54, 0
Ta 242,243,18048
Mava 60,236,172
Ta 244,243,19072
Mava 64,236,188
)A"
R"A(s_cmp_eq_u32 s55, s82
s_addc_u32 s54, s54, 0
Ta 246,243,20096
Mava 64,232,184
Ta 248,244,17024
Mava 64,228,180
s_cmp_eq_u32 s55, s82
s_addc_u32 s54, s54, 0
Ta 250,244,18048
Mava 64,224,176
Ta 252,244,19072
Mava 68,224,192
s_cmp_eq_u32 s55, s82
s_addc_u32 s54, s54, 0
Ta 254,244,20096
Mava 68,228,196
ds_read_b32 v254, v243
Mava 68,232,200
s_cmp_eq_u32 s55, s82
s_addc_u32 s54, s54, 0
Mava 68,236,204
Mava 72,236,220
s_cmp_eq_u32 s55, s82
s_addc_u32 s54, s54, 0
Mava 72,232,216
Mava 72,228,212
s_cmp_eq_u32 s55, s82
s_addc_u32 s54, s54, 0
Mava 72,224,208
Wl 1
Maaa 80,240,160
s_cmp_eq_u32 s55, s82
s_addc_u32 s54, s54, 0
Maaa 80,244,164
Tv 224,243,17032
Maaa 80,248,168
s_cmp_eq_u32 s55, s82
s_addc_u32 s54, s54, 0
Tv 226,243,18056
Maaa 80,252,172
Tv 228,243,19080
Maaa 84,252,188
s_cmp_eq_u32 s55, s82
s_addc_u32 s54, s54, 0
Tv 230,243,20104
Maaa 84,248,184
Tv 232,244,17032
Maaa 84,244,180
s_cmp_eq_u32 s55, s82
s_addc_u32 s54, s54, 0
Tv 234,244,18056
Maaa 84,240,176
Tv 236,244,19080
Maaa 88,240,192
s_cmp_eq_u32 s55, s82
s_addc_u32 s54, s54, 0
Tv 238,244,20104
Maaa 88,244,196
ds_read_b32 v254, v243
v_lshlrev_b32 v252, 12, v245
Maaa 88,248,200
s_cmp_eq_u32 s55, s82
s_addc_u32 s54, s54, 0
buffer_load_dword v253, v252, s[8:11], s26 offen
Maaa 88,252,204
Maaa 92,252,220
s_cmp_eq_u32 s55, s82
s_addc_u32 s54, s54, 0
Maaa 92,248,216
Maaa 92,244,212
s_cmp_eq_u32 s55, s82
s_addc_u32 s54, s54, 0
Maaa 92,240,208
Wl 1
Mava 100,224,160
s_cmp_eq_u32 s55, s82
s_addc_u32 s54, s54, 0
Mava 100,228,164
Ta 240,243,17280
Mava 100,232,168
s_cmp_eq_u32 s55, s82
s_addc_u32 s54, s54, 0
Ta 242,243,18304
Mava 100,236,172
Ta 244,243,19328
Mava 104,236,188
s_cmp_eq_u32 s55, s82
s_addc_u32 s54, s54, 0
Ta 246,243,20352
Mava 104,232,184
Ta 248,244,17280
Mava 104,228,180
s_cmp_eq_u32 s55, s82
s_addc_u32 s54, s54, 0
Ta 250,244,18304
Mava 104,224,176
Ta 252,244,19328
Mava 108,224,192
s_cmp_eq_u32 s55, s82
s_addc_u32 s54, s54, 0
Ta 254,244,20352
Mava 108,228,196
ds_read_b32 v254, v243
Mava 108,232,200
s_cmp_eq_u32 s55, s82
s_addc_u32 s54, s54, 0
Mava 108,236,204
Mava 112,236,220
s_cmp_eq_u32 s55, s82
s_addc_u32 s54, s54, 0
Mava 112,232,216
Mava 112,228,212
s_cmp_eq_u32 s55, s82
s_addc_u32 s54, s54, 0
Mava 112,224,208
Wl 1
Maaa 120,240,160
s_cmp_eq_u32 s55, s82
s_addc_u32 s54, s54, 0
Maaa 120,244,164
Tv 224,243,17288
Maaa 120,248,168
s_cmp_eq_u32 s55, s82
s_addc_u32 s54, s54, 0
Tv 226,243,18312
Maaa 120,252,172
Tv 228,243,19336
Maaa 124,252,188
s_cmp_eq_u32 s55, s82
s_addc_u32 s54, s54, 0
Tv 230,243,20360
Maaa 124,248,184
Tv 232,244,17288
Maaa 124,244,180
s_cmp_eq_u32 s55, s82
s_addc_u32 s54, s54, 0
Tv 234,244,18312
Maaa 124,240,176
Tv 236,244,19336
Maaa 128,240,192
s_cmp_eq_u32 s55, s82
s_addc_u32 s54, s54, 0
Tv 238,244,20360
Maaa 128,244,196
Maaa 128,248,200
s_cmp_eq_u32 s55, s82
s_addc_u32 s54, s54, 0
buffer_load_dword v220, v219, s[72:75], s52 offen
Maaa 128,252,204
s_add_u32 s52, s52, 1024
Maaa 132,252,220
s_cmp_eq_u32 s55, s82
s_addc_u32 s54, s54, 0
Maaa 132,248,216
Maaa 132,244,212
s_cmp_eq_u32 s55, s82
s_addc_u32 s54, s54, 0
Maaa 132,240,208
Wl 0
Mava 140,224,160
s_mov_b64 exec, 0
s_swappc_b64 s[12:13], s[14:15]
s_add_u32 s14, s14, 4096
s_addc_u32 s15, s15, 0
s_waitcnt vmcnt(3)
s_barrier
Mava 140,228,164
s_add_u32 m0, s78, 66048
Ta 240,243,0
Mava 140,232,168
s_mov_b32 s79, 3145984
Ta 242,243,1024
Mava 140,236,172
Ta 244,243,2048
buffer_load_dwordx4 v208, s[68:71], s79 offen offset:0 lds
Mava 144,236,188
Ta 246,243,3072
Ra 160,216,0
Mava 144,232,184
Ta 248,244,0
Ra 164,216,256
Mava 144,228,180
Ta 250,244,1024
Ra 168,216,512
Mava 144,224,176
Ta 252,244,2048
Ra 172,216,768
buffer_load_dwordx4 v209, s[68:71], s79 offen offset:1024 lds
Mava 148,224,192
Ta 254,244,3072
Ra 176,216,16512
Mava 148,228,196
Ra 180,216,16768
Mava 148,232,200
Ra 184,216,17024
Mava 148,236,204
Ra 188,216,17280
buffer_load_dwordx4 v210, s[68:71], s79 offen offset:2048 lds
Mava 152,236,220
Ra 192,217,0
Mava 152,232,216
Ra 196,217,256
Mava 152,228,212
Ra 200,217,512
Mava 152,224,208
Wl 7
Ra 204,217,768
buffer_load_dwordx4 v211, s[68:71], s79 offen offset:3072 lds
Mvaa 0,240,160
Ra 208,216,33024
Mvaa 0,244,164
s_add_u32 m0, s78, 132096
Tv 224,243,8
Ra 212,216,33280
Mvaa 0,248,168
s_mov_b32 s79, 3670272
Tv 226,243,1032
Ra 216,216,33536
Mvaa 0,252,172
Wl 9
Tv 228,243,2056
Ra 220,216,33792
buffer_load_dwordx4 v208, s[68:71], s79 offen offset:0 lds
Mvaa 4,252,188
Tv 230,243,3080
Mvaa 4,248,184
Tv 232,244,8
Mvaa 4,244,180
Tv 234,244,1032
Mvaa 4,240,176
Wl 10
Tv 236,244,2056
buffer_load_dwordx4 v209, s[68:71], s79 offen offset:1024 lds
Mvaa 8,240,192
Tv 238,244,3080
Mvaa 8,244,196
Mvaa 8,248,200
Mvaa 8,252,204
Wl 5
buffer_load_dwordx4 v210, s[68:71], s79 offen offset:2048 lds
Mvaa 12,252,220
Mvaa 12,248,216
Mvaa 12,244,212
Mvaa 12,240,208
Wl 0
buffer_load_dwordx4 v211, s[68:71], s79 offen offset:3072 lds
Mvva 20,224,160
s_barrier
Mvva 20,228,164
Ta 240,243,256
Mvva 20,232,168
Ta 242,243,1280
Mvva 20,236,172
Ta 244,243,2304
Mvva 24,236,188
Ta 246,243,3328
Mvva 24,232,184
Ta 248,244,256
Mvva 24,228,180
Ta 250,244,1280
Mvva 24,224,176
Ta 252,244,2304
Mvva 28,224,192
Ta 254,244,3328
Mvva 28,228,196
s_add_u32 m0, s80, 0
Mvva 28,232,200
s_mov_b32 s79, 0
Mvva 28,236,204
buffer_load_dwordx4 v242, s[36:39], s79 offen offset:0 nt lds
buffer_load_dwordx4 v241, s[36:39], s79 offen offset:1024 nt lds
Mvva 32,236,220
Mvva 32,232,216
Mvva 32,228,212
Mvva 32,224,208
Wl 0
Mvaa 40,240,160
v_mul_f32 v0, s82, v0
v_mul_f32 v1, s82, v1
Mvaa 40,244,164
Tv 224,243,264
v_mul_f32 v2, s82, v2
Mvaa 40,248,168
Tv 226,243,1288
v_mul_f32 v3, s82, v3
Mvaa 40,252,172
Tv 228,243,2312
Pk 160,0,1
Mvaa 44,252,188
Tv 230,243,3336
Pk 161,2,3
Mvaa 44,248,184
Tv 232,244,264
v_mul_f32 v20, s82, v20
Mvaa 44,244,180
Tv 234,244,1288
v_mul_f32 v21, s82, v21
Mvaa 44,240,176
Tv 236,244,2312
v_mul_f32 v22, s82, v22
Mvaa 48,240,192
Tv 238,244,3336
v_mul_f32 v23, s82, v23
Mvaa 48,244,196
s_cmp_gt_i32 s54, 0
s_cselect_b32 s55, 8192, 0
Pk 162,20,21
Pk 163,22,23
Mvaa 48,248,200
v_mov_b32 v255, s55
v_mul_f32 v4, s82, v4
Mvaa 48,252,204
ds_write_b32 v244, v255 offset:33024
v_mul_f32 v5, s82, v5
Mvaa 52,252,220
v_mul_f32 v6, s82, v6
v_mul_f32 v7, s82, v7
Mvaa 52,248,216
Pk 176,4,5
Pk 177,6,7
Mvaa 52,244,212
v_mul_f32 v24, s82, v24
v_mul_f32 v25, s82, v25
Mvaa 52,240,208
Wl 1
v_mul_f32 v26, s82, v26
v_mul_f32 v27, s82, v27
Mvva 60,224,160
Pk 178,24,25
Pk 179,26,27
Mvva 60,228,164
Ta 240,243,512
v_mul_f32 v8, s82, v8
Mvva 60,232,168
Ta 242,243,1536
v_mul_f32 v9, s82, v9
Mvva 60,236,172
Ta 244,243,2560
v_mul_f32 v10, s82, v10
Mvva 64,236,188
Ta 246,243,3584
v_mul_f32 v11, s82, v11
Mvva 64,232,184
Ta 248,244,512
Pk 192,8,9
Mvva 64,228,180
Ta 250,244,1536
Pk 193,10,11
Mvva 64,224,176
Ta 252,244,2560
v_mul_f32 v28, s82, v28
Mvva 68,224,192
Ta 254,244,3584
v_mul_f32 v29, s82, v29
Mvva 68,228,196
v_mul_f32 v30, s82, v30
v_mul_f32 v31, s82, v31
Mvva 68,232,200
Pk 194,28,29
Pk 195,30,31
Mvva 68,236,204
buffer_load_dwordx4 v242, s[36:39], s79 offen offset:2048 nt lds
buffer_load_dwordx4 v241, s[36:39], s79 offen offset:3072 nt lds
Mvva 72,236,220
v_mul_f32 v12, s82, v12
v_mul_f32 v13, s82, v13
Mvva 72,232,216
v_mul_f32 v14, s82, v14
v_mul_f32 v15, s82, v15
Mvva 72,228,212
Pk 208,12,13
Pk 209,14,15
Mvva 72,224,208
Wl 0
v_mul_f32 v32, s82, v32
v_mul_f32 v33, s82, v33
Mvaa 80,240,160
v_mul_f32 v34, s82, v34
v_mul_f32 v35, s82, v35
Mvaa 80,244,164
Tv 224,243,520
Pk 210,32,33
Mvaa 80,248,168
Tv 226,243,1544
Pk 211,34,35
Mvaa 80,252,172
Tv 228,243,2568
v_mul_f32 v40, s82, v40
Mvaa 84,252,188
Tv 230,243,3592
v_mul_f32 v41, s82, v41
Mvaa 84,248,184
Tv 232,244,520
v_mul_f32 v42, s82, v42
Mvaa 84,244,180
Tv 234,244,1544
v_mul_f32 v43, s82, v43
Mvaa 84,240,176
Tv 236,244,2568
Pk 164,40,41
)A"
R"A(Mvaa 88,240,192
Tv 238,244,3592
Pk 165,42,43
Mvaa 88,244,196
s_add_u32 m0, s80, 16512
v_mul_f32 v60, s82, v60
v_mul_f32 v61, s82, v61
Mvaa 88,248,200
s_mov_b32 s79, 1024
v_mul_f32 v62, s82, v62
v_mul_f32 v63, s82, v63
Mvaa 88,252,204
buffer_load_dwordx4 v242, s[36:39], s79 offen offset:0 nt lds
buffer_load_dwordx4 v241, s[36:39], s79 offen offset:1024 nt lds
Mvaa 92,252,220
Pk 166,60,61
Pk 167,62,63
Mvaa 92,248,216
v_mul_f32 v44, s82, v44
v_mul_f32 v45, s82, v45
Mvaa 92,244,212
v_mul_f32 v46, s82, v46
v_mul_f32 v47, s82, v47
Mvaa 92,240,208
Wl 0
Pk 180,44,45
Pk 181,46,47
Mvva 100,224,160
v_mul_f32 v64, s82, v64
v_mul_f32 v65, s82, v65
Mvva 100,228,164
Ta 240,243,768
v_mul_f32 v66, s82, v66
Mvva 100,232,168
Ta 242,243,1792
v_mul_f32 v67, s82, v67
Mvva 100,236,172
Ta 244,243,2816
Pk 182,64,65
Mvva 104,236,188
Ta 246,243,3840
Pk 183,66,67
Mvva 104,232,184
Ta 248,244,768
v_mul_f32 v48, s82, v48
Mvva 104,228,180
Ta 250,244,1792
v_mul_f32 v49, s82, v49
Mvva 104,224,176
Ta 252,244,2816
v_mul_f32 v50, s82, v50
Mvva 108,224,192
Ta 254,244,3840
v_mul_f32 v51, s82, v51
Mvva 108,228,196
Pk 196,48,49
Pk 197,50,51
Mvva 108,232,200
v_mul_f32 v68, s82, v68
v_mul_f32 v69, s82, v69
Mvva 108,236,204
v_mul_f32 v70, s82, v70
v_mul_f32 v71, s82, v71
Mvva 112,236,220
Pk 198,68,69
Pk 199,70,71
Mvva 112,232,216
v_mul_f32 v52, s82, v52
v_mul_f32 v53, s82, v53
Mvva 112,228,212
v_mul_f32 v54, s82, v54
v_mul_f32 v55, s82, v55
Mvva 112,224,208
Wl 0
Pk 212,52,53
Pk 213,54,55
Mvaa 120,240,160
v_mul_f32 v72, s82, v72
v_mul_f32 v73, s82, v73
Mvaa 120,244,164
Tv 224,243,776
v_mul_f32 v74, s82, v74
Mvaa 120,248,168
Tv 226,243,1800
v_mul_f32 v75, s82, v75
Mvaa 120,252,172
Tv 228,243,2824
Pk 214,72,73
Mvaa 124,252,188
Tv 230,243,3848
Pk 215,74,75
Mvaa 124,248,184
Tv 232,244,776
v_mul_f32 v80, s82, v80
Mvaa 124,244,180
Tv 234,244,1800
v_mul_f32 v81, s82, v81
Mvaa 124,240,176
Tv 236,244,2824
v_mul_f32 v82, s82, v82
Mvaa 128,240,192
Tv 238,244,3848
v_mul_f32 v83, s82, v83
Mvaa 128,244,196
Pk 168,80,81
Pk 169,82,83
Mvaa 128,248,200
v_mul_f32 v100, s82, v100
v_mul_f32 v101, s82, v101
Mvaa 128,252,204
buffer_load_dwordx4 v242, s[36:39], s79 offen offset:2048 nt lds
buffer_load_dwordx4 v241, s[36:39], s79 offen offset:3072 nt lds
Mvaa 132,252,220
v_mul_f32 v102, s82, v102
v_mul_f32 v103, s82, v103
Mvaa 132,248,216
Pk 170,100,101
Pk 171,102,103
Mvaa 132,244,212
v_mul_f32 v84, s82, v84
v_mul_f32 v85, s82, v85
Mvaa 132,240,208
Wl 0
v_mul_f32 v86, s82, v86
v_mul_f32 v87, s82, v87
Mvva 140,224,160
s_mov_b64 exec, 0
s_swappc_b64 s[12:13], s[14:15]
s_add_u32 s14, s14, 4096
s_addc_u32 s15, s15, 0
s_waitcnt vmcnt(8)
s_barrier
Pk 184,84,85
Pk 185,86,87
Mvva 140,228,164
Ta 240,243,16512
v_mul_f32 v104, s82, v104
Mvva 140,232,168
Ta 242,243,17536
v_mul_f32 v105, s82, v105
Mvva 140,236,172
Ta 244,243,18560
v_mul_f32 v106, s82, v106
Mvva 144,236,188
Ta 246,243,19584
v_mul_f32 v107, s82, v107
Mvva 144,232,184
Ta 248,244,16512
Pk 186,104,105
Mvva 144,228,180
Ta 250,244,17536
Pk 187,106,107
Mvva 144,224,176
Ta 252,244,18560
v_mul_f32 v88, s82, v88
Mvva 148,224,192
Ta 254,244,19584
v_mul_f32 v89, s82, v89
Mvva 148,228,196
v_mul_f32 v90, s82, v90
v_mul_f32 v91, s82, v91
Mvva 148,232,200
Pk 200,88,89
Pk 201,90,91
Mvva 148,236,204
v_mul_f32 v108, s82, v108
v_mul_f32 v109, s82, v109
Mvva 152,236,220
v_mul_f32 v110, s82, v110
v_mul_f32 v111, s82, v111
Mvva 152,232,216
Pk 202,108,109
Pk 203,110,111
Mvva 152,228,212
v_mul_f32 v92, s82, v92
v_mul_f32 v93, s82, v93
Mvva 152,224,208
Wl 0
v_mul_f32 v94, s82, v94
v_mul_f32 v95, s82, v95
Maaa 0,240,160
Pk 216,92,93
Pk 217,94,95
Maaa 0,244,164
Tv 224,243,16520
v_mul_f32 v112, s82, v112
Maaa 0,248,168
Tv 226,243,17544
v_mul_f32 v113, s82, v113
Maaa 0,252,172
Tv 228,243,18568
v_mul_f32 v114, s82, v114
Maaa 4,252,188
Tv 230,243,19592
v_mul_f32 v115, s82, v115
Maaa 4,248,184
Tv 232,244,16520
Pk 218,112,113
Maaa 4,244,180
Tv 234,244,17544
Pk 219,114,115
Maaa 4,240,176
Tv 236,244,18568
v_mul_f32 v120, s82, v120
Maaa 8,240,192
Tv 238,244,19592
v_mul_f32 v121, s82, v121
Maaa 8,244,196
s_add_u32 m0, s77, 82560
v_mul_f32 v122, s82, v122
v_mul_f32 v123, s82, v123
Maaa 8,248,200
s_mov_b32 s79, 0
Pk 172,120,121
Pk 173,122,123
Maaa 8,252,204
buffer_load_dwordx4 v242, s[40:43], s79 offen offset:0 nt lds
buffer_load_dwordx4 v241, s[40:43], s79 offen offset:1024 nt lds
Maaa 12,252,220
v_mul_f32 v140, s82, v140
v_mul_f32 v141, s82, v141
Maaa 12,248,216
v_mul_f32 v142, s82, v142
v_mul_f32 v143, s82, v143
Maaa 12,244,212
Pk 174,140,141
Pk 175,142,143
Maaa 12,240,208
Wl 0
v_mul_f32 v124, s82, v124
v_mul_f32 v125, s82, v125
Mava 20,224,160
v_mul_f32 v126, s82, v126
v_mul_f32 v127, s82, v127
Mava 20,228,164
Ta 240,243,16768
Pk 188,124,125
Mava 20,232,168
Ta 242,243,17792
Pk 189,126,127
Mava 20,236,172
Ta 244,243,18816
v_mul_f32 v144, s82, v144
Mava 24,236,188
Ta 246,243,19840
v_mul_f32 v145, s82, v145
Mava 24,232,184
Ta 248,244,16768
v_mul_f32 v146, s82, v146
Mava 24,228,180
Ta 250,244,17792
v_mul_f32 v147, s82, v147
Mava 24,224,176
Ta 252,244,18816
Pk 190,144,145
Mava 28,224,192
Ta 254,244,19840
Pk 191,146,147
Mava 28,228,196
v_mul_f32 v128, s82, v128
v_mul_f32 v129, s82, v129
Mava 28,232,200
v_mul_f32 v130, s82, v130
v_mul_f32 v131, s82, v131
Mava 28,236,204
buffer_load_dwordx4 v242, s[40:43], s79 offen offset:2048 nt lds
buffer_load_dwordx4 v241, s[40:43], s79 offen offset:3072 nt lds
Mava 32,236,220
Pk 204,128,129
Pk 205,130,131
Mava 32,232,216
v_mul_f32 v148, s82, v148
v_mul_f32 v149, s82, v149
Mava 32,228,212
v_mul_f32 v150, s82, v150
v_mul_f32 v151, s82, v151
Mava 32,224,208
Wl 0
Pk 206,148,149
Pk 207,150,151
Maaa 40,240,160
v_mul_f32 v132, s82, v132
v_mul_f32 v133, s82, v133
Maaa 40,244,164
Tv 224,243,16776
v_mul_f32 v134, s82, v134
Maaa 40,248,168
Tv 226,243,17800
v_mul_f32 v135, s82, v135
Maaa 40,252,172
Tv 228,243,18824
Pk 220,132,133
Maaa 44,252,188
Tv 230,243,19848
Pk 221,134,135
Maaa 44,248,184
Tv 232,244,16776
v_mul_f32 v152, s82, v152
Maaa 44,244,180
Tv 234,244,17800
v_mul_f32 v153, s82, v153
Maaa 44,240,176
Tv 236,244,18824
v_mul_f32 v154, s82, v154
Maaa 48,240,192
Tv 238,244,19848
v_mul_f32 v155, s82, v155
Maaa 48,244,196
Pk 222,152,153
Pk 223,154,155
Maaa 48,248,200
Ar 0,0
Ar 1,1
Maaa 48,252,204
Ar 2,2
Ar 3,3
Maaa 52,252,220
Ar 4,20
Ar 5,21
Maaa 52,248,216
Ar 6,22
Ar 7,23
Maaa 52,244,212
v_mul_f32 v0, s82, v0
v_mul_f32 v1, s82, v1
Maaa 52,240,208
Wl 0
v_mul_f32 v2, s82, v2
v_mul_f32 v3, s82, v3
Mava 60,224,160
v_mul_f32 v4, s82, v4
v_mul_f32 v5, s82, v5
Mava 60,228,164
Ta 240,243,17024
v_mul_f32 v6, s82, v6
Mava 60,232,168
Ta 242,243,18048
v_mul_f32 v7, s82, v7
Mava 60,236,172
Ta 244,243,19072
Pk 80,0,1
Mava 64,236,188
Ta 246,243,20096
Pk 81,2,3
Mava 64,232,184
Ta 248,244,17024
Pk 82,4,5
Mava 64,228,180
Ta 250,244,18048
Pk 83,6,7
Mava 64,224,176
Ta 252,244,19072
Ar 8,4
Mava 68,224,192
Ta 254,244,20096
Ar 9,5
Mava 68,228,196
s_add_u32 m0, s80, 33024
Ar 10,6
Ar 11,7
Mava 68,232,200
s_mov_b32 s79, 4096
Ar 12,24
Ar 13,25
Mava 68,236,204
buffer_load_dwordx4 v242, s[36:39], s79 offen offset:0 nt lds
buffer_load_dwordx4 v241, s[36:39], s79 offen offset:1024 nt lds
Mava 72,236,220
Ar 14,26
Ar 15,27
Mava 72,232,216
v_mul_f32 v8, s82, v8
v_mul_f32 v9, s82, v9
Mava 72,228,212
v_mul_f32 v10, s82, v10
v_mul_f32 v11, s82, v11
Mava 72,224,208
Wl 0
v_mul_f32 v12, s82, v12
v_mul_f32 v13, s82, v13
Maaa 80,240,160
v_mul_f32 v14, s82, v14
v_mul_f32 v15, s82, v15
Maaa 80,244,164
Tv 224,243,17032
Pk 84,8,9
Maaa 80,248,168
Tv 226,243,18056
Pk 85,10,11
Maaa 80,252,172
Tv 228,243,19080
Pk 86,12,13
Maaa 84,252,188
Tv 230,243,20104
Pk 87,14,15
Maaa 84,248,184
Tv 232,244,17032
Ar 0,8
Maaa 84,244,180
Tv 234,244,18056
)A"
R"A(Ar 1,9
Maaa 84,240,176
Tv 236,244,19080
Ar 2,10
Maaa 88,240,192
Tv 238,244,20104
Ar 3,11
Maaa 88,244,196
Ar 4,28
Ar 5,29
Maaa 88,248,200
Ar 6,30
Ar 7,31
Maaa 88,252,204
buffer_load_dwordx4 v242, s[36:39], s79 offen offset:2048 nt lds
buffer_load_dwordx4 v241, s[36:39], s79 offen offset:3072 nt lds
Maaa 92,252,220
v_mul_f32 v0, s82, v0
v_mul_f32 v1, s82, v1
Maaa 92,248,216
v_mul_f32 v2, s82, v2
v_mul_f32 v3, s82, v3
Maaa 92,244,212
v_mul_f32 v4, s82, v4
v_mul_f32 v5, s82, v5
Maaa 92,240,208
Wl 0
v_mul_f32 v6, s82, v6
v_mul_f32 v7, s82, v7
Mava 100,224,160
Pk 88,0,1
Pk 89,2,3
Mava 100,228,164
Ta 240,243,17280
Pk 90,4,5
Mava 100,232,168
Ta 242,243,18304
Pk 91,6,7
Mava 100,236,172
Ta 244,243,19328
Ar 8,12
Mava 104,236,188
Ta 246,243,20352
Ar 9,13
Mava 104,232,184
Ta 248,244,17280
Ar 10,14
Mava 104,228,180
Ta 250,244,18304
Ar 11,15
Mava 104,224,176
Ta 252,244,19328
Ar 12,32
Mava 108,224,192
Ta 254,244,20352
Ar 13,33
Mava 108,228,196
Ar 14,34
Ar 15,35
Mava 108,232,200
v_mul_f32 v8, s82, v8
v_mul_f32 v9, s82, v9
Mava 108,236,204
v_mul_f32 v10, s82, v10
v_mul_f32 v11, s82, v11
Mava 112,236,220
v_mul_f32 v12, s82, v12
v_mul_f32 v13, s82, v13
Mava 112,232,216
v_mul_f32 v14, s82, v14
v_mul_f32 v15, s82, v15
Mava 112,228,212
Pk 92,8,9
Pk 93,10,11
Mava 112,224,208
Wl 0
Pk 94,12,13
Pk 95,14,15
Maaa 120,240,160
Ar 0,40
Ar 1,41
Maaa 120,244,164
Tv 224,243,17288
Ar 2,42
Maaa 120,248,168
Tv 226,243,18312
Ar 3,43
Maaa 120,252,172
Tv 228,243,19336
Ar 4,60
Maaa 124,252,188
Tv 230,243,20360
Ar 5,61
Maaa 124,248,184
Tv 232,244,17288
Ar 6,62
Maaa 124,244,180
Tv 234,244,18312
Ar 7,63
Maaa 124,240,176
Tv 236,244,19336
v_mul_f32 v0, s82, v0
Maaa 128,240,192
Tv 238,244,20360
v_mul_f32 v1, s82, v1
Maaa 128,244,196
s_add_u32 m0, s77, 99072
v_mul_f32 v2, s82, v2
v_mul_f32 v3, s82, v3
Maaa 128,248,200
s_mov_b32 s79, 1024
v_mul_f32 v4, s82, v4
v_mul_f32 v5, s82, v5
Maaa 128,252,204
buffer_load_dwordx4 v242, s[40:43], s79 offen offset:0 nt lds
buffer_load_dwordx4 v241, s[40:43], s79 offen offset:1024 nt lds
Maaa 132,252,220
v_mul_f32 v6, s82, v6
v_mul_f32 v7, s82, v7
Maaa 132,248,216
Pk 100,0,1
Pk 101,2,3
Maaa 132,244,212
Pk 102,4,5
Pk 103,6,7
Maaa 132,240,208
Wl 0
Ar 8,44
Ar 9,45
Mava 140,224,160
Ar 10,46
Ar 11,47
Mava 140,228,164
Ar 12,64
Ar 13,65
Mava 140,232,168
Ar 14,66
Ar 15,67
Mava 140,236,172
v_mul_f32 v8, s82, v8
v_mul_f32 v9, s82, v9
Mava 144,236,188
v_mul_f32 v10, s82, v10
v_mul_f32 v11, s82, v11
Mava 144,232,184
v_mul_f32 v12, s82, v12
v_mul_f32 v13, s82, v13
Mava 144,228,180
v_mul_f32 v14, s82, v14
v_mul_f32 v15, s82, v15
Mava 144,224,176
Pk 104,8,9
Pk 105,10,11
Mava 148,224,192
Pk 106,12,13
Pk 107,14,15
Mava 148,228,196
Ar 0,48
Ar 1,49
Mava 148,232,200
Ar 2,50
Ar 3,51
Mava 148,236,204
Ar 4,68
Ar 5,69
Mava 152,236,220
Ar 6,70
Ar 7,71
Mava 152,232,216
v_mul_f32 v0, s82, v0
v_mul_f32 v1, s82, v1
Mava 152,228,212
v_mul_f32 v2, s82, v2
v_mul_f32 v3, s82, v3
Mava 152,224,208
v_mul_f32 v4, s82, v4
v_mul_f32 v5, s82, v5
.p2align 3
v_mul_f32 v6, s82, v6
v_mul_f32 v7, s82, v7
Pk 108,0,1
Pk 109,2,3
Pk 110,4,5
Pk 111,6,7
s_nop 7
.p2align 3
Ar 28,52
Ar 29,53
Ar 30,54
Ar 31,55
Ar 32,72
Ar 33,73
Ar 34,74
Ar 35,75
v_pk_mul_f32 v[28:29], v[28:29], s[82:83]
v_pk_mul_f32 v[30:31], v[30:31], s[82:83]
v_pk_mul_f32 v[32:33], v[32:33], s[82:83]
v_pk_mul_f32 v[34:35], v[34:35], s[82:83]
Pk 112,28,29
Pk 113,30,31
Pk 114,32,33
Pk 115,34,35
Ar 20,80
buffer_load_dwordx4 v242, s[40:43], s79 offen offset:2048 nt lds
buffer_load_dwordx4 v241, s[40:43], s79 offen offset:3072 nt lds
Ar 21,81
Ar 22,82
Ar 23,83
Ar 24,100
Ar 25,101
Ar 26,102
Ar 27,103
v_pk_mul_f32 v[20:21], v[20:21], s[82:83]
v_pk_mul_f32 v[22:23], v[22:23], s[82:83]
v_pk_mul_f32 v[24:25], v[24:25], s[82:83]
v_pk_mul_f32 v[26:27], v[26:27], s[82:83]
Pk 40,20,21
Pk 41,22,23
Pk 42,24,25
Pk 43,26,27
Ar 28,84
Ar 29,85
Ar 30,86
Ar 31,87
Ar 32,104
Ar 33,105
Ar 34,106
Ar 35,107
v_pk_mul_f32 v[28:29], v[28:29], s[82:83]
s_add_u32 m0, s80, 49536
s_mov_b32 s79, 5120
buffer_load_dwordx4 v242, s[36:39], s79 offen offset:0 nt lds
buffer_load_dwordx4 v241, s[36:39], s79 offen offset:1024 nt lds
v_pk_mul_f32 v[30:31], v[30:31], s[82:83]
v_pk_mul_f32 v[32:33], v[32:33], s[82:83]
v_pk_mul_f32 v[34:35], v[34:35], s[82:83]
Pk 44,28,29
Pk 45,30,31
Pk 46,32,33
Pk 47,34,35
Ar 20,88
Ar 21,89
Ar 22,90
Ar 23,91
Ar 24,108
Ar 25,109
Ar 26,110
Ar 27,111
v_pk_mul_f32 v[20:21], v[20:21], s[82:83]
v_pk_mul_f32 v[22:23], v[22:23], s[82:83]
v_pk_mul_f32 v[24:25], v[24:25], s[82:83]
v_pk_mul_f32 v[26:27], v[26:27], s[82:83]
Pk 48,20,21
Pk 49,22,23
Pk 50,24,25
Pk 51,26,27
Ar 28,92
buffer_load_dwordx4 v242, s[36:39], s79 offen offset:2048 nt lds
buffer_load_dwordx4 v241, s[36:39], s79 offen offset:3072 nt lds
Ar 29,93
Ar 30,94
Ar 31,95
Ar 32,112
Ar 33,113
Ar 34,114
Ar 35,115
v_pk_mul_f32 v[28:29], v[28:29], s[82:83]
v_pk_mul_f32 v[30:31], v[30:31], s[82:83]
v_pk_mul_f32 v[32:33], v[32:33], s[82:83]
v_pk_mul_f32 v[34:35], v[34:35], s[82:83]
Pk 52,28,29
Pk 53,30,31
Pk 54,32,33
Pk 55,34,35
Ar 20,120
Ar 21,121
Ar 22,122
Ar 23,123
Ar 24,140
Ar 25,141
Ar 26,142
Ar 27,143
v_pk_mul_f32 v[20:21], v[20:21], s[82:83]
s_add_u32 m0, s77, 115584
s_mov_b32 s79, 4096
buffer_load_dwordx4 v242, s[40:43], s79 offen offset:0 nt lds
buffer_load_dwordx4 v241, s[40:43], s79 offen offset:1024 nt lds
v_pk_mul_f32 v[22:23], v[22:23], s[82:83]
v_pk_mul_f32 v[24:25], v[24:25], s[82:83]
v_pk_mul_f32 v[26:27], v[26:27], s[82:83]
Pk 60,20,21
Pk 61,22,23
Pk 62,24,25
Pk 63,26,27
Ar 28,124
Ar 29,125
Ar 30,126
Ar 31,127
Ar 32,144
Ar 33,145
Ar 34,146
Ar 35,147
v_pk_mul_f32 v[28:29], v[28:29], s[82:83]
v_pk_mul_f32 v[30:31], v[30:31], s[82:83]
v_pk_mul_f32 v[32:33], v[32:33], s[82:83]
v_pk_mul_f32 v[34:35], v[34:35], s[82:83]
Pk 64,28,29
Pk 65,30,31
Pk 66,32,33
Pk 67,34,35
buffer_load_dwordx4 v242, s[40:43], s79 offen offset:2048 nt lds
buffer_load_dwordx4 v241, s[40:43], s79 offen offset:3072 nt lds
s_waitcnt vmcnt(20)
s_barrier
Ra 160,240,0
Ra 164,240,256
Ra 168,240,512
Ra 172,240,768
ds_read_b32 v36, v244 offset:33024
Ar 20,128
Ar 21,129
Ar 22,130
Ar 23,131
Ar 24,148
Ar 25,149
Ar 26,150
Ar 27,151
v_pk_mul_f32 v[20:21], v[20:21], s[82:83]
v_pk_mul_f32 v[22:23], v[22:23], s[82:83]
v_pk_mul_f32 v[24:25], v[24:25], s[82:83]
v_pk_mul_f32 v[26:27], v[26:27], s[82:83]
Pk 68,20,21
Pk 69,22,23
Pk 70,24,25
Pk 71,26,27
Ar 28,132
Ar 29,133
Ar 30,134
Ar 31,135
Ar 32,152
Ar 33,153
Ar 34,154
Ar 35,155
v_pk_mul_f32 v[28:29], v[28:29], s[82:83]
v_pk_mul_f32 v[30:31], v[30:31], s[82:83]
v_pk_mul_f32 v[32:33], v[32:33], s[82:83]
v_pk_mul_f32 v[34:35], v[34:35], s[82:83]
Pk 72,28,29
Pk 73,30,31
Pk 74,32,33
Pk 75,34,35
Wl 0
Zvav 0,160,160
v_readfirstlane_b32 s28, v36
v_accvgpr_write_b32 a192, v80
Mvav 0,164,164
v_accvgpr_write_b32 a193, v81
v_accvgpr_write_b32 a194, v82
Mvav 0,168,168
s_add_u32 s96, s96, s28
v_accvgpr_write_b32 a195, v83
v_accvgpr_write_b32 a208, v84
Mvav 0,172,172
s_lshl_b32 s66, s28, 18
v_accvgpr_write_b32 a209, v85
v_accvgpr_write_b32 a210, v86
Zvav 4,172,188
s_xor_b32 s77, s24, 4
v_accvgpr_write_b32 a211, v87
v_accvgpr_write_b32 a224, v88
Mvav 4,168,184
s_lshl_b32 s77, s77, 10
v_accvgpr_write_b32 a225, v89
v_accvgpr_write_b32 a226, v90
Mvav 4,164,180
s_add_u32 s77, s77, 64
v_accvgpr_write_b32 a227, v91
v_accvgpr_write_b32 a240, v92
Mvav 4,160,176
s_xor_b32 s78, s24, 8
v_accvgpr_write_b32 a241, v93
v_accvgpr_write_b32 a242, v94
Zvav 8,160,192
s_lshl_b32 s78, s78, 10
v_accvgpr_write_b32 a243, v95
v_accvgpr_write_b32 a196, v100
Mvav 8,164,196
s_add_u32 s78, s78, 128
v_accvgpr_write_b32 a197, v101
v_accvgpr_write_b32 a198, v102
Mvav 8,168,200
s_xor_b32 s80, s24, 12
v_accvgpr_write_b32 a199, v103
v_accvgpr_write_b32 a212, v104
Mvav 8,172,204
s_lshl_b32 s80, s80, 10
v_accvgpr_write_b32 a213, v105
v_accvgpr_write_b32 a214, v106
Zvav 12,172,220
s_add_u32 s80, s80, 192
)A"
R"A(Ra 172,240,1792
v_accvgpr_write_b32 a215, v107
Mvav 12,168,216
Ra 168,240,1536
v_accvgpr_write_b32 a228, v108
Mvav 12,164,212
Ra 164,240,1280
v_accvgpr_write_b32 a229, v109
Mvav 12,160,208
Ra 160,240,1024
v_accvgpr_write_b32 a230, v110
.p2align 3
v_accvgpr_write_b32 a231, v111
v_accvgpr_write_b32 a244, v112
v_accvgpr_write_b32 a245, v113
v_accvgpr_write_b32 a246, v114
v_accvgpr_write_b32 a247, v115
v_accvgpr_write_b32 a200, v40
v_accvgpr_write_b32 a201, v41
v_accvgpr_write_b32 a202, v42
v_accvgpr_write_b32 a203, v43
v_accvgpr_write_b32 a216, v44
v_accvgpr_write_b32 a217, v45
v_accvgpr_write_b32 a218, v46
v_accvgpr_write_b32 a219, v47
v_accvgpr_write_b32 a232, v48
v_accvgpr_write_b32 a233, v49
v_accvgpr_write_b32 a234, v50
v_accvgpr_write_b32 a235, v51
v_accvgpr_write_b32 a248, v52
v_accvgpr_write_b32 a249, v53
v_accvgpr_write_b32 a250, v54
v_accvgpr_write_b32 a251, v55
v_accvgpr_write_b32 a204, v60
v_accvgpr_write_b32 a205, v61
v_accvgpr_write_b32 a206, v62
v_accvgpr_write_b32 a207, v63
v_accvgpr_write_b32 a220, v64
v_accvgpr_write_b32 a221, v65
v_accvgpr_write_b32 a222, v66
v_accvgpr_write_b32 a223, v67
v_accvgpr_write_b32 a236, v68
v_accvgpr_write_b32 a237, v69
v_accvgpr_write_b32 a238, v70
v_accvgpr_write_b32 a239, v71
v_accvgpr_write_b32 a252, v72
v_accvgpr_write_b32 a253, v73
v_accvgpr_write_b32 a254, v74
v_accvgpr_write_b32 a255, v75
v_lshrrev_b32 v243, 4, v245
.p2align 3
v_mul_u32_u24 v241, 4128, v243
v_bfe_u32 v243, v245, 2, 2
v_lshl_add_u32 v241, v243, 6, v241
v_and_b32 v243, 3, v245
.p2align 3
v_lshl_add_u32 v241, v243, 3, v241
v_add_u32 v241, 82560, v241
v_max3_f32 v120, v0, v1, v2
v_max_f32 v120, v120, v3
v_max3_f32 v124, v4, v5, v6
v_max_f32 v124, v124, v7
v_max3_f32 v128, v8, v9, v10
v_max_f32 v128, v128, v11
v_max3_f32 v132, v12, v13, v14
v_max_f32 v132, v132, v15
v_mov_b32 v121, v120
v_mov_b32 v125, v124
v_mov_b32 v129, v128
v_mov_b32 v133, v132
s_nop 0
v_permlane32_swap_b32 v120, v121
v_permlane32_swap_b32 v124, v125
v_permlane32_swap_b32 v128, v129
v_permlane32_swap_b32 v132, v133
v_max_f32 v120, v120, v121
v_max_f32 v124, v124, v125
v_max_f32 v128, v128, v129
v_max_f32 v132, v132, v133
v_mov_b32 v121, v120
v_mov_b32 v125, v124
v_mov_b32 v129, v128
v_mov_b32 v133, v132
s_nop 0
v_permlane16_swap_b32 v120, v121
v_permlane16_swap_b32 v124, v125
v_permlane16_swap_b32 v128, v129
v_permlane16_swap_b32 v132, v133
v_max_f32 v120, v120, v121
v_max_f32 v124, v124, v125
v_max_f32 v128, v128, v129
v_max_f32 v132, v132, v133
v_pk_mul_f32 v[122:123], v[120:121], -1.0 op_sel_hi:[0,0]
v_pk_mul_f32 v[120:121], v[120:121], -1.0 op_sel_hi:[0,0]
v_pk_add_f32 v[0:1], v[0:1], v[122:123]
v_pk_add_f32 v[2:3], v[2:3], v[122:123]
v_pk_mov_b32 v[140:141], 0, 0
v_pk_mul_f32 v[126:127], v[124:125], -1.0 op_sel_hi:[0,0]
v_pk_mul_f32 v[124:125], v[124:125], -1.0 op_sel_hi:[0,0]
v_pk_add_f32 v[4:5], v[4:5], v[126:127]
v_pk_add_f32 v[6:7], v[6:7], v[126:127]
v_pk_mov_b32 v[142:143], 0, 0
v_pk_mul_f32 v[130:131], v[128:129], -1.0 op_sel_hi:[0,0]
v_pk_mul_f32 v[128:129], v[128:129], -1.0 op_sel_hi:[0,0]
v_pk_add_f32 v[8:9], v[8:9], v[130:131]
v_pk_add_f32 v[10:11], v[10:11], v[130:131]
v_pk_mov_b32 v[144:145], 0, 0
v_pk_mul_f32 v[134:135], v[132:133], -1.0 op_sel_hi:[0,0]
v_pk_mul_f32 v[132:133], v[132:133], -1.0 op_sel_hi:[0,0]
v_pk_add_f32 v[12:13], v[12:13], v[134:135]
v_pk_add_f32 v[14:15], v[14:15], v[134:135]
v_pk_mov_b32 v[146:147], 0, 0
Wl 0
s_mov_b32 s44, 8192
s_mov_b32 s46, 5120
s_mov_b32 s68, 117760
s_mov_b32 s69, 3072
v_subrev_u32 v150, 768, v248
v_subrev_u32 v151, 768, v249
v_subrev_u32 v152, 768, v250
v_subrev_u32 v153, 768, v251
v_mov_b32 v254, 0x3f803f80
v_accvgpr_write_b32 a156, v254
v_accvgpr_write_b32 a157, v254
v_accvgpr_write_b32 a158, v254
v_accvgpr_write_b32 a159, v254
v_lshlrev_b32 v252, 12, v245
s_mov_b32 s48, 0
Cvav 20,160,160,120
s_barrier
Ex 0
Mvav 20,164,164
Ex 1
Mvav 20,168,168
Ex 2
Mvav 20,172,172
Pk 40,0,1
Cvav 24,172,188,124
Ex 3
Ex 4
Mvav 24,168,184
Pk 41,2,3
Mvav 24,164,180
Ex 5
Mvav 24,160,176
Ex 6
Cvav 28,160,192,128
Pk 48,4,5
Ex 7
Mvav 28,164,196
Ex 8
Mvav 28,168,200
Pk 49,6,7
Mvav 28,172,204
Ex 9
Cvav 32,172,220,132
Ra 172,240,2816
Ex 10
Pk 56,8,9
Mvav 32,168,216
Ra 168,240,2560
Mvav 32,164,212
Ra 164,240,2304
Mvav 32,160,208
Ra 160,240,2048
Ex 11
Ex 12
Pk 57,10,11
Ex 13
Ex 14
Pk 64,12,13
Ex 15
s_nop 0
Pk 65,14,15
Wl 0
Cvav 0,160,160,120
Ex 20
Mvav 0,164,164
s_mov_b32 m0, 0
s_nop 0
buffer_load_dwordx4 v248, s[36:39], s44 offen offset:0 nt lds
buffer_load_dwordx4 v150, s[36:39], s44 offen offset:1024 nt lds
Ex 21
Mvav 0,168,168
Ex 22
Mvav 0,172,172
s_mov_b32 m0, 4096
s_nop 0
buffer_load_dwordx4 v249, s[36:39], s44 offen offset:0 nt lds
buffer_load_dwordx4 v151, s[36:39], s44 offen offset:1024 nt lds
Pk 42,20,21
Cvav 4,172,188,124
Ex 23
Ex 24
Mvav 4,168,184
s_mov_b32 m0, 8224
s_nop 0
buffer_load_dwordx4 v250, s[36:39], s44 offen offset:0 nt lds
buffer_load_dwordx4 v152, s[36:39], s44 offen offset:1024 nt lds
Pk 43,22,23
Mvav 4,164,180
Ex 25
Mvav 4,160,176
s_mov_b32 m0, 12320
s_nop 0
buffer_load_dwordx4 v251, s[36:39], s44 offen offset:0 nt lds
buffer_load_dwordx4 v153, s[36:39], s44 offen offset:1024 nt lds
Ex 26
Cvav 8,160,192,128
Pk 50,24,25
Ex 27
Mvav 8,164,196
Ex 28
Mvav 8,168,200
Pk 51,26,27
Mvav 8,172,204
Ex 29
Cvav 12,172,220,132
Ra 172,240,3840
Ex 30
Pk 58,28,29
Mvav 12,168,216
Ra 168,240,3584
Mvav 12,164,212
Ra 164,240,3328
Mvav 12,160,208
Ra 160,240,3072
Ex 31
Ex 32
Pk 59,30,31
Ex 33
Ex 34
Pk 66,32,33
Ex 35
s_nop 0
Pk 67,34,35
Wl 0
Cvav 20,160,160,120
Ex 0
Mvav 20,164,164
Ex 1
Mvav 20,168,168
Ex 2
Mvav 20,172,172
Pk 44,0,1
Cvav 24,172,188,124
Ex 3
Ex 4
Mvav 24,168,184
Pk 45,2,3
Mvav 24,164,180
Ex 5
Mvav 24,160,176
Ex 6
Cvav 28,160,192,128
Pk 52,4,5
Ex 7
Mvav 28,164,196
Ex 8
Mvav 28,168,200
Pk 53,6,7
Mvav 28,172,204
Ex 9
Cvav 32,172,220,132
Ra 172,240,17280
Ex 10
Pk 60,8,9
Mvav 32,168,216
Ra 168,240,17024
Mvav 32,164,212
Ra 164,240,16768
Mvav 32,160,208
Ra 160,240,16512
Ex 11
Ex 12
Pk 61,10,11
Ex 13
Ex 14
Pk 68,12,13
Ex 15
s_nop 0
Pk 69,14,15
Zaav 16,156,40
Zaav 36,156,48
Zaav 56,156,56
Zaav 76,156,64
Wl 0
Cvav 0,160,160,120
Ex 20
Mvav 0,164,164
Ex 21
Mvav 0,168,168
Ex 22
Mvav 0,172,172
Pk 46,20,21
Cvav 4,172,188,124
Ex 23
Ex 24
Mvav 4,168,184
Pk 47,22,23
Mvav 4,164,180
Ex 25
Mvav 4,160,176
Ex 26
Cvav 8,160,192,128
Pk 54,24,25
Ex 27
Mvav 8,164,196
Ex 28
Mvav 8,168,200
Pk 55,26,27
Mvav 8,172,204
Ex 29
Cvav 12,172,220,132
Ra 172,240,18304
Ex 30
Pk 62,28,29
Mvav 12,168,216
Ra 168,240,18048
Mvav 12,164,212
Ra 164,240,17792
Mvav 12,160,208
Ra 160,240,17536
Ex 31
Ex 32
Pk 63,30,31
Ex 33
Ex 34
Pk 70,32,33
Ex 35
s_nop 0
Pk 71,34,35
Wl 0
s_waitcnt vmcnt(8)
s_mov_b32 s47, 0
Cvav 20,160,160,120
s_waitcnt vmcnt(62)
s_barrier
s_mov_b32 m0, 132096
Ex 0
Mvav 20,164,164
Ta 176,241,0
buffer_load_dwordx4 v248, s[40:43], s46 offen offset:0 nt lds
buffer_load_dwordx4 v150, s[40:43], s46 offen offset:1024 nt lds
Mvav 20,168,168
Ex 1
Mvav 20,172,172
Ta 178,241,1024
Pk 80,0,1
Cvav 24,172,188,124
Ex 2
Mvav 24,168,184
Ta 180,241,2048
Mvav 24,164,180
Ex 3
Mvav 24,160,176
Ta 182,241,3072
Pk 81,2,3
Cvav 28,160,192,128
Ex 4
Mvav 28,164,196
Ta 184,241,32
buffer_load_dwordx4 v248, s[40:43], s46 offen offset:2048 nt lds
buffer_load_dwordx4 v150, s[40:43], s46 offen offset:3072 nt lds
Mvav 28,168,200
Ex 5
Mvav 28,172,204
Ta 186,241,1056
Pk 88,4,5
Cvav 32,172,220,132
Ex 6
Mvav 32,168,216
Ta 188,241,2080
Mvav 32,164,212
Ex 7
Mvav 32,160,208
Wl 3
Ta 190,241,3104
Pk 89,6,7
Zaav 0,176,40
Ex 8
Maav 0,180,44
Ra 160,240,18560
Zaav 4,176,48
s_mov_b32 m0, 136224
Ex 9
Maav 4,180,52
Ra 164,240,18816
buffer_load_dwordx4 v249, s[40:43], s46 offen offset:0 nt lds
buffer_load_dwordx4 v151, s[40:43], s46 offen offset:1024 nt lds
Zaav 8,176,56
Ex 10
Maav 8,180,60
Ra 168,240,19072
Pk 96,8,9
Zaav 12,176,64
Ex 11
Maav 12,180,68
Wl 3
Ra 172,240,19328
)A"
R"A(Pk 97,10,11
Zaav 20,184,40
Ex 12
Maav 20,188,44
Zaav 24,184,48
Ex 13
Maav 24,188,52
buffer_load_dwordx4 v249, s[40:43], s46 offen offset:2048 nt lds
buffer_load_dwordx4 v151, s[40:43], s46 offen offset:3072 nt lds
Zaav 28,184,56
Ex 14
Maav 28,188,60
Pk 104,12,13
Zaav 32,184,64
Ex 15
Maav 32,188,68
Pk 105,14,15
Maav 16,156,44
Maav 36,156,52
Maav 56,156,60
Maav 76,156,68
Wl 0
Cvav 0,160,160,120
s_mov_b32 m0, 140352
Ex 20
Mvav 0,164,164
Ta 176,241,256
buffer_load_dwordx4 v250, s[40:43], s46 offen offset:0 nt lds
buffer_load_dwordx4 v152, s[40:43], s46 offen offset:1024 nt lds
Mvav 0,168,168
Ex 21
Mvav 0,172,172
Ta 178,241,1280
Pk 82,20,21
Cvav 4,172,188,124
Ex 22
Mvav 4,168,184
Ta 180,241,2304
Mvav 4,164,180
Ex 23
Mvav 4,160,176
Ta 182,241,3328
Pk 83,22,23
Cvav 8,160,192,128
Ex 24
Mvav 8,164,196
Ta 184,241,288
buffer_load_dwordx4 v250, s[40:43], s46 offen offset:2048 nt lds
buffer_load_dwordx4 v152, s[40:43], s46 offen offset:3072 nt lds
Mvav 8,168,200
Ex 25
Mvav 8,172,204
Ta 186,241,1312
Pk 90,24,25
Cvav 12,172,220,132
Ex 26
Mvav 12,168,216
Ta 188,241,2336
Mvav 12,164,212
Ex 27
Mvav 12,160,208
Wl 3
Ta 190,241,3360
Pk 91,26,27
Zaav 40,176,40
s_mov_b32 m0, 144480
Ex 28
Maav 40,180,44
Ra 160,240,19584
buffer_load_dwordx4 v251, s[40:43], s46 offen offset:0 nt lds
buffer_load_dwordx4 v153, s[40:43], s46 offen offset:1024 nt lds
Zaav 44,176,48
Ex 29
Maav 44,180,52
Ra 164,240,19840
Pk 98,28,29
Zaav 48,176,56
Ex 30
Maav 48,180,60
Ra 168,240,20096
Zaav 52,176,64
Ex 31
Maav 52,180,68
Wl 3
Ra 172,240,20352
Pk 99,30,31
Zaav 60,184,40
Ex 32
Maav 60,188,44
buffer_load_dwordx4 v251, s[40:43], s46 offen offset:2048 nt lds
buffer_load_dwordx4 v153, s[40:43], s46 offen offset:3072 nt lds
Zaav 64,184,48
Ex 33
Maav 64,188,52
Pk 106,32,33
Zaav 68,184,56
Ex 34
Maav 68,188,60
Zaav 72,184,64
Ex 35
Maav 72,188,68
Wl 0
Pk 107,34,35
Cvav 20,160,160,120
s_add_u32 s46, s46, s69
s_xor_b32 s69, s69, 114688
s_mov_b32 m0, 0
Ex 0
Mvav 20,164,164
Ta 176,241,512
buffer_load_dwordx4 v248, s[36:39], s44 offen offset:2048 nt lds
buffer_load_dwordx4 v150, s[36:39], s44 offen offset:3072 nt lds
Mvav 20,168,168
Ex 1
Mvav 20,172,172
Ta 178,241,1536
Pk 84,0,1
Cvav 24,172,188,124
Ex 2
Mvav 24,168,184
Ta 180,241,2560
Mvav 24,164,180
Ex 3
Mvav 24,160,176
Ta 182,241,3584
Pk 85,2,3
Cvav 28,160,192,128
s_mov_b32 m0, 4096
Ex 4
Mvav 28,164,196
Ta 184,241,544
buffer_load_dwordx4 v249, s[36:39], s44 offen offset:2048 nt lds
buffer_load_dwordx4 v151, s[36:39], s44 offen offset:3072 nt lds
Mvav 28,168,200
Ex 5
Mvav 28,172,204
Ta 186,241,1568
Pk 92,4,5
Cvav 32,172,220,132
Ex 6
Mvav 32,168,216
Ta 188,241,2592
Mvav 32,164,212
Ex 7
Mvav 32,160,208
Wl 3
Ta 190,241,3616
Pk 93,6,7
Zaav 80,176,40
Ex 8
Maav 80,180,44
Ra 160,240,33024
Zaav 84,176,48
s_mov_b32 m0, 8224
Ex 9
Maav 84,180,52
Ra 164,240,33280
buffer_load_dwordx4 v250, s[36:39], s44 offen offset:2048 nt lds
buffer_load_dwordx4 v152, s[36:39], s44 offen offset:3072 nt lds
Zaav 88,176,56
Ex 10
Maav 88,180,60
Ra 168,240,33536
Pk 100,8,9
Zaav 92,176,64
Ex 11
Maav 92,180,68
Wl 3
Ra 172,240,33792
Pk 101,10,11
Zaav 100,184,40
Ex 12
Maav 100,188,44
Zaav 104,184,48
s_mov_b32 m0, 12320
Ex 13
Maav 104,188,52
buffer_load_dwordx4 v251, s[36:39], s44 offen offset:2048 nt lds
buffer_load_dwordx4 v153, s[36:39], s44 offen offset:3072 nt lds
Zaav 108,184,56
Ex 14
Maav 108,188,60
Pk 108,12,13
Zaav 112,184,64
Ex 15
Maav 112,188,68
Pk 109,14,15
Maav 16,156,80
Maav 36,156,88
Maav 56,156,96
Maav 76,156,104
Wl 0
Cvav 0,160,160,120
s_add_u32 s44, s44, 1024
s_mov_b32 m0, 16512
Ex 20
Mvav 0,164,164
Ta 176,241,768
buffer_load_dwordx4 v248, s[36:39], s44 offen offset:0 nt lds
buffer_load_dwordx4 v150, s[36:39], s44 offen offset:1024 nt lds
Mvav 0,168,168
Ex 21
Mvav 0,172,172
Ta 178,241,1792
Pk 86,20,21
Cvav 4,172,188,124
Ex 22
Mvav 4,168,184
Ta 180,241,2816
Mvav 4,164,180
Ex 23
Mvav 4,160,176
Ta 182,241,3840
Pk 87,22,23
Cvav 8,160,192,128
s_mov_b32 m0, 20608
Ex 24
Mvav 8,164,196
Ta 184,241,800
buffer_load_dwordx4 v249, s[36:39], s44 offen offset:0 nt lds
buffer_load_dwordx4 v151, s[36:39], s44 offen offset:1024 nt lds
Mvav 8,168,200
Ex 25
Mvav 8,172,204
Ta 186,241,1824
Pk 94,24,25
Cvav 12,172,220,132
Ex 26
Mvav 12,168,216
Ta 188,241,2848
Mvav 12,164,212
Ex 27
Mvav 12,160,208
Wl 3
Ta 190,241,3872
Pk 95,26,27
Zaav 120,176,40
s_mov_b32 m0, 24736
Ex 28
Maav 120,180,44
Ra 160,240,34048
buffer_load_dwordx4 v250, s[36:39], s44 offen offset:0 nt lds
buffer_load_dwordx4 v152, s[36:39], s44 offen offset:1024 nt lds
Zaav 124,176,48
Ex 29
Maav 124,180,52
Ra 164,240,34304
Pk 102,28,29
Zaav 128,176,56
Ex 30
Maav 128,180,60
Ra 168,240,34560
Zaav 132,176,64
Ex 31
Maav 132,180,68
Wl 3
Ra 172,240,34816
Pk 103,30,31
Zaav 140,184,40
s_mov_b32 m0, 28832
Ex 32
Maav 140,188,44
buffer_load_dwordx4 v251, s[36:39], s44 offen offset:0 nt lds
buffer_load_dwordx4 v153, s[36:39], s44 offen offset:1024 nt lds
Zaav 144,184,48
Ex 33
Maav 144,188,52
Pk 110,32,33
Zaav 148,184,56
Ex 34
Maav 148,188,60
Zaav 152,184,64
Ex 35
Maav 152,188,68
Wl 0
Pk 111,34,35
s_add_u32 s14, s8, PG0_%=-PC0_%=+4096*10
s_addc_u32 s15, s9, 0
LOOPB0_%=:
Cvav 20,160,160,120
s_waitcnt vmcnt(62)
s_barrier
s_mov_b32 m0, 82560
Ex 0
Mvav 20,164,164
Ta 176,241,16512
buffer_load_dwordx4 v248, s[40:43], s46 offen offset:0 nt lds
buffer_load_dwordx4 v150, s[40:43], s46 offen offset:1024 nt lds
Mvav 20,168,168
Ex 1
Mvav 20,172,172
Ta 178,241,17536
Pk 40,0,1
Cvav 24,172,188,124
Ex 2
Mvav 24,168,184
Ta 180,241,18560
Mvav 24,164,180
Ex 3
Mvav 24,160,176
Ta 182,241,19584
Pk 41,2,3
Cvav 28,160,192,128
Ex 4
Mvav 28,164,196
Ta 184,241,16544
buffer_load_dwordx4 v248, s[40:43], s46 offen offset:2048 nt lds
buffer_load_dwordx4 v150, s[40:43], s46 offen offset:3072 nt lds
Mvav 28,168,200
Ex 5
Mvav 28,172,204
Ta 186,241,17568
Pk 48,4,5
Cvav 32,172,220,132
Ex 6
Mvav 32,168,216
Ta 188,241,18592
Mvav 32,164,212
Ex 7
Mvav 32,160,208
Wl 3
Ta 190,241,19616
Pk 49,6,7
Maav 0,176,80
Ex 8
Maav 0,180,84
Ra 160,240,35072
Maav 4,176,88
s_mov_b32 m0, 86688
Ex 9
Maav 4,180,92
Ra 164,240,35328
buffer_load_dwordx4 v249, s[40:43], s46 offen offset:0 nt lds
buffer_load_dwordx4 v151, s[40:43], s46 offen offset:1024 nt lds
Maav 8,176,96
Ex 10
Maav 8,180,100
Ra 168,240,35584
Pk 56,8,9
Maav 12,176,104
Ex 11
Maav 12,180,108
Wl 3
Ra 172,240,35840
Pk 57,10,11
Maav 20,184,80
Ex 12
Maav 20,188,84
Maav 24,184,88
Ex 13
Maav 24,188,92
buffer_load_dwordx4 v249, s[40:43], s46 offen offset:2048 nt lds
buffer_load_dwordx4 v151, s[40:43], s46 offen offset:3072 nt lds
Maav 28,184,96
Ex 14
Maav 28,188,100
Pk 64,12,13
Maav 32,184,104
Ex 15
Maav 32,188,108
Pk 65,14,15
Maav 16,156,84
Maav 36,156,92
Maav 56,156,100
Maav 76,156,108
Wl 0
Cvav 0,160,160,120
s_mov_b32 m0, 90816
Ex 20
Mvav 0,164,164
Ta 176,241,16768
buffer_load_dwordx4 v250, s[40:43], s46 offen offset:0 nt lds
buffer_load_dwordx4 v152, s[40:43], s46 offen offset:1024 nt lds
Mvav 0,168,168
Ex 21
Mvav 0,172,172
Ta 178,241,17792
Pk 42,20,21
Cvav 4,172,188,124
Ex 22
Mvav 4,168,184
Ta 180,241,18816
Mvav 4,164,180
Ex 23
Mvav 4,160,176
Ta 182,241,19840
Pk 43,22,23
Cvav 8,160,192,128
Ex 24
Mvav 8,164,196
Ta 184,241,16800
buffer_load_dwordx4 v250, s[40:43], s46 offen offset:2048 nt lds
buffer_load_dwordx4 v152, s[40:43], s46 offen offset:3072 nt lds
Mvav 8,168,200
Ex 25
Mvav 8,172,204
Ta 186,241,17824
Pk 50,24,25
Cvav 12,172,220,132
Ex 26
)A"
R"A(Mvav 12,168,216
Ta 188,241,18848
Mvav 12,164,212
Ex 27
Mvav 12,160,208
Wl 3
Ta 190,241,19872
Pk 51,26,27
Maav 40,176,80
s_mov_b32 m0, 94944
Ex 28
Maav 40,180,84
Ra 160,240,36096
buffer_load_dwordx4 v251, s[40:43], s46 offen offset:0 nt lds
buffer_load_dwordx4 v153, s[40:43], s46 offen offset:1024 nt lds
Maav 44,176,88
Ex 29
Maav 44,180,92
Ra 164,240,36352
Pk 58,28,29
Maav 48,176,96
Ex 30
Maav 48,180,100
Ra 168,240,36608
Maav 52,176,104
Ex 31
Maav 52,180,108
Wl 3
Ra 172,240,36864
Pk 59,30,31
Maav 60,184,80
Ex 32
Maav 60,188,84
buffer_load_dwordx4 v251, s[40:43], s46 offen offset:2048 nt lds
buffer_load_dwordx4 v153, s[40:43], s46 offen offset:3072 nt lds
Maav 64,184,88
Ex 33
Maav 64,188,92
Pk 66,32,33
Maav 68,184,96
Ex 34
Maav 68,188,100
Maav 72,184,104
Ex 35
Maav 72,188,108
Wl 0
Pk 67,34,35
s_mov_b64 exec, 0
s_swappc_b64 s[12:13], s[14:15]
s_add_u32 s14, s14, 4096
s_addc_u32 s15, s15, 0
Cvav 20,160,160,120
s_add_u32 s46, s46, 1024
s_mov_b32 m0, 16512
Ex 0
Mvav 20,164,164
Ta 176,241,17024
buffer_load_dwordx4 v248, s[36:39], s44 offen offset:2048 nt lds
buffer_load_dwordx4 v150, s[36:39], s44 offen offset:3072 nt lds
Mvav 20,168,168
Ex 1
Mvav 20,172,172
Ta 178,241,18048
Pk 44,0,1
Cvav 24,172,188,124
Ex 2
Mvav 24,168,184
Ta 180,241,19072
Mvav 24,164,180
Ex 3
Mvav 24,160,176
Ta 182,241,20096
Pk 45,2,3
Cvav 28,160,192,128
s_mov_b32 m0, 20608
Ex 4
Mvav 28,164,196
Ta 184,241,17056
buffer_load_dwordx4 v249, s[36:39], s44 offen offset:2048 nt lds
buffer_load_dwordx4 v151, s[36:39], s44 offen offset:3072 nt lds
Mvav 28,168,200
Ex 5
Mvav 28,172,204
Ta 186,241,18080
Pk 52,4,5
Cvav 32,172,220,132
Ex 6
Mvav 32,168,216
Ta 188,241,19104
Mvav 32,164,212
Ex 7
Mvav 32,160,208
Wl 3
Ta 190,241,20128
Pk 53,6,7
Maav 80,176,80
Ex 8
Maav 80,180,84
Ra 160,240,49536
Maav 84,176,88
s_mov_b32 m0, 24736
Ex 9
Maav 84,180,92
Ra 164,240,49792
buffer_load_dwordx4 v250, s[36:39], s44 offen offset:2048 nt lds
buffer_load_dwordx4 v152, s[36:39], s44 offen offset:3072 nt lds
Maav 88,176,96
Ex 10
Maav 88,180,100
Ra 168,240,50048
Pk 60,8,9
Maav 92,176,104
Ex 11
Maav 92,180,108
Wl 3
Ra 172,240,50304
Pk 61,10,11
Maav 100,184,80
Ex 12
Maav 100,188,84
Maav 104,184,88
s_mov_b32 m0, 28832
Ex 13
Maav 104,188,92
buffer_load_dwordx4 v251, s[36:39], s44 offen offset:2048 nt lds
buffer_load_dwordx4 v153, s[36:39], s44 offen offset:3072 nt lds
Maav 108,184,96
Ex 14
Maav 108,188,100
Pk 68,12,13
Maav 112,184,104
Ex 15
Maav 112,188,108
Pk 69,14,15
Maav 16,156,40
Maav 36,156,48
Maav 56,156,56
Maav 76,156,64
Wl 0
Cvav 0,160,160,120
s_add_u32 s44, s44, 3072
s_mov_b32 m0, 33024
Ex 20
Mvav 0,164,164
Ta 176,241,17280
buffer_load_dwordx4 v248, s[36:39], s44 offen offset:0 nt lds
buffer_load_dwordx4 v150, s[36:39], s44 offen offset:1024 nt lds
Mvav 0,168,168
Ex 21
Mvav 0,172,172
Ta 178,241,18304
Pk 46,20,21
Cvav 4,172,188,124
Ex 22
Mvav 4,168,184
Ta 180,241,19328
Mvav 4,164,180
Ex 23
Mvav 4,160,176
Ta 182,241,20352
Pk 47,22,23
Cvav 8,160,192,128
s_mov_b32 m0, 37120
Ex 24
Mvav 8,164,196
Ta 184,241,17312
buffer_load_dwordx4 v249, s[36:39], s44 offen offset:0 nt lds
buffer_load_dwordx4 v151, s[36:39], s44 offen offset:1024 nt lds
Mvav 8,168,200
Ex 25
Mvav 8,172,204
Ta 186,241,18336
Pk 54,24,25
Cvav 12,172,220,132
Ex 26
Mvav 12,168,216
Ta 188,241,19360
Mvav 12,164,212
Ex 27
Mvav 12,160,208
Wl 3
Ta 190,241,20384
Pk 55,26,27
Maav 120,176,80
s_mov_b32 m0, 41248
Ex 28
Maav 120,180,84
Ra 160,240,50560
buffer_load_dwordx4 v250, s[36:39], s44 offen offset:0 nt lds
buffer_load_dwordx4 v152, s[36:39], s44 offen offset:1024 nt lds
Maav 124,176,88
Ex 29
Maav 124,180,92
Ra 164,240,50816
Pk 62,28,29
Maav 128,176,96
Ex 30
Maav 128,180,100
Ra 168,240,51072
Maav 132,176,104
Ex 31
Maav 132,180,108
Wl 3
Ra 172,240,51328
Pk 63,30,31
Maav 140,184,80
s_mov_b32 m0, 45344
Ex 32
Maav 140,188,84
buffer_load_dwordx4 v251, s[36:39], s44 offen offset:0 nt lds
buffer_load_dwordx4 v153, s[36:39], s44 offen offset:1024 nt lds
Maav 144,184,88
Ex 33
Maav 144,188,92
Pk 70,32,33
Maav 148,184,96
Ex 34
Maav 148,188,100
Maav 152,184,104
Ex 35
Maav 152,188,108
Wl 0
Pk 71,34,35
Cvav 20,160,160,120
s_waitcnt vmcnt(62)
s_barrier
s_mov_b32 m0, 99072
Ex 0
Mvav 20,164,164
Ta 176,241,33024
buffer_load_dwordx4 v248, s[40:43], s46 offen offset:0 nt lds
buffer_load_dwordx4 v150, s[40:43], s46 offen offset:1024 nt lds
Mvav 20,168,168
Ex 1
Mvav 20,172,172
Ta 178,241,34048
Pk 80,0,1
Cvav 24,172,188,124
Ex 2
Mvav 24,168,184
Ta 180,241,35072
Mvav 24,164,180
Ex 3
Mvav 24,160,176
Ta 182,241,36096
Pk 81,2,3
Cvav 28,160,192,128
Ex 4
Mvav 28,164,196
Ta 184,241,33056
buffer_load_dwordx4 v248, s[40:43], s46 offen offset:2048 nt lds
buffer_load_dwordx4 v150, s[40:43], s46 offen offset:3072 nt lds
Mvav 28,168,200
Ex 5
Mvav 28,172,204
Ta 186,241,34080
Pk 88,4,5
Cvav 32,172,220,132
Ex 6
Mvav 32,168,216
Ta 188,241,35104
Mvav 32,164,212
Ex 7
Mvav 32,160,208
Wl 3
Ta 190,241,36128
Pk 89,6,7
Maav 0,176,40
Ex 8
Maav 0,180,44
Ra 160,240,51584
Maav 4,176,48
s_mov_b32 m0, 103200
Ex 9
Maav 4,180,52
Ra 164,240,51840
buffer_load_dwordx4 v249, s[40:43], s46 offen offset:0 nt lds
buffer_load_dwordx4 v151, s[40:43], s46 offen offset:1024 nt lds
Maav 8,176,56
Ex 10
Maav 8,180,60
Ra 168,240,52096
Pk 96,8,9
Maav 12,176,64
Ex 11
Maav 12,180,68
Wl 3
Ra 172,240,52352
Pk 97,10,11
Maav 20,184,40
Ex 12
Maav 20,188,44
Maav 24,184,48
Ex 13
Maav 24,188,52
buffer_load_dwordx4 v249, s[40:43], s46 offen offset:2048 nt lds
buffer_load_dwordx4 v151, s[40:43], s46 offen offset:3072 nt lds
Maav 28,184,56
Ex 14
Maav 28,188,60
Pk 104,12,13
Maav 32,184,64
Ex 15
Maav 32,188,68
Pk 105,14,15
Maav 16,156,44
Maav 36,156,52
Maav 56,156,60
Maav 76,156,68
Wl 0
Cvav 0,160,160,120
s_mov_b32 m0, 107328
Ex 20
Mvav 0,164,164
Ta 176,241,33280
buffer_load_dwordx4 v250, s[40:43], s46 offen offset:0 nt lds
buffer_load_dwordx4 v152, s[40:43], s46 offen offset:1024 nt lds
Mvav 0,168,168
Ex 21
Mvav 0,172,172
Ta 178,241,34304
Pk 82,20,21
Cvav 4,172,188,124
Ex 22
Mvav 4,168,184
Ta 180,241,35328
Mvav 4,164,180
Ex 23
Mvav 4,160,176
Ta 182,241,36352
Pk 83,22,23
Cvav 8,160,192,128
Ex 24
Mvav 8,164,196
Ta 184,241,33312
buffer_load_dwordx4 v250, s[40:43], s46 offen offset:2048 nt lds
buffer_load_dwordx4 v152, s[40:43], s46 offen offset:3072 nt lds
Mvav 8,168,200
Ex 25
Mvav 8,172,204
Ta 186,241,34336
Pk 90,24,25
Cvav 12,172,220,132
Ex 26
Mvav 12,168,216
Ta 188,241,35360
Mvav 12,164,212
Ex 27
Mvav 12,160,208
Wl 3
Ta 190,241,36384
Pk 91,26,27
Maav 40,176,40
s_mov_b32 m0, 111456
Ex 28
Maav 40,180,44
Ra 160,240,52608
buffer_load_dwordx4 v251, s[40:43], s46 offen offset:0 nt lds
buffer_load_dwordx4 v153, s[40:43], s46 offen offset:1024 nt lds
Maav 44,176,48
Ex 29
Maav 44,180,52
Ra 164,240,52864
Pk 98,28,29
Maav 48,176,56
Ex 30
Maav 48,180,60
Ra 168,240,53120
Maav 52,176,64
Ex 31
Maav 52,180,68
Wl 3
Ra 172,240,53376
Pk 99,30,31
Maav 60,184,40
Ex 32
Maav 60,188,44
buffer_load_dwordx4 v251, s[40:43], s46 offen offset:2048 nt lds
buffer_load_dwordx4 v153, s[40:43], s46 offen offset:3072 nt lds
Maav 64,184,48
Ex 33
Maav 64,188,52
Pk 106,32,33
Maav 68,184,56
Ex 34
Maav 68,188,60
Maav 72,184,64
Ex 35
Maav 72,188,68
Wl 0
Pk 107,34,35
Cvav 20,160,160,120
s_add_u32 s46, s46, 3072
s_mov_b32 m0, 33024
Ex 0
Mvav 20,164,164
Ta 176,241,33536
buffer_load_dwordx4 v248, s[36:39], s44 offen offset:2048 nt lds
buffer_load_dwordx4 v150, s[36:39], s44 offen offset:3072 nt lds
Mvav 20,168,168
)A"
R"A(Ex 1
Mvav 20,172,172
Ta 178,241,34560
Pk 84,0,1
Cvav 24,172,188,124
Ex 2
Mvav 24,168,184
Ta 180,241,35584
Mvav 24,164,180
Ex 3
Mvav 24,160,176
Ta 182,241,36608
Pk 85,2,3
Cvav 28,160,192,128
s_mov_b32 m0, 37120
Ex 4
Mvav 28,164,196
Ta 184,241,33568
buffer_load_dwordx4 v249, s[36:39], s44 offen offset:2048 nt lds
buffer_load_dwordx4 v151, s[36:39], s44 offen offset:3072 nt lds
Mvav 28,168,200
Ex 5
Mvav 28,172,204
Ta 186,241,34592
Pk 92,4,5
Cvav 32,172,220,132
Ex 6
Mvav 32,168,216
Ta 188,241,35616
Mvav 32,164,212
Ex 7
Mvav 32,160,208
Wl 3
Ta 190,241,36640
Pk 93,6,7
Maav 80,176,40
Ex 8
Maav 80,180,44
Ra 160,240,0
Maav 84,176,48
s_mov_b32 m0, 41248
Ex 9
Maav 84,180,52
Ra 164,240,256
buffer_load_dwordx4 v250, s[36:39], s44 offen offset:2048 nt lds
buffer_load_dwordx4 v152, s[36:39], s44 offen offset:3072 nt lds
Maav 88,176,56
Ex 10
Maav 88,180,60
Ra 168,240,512
Pk 100,8,9
Maav 92,176,64
Ex 11
Maav 92,180,68
Wl 3
Ra 172,240,768
Pk 101,10,11
Maav 100,184,40
Ex 12
Maav 100,188,44
Maav 104,184,48
s_mov_b32 m0, 45344
Ex 13
Maav 104,188,52
buffer_load_dwordx4 v251, s[36:39], s44 offen offset:2048 nt lds
buffer_load_dwordx4 v153, s[36:39], s44 offen offset:3072 nt lds
Maav 108,184,56
Ex 14
Maav 108,188,60
Pk 108,12,13
Maav 112,184,64
Ex 15
Maav 112,188,68
Pk 109,14,15
Maav 16,156,80
Maav 36,156,88
Maav 56,156,96
s_cmp_eq_u32 s47, 3
Maav 76,156,104
Wl 0
s_cbranch_scc1 EXITB0_%=
Cvav 0,160,160,120
s_add_u32 s44, s44, 1024
s_mov_b32 m0, 49536
Ex 20
Mvav 0,164,164
Ta 176,241,33792
buffer_load_dwordx4 v248, s[36:39], s44 offen offset:0 nt lds
buffer_load_dwordx4 v150, s[36:39], s44 offen offset:1024 nt lds
Mvav 0,168,168
Ex 21
Mvav 0,172,172
Ta 178,241,34816
Pk 86,20,21
Cvav 4,172,188,124
Ex 22
Mvav 4,168,184
Ta 180,241,35840
Mvav 4,164,180
Ex 23
Mvav 4,160,176
Ta 182,241,36864
Pk 87,22,23
Cvav 8,160,192,128
s_mov_b32 m0, 53632
Ex 24
Mvav 8,164,196
Ta 184,241,33824
buffer_load_dwordx4 v249, s[36:39], s44 offen offset:0 nt lds
buffer_load_dwordx4 v151, s[36:39], s44 offen offset:1024 nt lds
Mvav 8,168,200
Ex 25
Mvav 8,172,204
Ta 186,241,34848
Pk 94,24,25
Cvav 12,172,220,132
Ex 26
Mvav 12,168,216
Ta 188,241,35872
Mvav 12,164,212
Ex 27
Mvav 12,160,208
Wl 3
Ta 190,241,36896
Pk 95,26,27
Maav 120,176,40
s_mov_b32 m0, 57760
Ex 28
Maav 120,180,44
Ra 160,240,1024
buffer_load_dwordx4 v250, s[36:39], s44 offen offset:0 nt lds
buffer_load_dwordx4 v152, s[36:39], s44 offen offset:1024 nt lds
Maav 124,176,48
Ex 29
Maav 124,180,52
Ra 164,240,1280
Pk 102,28,29
Maav 128,176,56
Ex 30
Maav 128,180,60
Ra 168,240,1536
Maav 132,176,64
Ex 31
Maav 132,180,68
Wl 3
Ra 172,240,1792
Pk 103,30,31
Maav 140,184,40
s_mov_b32 m0, 61856
Ex 32
Maav 140,188,44
buffer_load_dwordx4 v251, s[36:39], s44 offen offset:0 nt lds
buffer_load_dwordx4 v153, s[36:39], s44 offen offset:1024 nt lds
Maav 144,184,48
Ex 33
Maav 144,188,52
Pk 110,32,33
Maav 148,184,56
Ex 34
Maav 148,188,60
Maav 152,184,64
Ex 35
Maav 152,188,68
Wl 0
Pk 111,34,35
Cvav 20,160,160,120
s_waitcnt vmcnt(62)
s_barrier
s_mov_b32 m0, 115584
Ex 0
Mvav 20,164,164
Ta 176,241,49536
buffer_load_dwordx4 v248, s[40:43], s46 offen offset:0 nt lds
buffer_load_dwordx4 v150, s[40:43], s46 offen offset:1024 nt lds
Mvav 20,168,168
Ex 1
Mvav 20,172,172
Ta 178,241,50560
Pk 40,0,1
Cvav 24,172,188,124
Ex 2
Mvav 24,168,184
Ta 180,241,51584
Mvav 24,164,180
Ex 3
Mvav 24,160,176
Ta 182,241,52608
Pk 41,2,3
Cvav 28,160,192,128
Ex 4
Mvav 28,164,196
Ta 184,241,49568
buffer_load_dwordx4 v248, s[40:43], s46 offen offset:2048 nt lds
buffer_load_dwordx4 v150, s[40:43], s46 offen offset:3072 nt lds
Mvav 28,168,200
Ex 5
Mvav 28,172,204
Ta 186,241,50592
Pk 48,4,5
Cvav 32,172,220,132
Ex 6
Mvav 32,168,216
Ta 188,241,51616
Mvav 32,164,212
Ex 7
Mvav 32,160,208
Wl 3
Ta 190,241,52640
Pk 49,6,7
Maav 0,176,80
Ex 8
Maav 0,180,84
Ra 160,240,2048
Maav 4,176,88
s_mov_b32 m0, 119712
Ex 9
Maav 4,180,92
Ra 164,240,2304
buffer_load_dwordx4 v249, s[40:43], s46 offen offset:0 nt lds
buffer_load_dwordx4 v151, s[40:43], s46 offen offset:1024 nt lds
Maav 8,176,96
Ex 10
Maav 8,180,100
Ra 168,240,2560
Pk 56,8,9
Maav 12,176,104
Ex 11
Maav 12,180,108
Wl 3
Ra 172,240,2816
Pk 57,10,11
Maav 20,184,80
Ex 12
Maav 20,188,84
Maav 24,184,88
Ex 13
Maav 24,188,92
buffer_load_dwordx4 v249, s[40:43], s46 offen offset:2048 nt lds
buffer_load_dwordx4 v151, s[40:43], s46 offen offset:3072 nt lds
Maav 28,184,96
Ex 14
Maav 28,188,100
Pk 64,12,13
Maav 32,184,104
Ex 15
Maav 32,188,108
Pk 65,14,15
Maav 16,156,84
Maav 36,156,92
Maav 56,156,100
Maav 76,156,108
Wl 0
Cvav 0,160,160,120
s_mov_b32 m0, 123840
Ex 20
Mvav 0,164,164
Ta 176,241,49792
buffer_load_dwordx4 v250, s[40:43], s46 offen offset:0 nt lds
buffer_load_dwordx4 v152, s[40:43], s46 offen offset:1024 nt lds
Mvav 0,168,168
Ex 21
Mvav 0,172,172
Ta 178,241,50816
Pk 42,20,21
Cvav 4,172,188,124
Ex 22
Mvav 4,168,184
Ta 180,241,51840
Mvav 4,164,180
Ex 23
Mvav 4,160,176
Ta 182,241,52864
Pk 43,22,23
Cvav 8,160,192,128
Ex 24
Mvav 8,164,196
Ta 184,241,49824
buffer_load_dwordx4 v250, s[40:43], s46 offen offset:2048 nt lds
buffer_load_dwordx4 v152, s[40:43], s46 offen offset:3072 nt lds
Mvav 8,168,200
Ex 25
Mvav 8,172,204
Ta 186,241,50848
Pk 50,24,25
Cvav 12,172,220,132
Ex 26
Mvav 12,168,216
Ta 188,241,51872
Mvav 12,164,212
Ex 27
Mvav 12,160,208
Wl 3
Ta 190,241,52896
Pk 51,26,27
Maav 40,176,80
s_mov_b32 m0, 127968
Ex 28
Maav 40,180,84
Ra 160,240,3072
buffer_load_dwordx4 v251, s[40:43], s46 offen offset:0 nt lds
buffer_load_dwordx4 v153, s[40:43], s46 offen offset:1024 nt lds
Maav 44,176,88
Ex 29
Maav 44,180,92
Ra 164,240,3328
Pk 58,28,29
Maav 48,176,96
Ex 30
Maav 48,180,100
Ra 168,240,3584
Maav 52,176,104
Ex 31
Maav 52,180,108
Wl 3
Ra 172,240,3840
Pk 59,30,31
Maav 60,184,80
Ex 32
Maav 60,188,84
buffer_load_dwordx4 v251, s[40:43], s46 offen offset:2048 nt lds
buffer_load_dwordx4 v153, s[40:43], s46 offen offset:3072 nt lds
Maav 64,184,88
Ex 33
Maav 64,188,92
Pk 66,32,33
Maav 68,184,96
Ex 34
Maav 68,188,100
Maav 72,184,104
Ex 35
Maav 72,188,108
Wl 0
Pk 67,34,35
Cvav 20,160,160,120
s_add_u32 s46, s46, 1024
s_mov_b32 m0, 49536
Ex 0
Mvav 20,164,164
Ta 176,241,50048
buffer_load_dwordx4 v248, s[36:39], s44 offen offset:2048 nt lds
buffer_load_dwordx4 v150, s[36:39], s44 offen offset:3072 nt lds
Mvav 20,168,168
Ex 1
Mvav 20,172,172
Ta 178,241,51072
Pk 44,0,1
Cvav 24,172,188,124
Ex 2
Mvav 24,168,184
Ta 180,241,52096
Mvav 24,164,180
Ex 3
Mvav 24,160,176
Ta 182,241,53120
Pk 45,2,3
Cvav 28,160,192,128
s_mov_b32 m0, 53632
Ex 4
Mvav 28,164,196
Ta 184,241,50080
buffer_load_dwordx4 v249, s[36:39], s44 offen offset:2048 nt lds
buffer_load_dwordx4 v151, s[36:39], s44 offen offset:3072 nt lds
Mvav 28,168,200
Ex 5
Mvav 28,172,204
Ta 186,241,51104
Pk 52,4,5
Cvav 32,172,220,132
Ex 6
Mvav 32,168,216
Ta 188,241,52128
Mvav 32,164,212
Ex 7
Mvav 32,160,208
Wl 3
Ta 190,241,53152
Pk 53,6,7
Maav 80,176,80
Ex 8
Maav 80,180,84
Ra 160,240,16512
Maav 84,176,88
s_mov_b32 m0, 57760
Ex 9
Maav 84,180,92
Ra 164,240,16768
buffer_load_dwordx4 v250, s[36:39], s44 offen offset:2048 nt lds
buffer_load_dwordx4 v152, s[36:39], s44 offen offset:3072 nt lds
Maav 88,176,96
Ex 10
Maav 88,180,100
Ra 168,240,17024
Pk 60,8,9
Maav 92,176,104
Ex 11
Maav 92,180,108
Wl 3
Ra 172,240,17280
Pk 61,10,11
Maav 100,184,80
)A"
R"A(Ex 12
Maav 100,188,84
Maav 104,184,88
s_mov_b32 m0, 61856
Ex 13
Maav 104,188,92
buffer_load_dwordx4 v251, s[36:39], s44 offen offset:2048 nt lds
buffer_load_dwordx4 v153, s[36:39], s44 offen offset:3072 nt lds
Maav 108,184,96
Ex 14
Maav 108,188,100
Pk 68,12,13
Maav 112,184,104
Ex 15
Maav 112,188,108
Pk 69,14,15
Maav 16,156,40
Maav 36,156,48
Maav 56,156,56
Maav 76,156,64
Wl 0
Cvav 0,160,160,120
s_add_u32 s44, s44, s68
s_xor_b32 s68, s68, 114688
s_mov_b32 m0, 0
Ex 20
Mvav 0,164,164
Ta 176,241,50304
buffer_load_dwordx4 v248, s[36:39], s44 offen offset:0 nt lds
buffer_load_dwordx4 v150, s[36:39], s44 offen offset:1024 nt lds
Mvav 0,168,168
Ex 21
Mvav 0,172,172
Ta 178,241,51328
Pk 46,20,21
Cvav 4,172,188,124
Ex 22
Mvav 4,168,184
Ta 180,241,52352
Mvav 4,164,180
Ex 23
Mvav 4,160,176
Ta 182,241,53376
Pk 47,22,23
Cvav 8,160,192,128
s_mov_b32 m0, 4096
Ex 24
Mvav 8,164,196
Ta 184,241,50336
buffer_load_dwordx4 v249, s[36:39], s44 offen offset:0 nt lds
buffer_load_dwordx4 v151, s[36:39], s44 offen offset:1024 nt lds
Mvav 8,168,200
Ex 25
Mvav 8,172,204
Ta 186,241,51360
Pk 54,24,25
Cvav 12,172,220,132
Ex 26
Mvav 12,168,216
Ta 188,241,52384
Mvav 12,164,212
Ex 27
Mvav 12,160,208
Wl 3
Ta 190,241,53408
Pk 55,26,27
Maav 120,176,80
s_mov_b32 m0, 8224
Ex 28
Maav 120,180,84
Ra 160,240,17536
buffer_load_dwordx4 v250, s[36:39], s44 offen offset:0 nt lds
buffer_load_dwordx4 v152, s[36:39], s44 offen offset:1024 nt lds
Maav 124,176,88
Ex 29
Maav 124,180,92
Ra 164,240,17792
Pk 62,28,29
Maav 128,176,96
Ex 30
Maav 128,180,100
Ra 168,240,18048
Maav 132,176,104
Ex 31
Maav 132,180,108
Wl 3
Ra 172,240,18304
Pk 63,30,31
Maav 140,184,80
s_mov_b32 m0, 12320
Ex 32
Maav 140,188,84
buffer_load_dwordx4 v251, s[36:39], s44 offen offset:0 nt lds
buffer_load_dwordx4 v153, s[36:39], s44 offen offset:1024 nt lds
Maav 144,184,88
Ex 33
Maav 144,188,92
Pk 70,32,33
Maav 148,184,96
Ex 34
Maav 148,188,100
Maav 152,184,104
Ex 35
Maav 152,188,108
Wl 0
Pk 71,34,35
Cvav 20,160,160,120
s_waitcnt vmcnt(62)
s_barrier
s_mov_b32 m0, 132096
Ex 0
Mvav 20,164,164
Ta 176,241,0
buffer_load_dwordx4 v248, s[40:43], s46 offen offset:0 nt lds
buffer_load_dwordx4 v150, s[40:43], s46 offen offset:1024 nt lds
Mvav 20,168,168
Ex 1
Mvav 20,172,172
Ta 178,241,1024
Pk 80,0,1
Cvav 24,172,188,124
Ex 2
Mvav 24,168,184
Ta 180,241,2048
Mvav 24,164,180
Ex 3
Mvav 24,160,176
Ta 182,241,3072
Pk 81,2,3
Cvav 28,160,192,128
Ex 4
Mvav 28,164,196
Ta 184,241,32
buffer_load_dwordx4 v248, s[40:43], s46 offen offset:2048 nt lds
buffer_load_dwordx4 v150, s[40:43], s46 offen offset:3072 nt lds
Mvav 28,168,200
Ex 5
Mvav 28,172,204
Ta 186,241,1056
Pk 88,4,5
Cvav 32,172,220,132
Ex 6
Mvav 32,168,216
Ta 188,241,2080
Mvav 32,164,212
Ex 7
Mvav 32,160,208
Wl 3
Ta 190,241,3104
Pk 89,6,7
Maav 0,176,40
Ex 8
Maav 0,180,44
Ra 160,240,18560
Maav 4,176,48
s_mov_b32 m0, 136224
Ex 9
Maav 4,180,52
Ra 164,240,18816
buffer_load_dwordx4 v249, s[40:43], s46 offen offset:0 nt lds
buffer_load_dwordx4 v151, s[40:43], s46 offen offset:1024 nt lds
Maav 8,176,56
Ex 10
Maav 8,180,60
Ra 168,240,19072
Pk 96,8,9
Maav 12,176,64
Ex 11
Maav 12,180,68
Wl 3
Ra 172,240,19328
Pk 97,10,11
Maav 20,184,40
Ex 12
Maav 20,188,44
Maav 24,184,48
Ex 13
Maav 24,188,52
buffer_load_dwordx4 v249, s[40:43], s46 offen offset:2048 nt lds
buffer_load_dwordx4 v151, s[40:43], s46 offen offset:3072 nt lds
Maav 28,184,56
Ex 14
Maav 28,188,60
Pk 104,12,13
Maav 32,184,64
Ex 15
Maav 32,188,68
Pk 105,14,15
Maav 16,156,44
Maav 36,156,52
Maav 56,156,60
Maav 76,156,68
Wl 0
Cvav 0,160,160,120
s_mov_b32 m0, 140352
Ex 20
Mvav 0,164,164
Ta 176,241,256
buffer_load_dwordx4 v250, s[40:43], s46 offen offset:0 nt lds
buffer_load_dwordx4 v152, s[40:43], s46 offen offset:1024 nt lds
Mvav 0,168,168
Ex 21
Mvav 0,172,172
Ta 178,241,1280
Pk 82,20,21
Cvav 4,172,188,124
Ex 22
Mvav 4,168,184
Ta 180,241,2304
Mvav 4,164,180
Ex 23
Mvav 4,160,176
Ta 182,241,3328
Pk 83,22,23
Cvav 8,160,192,128
Ex 24
Mvav 8,164,196
Ta 184,241,288
buffer_load_dwordx4 v250, s[40:43], s46 offen offset:2048 nt lds
buffer_load_dwordx4 v152, s[40:43], s46 offen offset:3072 nt lds
Mvav 8,168,200
Ex 25
Mvav 8,172,204
Ta 186,241,1312
Pk 90,24,25
Cvav 12,172,220,132
Ex 26
Mvav 12,168,216
Ta 188,241,2336
Mvav 12,164,212
Ex 27
Mvav 12,160,208
Wl 3
Ta 190,241,3360
Pk 91,26,27
Maav 40,176,40
s_mov_b32 m0, 144480
Ex 28
Maav 40,180,44
Ra 160,240,19584
buffer_load_dwordx4 v251, s[40:43], s46 offen offset:0 nt lds
buffer_load_dwordx4 v153, s[40:43], s46 offen offset:1024 nt lds
Maav 44,176,48
Ex 29
Maav 44,180,52
Ra 164,240,19840
Pk 98,28,29
Maav 48,176,56
Ex 30
Maav 48,180,60
Ra 168,240,20096
Maav 52,176,64
Ex 31
Maav 52,180,68
Wl 3
Ra 172,240,20352
Pk 99,30,31
Maav 60,184,40
Ex 32
Maav 60,188,44
buffer_load_dwordx4 v251, s[40:43], s46 offen offset:2048 nt lds
buffer_load_dwordx4 v153, s[40:43], s46 offen offset:3072 nt lds
Maav 64,184,48
Ex 33
Maav 64,188,52
Pk 106,32,33
Maav 68,184,56
Ex 34
Maav 68,188,60
Maav 72,184,64
Ex 35
Maav 72,188,68
Wl 0
Pk 107,34,35
Cvav 20,160,160,120
s_add_u32 s46, s46, s69
s_xor_b32 s69, s69, 114688
s_mov_b32 m0, 0
Ex 0
Mvav 20,164,164
Ta 176,241,512
buffer_load_dwordx4 v248, s[36:39], s44 offen offset:2048 nt lds
buffer_load_dwordx4 v150, s[36:39], s44 offen offset:3072 nt lds
Mvav 20,168,168
Ex 1
Mvav 20,172,172
Ta 178,241,1536
Pk 84,0,1
Cvav 24,172,188,124
Ex 2
Mvav 24,168,184
Ta 180,241,2560
Mvav 24,164,180
Ex 3
Mvav 24,160,176
Ta 182,241,3584
Pk 85,2,3
Cvav 28,160,192,128
s_mov_b32 m0, 4096
Ex 4
Mvav 28,164,196
Ta 184,241,544
buffer_load_dwordx4 v249, s[36:39], s44 offen offset:2048 nt lds
buffer_load_dwordx4 v151, s[36:39], s44 offen offset:3072 nt lds
Mvav 28,168,200
Ex 5
Mvav 28,172,204
Ta 186,241,1568
Pk 92,4,5
Cvav 32,172,220,132
Ex 6
Mvav 32,168,216
Ta 188,241,2592
Mvav 32,164,212
Ex 7
Mvav 32,160,208
Wl 3
Ta 190,241,3616
Pk 93,6,7
Maav 80,176,40
Ex 8
Maav 80,180,44
Ra 160,240,33024
Maav 84,176,48
s_mov_b32 m0, 8224
Ex 9
Maav 84,180,52
Ra 164,240,33280
buffer_load_dwordx4 v250, s[36:39], s44 offen offset:2048 nt lds
buffer_load_dwordx4 v152, s[36:39], s44 offen offset:3072 nt lds
Maav 88,176,56
Ex 10
Maav 88,180,60
Ra 168,240,33536
Pk 100,8,9
Maav 92,176,64
Ex 11
Maav 92,180,68
Wl 3
Ra 172,240,33792
Pk 101,10,11
Maav 100,184,40
Ex 12
Maav 100,188,44
Maav 104,184,48
s_mov_b32 m0, 12320
Ex 13
Maav 104,188,52
buffer_load_dwordx4 v251, s[36:39], s44 offen offset:2048 nt lds
buffer_load_dwordx4 v153, s[36:39], s44 offen offset:3072 nt lds
Maav 108,184,56
Ex 14
Maav 108,188,60
Pk 108,12,13
Maav 112,184,64
Ex 15
Maav 112,188,68
Pk 109,14,15
Maav 16,156,80
Maav 36,156,88
Maav 56,156,96
Maav 76,156,104
Wl 0
Cvav 0,160,160,120
s_add_u32 s44, s44, 1024
s_mov_b32 m0, 16512
Ex 20
Mvav 0,164,164
Ta 176,241,768
buffer_load_dwordx4 v248, s[36:39], s44 offen offset:0 nt lds
buffer_load_dwordx4 v150, s[36:39], s44 offen offset:1024 nt lds
Mvav 0,168,168
Ex 21
Mvav 0,172,172
Ta 178,241,1792
Pk 86,20,21
Cvav 4,172,188,124
Ex 22
Mvav 4,168,184
Ta 180,241,2816
Mvav 4,164,180
Ex 23
Mvav 4,160,176
Ta 182,241,3840
Pk 87,22,23
Cvav 8,160,192,128
s_mov_b32 m0, 20608
Ex 24
Mvav 8,164,196
Ta 184,241,800
buffer_load_dwordx4 v249, s[36:39], s44 offen offset:0 nt lds
buffer_load_dwordx4 v151, s[36:39], s44 offen offset:1024 nt lds
Mvav 8,168,200
Ex 25
Mvav 8,172,204
Ta 186,241,1824
Pk 94,24,25
Cvav 12,172,220,132
Ex 26
Mvav 12,168,216
)A"
R"A(Ta 188,241,2848
Mvav 12,164,212
Ex 27
Mvav 12,160,208
Wl 3
Ta 190,241,3872
Pk 95,26,27
Maav 120,176,40
s_mov_b32 m0, 24736
Ex 28
Maav 120,180,44
Ra 160,240,34048
buffer_load_dwordx4 v250, s[36:39], s44 offen offset:0 nt lds
buffer_load_dwordx4 v152, s[36:39], s44 offen offset:1024 nt lds
Maav 124,176,48
Ex 29
Maav 124,180,52
Ra 164,240,34304
Pk 102,28,29
Maav 128,176,56
Ex 30
Maav 128,180,60
Ra 168,240,34560
Maav 132,176,64
Ex 31
Maav 132,180,68
Wl 3
Ra 172,240,34816
Pk 103,30,31
Maav 140,184,40
s_mov_b32 m0, 28832
Ex 32
Maav 140,188,44
buffer_load_dwordx4 v251, s[36:39], s44 offen offset:0 nt lds
buffer_load_dwordx4 v153, s[36:39], s44 offen offset:1024 nt lds
Maav 144,184,48
Ex 33
Maav 144,188,52
Pk 110,32,33
Maav 148,184,56
Ex 34
Maav 148,188,60
Maav 152,184,64
s_add_u32 s47, s47, 1
Ex 35
Maav 152,188,68
Wl 0
Pk 111,34,35
s_branch LOOPB0_%=
EXITB0_%=:
Zvaa 0,160,192
s_add_u32 s44, s44, 1024
s_mov_b32 m0, 49536
Ex 20
Mvaa 0,164,196
Ta 176,241,33792
buffer_load_dwordx4 v248, s[36:39], s44 offen offset:0 nt lds
buffer_load_dwordx4 v150, s[36:39], s44 offen offset:1024 nt lds
Mvaa 0,168,200
Ex 21
Mvaa 0,172,204
Ta 178,241,34816
Pk 86,20,21
Zvaa 4,172,220
Ex 22
Mvaa 4,168,216
Ta 180,241,35840
Mvaa 4,164,212
Ex 23
Mvaa 4,160,208
Ta 182,241,36864
Pk 87,22,23
Zvaa 8,160,224
s_mov_b32 m0, 53632
Ex 24
Mvaa 8,164,228
Ta 184,241,33824
buffer_load_dwordx4 v249, s[36:39], s44 offen offset:0 nt lds
buffer_load_dwordx4 v151, s[36:39], s44 offen offset:1024 nt lds
Mvaa 8,168,232
Ex 25
Mvaa 8,172,236
Ta 186,241,34848
Pk 94,24,25
Zvaa 12,172,252
Ex 26
Mvaa 12,168,248
Ta 188,241,35872
Mvaa 12,164,244
Ex 27
Mvaa 12,160,240
Wl 3
Ta 190,241,36896
Pk 95,26,27
Maav 120,176,40
s_mov_b32 m0, 57760
Ex 28
Maav 120,180,44
Ra 160,240,1024
buffer_load_dwordx4 v250, s[36:39], s44 offen offset:0 nt lds
buffer_load_dwordx4 v152, s[36:39], s44 offen offset:1024 nt lds
Maav 124,176,48
Ex 29
Maav 124,180,52
Ra 164,240,1280
Pk 102,28,29
Maav 128,176,56
Ex 30
Maav 128,180,60
Ra 168,240,1536
Maav 132,176,64
Ex 31
Maav 132,180,68
Wl 3
Ra 172,240,1792
Pk 103,30,31
Maav 140,184,40
s_mov_b32 m0, 61856
Ex 32
Maav 140,188,44
buffer_load_dwordx4 v251, s[36:39], s44 offen offset:0 nt lds
buffer_load_dwordx4 v153, s[36:39], s44 offen offset:1024 nt lds
Maav 144,184,48
Ex 33
Maav 144,188,52
Pk 110,32,33
Maav 148,184,56
Ex 34
Maav 148,188,60
Maav 152,184,64
Ex 35
Maav 152,188,68
Wl 0
Pk 111,34,35
s_nop 1
Maav 16,156,84
Maav 36,156,92
Maav 56,156,100
Maav 76,156,108
s_nop 7
Ar 140,16
Ar 142,36
Ar 144,56
Ar 146,76
v_add_f32 v141, v140, v142
Ad 141,144
Ad 141,146
v_cmp_nge_f32 vcc, 0x5d800000, v141
v_rcp_f32 v158, v140
v_rcp_f32 v159, v142
v_rcp_f32 v242, v144
v_rcp_f32 v243, v146
s_or_b32 s54, vcc_lo, vcc_hi
v_max3_f32 v120, v0, v1, v2
v_max_f32 v120, v120, v3
v_max3_f32 v124, v4, v5, v6
v_max_f32 v124, v124, v7
v_max3_f32 v128, v8, v9, v10
v_max_f32 v128, v128, v11
v_max3_f32 v132, v12, v13, v14
v_max_f32 v132, v132, v15
v_mov_b32 v121, v120
v_mov_b32 v125, v124
v_mov_b32 v129, v128
v_mov_b32 v133, v132
s_nop 0
v_permlane32_swap_b32 v120, v121
v_permlane32_swap_b32 v124, v125
v_permlane32_swap_b32 v128, v129
v_permlane32_swap_b32 v132, v133
v_max_f32 v120, v120, v121
v_max_f32 v124, v124, v125
v_max_f32 v128, v128, v129
v_max_f32 v132, v132, v133
v_mov_b32 v121, v120
v_mov_b32 v125, v124
v_mov_b32 v129, v128
v_mov_b32 v133, v132
s_nop 0
v_permlane16_swap_b32 v120, v121
v_permlane16_swap_b32 v124, v125
v_permlane16_swap_b32 v128, v129
v_permlane16_swap_b32 v132, v133
v_max_f32 v120, v120, v121
v_max_f32 v124, v124, v125
v_max_f32 v128, v128, v129
v_max_f32 v132, v132, v133
v_pk_mul_f32 v[122:123], v[120:121], -1.0 op_sel_hi:[0,0]
v_pk_mul_f32 v[120:121], v[120:121], -1.0 op_sel_hi:[0,0]
v_pk_add_f32 v[0:1], v[0:1], v[122:123]
v_pk_add_f32 v[2:3], v[2:3], v[122:123]
v_pk_mov_b32 v[140:141], 0, 0
v_pk_mul_f32 v[126:127], v[124:125], -1.0 op_sel_hi:[0,0]
v_pk_mul_f32 v[124:125], v[124:125], -1.0 op_sel_hi:[0,0]
v_pk_add_f32 v[4:5], v[4:5], v[126:127]
v_pk_add_f32 v[6:7], v[6:7], v[126:127]
v_pk_mov_b32 v[142:143], 0, 0
v_pk_mul_f32 v[130:131], v[128:129], -1.0 op_sel_hi:[0,0]
v_pk_mul_f32 v[128:129], v[128:129], -1.0 op_sel_hi:[0,0]
v_pk_add_f32 v[8:9], v[8:9], v[130:131]
v_pk_add_f32 v[10:11], v[10:11], v[130:131]
v_pk_mov_b32 v[144:145], 0, 0
v_pk_mul_f32 v[134:135], v[132:133], -1.0 op_sel_hi:[0,0]
v_pk_mul_f32 v[132:133], v[132:133], -1.0 op_sel_hi:[0,0]
v_pk_add_f32 v[12:13], v[12:13], v[134:135]
v_pk_add_f32 v[14:15], v[14:15], v[134:135]
v_pk_mov_b32 v[146:147], 0, 0
s_nop 0
Cvaa 20,160,192,120
s_waitcnt vmcnt(62)
s_barrier
s_mov_b32 m0, 115584
Ex 0
Mvaa 20,164,196
Ta 176,241,49536
buffer_load_dwordx4 v248, s[40:43], s46 offen offset:0 nt lds
buffer_load_dwordx4 v150, s[40:43], s46 offen offset:1024 nt lds
Mvaa 20,168,200
Ex 1
Mvaa 20,172,204
Ta 178,241,50560
Pk 40,0,1
Cvaa 24,172,220,124
Ex 2
Mvaa 24,168,216
Ta 180,241,51584
Mvaa 24,164,212
Ex 3
Mvaa 24,160,208
Ta 182,241,52608
Pk 41,2,3
Cvaa 28,160,224,128
Ex 4
Mvaa 28,164,228
Ta 184,241,49568
buffer_load_dwordx4 v248, s[40:43], s46 offen offset:2048 nt lds
buffer_load_dwordx4 v150, s[40:43], s46 offen offset:3072 nt lds
Mvaa 28,168,232
Ex 5
Mvaa 28,172,236
Ta 186,241,50592
Pk 48,4,5
Cvaa 32,172,252,132
Ex 6
Mvaa 32,168,248
Ta 188,241,51616
Mvaa 32,164,244
Ex 7
Mvaa 32,160,240
Wl 3
Ta 190,241,52640
Pk 49,6,7
Maav 0,176,80
s_mov_b32 m0, 119712
Ex 8
Maav 0,180,84
Ra 160,240,2048
buffer_load_dwordx4 v249, s[40:43], s46 offen offset:0 nt lds
buffer_load_dwordx4 v151, s[40:43], s46 offen offset:1024 nt lds
Maav 4,176,88
Ex 9
Maav 4,180,92
Ra 164,240,2304
Pk 56,8,9
Maav 8,176,96
Ex 10
Maav 8,180,100
Ra 168,240,2560
Maav 12,176,104
Ex 11
Maav 12,180,108
Wl 3
Ra 172,240,2816
Pk 57,10,11
Maav 20,184,80
Ex 12
Maav 20,188,84
buffer_load_dwordx4 v249, s[40:43], s46 offen offset:2048 nt lds
buffer_load_dwordx4 v151, s[40:43], s46 offen offset:3072 nt lds
Maav 24,184,88
Ex 13
Maav 24,188,92
Pk 64,12,13
Maav 28,184,96
Ex 14
Maav 28,188,100
Maav 32,184,104
Ex 15
Maav 32,188,108
Wl 0
Pk 65,14,15
Cvaa 0,160,192,120
s_mov_b32 m0, 123840
Ex 20
Mvaa 0,164,196
Ta 176,241,49792
buffer_load_dwordx4 v250, s[40:43], s46 offen offset:0 nt lds
buffer_load_dwordx4 v152, s[40:43], s46 offen offset:1024 nt lds
Mvaa 0,168,200
Ex 21
Mvaa 0,172,204
Ta 178,241,50816
Pk 42,20,21
Cvaa 4,172,220,124
Ex 22
Mvaa 4,168,216
Ta 180,241,51840
Mvaa 4,164,212
Ex 23
Mvaa 4,160,208
Ta 182,241,52864
Pk 43,22,23
Cvaa 8,160,224,128
Ex 24
Mvaa 8,164,228
Ta 184,241,49824
buffer_load_dwordx4 v250, s[40:43], s46 offen offset:2048 nt lds
buffer_load_dwordx4 v152, s[40:43], s46 offen offset:3072 nt lds
Mvaa 8,168,232
Ex 25
Mvaa 8,172,236
Ta 186,241,50848
Pk 50,24,25
Cvaa 12,172,252,132
Ex 26
Mvaa 12,168,248
Ta 188,241,51872
Mvaa 12,164,244
Ex 27
Mvaa 12,160,240
Wl 3
Ta 190,241,52896
Pk 51,26,27
Maav 40,176,80
s_mov_b32 m0, 127968
Ex 28
Maav 40,180,84
Ra 160,240,3072
buffer_load_dwordx4 v251, s[40:43], s46 offen offset:0 nt lds
buffer_load_dwordx4 v153, s[40:43], s46 offen offset:1024 nt lds
Maav 44,176,88
Ex 29
Maav 44,180,92
Ra 164,240,3328
Pk 58,28,29
Maav 48,176,96
Ex 30
Maav 48,180,100
Ra 168,240,3584
Maav 52,176,104
Ex 31
Maav 52,180,108
Wl 3
Ra 172,240,3840
Pk 59,30,31
Maav 60,184,80
Ex 32
Maav 60,188,84
buffer_load_dwordx4 v251, s[40:43], s46 offen offset:2048 nt lds
buffer_load_dwordx4 v153, s[40:43], s46 offen offset:3072 nt lds
Maav 64,184,88
Ex 33
Maav 64,188,92
Pk 66,32,33
Maav 68,184,96
Ex 34
Maav 68,188,100
Maav 72,184,104
Ex 35
Maav 72,188,108
Wl 0
Pk 67,34,35
Cvaa 20,160,192,120
s_add_u32 s46, s46, 1024
s_mov_b32 m0, 49536
Ex 0
Mvaa 20,164,196
Ta 176,241,50048
buffer_load_dwordx4 v248, s[36:39], s44 offen offset:2048 nt lds
buffer_load_dwordx4 v150, s[36:39], s44 offen offset:3072 nt lds
Mvaa 20,168,200
Ex 1
Mvaa 20,172,204
Ta 178,241,51072
Pk 44,0,1
Cvaa 24,172,220,124
Ex 2
Mvaa 24,168,216
Ta 180,241,52096
Mvaa 24,164,212
Ex 3
Mvaa 24,160,208
Ta 182,241,53120
Pk 45,2,3
Cvaa 28,160,224,128
s_mov_b32 m0, 53632
Ex 4
Mvaa 28,164,228
Ta 184,241,50080
buffer_load_dwordx4 v249, s[36:39], s44 offen offset:2048 nt lds
)A"
R"A(buffer_load_dwordx4 v151, s[36:39], s44 offen offset:3072 nt lds
Mvaa 28,168,232
Ex 5
Mvaa 28,172,236
Ta 186,241,51104
Pk 52,4,5
Cvaa 32,172,252,132
Ex 6
Mvaa 32,168,248
Ta 188,241,52128
Mvaa 32,164,244
Ex 7
Mvaa 32,160,240
Wl 3
Ta 190,241,53152
Pk 53,6,7
Maav 80,176,80
Ex 8
Maav 80,180,84
Ra 160,240,16512
Maav 84,176,88
s_mov_b32 m0, 57760
Ex 9
Maav 84,180,92
Ra 164,240,16768
buffer_load_dwordx4 v250, s[36:39], s44 offen offset:2048 nt lds
buffer_load_dwordx4 v152, s[36:39], s44 offen offset:3072 nt lds
Maav 88,176,96
Ex 10
Maav 88,180,100
Ra 168,240,17024
Pk 60,8,9
Maav 92,176,104
Ex 11
Maav 92,180,108
Wl 3
Ra 172,240,17280
Pk 61,10,11
Maav 100,184,80
Ex 12
Maav 100,188,84
Maav 104,184,88
s_mov_b32 m0, 61856
Ex 13
Maav 104,188,92
buffer_load_dwordx4 v251, s[36:39], s44 offen offset:2048 nt lds
buffer_load_dwordx4 v153, s[36:39], s44 offen offset:3072 nt lds
Maav 108,184,96
Ex 14
Maav 108,188,100
Pk 68,12,13
Maav 112,184,104
Ex 15
Maav 112,188,108
Pk 69,14,15
Zaav 16,156,40
Zaav 36,156,48
Zaav 56,156,56
Zaav 76,156,64
Wl 0
Cvaa 0,160,192,120
s_add_u32 s44, s44, s68
s_xor_b32 s68, s68, 114688
s_mov_b32 m0, 0
Ex 20
Mvaa 0,164,196
Ta 176,241,50304
buffer_load_dwordx4 v248, s[36:39], s44 offen offset:0 nt lds
buffer_load_dwordx4 v150, s[36:39], s44 offen offset:1024 nt lds
Mvaa 0,168,200
Ex 21
Mvaa 0,172,204
Ta 178,241,51328
Pk 46,20,21
Cvaa 4,172,220,124
Ex 22
Mvaa 4,168,216
Ta 180,241,52352
Mvaa 4,164,212
Ex 23
Mvaa 4,160,208
Ta 182,241,53376
Pk 47,22,23
Cvaa 8,160,224,128
s_mov_b32 m0, 4096
Ex 24
Mvaa 8,164,228
Ta 184,241,50336
buffer_load_dwordx4 v249, s[36:39], s44 offen offset:0 nt lds
buffer_load_dwordx4 v151, s[36:39], s44 offen offset:1024 nt lds
Mvaa 8,168,232
Ex 25
Mvaa 8,172,236
Ta 186,241,51360
Pk 54,24,25
Cvaa 12,172,252,132
Ex 26
Mvaa 12,168,248
Ta 188,241,52384
Mvaa 12,164,244
Ex 27
Mvaa 12,160,240
Wl 3
Ta 190,241,53408
Pk 55,26,27
Maav 120,176,80
s_mov_b32 m0, 8224
Ex 28
Maav 120,180,84
Ra 160,240,17536
buffer_load_dwordx4 v250, s[36:39], s44 offen offset:0 nt lds
buffer_load_dwordx4 v152, s[36:39], s44 offen offset:1024 nt lds
Maav 124,176,88
Ex 29
Maav 124,180,92
Ra 164,240,17792
Pk 62,28,29
Maav 128,176,96
Ex 30
Maav 128,180,100
Ra 168,240,18048
Maav 132,176,104
Ex 31
Maav 132,180,108
Wl 3
Ra 172,240,18304
Pk 63,30,31
Maav 140,184,80
s_mov_b32 m0, 12320
Ex 32
Maav 140,188,84
buffer_load_dwordx4 v251, s[36:39], s44 offen offset:0 nt lds
buffer_load_dwordx4 v153, s[36:39], s44 offen offset:1024 nt lds
Maav 144,184,88
Ex 33
Maav 144,188,92
Pk 70,32,33
Maav 148,184,96
Ex 34
Maav 148,188,100
Maav 152,184,104
Ex 35
Maav 152,188,108
Wl 0
Pk 71,34,35
s_cmp_lg_u32 s54, 0
s_cbranch_scc1 SLOWB_%=
RET0B_%=:
.p2align 3
Ar 80,0
Ar 81,1
Ar 82,2
Ar 83,3
Ar 84,20
Ar 85,21
Ar 86,22
Ar 87,23
v_pk_mul_f32 v[80:81], v[80:81], v[158:159] op_sel_hi:[1,0]
v_pk_mul_f32 v[82:83], v[82:83], v[158:159] op_sel_hi:[1,0]
v_pk_mul_f32 v[84:85], v[84:85], v[158:159] op_sel_hi:[1,0]
v_pk_mul_f32 v[86:87], v[86:87], v[158:159] op_sel_hi:[1,0]
Pk 160,80,81
Pk 161,82,83
Pk 162,84,85
Pk 163,86,87
Ar 88,4
Ar 89,5
Ar 90,6
Ar 91,7
Ar 92,24
Ar 93,25
Ar 94,26
Ar 95,27
v_pk_mul_f32 v[88:89], v[88:89], v[158:159] op_sel:[0,1] op_sel_hi:[1,1]
v_pk_mul_f32 v[90:91], v[90:91], v[158:159] op_sel:[0,1] op_sel_hi:[1,1]
v_pk_mul_f32 v[92:93], v[92:93], v[158:159] op_sel:[0,1] op_sel_hi:[1,1]
v_pk_mul_f32 v[94:95], v[94:95], v[158:159] op_sel:[0,1] op_sel_hi:[1,1]
Pk 176,88,89
Pk 177,90,91
Pk 178,92,93
Pk 179,94,95
Ar 80,8
Ar 81,9
Ar 82,10
Ar 83,11
Ar 84,28
Ar 85,29
Ar 86,30
Ar 87,31
v_pk_mul_f32 v[80:81], v[80:81], v[242:243] op_sel_hi:[1,0]
v_pk_mul_f32 v[82:83], v[82:83], v[242:243] op_sel_hi:[1,0]
v_pk_mul_f32 v[84:85], v[84:85], v[242:243] op_sel_hi:[1,0]
v_pk_mul_f32 v[86:87], v[86:87], v[242:243] op_sel_hi:[1,0]
Pk 192,80,81
Pk 193,82,83
Pk 194,84,85
Pk 195,86,87
Ar 88,12
Ar 89,13
Ar 90,14
Ar 91,15
Ar 92,32
Ar 93,33
Ar 94,34
Ar 95,35
v_pk_mul_f32 v[88:89], v[88:89], v[242:243] op_sel:[0,1] op_sel_hi:[1,1]
v_pk_mul_f32 v[90:91], v[90:91], v[242:243] op_sel:[0,1] op_sel_hi:[1,1]
v_pk_mul_f32 v[92:93], v[92:93], v[242:243] op_sel:[0,1] op_sel_hi:[1,1]
v_pk_mul_f32 v[94:95], v[94:95], v[242:243] op_sel:[0,1] op_sel_hi:[1,1]
Pk 208,88,89
Pk 209,90,91
Pk 210,92,93
Pk 211,94,95
Ar 80,40
Ar 81,41
Ar 82,42
Ar 83,43
Ar 84,60
Ar 85,61
Ar 86,62
Ar 87,63
v_pk_mul_f32 v[80:81], v[80:81], v[158:159] op_sel_hi:[1,0]
v_pk_mul_f32 v[82:83], v[82:83], v[158:159] op_sel_hi:[1,0]
v_pk_mul_f32 v[84:85], v[84:85], v[158:159] op_sel_hi:[1,0]
v_pk_mul_f32 v[86:87], v[86:87], v[158:159] op_sel_hi:[1,0]
Pk 164,80,81
Pk 165,82,83
Pk 166,84,85
Pk 167,86,87
Ar 88,44
Ar 89,45
Ar 90,46
Ar 91,47
Ar 92,64
Ar 93,65
Ar 94,66
Ar 95,67
v_pk_mul_f32 v[88:89], v[88:89], v[158:159] op_sel:[0,1] op_sel_hi:[1,1]
v_pk_mul_f32 v[90:91], v[90:91], v[158:159] op_sel:[0,1] op_sel_hi:[1,1]
v_pk_mul_f32 v[92:93], v[92:93], v[158:159] op_sel:[0,1] op_sel_hi:[1,1]
v_pk_mul_f32 v[94:95], v[94:95], v[158:159] op_sel:[0,1] op_sel_hi:[1,1]
Pk 180,88,89
Pk 181,90,91
Pk 182,92,93
Pk 183,94,95
Ar 80,48
Ar 81,49
Ar 82,50
Ar 83,51
Ar 84,68
Ar 85,69
Ar 86,70
Ar 87,71
v_pk_mul_f32 v[80:81], v[80:81], v[242:243] op_sel_hi:[1,0]
v_pk_mul_f32 v[82:83], v[82:83], v[242:243] op_sel_hi:[1,0]
v_pk_mul_f32 v[84:85], v[84:85], v[242:243] op_sel_hi:[1,0]
v_pk_mul_f32 v[86:87], v[86:87], v[242:243] op_sel_hi:[1,0]
Pk 196,80,81
Pk 197,82,83
Pk 198,84,85
Pk 199,86,87
Ar 88,52
Ar 89,53
Ar 90,54
Ar 91,55
Ar 92,72
Ar 93,73
Ar 94,74
Ar 95,75
v_pk_mul_f32 v[88:89], v[88:89], v[242:243] op_sel:[0,1] op_sel_hi:[1,1]
v_pk_mul_f32 v[90:91], v[90:91], v[242:243] op_sel:[0,1] op_sel_hi:[1,1]
v_pk_mul_f32 v[92:93], v[92:93], v[242:243] op_sel:[0,1] op_sel_hi:[1,1]
v_pk_mul_f32 v[94:95], v[94:95], v[242:243] op_sel:[0,1] op_sel_hi:[1,1]
Pk 212,88,89
Pk 213,90,91
Pk 214,92,93
Pk 215,94,95
Ar 80,80
Ar 81,81
Ar 82,82
Ar 83,83
Ar 84,100
Ar 85,101
Ar 86,102
Ar 87,103
v_pk_mul_f32 v[80:81], v[80:81], v[158:159] op_sel_hi:[1,0]
v_pk_mul_f32 v[82:83], v[82:83], v[158:159] op_sel_hi:[1,0]
v_pk_mul_f32 v[84:85], v[84:85], v[158:159] op_sel_hi:[1,0]
v_pk_mul_f32 v[86:87], v[86:87], v[158:159] op_sel_hi:[1,0]
Pk 168,80,81
Pk 169,82,83
Pk 170,84,85
Pk 171,86,87
Ar 88,84
Ar 89,85
Ar 90,86
Ar 91,87
Ar 92,104
Ar 93,105
Ar 94,106
Ar 95,107
v_pk_mul_f32 v[88:89], v[88:89], v[158:159] op_sel:[0,1] op_sel_hi:[1,1]
v_pk_mul_f32 v[90:91], v[90:91], v[158:159] op_sel:[0,1] op_sel_hi:[1,1]
v_pk_mul_f32 v[92:93], v[92:93], v[158:159] op_sel:[0,1] op_sel_hi:[1,1]
v_pk_mul_f32 v[94:95], v[94:95], v[158:159] op_sel:[0,1] op_sel_hi:[1,1]
Pk 184,88,89
Pk 185,90,91
Pk 186,92,93
Pk 187,94,95
Ar 80,88
Ar 81,89
Ar 82,90
Ar 83,91
Ar 84,108
Ar 85,109
Ar 86,110
Ar 87,111
v_pk_mul_f32 v[80:81], v[80:81], v[242:243] op_sel_hi:[1,0]
v_pk_mul_f32 v[82:83], v[82:83], v[242:243] op_sel_hi:[1,0]
v_pk_mul_f32 v[84:85], v[84:85], v[242:243] op_sel_hi:[1,0]
v_pk_mul_f32 v[86:87], v[86:87], v[242:243] op_sel_hi:[1,0]
Pk 200,80,81
Pk 201,82,83
Pk 202,84,85
Pk 203,86,87
Ar 88,92
Ar 89,93
Ar 90,94
Ar 91,95
Ar 92,112
Ar 93,113
Ar 94,114
Ar 95,115
v_pk_mul_f32 v[88:89], v[88:89], v[242:243] op_sel:[0,1] op_sel_hi:[1,1]
v_pk_mul_f32 v[90:91], v[90:91], v[242:243] op_sel:[0,1] op_sel_hi:[1,1]
v_pk_mul_f32 v[92:93], v[92:93], v[242:243] op_sel:[0,1] op_sel_hi:[1,1]
v_pk_mul_f32 v[94:95], v[94:95], v[242:243] op_sel:[0,1] op_sel_hi:[1,1]
Pk 216,88,89
Pk 217,90,91
Pk 218,92,93
Pk 219,94,95
Ar 80,120
Ar 81,121
Ar 82,122
Ar 83,123
Ar 84,140
Ar 85,141
Ar 86,142
Ar 87,143
v_pk_mul_f32 v[80:81], v[80:81], v[158:159] op_sel_hi:[1,0]
v_pk_mul_f32 v[82:83], v[82:83], v[158:159] op_sel_hi:[1,0]
v_pk_mul_f32 v[84:85], v[84:85], v[158:159] op_sel_hi:[1,0]
v_pk_mul_f32 v[86:87], v[86:87], v[158:159] op_sel_hi:[1,0]
Pk 172,80,81
Pk 173,82,83
Pk 174,84,85
Pk 175,86,87
Ar 88,124
Ar 89,125
Ar 90,126
Ar 91,127
Ar 92,144
Ar 93,145
Ar 94,146
Ar 95,147
v_pk_mul_f32 v[88:89], v[88:89], v[158:159] op_sel:[0,1] op_sel_hi:[1,1]
v_pk_mul_f32 v[90:91], v[90:91], v[158:159] op_sel:[0,1] op_sel_hi:[1,1]
v_pk_mul_f32 v[92:93], v[92:93], v[158:159] op_sel:[0,1] op_sel_hi:[1,1]
v_pk_mul_f32 v[94:95], v[94:95], v[158:159] op_sel:[0,1] op_sel_hi:[1,1]
Pk 188,88,89
Pk 189,90,91
Pk 190,92,93
Pk 191,94,95
Ar 80,128
Ar 81,129
Ar 82,130
Ar 83,131
Ar 84,148
Ar 85,149
Ar 86,150
Ar 87,151
v_pk_mul_f32 v[80:81], v[80:81], v[242:243] op_sel_hi:[1,0]
v_pk_mul_f32 v[82:83], v[82:83], v[242:243] op_sel_hi:[1,0]
v_pk_mul_f32 v[84:85], v[84:85], v[242:243] op_sel_hi:[1,0]
v_pk_mul_f32 v[86:87], v[86:87], v[242:243] op_sel_hi:[1,0]
Pk 204,80,81
Pk 205,82,83
Pk 206,84,85
Pk 207,86,87
Ar 88,132
Ar 89,133
Ar 90,134
Ar 91,135
Ar 92,152
Ar 93,153
Ar 94,154
Ar 95,155
v_pk_mul_f32 v[88:89], v[88:89], v[242:243] op_sel:[0,1] op_sel_hi:[1,1]
v_pk_mul_f32 v[90:91], v[90:91], v[242:243] op_sel:[0,1] op_sel_hi:[1,1]
v_pk_mul_f32 v[92:93], v[92:93], v[242:243] op_sel:[0,1] op_sel_hi:[1,1]
)A"
R"A(v_pk_mul_f32 v[94:95], v[94:95], v[242:243] op_sel:[0,1] op_sel_hi:[1,1]
Pk 220,88,89
Pk 221,90,91
Pk 222,92,93
Pk 223,94,95
v_and_b32 v159, 3, v245
v_lshl_add_u32 v159, v159, 10, s59
v_writelane_b32 v159, s77, 4
v_writelane_b32 v159, s78, 5
v_writelane_b32 v159, s80, 6
global_load_dword v158, v159, s[92:93] sc1
v_lshlrev_b32 v242, 4, v245
s_mov_b32 s48, 1
s_mov_b32 s47, 0
v_lshlrev_b32 v243, 2, v245
v_bfe_u32 v112, v245, 2, 2
v_and_b32 v113, 3, v245
v_lshrrev_b32 v114, 4, v245
v_lshlrev_b32 v113, 4, v113
v_lshl_add_u32 v113, v112, 10, v113
s_lshr_b32 s56, s24, 2
s_lshl_b32 s53, s24, 18
v_xor_b32 v114, s56, v114
v_lshl_add_u32 v72, v114, 6, v113
v_add_u32 v73, 3064, v72
v_add_u32 v74, 6128, v72
v_add_u32 v75, 9192, v72
Cvaa 20,160,192,120
s_waitcnt vmcnt(62)
s_barrier
s_mov_b32 m0, 132096
Ex 0
Mvaa 20,164,196
Ta 176,241,0
buffer_load_dwordx4 v248, s[40:43], s46 offen offset:0 nt lds
buffer_load_dwordx4 v150, s[40:43], s46 offen offset:1024 nt lds
Mvaa 20,168,200
Ex 1
Mvaa 20,172,204
Ta 178,241,1024
Pk 80,0,1
Cvaa 24,172,220,124
Ex 2
Mvaa 24,168,216
Ta 180,241,2048
Mvaa 24,164,212
Ex 3
Mvaa 24,160,208
Ta 182,241,3072
Pk 81,2,3
Cvaa 28,160,224,128
s_mov_b32 s57, 0
Ex 4
Mvaa 28,164,228
Ta 184,241,32
buffer_store_dwordx4 v[160:163], v242, s[96:99], s57 offen offset:0 sc1
Mvaa 28,168,232
Ex 5
Mvaa 28,172,236
Ta 186,241,1056
Pk 88,4,5
Cvaa 32,172,252,132
Ex 6
Mvaa 32,168,248
Ta 188,241,2080
buffer_load_dwordx4 v248, s[40:43], s46 offen offset:2048 nt lds
buffer_load_dwordx4 v150, s[40:43], s46 offen offset:3072 nt lds
Mvaa 32,164,244
Ex 7
Mvaa 32,160,240
Wl 3
Ta 190,241,3104
Pk 89,6,7
Zaav 0,176,40
Ex 8
Maav 0,180,44
Ra 160,240,18560
Zaav 4,176,48
Ex 9
Maav 4,180,52
Ra 164,240,18816
Pk 96,8,9
Zaav 8,176,56
s_mov_b32 m0, 136224
Ex 10
Maav 8,180,60
Ra 168,240,19072
buffer_load_dwordx4 v249, s[40:43], s46 offen offset:0 nt lds
buffer_load_dwordx4 v151, s[40:43], s46 offen offset:1024 nt lds
Zaav 12,176,64
Ex 11
Maav 12,180,68
Wl 3
Ra 172,240,19328
Pk 97,10,11
Zaav 20,184,40
Ex 12
Maav 20,188,44
Zaav 24,184,48
Ex 13
Maav 24,188,52
Pk 104,12,13
Zaav 28,184,56
Ex 14
Maav 28,188,60
buffer_load_dwordx4 v249, s[40:43], s46 offen offset:2048 nt lds
buffer_load_dwordx4 v151, s[40:43], s46 offen offset:3072 nt lds
Zaav 32,184,64
Ex 15
Maav 32,188,68
Pk 105,14,15
Maav 16,156,44
Maav 36,156,52
Maav 56,156,60
Maav 76,156,68
Wl 0
Cvaa 0,160,192,120
s_mov_b32 m0, 140352
Ex 20
Mvaa 0,164,196
Ta 176,241,256
buffer_load_dwordx4 v250, s[40:43], s46 offen offset:0 nt lds
buffer_load_dwordx4 v152, s[40:43], s46 offen offset:1024 nt lds
Mvaa 0,168,200
Ex 21
Mvaa 0,172,204
Ta 178,241,1280
Pk 82,20,21
Cvaa 4,172,220,124
Ex 22
Mvaa 4,168,216
Ta 180,241,2304
Mvaa 4,164,212
s_mov_b32 s57, 0
Ex 23
Mvaa 4,160,208
Ta 182,241,3328
buffer_store_dwordx4 v[164:167], v242, s[96:99], s57 offen offset:1024 sc1
Cvaa 8,160,224,128
Ex 24
Mvaa 8,164,228
Ta 184,241,288
Pk 83,22,23
Mvaa 8,168,232
Ex 25
Mvaa 8,172,236
Ta 186,241,1312
buffer_load_dwordx4 v250, s[40:43], s46 offen offset:2048 nt lds
buffer_load_dwordx4 v152, s[40:43], s46 offen offset:3072 nt lds
Cvaa 12,172,252,132
Ex 26
Mvaa 12,168,248
Ta 188,241,2336
Pk 90,24,25
Mvaa 12,164,244
Ex 27
Mvaa 12,160,240
Wl 3
Ta 190,241,3360
Pk 91,26,27
Zaav 40,176,40
s_mov_b32 m0, 144480
Ex 28
Maav 40,180,44
Ra 160,240,19584
buffer_load_dwordx4 v251, s[40:43], s46 offen offset:0 nt lds
buffer_load_dwordx4 v153, s[40:43], s46 offen offset:1024 nt lds
Zaav 44,176,48
Ex 29
Maav 44,180,52
Ra 164,240,19840
Pk 98,28,29
Zaav 48,176,56
Ex 30
Maav 48,180,60
Ra 168,240,20096
Zaav 52,176,64
Ex 31
Maav 52,180,68
Wl 3
Ra 172,240,20352
Pk 99,30,31
Zaav 60,184,40
Ex 32
Maav 60,188,44
buffer_load_dwordx4 v251, s[40:43], s46 offen offset:2048 nt lds
buffer_load_dwordx4 v153, s[40:43], s46 offen offset:3072 nt lds
Zaav 64,184,48
Ex 33
Maav 64,188,52
Pk 106,32,33
Zaav 68,184,56
Ex 34
Maav 68,188,60
Zaav 72,184,64
Ex 35
Maav 72,188,68
Wl 0
Pk 107,34,35
s_mov_b64 exec, 0
s_swappc_b64 s[12:13], s[14:15]
s_add_u32 s14, s14, 4096
s_addc_u32 s15, s15, 0
Cvaa 20,160,192,120
s_add_u32 s46, s46, s69
s_xor_b32 s69, s69, 114688
s_mov_b32 m0, 0
Ex 0
Mvaa 20,164,196
Ta 176,241,512
buffer_load_dwordx4 v248, s[36:39], s44 offen offset:2048 nt lds
buffer_load_dwordx4 v150, s[36:39], s44 offen offset:3072 nt lds
Mvaa 20,168,200
Ex 1
Mvaa 20,172,204
Ta 178,241,1536
Pk 84,0,1
Cvaa 24,172,220,124
Ex 2
Mvaa 24,168,216
Ta 180,241,2560
Mvaa 24,164,212
Ex 3
Mvaa 24,160,208
Ta 182,241,3584
Pk 85,2,3
Cvaa 28,160,224,128
s_mov_b32 m0, 4096
Ex 4
Mvaa 28,164,228
Ta 184,241,544
buffer_load_dwordx4 v249, s[36:39], s44 offen offset:2048 nt lds
buffer_load_dwordx4 v151, s[36:39], s44 offen offset:3072 nt lds
Mvaa 28,168,232
Ex 5
Mvaa 28,172,236
Ta 186,241,1568
Pk 92,4,5
Cvaa 32,172,252,132
Ex 6
Mvaa 32,168,248
Ta 188,241,2592
Mvaa 32,164,244
Ex 7
Mvaa 32,160,240
Wl 3
Ta 190,241,3616
Pk 93,6,7
Zaav 80,176,40
Ex 8
Maav 80,180,44
Ra 160,240,33024
Zaav 84,176,48
s_mov_b32 m0, 8224
Ex 9
Maav 84,180,52
Ra 164,240,33280
buffer_load_dwordx4 v250, s[36:39], s44 offen offset:2048 nt lds
buffer_load_dwordx4 v152, s[36:39], s44 offen offset:3072 nt lds
Zaav 88,176,56
Ex 10
Maav 88,180,60
Ra 168,240,33536
Pk 100,8,9
Zaav 92,176,64
Ex 11
Maav 92,180,68
Wl 3
Ra 172,240,33792
Pk 101,10,11
Zaav 100,184,40
Ex 12
Maav 100,188,44
Zaav 104,184,48
s_mov_b32 m0, 12320
Ex 13
Maav 104,188,52
buffer_load_dwordx4 v251, s[36:39], s44 offen offset:2048 nt lds
buffer_load_dwordx4 v153, s[36:39], s44 offen offset:3072 nt lds
Zaav 108,184,56
Ex 14
Maav 108,188,60
Pk 108,12,13
Zaav 112,184,64
Ex 15
Maav 112,188,68
Pk 109,14,15
Maav 16,156,80
Maav 36,156,88
Maav 56,156,96
Maav 76,156,104
Wl 0
Cvaa 0,160,192,120
s_add_u32 s44, s44, 1024
s_mov_b32 m0, 16512
Ex 20
Mvaa 0,164,196
Ta 176,241,768
buffer_load_dwordx4 v248, s[36:39], s44 offen offset:0 nt lds
buffer_load_dwordx4 v150, s[36:39], s44 offen offset:1024 nt lds
Mvaa 0,168,200
Ex 21
Mvaa 0,172,204
Ta 178,241,1792
Pk 86,20,21
Cvaa 4,172,220,124
Ex 22
Mvaa 4,168,216
Ta 180,241,2816
Mvaa 4,164,212
s_mov_b32 s57, 0
Ex 23
Mvaa 4,160,208
Ta 182,241,3840
buffer_store_dwordx4 v[168:171], v242, s[96:99], s57 offen offset:2048 sc1
Cvaa 8,160,224,128
Ex 24
Mvaa 8,164,228
Ta 184,241,800
Pk 87,22,23
Mvaa 8,168,232
s_mov_b32 m0, 20608
Ex 25
Mvaa 8,172,236
Ta 186,241,1824
buffer_load_dwordx4 v249, s[36:39], s44 offen offset:0 nt lds
buffer_load_dwordx4 v151, s[36:39], s44 offen offset:1024 nt lds
Cvaa 12,172,252,132
Ex 26
Mvaa 12,168,248
Ta 188,241,2848
Pk 94,24,25
Mvaa 12,164,244
Ex 27
Mvaa 12,160,240
Wl 3
Ta 190,241,3872
Pk 95,26,27
Zaav 120,176,40
s_mov_b32 m0, 24736
Ex 28
Maav 120,180,44
Ra 160,240,34048
buffer_load_dwordx4 v250, s[36:39], s44 offen offset:0 nt lds
buffer_load_dwordx4 v152, s[36:39], s44 offen offset:1024 nt lds
Zaav 124,176,48
Ex 29
Maav 124,180,52
Ra 164,240,34304
Pk 102,28,29
Zaav 128,176,56
Ex 30
Maav 128,180,60
Ra 168,240,34560
Zaav 132,176,64
Ex 31
Maav 132,180,68
Wl 3
Ra 172,240,34816
Pk 103,30,31
Zaav 140,184,40
s_mov_b32 m0, 28832
Ex 32
Maav 140,188,44
buffer_load_dwordx4 v251, s[36:39], s44 offen offset:0 nt lds
buffer_load_dwordx4 v153, s[36:39], s44 offen offset:1024 nt lds
Zaav 144,184,48
Ex 33
Maav 144,188,52
Pk 110,32,33
Zaav 148,184,56
Ex 34
Maav 148,188,60
Zaav 152,184,64
Ex 35
Maav 152,188,68
Wl 0
Pk 111,34,35
Cvaa 20,160,192,120
s_waitcnt vmcnt(62)
s_barrier
s_mov_b32 m0, 82560
Ex 0
Mvaa 20,164,196
Ta 176,241,16512
buffer_load_dwordx4 v248, s[40:43], s46 offen offset:0 nt lds
buffer_load_dwordx4 v150, s[40:43], s46 offen offset:1024 nt lds
Mvaa 20,168,200
Ex 1
Mvaa 20,172,204
Ta 178,241,17536
Pk 40,0,1
Cvaa 24,172,220,124
Ex 2
Mvaa 24,168,216
Ta 180,241,18560
Mvaa 24,164,212
Ex 3
Mvaa 24,160,208
Ta 182,241,19584
)A"
R"A(Pk 41,2,3
Cvaa 28,160,224,128
s_mov_b32 s57, 0
Ex 4
Mvaa 28,164,228
Ta 184,241,16544
buffer_store_dwordx4 v[172:175], v242, s[96:99], s57 offen offset:3072 sc1
Mvaa 28,168,232
Ex 5
Mvaa 28,172,236
Ta 186,241,17568
Pk 48,4,5
Cvaa 32,172,252,132
Ex 6
Mvaa 32,168,248
Ta 188,241,18592
buffer_load_dwordx4 v248, s[40:43], s46 offen offset:2048 nt lds
buffer_load_dwordx4 v150, s[40:43], s46 offen offset:3072 nt lds
Mvaa 32,164,244
Ex 7
Mvaa 32,160,240
Wl 3
Ta 190,241,19616
Pk 49,6,7
Maav 0,176,80
Ex 8
Maav 0,180,84
Ra 160,240,35072
Maav 4,176,88
Ex 9
Maav 4,180,92
Ra 164,240,35328
Pk 56,8,9
Maav 8,176,96
s_mov_b32 m0, 86688
Ex 10
Maav 8,180,100
Ra 168,240,35584
buffer_load_dwordx4 v249, s[40:43], s46 offen offset:0 nt lds
buffer_load_dwordx4 v151, s[40:43], s46 offen offset:1024 nt lds
Maav 12,176,104
Ex 11
Maav 12,180,108
Wl 3
Ra 172,240,35840
Pk 57,10,11
Maav 20,184,80
Ex 12
Maav 20,188,84
Maav 24,184,88
Ex 13
Maav 24,188,92
Pk 64,12,13
Maav 28,184,96
Ex 14
Maav 28,188,100
buffer_load_dwordx4 v249, s[40:43], s46 offen offset:2048 nt lds
buffer_load_dwordx4 v151, s[40:43], s46 offen offset:3072 nt lds
Maav 32,184,104
Ex 15
Maav 32,188,108
Pk 65,14,15
Maav 16,156,84
Maav 36,156,92
Maav 56,156,100
Maav 76,156,108
Wl 0
Cvaa 0,160,192,120
s_mov_b32 m0, 90816
Ex 20
Mvaa 0,164,196
Ta 176,241,16768
buffer_load_dwordx4 v250, s[40:43], s46 offen offset:0 nt lds
buffer_load_dwordx4 v152, s[40:43], s46 offen offset:1024 nt lds
Mvaa 0,168,200
Ex 21
Mvaa 0,172,204
Ta 178,241,17792
Pk 42,20,21
Cvaa 4,172,220,124
Ex 22
Mvaa 4,168,216
Ta 180,241,18816
Mvaa 4,164,212
Ex 23
Mvaa 4,160,208
Ta 182,241,19840
Pk 43,22,23
Cvaa 8,160,224,128
Ex 24
Mvaa 8,164,228
Ta 184,241,16800
buffer_load_dwordx4 v250, s[40:43], s46 offen offset:2048 nt lds
buffer_load_dwordx4 v152, s[40:43], s46 offen offset:3072 nt lds
Mvaa 8,168,232
Ex 25
Mvaa 8,172,236
Ta 186,241,17824
Pk 50,24,25
Cvaa 12,172,252,132
Ex 26
Mvaa 12,168,248
Ta 188,241,18848
Mvaa 12,164,244
Ex 27
Mvaa 12,160,240
Wl 3
Ta 190,241,19872
Pk 51,26,27
Maav 40,176,80
s_mov_b32 m0, 94944
Ex 28
Maav 40,180,84
Ra 160,240,36096
buffer_load_dwordx4 v251, s[40:43], s46 offen offset:0 nt lds
buffer_load_dwordx4 v153, s[40:43], s46 offen offset:1024 nt lds
Maav 44,176,88
Ex 29
Maav 44,180,92
Ra 164,240,36352
Pk 58,28,29
Maav 48,176,96
Ex 30
Maav 48,180,100
Ra 168,240,36608
Maav 52,176,104
Ex 31
Maav 52,180,108
Wl 3
Ra 172,240,36864
Pk 59,30,31
Maav 60,184,80
Ex 32
Maav 60,188,84
buffer_load_dwordx4 v251, s[40:43], s46 offen offset:2048 nt lds
buffer_load_dwordx4 v153, s[40:43], s46 offen offset:3072 nt lds
Maav 64,184,88
Ex 33
Maav 64,188,92
Pk 66,32,33
Maav 68,184,96
Ex 34
Maav 68,188,100
Maav 72,184,104
Ex 35
Maav 72,188,108
Wl 0
Pk 67,34,35
s_mov_b64 exec, 0
s_swappc_b64 s[12:13], s[14:15]
s_add_u32 s14, s14, 4096
s_addc_u32 s15, s15, 0
Cvaa 20,160,192,120
s_add_u32 s46, s46, 1024
s_mov_b32 m0, 16512
Ex 0
Mvaa 20,164,196
Ta 176,241,17024
buffer_load_dwordx4 v248, s[36:39], s44 offen offset:2048 nt lds
buffer_load_dwordx4 v150, s[36:39], s44 offen offset:3072 nt lds
Mvaa 20,168,200
Ex 1
Mvaa 20,172,204
Ta 178,241,18048
Pk 44,0,1
Cvaa 24,172,220,124
Ex 2
Mvaa 24,168,216
Ta 180,241,19072
Mvaa 24,164,212
Ex 3
Mvaa 24,160,208
Ta 182,241,20096
Pk 45,2,3
Cvaa 28,160,224,128
s_mov_b32 s57, 16384
Ex 4
Mvaa 28,164,228
Ta 184,241,17056
buffer_store_dwordx4 v[176:179], v242, s[96:99], s57 offen offset:0 sc1
Mvaa 28,168,232
Ex 5
Mvaa 28,172,236
Ta 186,241,18080
Pk 52,4,5
Cvaa 32,172,252,132
s_mov_b32 m0, 20608
Ex 6
Mvaa 32,168,248
Ta 188,241,19104
buffer_load_dwordx4 v249, s[36:39], s44 offen offset:2048 nt lds
buffer_load_dwordx4 v151, s[36:39], s44 offen offset:3072 nt lds
Mvaa 32,164,244
Ex 7
Mvaa 32,160,240
Wl 3
Ta 190,241,20128
Pk 53,6,7
Maav 80,176,80
Ex 8
Maav 80,180,84
Ra 160,240,49536
Maav 84,176,88
Ex 9
Maav 84,180,92
Ra 164,240,49792
Pk 60,8,9
Maav 88,176,96
s_mov_b32 m0, 24736
Ex 10
Maav 88,180,100
Ra 168,240,50048
buffer_load_dwordx4 v250, s[36:39], s44 offen offset:2048 nt lds
buffer_load_dwordx4 v152, s[36:39], s44 offen offset:3072 nt lds
Maav 92,176,104
Ex 11
Maav 92,180,108
Wl 3
Ra 172,240,50304
Pk 61,10,11
Maav 100,184,80
Ex 12
Maav 100,188,84
Maav 104,184,88
Ex 13
Maav 104,188,92
Pk 68,12,13
Maav 108,184,96
s_mov_b32 m0, 28832
Ex 14
Maav 108,188,100
buffer_load_dwordx4 v251, s[36:39], s44 offen offset:2048 nt lds
buffer_load_dwordx4 v153, s[36:39], s44 offen offset:3072 nt lds
Maav 112,184,104
Ex 15
Maav 112,188,108
Pk 69,14,15
Maav 16,156,40
Maav 36,156,48
Maav 56,156,56
Maav 76,156,64
Wl 0
Cvaa 0,160,192,120
s_add_u32 s44, s44, 3072
s_mov_b32 m0, 33024
Ex 20
Mvaa 0,164,196
Ta 176,241,17280
buffer_load_dwordx4 v248, s[36:39], s44 offen offset:0 nt lds
buffer_load_dwordx4 v150, s[36:39], s44 offen offset:1024 nt lds
Mvaa 0,168,200
Ex 21
Mvaa 0,172,204
Ta 178,241,18304
Pk 46,20,21
Cvaa 4,172,220,124
Ex 22
Mvaa 4,168,216
Ta 180,241,19328
Mvaa 4,164,212
Ex 23
Mvaa 4,160,208
Ta 182,241,20352
Pk 47,22,23
Cvaa 8,160,224,128
s_mov_b32 m0, 37120
Ex 24
Mvaa 8,164,228
Ta 184,241,17312
buffer_load_dwordx4 v249, s[36:39], s44 offen offset:0 nt lds
buffer_load_dwordx4 v151, s[36:39], s44 offen offset:1024 nt lds
Mvaa 8,168,232
Ex 25
Mvaa 8,172,236
Ta 186,241,18336
Pk 54,24,25
Cvaa 12,172,252,132
Ex 26
Mvaa 12,168,248
Ta 188,241,19360
Mvaa 12,164,244
Ex 27
Mvaa 12,160,240
Wl 3
Ta 190,241,20384
Pk 55,26,27
Maav 120,176,80
s_mov_b32 m0, 41248
Ex 28
Maav 120,180,84
Ra 160,240,50560
buffer_load_dwordx4 v250, s[36:39], s44 offen offset:0 nt lds
buffer_load_dwordx4 v152, s[36:39], s44 offen offset:1024 nt lds
Maav 124,176,88
Ex 29
Maav 124,180,92
Ra 164,240,50816
Pk 62,28,29
Maav 128,176,96
Ex 30
Maav 128,180,100
Ra 168,240,51072
Maav 132,176,104
Ex 31
Maav 132,180,108
Wl 3
Ra 172,240,51328
Pk 63,30,31
Maav 140,184,80
s_mov_b32 m0, 45344
Ex 32
Maav 140,188,84
buffer_load_dwordx4 v251, s[36:39], s44 offen offset:0 nt lds
buffer_load_dwordx4 v153, s[36:39], s44 offen offset:1024 nt lds
Maav 144,184,88
Ex 33
Maav 144,188,92
Pk 70,32,33
Maav 148,184,96
Ex 34
Maav 148,188,100
Maav 152,184,104
Ex 35
Maav 152,188,108
Wl 0
Pk 71,34,35
Cvaa 20,160,192,120
s_waitcnt vmcnt(62)
s_barrier
s_mov_b32 m0, 99072
Ex 0
Mvaa 20,164,196
Ta 176,241,33024
buffer_load_dwordx4 v248, s[40:43], s46 offen offset:0 nt lds
buffer_load_dwordx4 v150, s[40:43], s46 offen offset:1024 nt lds
Mvaa 20,168,200
Ex 1
Mvaa 20,172,204
Ta 178,241,34048
Pk 80,0,1
Cvaa 24,172,220,124
Ex 2
Mvaa 24,168,216
Ta 180,241,35072
Mvaa 24,164,212
Ex 3
Mvaa 24,160,208
Ta 182,241,36096
Pk 81,2,3
Cvaa 28,160,224,128
s_mov_b32 s57, 16384
Ex 4
Mvaa 28,164,228
Ta 184,241,33056
buffer_store_dwordx4 v[180:183], v242, s[96:99], s57 offen offset:1024 sc1
Mvaa 28,168,232
Ex 5
Mvaa 28,172,236
Ta 186,241,34080
Pk 88,4,5
Cvaa 32,172,252,132
Ex 6
Mvaa 32,168,248
Ta 188,241,35104
buffer_load_dwordx4 v248, s[40:43], s46 offen offset:2048 nt lds
buffer_load_dwordx4 v150, s[40:43], s46 offen offset:3072 nt lds
Mvaa 32,164,244
Ex 7
Mvaa 32,160,240
Wl 3
Ta 190,241,36128
Pk 89,6,7
Maav 0,176,40
Ex 8
Maav 0,180,44
Ra 160,240,51584
Maav 4,176,48
Ex 9
Maav 4,180,52
Ra 164,240,51840
Pk 96,8,9
Maav 8,176,56
s_mov_b32 m0, 103200
Ex 10
Maav 8,180,60
Ra 168,240,52096
buffer_load_dwordx4 v249, s[40:43], s46 offen offset:0 nt lds
buffer_load_dwordx4 v151, s[40:43], s46 offen offset:1024 nt lds
Maav 12,176,64
Ex 11
Maav 12,180,68
Wl 3
Ra 172,240,52352
Pk 97,10,11
Maav 20,184,40
Ex 12
Maav 20,188,44
Maav 24,184,48
Ex 13
Maav 24,188,52
)A"
R"A(Pk 104,12,13
Maav 28,184,56
Ex 14
Maav 28,188,60
buffer_load_dwordx4 v249, s[40:43], s46 offen offset:2048 nt lds
buffer_load_dwordx4 v151, s[40:43], s46 offen offset:3072 nt lds
Maav 32,184,64
Ex 15
Maav 32,188,68
Pk 105,14,15
Maav 16,156,44
Maav 36,156,52
Maav 56,156,60
Maav 76,156,68
Wl 0
Cvaa 0,160,192,120
s_mov_b32 m0, 107328
Ex 20
Mvaa 0,164,196
Ta 176,241,33280
buffer_load_dwordx4 v250, s[40:43], s46 offen offset:0 nt lds
buffer_load_dwordx4 v152, s[40:43], s46 offen offset:1024 nt lds
Mvaa 0,168,200
Ex 21
Mvaa 0,172,204
Ta 178,241,34304
Pk 82,20,21
Cvaa 4,172,220,124
Ex 22
Mvaa 4,168,216
Ta 180,241,35328
Mvaa 4,164,212
s_mov_b32 s57, 16384
Ex 23
Mvaa 4,160,208
Ta 182,241,36352
buffer_store_dwordx4 v[184:187], v242, s[96:99], s57 offen offset:2048 sc1
Cvaa 8,160,224,128
Ex 24
Mvaa 8,164,228
Ta 184,241,33312
Pk 83,22,23
Mvaa 8,168,232
Ex 25
Mvaa 8,172,236
Ta 186,241,34336
buffer_load_dwordx4 v250, s[40:43], s46 offen offset:2048 nt lds
buffer_load_dwordx4 v152, s[40:43], s46 offen offset:3072 nt lds
Cvaa 12,172,252,132
Ex 26
Mvaa 12,168,248
Ta 188,241,35360
Pk 90,24,25
Mvaa 12,164,244
Ex 27
Mvaa 12,160,240
Wl 3
Ta 190,241,36384
Pk 91,26,27
Maav 40,176,40
s_mov_b32 m0, 111456
Ex 28
Maav 40,180,44
Ra 160,240,52608
buffer_load_dwordx4 v251, s[40:43], s46 offen offset:0 nt lds
buffer_load_dwordx4 v153, s[40:43], s46 offen offset:1024 nt lds
Maav 44,176,48
Ex 29
Maav 44,180,52
Ra 164,240,52864
Pk 98,28,29
Maav 48,176,56
Ex 30
Maav 48,180,60
Ra 168,240,53120
Maav 52,176,64
Ex 31
Maav 52,180,68
Wl 3
Ra 172,240,53376
Pk 99,30,31
Maav 60,184,40
Ex 32
Maav 60,188,44
buffer_load_dwordx4 v251, s[40:43], s46 offen offset:2048 nt lds
buffer_load_dwordx4 v153, s[40:43], s46 offen offset:3072 nt lds
Maav 64,184,48
Ex 33
Maav 64,188,52
Pk 106,32,33
Maav 68,184,56
Ex 34
Maav 68,188,60
Maav 72,184,64
Ex 35
Maav 72,188,68
Wl 0
Pk 107,34,35
Cvaa 20,160,192,120
s_add_u32 s46, s46, 3072
s_mov_b32 m0, 33024
Ex 0
Mvaa 20,164,196
Ta 176,241,33536
buffer_load_dwordx4 v248, s[36:39], s44 offen offset:2048 nt lds
buffer_load_dwordx4 v150, s[36:39], s44 offen offset:3072 nt lds
Mvaa 20,168,200
Ex 1
Mvaa 20,172,204
Ta 178,241,34560
Pk 84,0,1
Cvaa 24,172,220,124
Ex 2
Mvaa 24,168,216
Ta 180,241,35584
Mvaa 24,164,212
Ex 3
Mvaa 24,160,208
Ta 182,241,36608
Pk 85,2,3
Cvaa 28,160,224,128
s_mov_b32 m0, 37120
Ex 4
Mvaa 28,164,228
Ta 184,241,33568
buffer_load_dwordx4 v249, s[36:39], s44 offen offset:2048 nt lds
buffer_load_dwordx4 v151, s[36:39], s44 offen offset:3072 nt lds
Mvaa 28,168,232
Ex 5
Mvaa 28,172,236
Ta 186,241,34592
Pk 92,4,5
Cvaa 32,172,252,132
Ex 6
Mvaa 32,168,248
Ta 188,241,35616
Mvaa 32,164,244
Ex 7
Mvaa 32,160,240
Wl 3
Ta 190,241,36640
Pk 93,6,7
Maav 80,176,40
Ex 8
Maav 80,180,44
Ra 160,240,0
Maav 84,176,48
s_mov_b32 m0, 41248
Ex 9
Maav 84,180,52
Ra 164,240,256
buffer_load_dwordx4 v250, s[36:39], s44 offen offset:2048 nt lds
buffer_load_dwordx4 v152, s[36:39], s44 offen offset:3072 nt lds
Maav 88,176,56
Ex 10
Maav 88,180,60
Ra 168,240,512
Pk 100,8,9
Maav 92,176,64
Ex 11
Maav 92,180,68
Wl 3
Ra 172,240,768
Pk 101,10,11
Maav 100,184,40
Ex 12
Maav 100,188,44
Maav 104,184,48
s_mov_b32 m0, 45344
Ex 13
Maav 104,188,52
buffer_load_dwordx4 v251, s[36:39], s44 offen offset:2048 nt lds
buffer_load_dwordx4 v153, s[36:39], s44 offen offset:3072 nt lds
Maav 108,184,56
Ex 14
Maav 108,188,60
Pk 108,12,13
Maav 112,184,64
Ex 15
Maav 112,188,68
Pk 109,14,15
Maav 16,156,80
Maav 36,156,88
Maav 56,156,96
Maav 76,156,104
Wl 0
Cvaa 0,160,192,120
s_add_u32 s44, s44, 1024
s_mov_b32 m0, 49536
Ex 20
Mvaa 0,164,196
Ta 176,241,33792
buffer_load_dwordx4 v248, s[36:39], s44 offen offset:0 nt lds
buffer_load_dwordx4 v150, s[36:39], s44 offen offset:1024 nt lds
Mvaa 0,168,200
Ex 21
Mvaa 0,172,204
Ta 178,241,34816
Pk 86,20,21
Cvaa 4,172,220,124
Ex 22
Mvaa 4,168,216
Ta 180,241,35840
Mvaa 4,164,212
s_mov_b32 s57, 16384
Ex 23
Mvaa 4,160,208
Ta 182,241,36864
buffer_store_dwordx4 v[188:191], v242, s[96:99], s57 offen offset:3072 sc1
Cvaa 8,160,224,128
Ex 24
Mvaa 8,164,228
Ta 184,241,33824
Pk 87,22,23
Mvaa 8,168,232
s_mov_b32 m0, 53632
Ex 25
Mvaa 8,172,236
Ta 186,241,34848
buffer_load_dwordx4 v249, s[36:39], s44 offen offset:0 nt lds
buffer_load_dwordx4 v151, s[36:39], s44 offen offset:1024 nt lds
Cvaa 12,172,252,132
Ex 26
Mvaa 12,168,248
Ta 188,241,35872
Pk 94,24,25
Mvaa 12,164,244
Ex 27
Mvaa 12,160,240
Wl 3
Ta 190,241,36896
Pk 95,26,27
Maav 120,176,40
s_mov_b32 m0, 57760
Ex 28
Maav 120,180,44
Ra 160,240,1024
buffer_load_dwordx4 v250, s[36:39], s44 offen offset:0 nt lds
buffer_load_dwordx4 v152, s[36:39], s44 offen offset:1024 nt lds
Maav 124,176,48
Ex 29
Maav 124,180,52
Ra 164,240,1280
Pk 102,28,29
Maav 128,176,56
Ex 30
Maav 128,180,60
Ra 168,240,1536
Maav 132,176,64
Ex 31
Maav 132,180,68
Wl 3
Ra 172,240,1792
Pk 103,30,31
Maav 140,184,40
s_mov_b32 m0, 61856
Ex 32
Maav 140,188,44
buffer_load_dwordx4 v251, s[36:39], s44 offen offset:0 nt lds
buffer_load_dwordx4 v153, s[36:39], s44 offen offset:1024 nt lds
Maav 144,184,48
Ex 33
Maav 144,188,52
Pk 110,32,33
Maav 148,184,56
Ex 34
Maav 148,188,60
Maav 152,184,64
Ex 35
Maav 152,188,68
Wl 0
Pk 111,34,35
Cvaa 20,160,192,120
s_waitcnt vmcnt(62)
s_barrier
s_mov_b32 m0, 115584
Ex 0
Mvaa 20,164,196
Ta 176,241,49536
buffer_load_dwordx4 v248, s[40:43], s46 offen offset:0 nt lds
buffer_load_dwordx4 v150, s[40:43], s46 offen offset:1024 nt lds
Mvaa 20,168,200
Ex 1
Mvaa 20,172,204
Ta 178,241,50560
Pk 40,0,1
Cvaa 24,172,220,124
Ex 2
Mvaa 24,168,216
Ta 180,241,51584
Mvaa 24,164,212
Ex 3
Mvaa 24,160,208
Ta 182,241,52608
Pk 41,2,3
Cvaa 28,160,224,128
s_mov_b32 s57, 32768
Ex 4
Mvaa 28,164,228
Ta 184,241,49568
buffer_store_dwordx4 v[192:195], v242, s[96:99], s57 offen offset:0 sc1
Mvaa 28,168,232
Ex 5
Mvaa 28,172,236
Ta 186,241,50592
Pk 48,4,5
Cvaa 32,172,252,132
Ex 6
Mvaa 32,168,248
Ta 188,241,51616
buffer_load_dwordx4 v248, s[40:43], s46 offen offset:2048 nt lds
buffer_load_dwordx4 v150, s[40:43], s46 offen offset:3072 nt lds
Mvaa 32,164,244
Ex 7
Mvaa 32,160,240
Wl 3
Ta 190,241,52640
Pk 49,6,7
Maav 0,176,80
Ex 8
Maav 0,180,84
Ra 160,240,2048
Maav 4,176,88
Ex 9
Maav 4,180,92
Ra 164,240,2304
Pk 56,8,9
Maav 8,176,96
s_mov_b32 m0, 119712
Ex 10
Maav 8,180,100
Ra 168,240,2560
buffer_load_dwordx4 v249, s[40:43], s46 offen offset:0 nt lds
buffer_load_dwordx4 v151, s[40:43], s46 offen offset:1024 nt lds
Maav 12,176,104
Ex 11
Maav 12,180,108
Wl 3
Ra 172,240,2816
Pk 57,10,11
Maav 20,184,80
Ex 12
Maav 20,188,84
Maav 24,184,88
Ex 13
Maav 24,188,92
Pk 64,12,13
Maav 28,184,96
Ex 14
Maav 28,188,100
buffer_load_dwordx4 v249, s[40:43], s46 offen offset:2048 nt lds
buffer_load_dwordx4 v151, s[40:43], s46 offen offset:3072 nt lds
Maav 32,184,104
Ex 15
Maav 32,188,108
Pk 65,14,15
Maav 16,156,84
Maav 36,156,92
Maav 56,156,100
Maav 76,156,108
Wl 0
Cvaa 0,160,192,120
s_mov_b32 m0, 123840
Ex 20
Mvaa 0,164,196
Ta 176,241,49792
buffer_load_dwordx4 v250, s[40:43], s46 offen offset:0 nt lds
buffer_load_dwordx4 v152, s[40:43], s46 offen offset:1024 nt lds
Mvaa 0,168,200
Ex 21
Mvaa 0,172,204
Ta 178,241,50816
Pk 42,20,21
Cvaa 4,172,220,124
Ex 22
Mvaa 4,168,216
Ta 180,241,51840
Mvaa 4,164,212
Ex 23
Mvaa 4,160,208
Ta 182,241,52864
Pk 43,22,23
Cvaa 8,160,224,128
Ex 24
Mvaa 8,164,228
Ta 184,241,49824
buffer_load_dwordx4 v250, s[40:43], s46 offen offset:2048 nt lds
buffer_load_dwordx4 v152, s[40:43], s46 offen offset:3072 nt lds
Mvaa 8,168,232
Ex 25
Mvaa 8,172,236
Ta 186,241,50848
Pk 50,24,25
Cvaa 12,172,252,132
Ex 26
Mvaa 12,168,248
Ta 188,241,51872
Mvaa 12,164,244
Ex 27
Mvaa 12,160,240
)A"
R"A(Wl 3
Ta 190,241,52896
Pk 51,26,27
Maav 40,176,80
s_mov_b32 m0, 127968
Ex 28
Maav 40,180,84
Ra 160,240,3072
buffer_load_dwordx4 v251, s[40:43], s46 offen offset:0 nt lds
buffer_load_dwordx4 v153, s[40:43], s46 offen offset:1024 nt lds
Maav 44,176,88
Ex 29
Maav 44,180,92
Ra 164,240,3328
Pk 58,28,29
Maav 48,176,96
Ex 30
Maav 48,180,100
Ra 168,240,3584
Maav 52,176,104
Ex 31
Maav 52,180,108
Wl 3
Ra 172,240,3840
Pk 59,30,31
Maav 60,184,80
Ex 32
Maav 60,188,84
buffer_load_dwordx4 v251, s[40:43], s46 offen offset:2048 nt lds
buffer_load_dwordx4 v153, s[40:43], s46 offen offset:3072 nt lds
Maav 64,184,88
Ex 33
Maav 64,188,92
Pk 66,32,33
Maav 68,184,96
Ex 34
Maav 68,188,100
Maav 72,184,104
Ex 35
Maav 72,188,108
Wl 0
Pk 67,34,35
s_mov_b64 exec, 0
s_swappc_b64 s[12:13], s[14:15]
s_add_u32 s14, s14, 4096
s_addc_u32 s15, s15, 0
Cvaa 20,160,192,120
s_add_u32 s46, s46, 1024
s_mov_b32 m0, 49536
Ex 0
Mvaa 20,164,196
Ta 176,241,50048
buffer_load_dwordx4 v248, s[36:39], s44 offen offset:2048 nt lds
buffer_load_dwordx4 v150, s[36:39], s44 offen offset:3072 nt lds
Mvaa 20,168,200
Ex 1
Mvaa 20,172,204
Ta 178,241,51072
Pk 44,0,1
Cvaa 24,172,220,124
Ex 2
Mvaa 24,168,216
Ta 180,241,52096
Mvaa 24,164,212
Ex 3
Mvaa 24,160,208
Ta 182,241,53120
Pk 45,2,3
Cvaa 28,160,224,128
s_mov_b32 s57, 32768
Ex 4
Mvaa 28,164,228
Ta 184,241,50080
buffer_store_dwordx4 v[196:199], v242, s[96:99], s57 offen offset:1024 sc1
Mvaa 28,168,232
Ex 5
Mvaa 28,172,236
Ta 186,241,51104
Pk 52,4,5
Cvaa 32,172,252,132
s_mov_b32 m0, 53632
Ex 6
Mvaa 32,168,248
Ta 188,241,52128
buffer_load_dwordx4 v249, s[36:39], s44 offen offset:2048 nt lds
buffer_load_dwordx4 v151, s[36:39], s44 offen offset:3072 nt lds
Mvaa 32,164,244
Ex 7
Mvaa 32,160,240
Wl 3
Ta 190,241,53152
Pk 53,6,7
Maav 80,176,80
Ex 8
Maav 80,180,84
Ra 160,240,16512
Maav 84,176,88
Ex 9
Maav 84,180,92
Ra 164,240,16768
Pk 60,8,9
Maav 88,176,96
s_mov_b32 m0, 57760
Ex 10
Maav 88,180,100
Ra 168,240,17024
buffer_load_dwordx4 v250, s[36:39], s44 offen offset:2048 nt lds
buffer_load_dwordx4 v152, s[36:39], s44 offen offset:3072 nt lds
Maav 92,176,104
Ex 11
Maav 92,180,108
Wl 3
Ra 172,240,17280
Pk 61,10,11
Maav 100,184,80
Ex 12
Maav 100,188,84
Maav 104,184,88
Ex 13
Maav 104,188,92
Pk 68,12,13
Maav 108,184,96
s_mov_b32 m0, 61856
Ex 14
Maav 108,188,100
buffer_load_dwordx4 v251, s[36:39], s44 offen offset:2048 nt lds
buffer_load_dwordx4 v153, s[36:39], s44 offen offset:3072 nt lds
Maav 112,184,104
Ex 15
Maav 112,188,108
Pk 69,14,15
Maav 16,156,40
Maav 36,156,48
Maav 56,156,56
Maav 76,156,64
Wl 0
Cvaa 0,160,192,120
s_add_u32 s44, s44, s68
s_xor_b32 s68, s68, 114688
s_mov_b32 m0, 0
Ex 20
Mvaa 0,164,196
Ta 176,241,50304
buffer_load_dwordx4 v248, s[36:39], s44 offen offset:0 nt lds
buffer_load_dwordx4 v150, s[36:39], s44 offen offset:1024 nt lds
Mvaa 0,168,200
Ex 21
Mvaa 0,172,204
Ta 178,241,51328
Pk 46,20,21
Cvaa 4,172,220,124
Ex 22
Mvaa 4,168,216
Ta 180,241,52352
Mvaa 4,164,212
Ex 23
Mvaa 4,160,208
Ta 182,241,53376
Pk 47,22,23
Cvaa 8,160,224,128
s_mov_b32 m0, 4096
Ex 24
Mvaa 8,164,228
Ta 184,241,50336
buffer_load_dwordx4 v249, s[36:39], s44 offen offset:0 nt lds
buffer_load_dwordx4 v151, s[36:39], s44 offen offset:1024 nt lds
Mvaa 8,168,232
Ex 25
Mvaa 8,172,236
Ta 186,241,51360
Pk 54,24,25
Cvaa 12,172,252,132
Ex 26
Mvaa 12,168,248
Ta 188,241,52384
Mvaa 12,164,244
Ex 27
Mvaa 12,160,240
Wl 3
Ta 190,241,53408
Pk 55,26,27
Maav 120,176,80
s_mov_b32 m0, 8224
Ex 28
Maav 120,180,84
Ra 160,240,17536
buffer_load_dwordx4 v250, s[36:39], s44 offen offset:0 nt lds
buffer_load_dwordx4 v152, s[36:39], s44 offen offset:1024 nt lds
Maav 124,176,88
Ex 29
Maav 124,180,92
Ra 164,240,17792
Pk 62,28,29
Maav 128,176,96
Ex 30
Maav 128,180,100
Ra 168,240,18048
Maav 132,176,104
Ex 31
Maav 132,180,108
Wl 3
Ra 172,240,18304
Pk 63,30,31
Maav 140,184,80
s_mov_b32 m0, 12320
Ex 32
Maav 140,188,84
buffer_load_dwordx4 v251, s[36:39], s44 offen offset:0 nt lds
buffer_load_dwordx4 v153, s[36:39], s44 offen offset:1024 nt lds
Maav 144,184,88
Ex 33
Maav 144,188,92
Pk 70,32,33
Maav 148,184,96
Ex 34
Maav 148,188,100
Maav 152,184,104
Ex 35
Maav 152,188,108
Wl 0
Pk 71,34,35
Cvaa 20,160,192,120
s_waitcnt vmcnt(62)
s_barrier
s_mov_b32 m0, 132096
Ex 0
Mvaa 20,164,196
Ta 176,241,0
buffer_load_dwordx4 v248, s[40:43], s46 offen offset:0 nt lds
buffer_load_dwordx4 v150, s[40:43], s46 offen offset:1024 nt lds
Mvaa 20,168,200
Ex 1
Mvaa 20,172,204
Ta 178,241,1024
Pk 80,0,1
Cvaa 24,172,220,124
Ex 2
Mvaa 24,168,216
Ta 180,241,2048
Mvaa 24,164,212
Ex 3
Mvaa 24,160,208
Ta 182,241,3072
Pk 81,2,3
Cvaa 28,160,224,128
s_mov_b32 s57, 32768
Ex 4
Mvaa 28,164,228
Ta 184,241,32
buffer_store_dwordx4 v[200:203], v242, s[96:99], s57 offen offset:2048 sc1
Mvaa 28,168,232
Ex 5
Mvaa 28,172,236
Ta 186,241,1056
Pk 88,4,5
Cvaa 32,172,252,132
Ex 6
Mvaa 32,168,248
Ta 188,241,2080
buffer_load_dwordx4 v248, s[40:43], s46 offen offset:2048 nt lds
buffer_load_dwordx4 v150, s[40:43], s46 offen offset:3072 nt lds
Mvaa 32,164,244
Ex 7
Mvaa 32,160,240
Wl 3
Ta 190,241,3104
Pk 89,6,7
Maav 0,176,40
Ex 8
Maav 0,180,44
Ra 160,240,18560
Maav 4,176,48
Ex 9
Maav 4,180,52
Ra 164,240,18816
Pk 96,8,9
Maav 8,176,56
s_mov_b32 m0, 136224
Ex 10
Maav 8,180,60
Ra 168,240,19072
buffer_load_dwordx4 v249, s[40:43], s46 offen offset:0 nt lds
buffer_load_dwordx4 v151, s[40:43], s46 offen offset:1024 nt lds
Maav 12,176,64
Ex 11
Maav 12,180,68
Wl 3
Ra 172,240,19328
Pk 97,10,11
Maav 20,184,40
Ex 12
Maav 20,188,44
Maav 24,184,48
Ex 13
Maav 24,188,52
Pk 104,12,13
Maav 28,184,56
Ex 14
Maav 28,188,60
buffer_load_dwordx4 v249, s[40:43], s46 offen offset:2048 nt lds
buffer_load_dwordx4 v151, s[40:43], s46 offen offset:3072 nt lds
Maav 32,184,64
Ex 15
Maav 32,188,68
Pk 105,14,15
Maav 16,156,44
Maav 36,156,52
Maav 56,156,60
Maav 76,156,68
Wl 0
Cvaa 0,160,192,120
s_mov_b32 m0, 140352
Ex 20
Mvaa 0,164,196
Ta 176,241,256
buffer_load_dwordx4 v250, s[40:43], s46 offen offset:0 nt lds
buffer_load_dwordx4 v152, s[40:43], s46 offen offset:1024 nt lds
Mvaa 0,168,200
Ex 21
Mvaa 0,172,204
Ta 178,241,1280
Pk 82,20,21
Cvaa 4,172,220,124
Ex 22
Mvaa 4,168,216
Ta 180,241,2304
Mvaa 4,164,212
s_mov_b32 s57, 32768
Ex 23
Mvaa 4,160,208
Ta 182,241,3328
buffer_store_dwordx4 v[204:207], v242, s[96:99], s57 offen offset:3072 sc1
Cvaa 8,160,224,128
Ex 24
Mvaa 8,164,228
Ta 184,241,288
Pk 83,22,23
Mvaa 8,168,232
Ex 25
Mvaa 8,172,236
Ta 186,241,1312
buffer_load_dwordx4 v250, s[40:43], s46 offen offset:2048 nt lds
buffer_load_dwordx4 v152, s[40:43], s46 offen offset:3072 nt lds
Cvaa 12,172,252,132
Ex 26
Mvaa 12,168,248
Ta 188,241,2336
Pk 90,24,25
Mvaa 12,164,244
Ex 27
Mvaa 12,160,240
Wl 3
Ta 190,241,3360
Pk 91,26,27
Maav 40,176,40
s_mov_b32 m0, 144480
Ex 28
Maav 40,180,44
Ra 160,240,19584
buffer_load_dwordx4 v251, s[40:43], s46 offen offset:0 nt lds
buffer_load_dwordx4 v153, s[40:43], s46 offen offset:1024 nt lds
Maav 44,176,48
Ex 29
Maav 44,180,52
Ra 164,240,19840
Pk 98,28,29
Maav 48,176,56
Ex 30
Maav 48,180,60
Ra 168,240,20096
Maav 52,176,64
Ex 31
Maav 52,180,68
Wl 3
Ra 172,240,20352
Pk 99,30,31
Maav 60,184,40
Ex 32
Maav 60,188,44
buffer_load_dwordx4 v251, s[40:43], s46 offen offset:2048 nt lds
buffer_load_dwordx4 v153, s[40:43], s46 offen offset:3072 nt lds
Maav 64,184,48
Ex 33
Maav 64,188,52
Pk 106,32,33
Maav 68,184,56
Ex 34
Maav 68,188,60
Maav 72,184,64
Ex 35
Maav 72,188,68
Wl 0
Pk 107,34,35
s_mov_b64 exec, 0
s_swappc_b64 s[12:13], s[14:15]
s_add_u32 s14, s14, 4096
s_addc_u32 s15, s15, 0
Cvaa 20,160,192,120
s_add_u32 s46, s46, s69
s_xor_b32 s69, s69, 114688
)A"
R"A(s_mov_b32 m0, 0
Ex 0
Mvaa 20,164,196
Ta 176,241,512
buffer_load_dwordx4 v248, s[36:39], s44 offen offset:2048 nt lds
buffer_load_dwordx4 v150, s[36:39], s44 offen offset:3072 nt lds
Mvaa 20,168,200
Ex 1
Mvaa 20,172,204
Ta 178,241,1536
Pk 84,0,1
Cvaa 24,172,220,124
Ex 2
Mvaa 24,168,216
Ta 180,241,2560
Mvaa 24,164,212
Ex 3
Mvaa 24,160,208
Ta 182,241,3584
Pk 85,2,3
Cvaa 28,160,224,128
s_mov_b32 m0, 4096
Ex 4
Mvaa 28,164,228
Ta 184,241,544
buffer_load_dwordx4 v249, s[36:39], s44 offen offset:2048 nt lds
buffer_load_dwordx4 v151, s[36:39], s44 offen offset:3072 nt lds
Mvaa 28,168,232
Ex 5
Mvaa 28,172,236
Ta 186,241,1568
Pk 92,4,5
Cvaa 32,172,252,132
Ex 6
Mvaa 32,168,248
Ta 188,241,2592
Mvaa 32,164,244
Ex 7
Mvaa 32,160,240
Wl 3
Ta 190,241,3616
Pk 93,6,7
Maav 80,176,40
Ex 8
Maav 80,180,44
Ra 160,240,33024
Maav 84,176,48
s_mov_b32 m0, 8224
Ex 9
Maav 84,180,52
Ra 164,240,33280
buffer_load_dwordx4 v250, s[36:39], s44 offen offset:2048 nt lds
buffer_load_dwordx4 v152, s[36:39], s44 offen offset:3072 nt lds
Maav 88,176,56
Ex 10
Maav 88,180,60
Ra 168,240,33536
Pk 100,8,9
Maav 92,176,64
Ex 11
Maav 92,180,68
Wl 3
Ra 172,240,33792
Pk 101,10,11
Maav 100,184,40
Ex 12
Maav 100,188,44
Maav 104,184,48
s_mov_b32 m0, 12320
Ex 13
Maav 104,188,52
buffer_load_dwordx4 v251, s[36:39], s44 offen offset:2048 nt lds
buffer_load_dwordx4 v153, s[36:39], s44 offen offset:3072 nt lds
Maav 108,184,56
Ex 14
Maav 108,188,60
Pk 108,12,13
Maav 112,184,64
Ex 15
Maav 112,188,68
Pk 109,14,15
Maav 16,156,80
Maav 36,156,88
Maav 56,156,96
Maav 76,156,104
Wl 0
Cvaa 0,160,192,120
s_add_u32 s44, s44, 1024
s_mov_b32 m0, 16512
Ex 20
Mvaa 0,164,196
Ta 176,241,768
buffer_load_dwordx4 v248, s[36:39], s44 offen offset:0 nt lds
buffer_load_dwordx4 v150, s[36:39], s44 offen offset:1024 nt lds
Mvaa 0,168,200
Ex 21
Mvaa 0,172,204
Ta 178,241,1792
Pk 86,20,21
Cvaa 4,172,220,124
Ex 22
Mvaa 4,168,216
Ta 180,241,2816
Mvaa 4,164,212
s_mov_b32 s57, 49152
Ex 23
Mvaa 4,160,208
Ta 182,241,3840
buffer_store_dwordx4 v[208:211], v242, s[96:99], s57 offen offset:0 sc1
Cvaa 8,160,224,128
Ex 24
Mvaa 8,164,228
Ta 184,241,800
Pk 87,22,23
Mvaa 8,168,232
s_mov_b32 m0, 20608
Ex 25
Mvaa 8,172,236
Ta 186,241,1824
buffer_load_dwordx4 v249, s[36:39], s44 offen offset:0 nt lds
buffer_load_dwordx4 v151, s[36:39], s44 offen offset:1024 nt lds
Cvaa 12,172,252,132
Ex 26
Mvaa 12,168,248
Ta 188,241,2848
Pk 94,24,25
Mvaa 12,164,244
Ex 27
Mvaa 12,160,240
Wl 3
Ta 190,241,3872
Pk 95,26,27
Maav 120,176,40
s_mov_b32 m0, 24736
Ex 28
Maav 120,180,44
Ra 160,240,34048
buffer_load_dwordx4 v250, s[36:39], s44 offen offset:0 nt lds
buffer_load_dwordx4 v152, s[36:39], s44 offen offset:1024 nt lds
Maav 124,176,48
Ex 29
Maav 124,180,52
Ra 164,240,34304
Pk 102,28,29
Maav 128,176,56
Ex 30
Maav 128,180,60
Ra 168,240,34560
Maav 132,176,64
Ex 31
Maav 132,180,68
Wl 3
Ra 172,240,34816
Pk 103,30,31
Maav 140,184,40
s_mov_b32 m0, 28832
Ex 32
Maav 140,188,44
buffer_load_dwordx4 v251, s[36:39], s44 offen offset:0 nt lds
buffer_load_dwordx4 v153, s[36:39], s44 offen offset:1024 nt lds
Maav 144,184,48
Ex 33
Maav 144,188,52
Pk 110,32,33
Maav 148,184,56
Ex 34
Maav 148,188,60
Maav 152,184,64
Ex 35
Maav 152,188,68
Wl 0
Pk 111,34,35
Cvaa 20,160,192,120
s_waitcnt vmcnt(62)
s_barrier
s_mov_b32 m0, 82560
Ex 0
Mvaa 20,164,196
Ta 176,241,16512
buffer_load_dwordx4 v248, s[40:43], s46 offen offset:0 nt lds
buffer_load_dwordx4 v150, s[40:43], s46 offen offset:1024 nt lds
Mvaa 20,168,200
Ex 1
Mvaa 20,172,204
Ta 178,241,17536
Pk 40,0,1
Cvaa 24,172,220,124
Ex 2
Mvaa 24,168,216
Ta 180,241,18560
Mvaa 24,164,212
Ex 3
Mvaa 24,160,208
Ta 182,241,19584
Pk 41,2,3
Cvaa 28,160,224,128
s_mov_b32 s57, 49152
Ex 4
Mvaa 28,164,228
Ta 184,241,16544
buffer_store_dwordx4 v[212:215], v242, s[96:99], s57 offen offset:1024 sc1
Mvaa 28,168,232
Ex 5
Mvaa 28,172,236
Ta 186,241,17568
Pk 48,4,5
Cvaa 32,172,252,132
Ex 6
Mvaa 32,168,248
Ta 188,241,18592
buffer_load_dwordx4 v248, s[40:43], s46 offen offset:2048 nt lds
buffer_load_dwordx4 v150, s[40:43], s46 offen offset:3072 nt lds
Mvaa 32,164,244
Ex 7
Mvaa 32,160,240
Wl 3
Ta 190,241,19616
Pk 49,6,7
Maav 0,176,80
Ex 8
Maav 0,180,84
Ra 160,240,35072
Maav 4,176,88
Ex 9
Maav 4,180,92
Ra 164,240,35328
Pk 56,8,9
Maav 8,176,96
s_mov_b32 m0, 86688
Ex 10
Maav 8,180,100
Ra 168,240,35584
buffer_load_dwordx4 v249, s[40:43], s46 offen offset:0 nt lds
buffer_load_dwordx4 v151, s[40:43], s46 offen offset:1024 nt lds
Maav 12,176,104
Ex 11
Maav 12,180,108
Wl 3
Ra 172,240,35840
Pk 57,10,11
Maav 20,184,80
Ex 12
Maav 20,188,84
Maav 24,184,88
Ex 13
Maav 24,188,92
Pk 64,12,13
Maav 28,184,96
Ex 14
Maav 28,188,100
buffer_load_dwordx4 v249, s[40:43], s46 offen offset:2048 nt lds
buffer_load_dwordx4 v151, s[40:43], s46 offen offset:3072 nt lds
Maav 32,184,104
Ex 15
Maav 32,188,108
Pk 65,14,15
Maav 16,156,84
Maav 36,156,92
Maav 56,156,100
Maav 76,156,108
Wl 0
Cvaa 0,160,192,120
s_mov_b32 m0, 90816
Ex 20
Mvaa 0,164,196
Ta 176,241,16768
buffer_load_dwordx4 v250, s[40:43], s46 offen offset:0 nt lds
buffer_load_dwordx4 v152, s[40:43], s46 offen offset:1024 nt lds
Mvaa 0,168,200
Ex 21
Mvaa 0,172,204
Ta 178,241,17792
Pk 42,20,21
Cvaa 4,172,220,124
Ex 22
Mvaa 4,168,216
Ta 180,241,18816
Mvaa 4,164,212
Ex 23
Mvaa 4,160,208
Ta 182,241,19840
Pk 43,22,23
Cvaa 8,160,224,128
Ex 24
Mvaa 8,164,228
Ta 184,241,16800
buffer_load_dwordx4 v250, s[40:43], s46 offen offset:2048 nt lds
buffer_load_dwordx4 v152, s[40:43], s46 offen offset:3072 nt lds
Mvaa 8,168,232
Ex 25
Mvaa 8,172,236
Ta 186,241,17824
Pk 50,24,25
Cvaa 12,172,252,132
Ex 26
Mvaa 12,168,248
Ta 188,241,18848
Mvaa 12,164,244
Ex 27
Mvaa 12,160,240
Wl 3
Ta 190,241,19872
Pk 51,26,27
Maav 40,176,80
s_mov_b32 m0, 94944
Ex 28
Maav 40,180,84
Ra 160,240,36096
buffer_load_dwordx4 v251, s[40:43], s46 offen offset:0 nt lds
buffer_load_dwordx4 v153, s[40:43], s46 offen offset:1024 nt lds
Maav 44,176,88
Ex 29
Maav 44,180,92
Ra 164,240,36352
Pk 58,28,29
Maav 48,176,96
Ex 30
Maav 48,180,100
Ra 168,240,36608
Maav 52,176,104
Ex 31
Maav 52,180,108
Wl 3
Ra 172,240,36864
Pk 59,30,31
Maav 60,184,80
Ex 32
Maav 60,188,84
buffer_load_dwordx4 v251, s[40:43], s46 offen offset:2048 nt lds
buffer_load_dwordx4 v153, s[40:43], s46 offen offset:3072 nt lds
Maav 64,184,88
Ex 33
Maav 64,188,92
Pk 66,32,33
Maav 68,184,96
Ex 34
Maav 68,188,100
Maav 72,184,104
Ex 35
Maav 72,188,108
Wl 0
Pk 67,34,35
Cvaa 20,160,192,120
s_add_u32 s46, s46, 1024
s_mov_b32 m0, 16512
Ex 0
Mvaa 20,164,196
Ta 176,241,17024
buffer_load_dwordx4 v248, s[36:39], s44 offen offset:2048 nt lds
buffer_load_dwordx4 v150, s[36:39], s44 offen offset:3072 nt lds
Mvaa 20,168,200
Ex 1
Mvaa 20,172,204
Ta 178,241,18048
Pk 44,0,1
Cvaa 24,172,220,124
Ex 2
Mvaa 24,168,216
Ta 180,241,19072
Mvaa 24,164,212
Ex 3
Mvaa 24,160,208
Ta 182,241,20096
Pk 45,2,3
Cvaa 28,160,224,128
s_mov_b32 s57, 49152
Ex 4
Mvaa 28,164,228
Ta 184,241,17056
buffer_store_dwordx4 v[216:219], v242, s[96:99], s57 offen offset:2048 sc1
Mvaa 28,168,232
Ex 5
Mvaa 28,172,236
Ta 186,241,18080
Pk 52,4,5
Cvaa 32,172,252,132
s_mov_b32 m0, 20608
Ex 6
Mvaa 32,168,248
Ta 188,241,19104
buffer_load_dwordx4 v249, s[36:39], s44 offen offset:2048 nt lds
buffer_load_dwordx4 v151, s[36:39], s44 offen offset:3072 nt lds
Mvaa 32,164,244
Ex 7
Mvaa 32,160,240
Wl 3
Ta 190,241,20128
Pk 53,6,7
Maav 80,176,80
Ex 8
Maav 80,180,84
Ra 160,240,49536
Maav 84,176,88
Ex 9
Maav 84,180,92
Ra 164,240,49792
Pk 60,8,9
Maav 88,176,96
s_mov_b32 m0, 24736
Ex 10
)A"
R"A(Maav 88,180,100
Ra 168,240,50048
buffer_load_dwordx4 v250, s[36:39], s44 offen offset:2048 nt lds
buffer_load_dwordx4 v152, s[36:39], s44 offen offset:3072 nt lds
Maav 92,176,104
Ex 11
Maav 92,180,108
Wl 3
Ra 172,240,50304
Pk 61,10,11
Maav 100,184,80
Ex 12
Maav 100,188,84
Maav 104,184,88
Ex 13
Maav 104,188,92
Pk 68,12,13
Maav 108,184,96
s_mov_b32 m0, 28832
Ex 14
Maav 108,188,100
buffer_load_dwordx4 v251, s[36:39], s44 offen offset:2048 nt lds
buffer_load_dwordx4 v153, s[36:39], s44 offen offset:3072 nt lds
Maav 112,184,104
Ex 15
Maav 112,188,108
Pk 69,14,15
Maav 16,156,40
Maav 36,156,48
Maav 56,156,56
Maav 76,156,64
Wl 0
Cvaa 0,160,192,120
s_add_u32 s44, s44, 3072
s_mov_b32 m0, 33024
Ex 20
Mvaa 0,164,196
Ta 176,241,17280
buffer_load_dwordx4 v248, s[36:39], s44 offen offset:0 nt lds
buffer_load_dwordx4 v150, s[36:39], s44 offen offset:1024 nt lds
Mvaa 0,168,200
Ex 21
Mvaa 0,172,204
Ta 178,241,18304
Pk 46,20,21
Cvaa 4,172,220,124
Ex 22
Mvaa 4,168,216
Ta 180,241,19328
Mvaa 4,164,212
Ex 23
Mvaa 4,160,208
Ta 182,241,20352
Pk 47,22,23
Cvaa 8,160,224,128
s_mov_b32 m0, 37120
Ex 24
Mvaa 8,164,228
Ta 184,241,17312
buffer_load_dwordx4 v249, s[36:39], s44 offen offset:0 nt lds
buffer_load_dwordx4 v151, s[36:39], s44 offen offset:1024 nt lds
Mvaa 8,168,232
Ex 25
Mvaa 8,172,236
Ta 186,241,18336
Pk 54,24,25
Cvaa 12,172,252,132
Ex 26
Mvaa 12,168,248
Ta 188,241,19360
Mvaa 12,164,244
Ex 27
Mvaa 12,160,240
Wl 3
Ta 190,241,20384
Pk 55,26,27
Maav 120,176,80
s_mov_b32 m0, 41248
Ex 28
Maav 120,180,84
Ra 160,240,50560
buffer_load_dwordx4 v250, s[36:39], s44 offen offset:0 nt lds
buffer_load_dwordx4 v152, s[36:39], s44 offen offset:1024 nt lds
Maav 124,176,88
Ex 29
Maav 124,180,92
Ra 164,240,50816
Pk 62,28,29
Maav 128,176,96
Ex 30
Maav 128,180,100
Ra 168,240,51072
Maav 132,176,104
Ex 31
Maav 132,180,108
Wl 3
Ra 172,240,51328
Pk 63,30,31
Maav 140,184,80
s_mov_b32 m0, 45344
Ex 32
Maav 140,188,84
buffer_load_dwordx4 v251, s[36:39], s44 offen offset:0 nt lds
buffer_load_dwordx4 v153, s[36:39], s44 offen offset:1024 nt lds
Maav 144,184,88
Ex 33
Maav 144,188,92
Pk 70,32,33
Maav 148,184,96
Ex 34
Maav 148,188,100
Maav 152,184,104
Ex 35
Maav 152,188,108
Wl 0
Pk 71,34,35
Cvaa 20,160,192,120
s_waitcnt vmcnt(62)
s_barrier
s_mov_b32 m0, 99072
Ex 0
Mvaa 20,164,196
Ta 176,241,33024
buffer_load_dwordx4 v248, s[40:43], s46 offen offset:0 nt lds
buffer_load_dwordx4 v150, s[40:43], s46 offen offset:1024 nt lds
Mvaa 20,168,200
Ex 1
Mvaa 20,172,204
Ta 178,241,34048
Pk 80,0,1
Cvaa 24,172,220,124
Ex 2
Mvaa 24,168,216
Ta 180,241,35072
Mvaa 24,164,212
Ex 3
Mvaa 24,160,208
Ta 182,241,36096
Pk 81,2,3
Cvaa 28,160,224,128
s_mov_b32 s57, 49152
Ex 4
Mvaa 28,164,228
Ta 184,241,33056
buffer_store_dwordx4 v[220:223], v242, s[96:99], s57 offen offset:3072 sc1
Mvaa 28,168,232
Ex 5
Mvaa 28,172,236
Ta 186,241,34080
Pk 88,4,5
Cvaa 32,172,252,132
Ex 6
Mvaa 32,168,248
Ta 188,241,35104
buffer_load_dwordx4 v248, s[40:43], s46 offen offset:2048 nt lds
buffer_load_dwordx4 v150, s[40:43], s46 offen offset:3072 nt lds
Mvaa 32,164,244
Ex 7
Mvaa 32,160,240
Wl 3
Ta 190,241,36128
Pk 89,6,7
Maav 0,176,40
Ex 8
Maav 0,180,44
Ra 160,240,51584
Maav 4,176,48
Ex 9
Maav 4,180,52
Ra 164,240,51840
Pk 96,8,9
Maav 8,176,56
s_mov_b32 m0, 103200
Ex 10
Maav 8,180,60
Ra 168,240,52096
buffer_load_dwordx4 v249, s[40:43], s46 offen offset:0 nt lds
buffer_load_dwordx4 v151, s[40:43], s46 offen offset:1024 nt lds
Maav 12,176,64
Ex 11
Maav 12,180,68
Wl 3
Ra 172,240,52352
Pk 97,10,11
Maav 20,184,40
Ex 12
Maav 20,188,44
Maav 24,184,48
Ex 13
Maav 24,188,52
Pk 104,12,13
Maav 28,184,56
Ex 14
Maav 28,188,60
buffer_load_dwordx4 v249, s[40:43], s46 offen offset:2048 nt lds
buffer_load_dwordx4 v151, s[40:43], s46 offen offset:3072 nt lds
Maav 32,184,64
Ex 15
Maav 32,188,68
Pk 105,14,15
Maav 16,156,44
Maav 36,156,52
Maav 56,156,60
Maav 76,156,68
Wl 0
Cvaa 0,160,192,120
s_mov_b32 m0, 107328
Ex 20
Mvaa 0,164,196
Ta 176,241,33280
buffer_load_dwordx4 v250, s[40:43], s46 offen offset:0 nt lds
buffer_load_dwordx4 v152, s[40:43], s46 offen offset:1024 nt lds
Mvaa 0,168,200
Ex 21
Mvaa 0,172,204
Ta 178,241,34304
Pk 82,20,21
Cvaa 4,172,220,124
Ex 22
Mvaa 4,168,216
Ta 180,241,35328
Mvaa 4,164,212
Ex 23
Mvaa 4,160,208
Ta 182,241,36352
Pk 83,22,23
Cvaa 8,160,224,128
Ex 24
Mvaa 8,164,228
Ta 184,241,33312
buffer_load_dwordx4 v250, s[40:43], s46 offen offset:2048 nt lds
buffer_load_dwordx4 v152, s[40:43], s46 offen offset:3072 nt lds
Mvaa 8,168,232
Ex 25
Mvaa 8,172,236
Ta 186,241,34336
Pk 90,24,25
Cvaa 12,172,252,132
Ex 26
Mvaa 12,168,248
Ta 188,241,35360
Mvaa 12,164,244
Ex 27
Mvaa 12,160,240
Wl 3
Ta 190,241,36384
Pk 91,26,27
Maav 40,176,40
s_mov_b32 m0, 111456
Ex 28
Maav 40,180,44
Ra 160,240,52608
buffer_load_dwordx4 v251, s[40:43], s46 offen offset:0 nt lds
buffer_load_dwordx4 v153, s[40:43], s46 offen offset:1024 nt lds
Maav 44,176,48
Ex 29
Maav 44,180,52
Ra 164,240,52864
Pk 98,28,29
Maav 48,176,56
Ex 30
Maav 48,180,60
Ra 168,240,53120
Maav 52,176,64
Ex 31
Maav 52,180,68
Wl 3
Ra 172,240,53376
Pk 99,30,31
Maav 60,184,40
Ex 32
Maav 60,188,44
buffer_load_dwordx4 v251, s[40:43], s46 offen offset:2048 nt lds
buffer_load_dwordx4 v153, s[40:43], s46 offen offset:3072 nt lds
Maav 64,184,48
Ex 33
Maav 64,188,52
Pk 106,32,33
Maav 68,184,56
Ex 34
Maav 68,188,60
Maav 72,184,64
Ex 35
Maav 72,188,68
Wl 0
Pk 107,34,35
s_mov_b64 exec, 0
s_swappc_b64 s[12:13], s[14:15]
s_add_u32 s14, s14, 4096
s_addc_u32 s15, s15, 0
Cvaa 20,160,192,120
s_add_u32 s46, s46, 3072
s_mov_b32 m0, 33024
Ex 0
Mvaa 20,164,196
Ta 176,241,33536
buffer_load_dwordx4 v248, s[36:39], s44 offen offset:2048 nt lds
buffer_load_dwordx4 v150, s[36:39], s44 offen offset:3072 nt lds
Mvaa 20,168,200
Ex 1
Mvaa 20,172,204
Ta 178,241,34560
Pk 84,0,1
Cvaa 24,172,220,124
Ex 2
Mvaa 24,168,216
Ta 180,241,35584
Mvaa 24,164,212
Ex 3
Mvaa 24,160,208
Ta 182,241,36608
Pk 85,2,3
Cvaa 28,160,224,128
s_mov_b32 m0, 37120
Ex 4
Mvaa 28,164,228
Ta 184,241,33568
buffer_load_dwordx4 v249, s[36:39], s44 offen offset:2048 nt lds
buffer_load_dwordx4 v151, s[36:39], s44 offen offset:3072 nt lds
Mvaa 28,168,232
Ex 5
Mvaa 28,172,236
Ta 186,241,34592
Pk 92,4,5
Cvaa 32,172,252,132
Ex 6
Mvaa 32,168,248
Ta 188,241,35616
Mvaa 32,164,244
Ex 7
Mvaa 32,160,240
Wl 3
Ta 190,241,36640
Pk 93,6,7
Maav 80,176,40
Ex 8
Maav 80,180,44
Ra 160,240,0
Maav 84,176,48
s_mov_b32 m0, 41248
Ex 9
Maav 84,180,52
Ra 164,240,256
buffer_load_dwordx4 v250, s[36:39], s44 offen offset:2048 nt lds
buffer_load_dwordx4 v152, s[36:39], s44 offen offset:3072 nt lds
Maav 88,176,56
Ex 10
Maav 88,180,60
Ra 168,240,512
Pk 100,8,9
Maav 92,176,64
Ex 11
Maav 92,180,68
Wl 3
Ra 172,240,768
Pk 101,10,11
Maav 100,184,40
Ex 12
Maav 100,188,44
Maav 104,184,48
s_mov_b32 m0, 45344
Ex 13
Maav 104,188,52
buffer_load_dwordx4 v251, s[36:39], s44 offen offset:2048 nt lds
buffer_load_dwordx4 v153, s[36:39], s44 offen offset:3072 nt lds
Maav 108,184,56
Ex 14
Maav 108,188,60
Pk 108,12,13
Maav 112,184,64
Ex 15
Maav 112,188,68
Pk 109,14,15
Maav 16,156,80
Maav 36,156,88
Maav 56,156,96
Maav 76,156,104
Wl 0
Cvaa 0,160,192,120
s_add_u32 s44, s44, 1024
s_mov_b32 m0, 49536
Ex 20
Mvaa 0,164,196
Ta 176,241,33792
buffer_load_dwordx4 v248, s[36:39], s44 offen offset:0 nt lds
buffer_load_dwordx4 v150, s[36:39], s44 offen offset:1024 nt lds
Mvaa 0,168,200
Ex 21
Mvaa 0,172,204
Ta 178,241,34816
Pk 86,20,21
Cvaa 4,172,220,124
Ex 22
Mvaa 4,168,216
Ta 180,241,35840
Mvaa 4,164,212
Ex 23
Mvaa 4,160,208
Ta 182,241,36864
Pk 87,22,23
)A"
R"A(Cvaa 8,160,224,128
s_mov_b32 m0, 53632
Ex 24
Mvaa 8,164,228
Ta 184,241,33824
buffer_load_dwordx4 v249, s[36:39], s44 offen offset:0 nt lds
buffer_load_dwordx4 v151, s[36:39], s44 offen offset:1024 nt lds
Mvaa 8,168,232
Ex 25
Mvaa 8,172,236
Ta 186,241,34848
Pk 94,24,25
Cvaa 12,172,252,132
Ex 26
Mvaa 12,168,248
Ta 188,241,35872
Mvaa 12,164,244
Ex 27
Mvaa 12,160,240
Wl 3
Ta 190,241,36896
Pk 95,26,27
Maav 120,176,40
s_mov_b32 m0, 57760
Ex 28
Maav 120,180,44
Ra 160,240,1024
buffer_load_dwordx4 v250, s[36:39], s44 offen offset:0 nt lds
buffer_load_dwordx4 v152, s[36:39], s44 offen offset:1024 nt lds
Maav 124,176,48
Ex 29
Maav 124,180,52
Ra 164,240,1280
Pk 102,28,29
Maav 128,176,56
Ex 30
Maav 128,180,60
Ra 168,240,1536
Maav 132,176,64
Ex 31
Maav 132,180,68
Wl 3
Ra 172,240,1792
Pk 103,30,31
Maav 140,184,40
s_mov_b32 m0, 61856
Ex 32
Maav 140,188,44
buffer_load_dwordx4 v251, s[36:39], s44 offen offset:0 nt lds
buffer_load_dwordx4 v153, s[36:39], s44 offen offset:1024 nt lds
Maav 144,184,48
Ex 33
Maav 144,188,52
Pk 110,32,33
Maav 148,184,56
Ex 34
Maav 148,188,60
Maav 152,184,64
Ex 35
Maav 152,188,68
Wl 0
Pk 111,34,35
Cvaa 20,160,192,120
s_waitcnt vmcnt(62)
s_barrier
s_mov_b32 m0, 115584
Ex 0
Mvaa 20,164,196
Ta 176,241,49536
buffer_load_dwordx4 v248, s[40:43], s46 offen offset:0 nt lds
buffer_load_dwordx4 v150, s[40:43], s46 offen offset:1024 nt lds
Mvaa 20,168,200
Ex 1
Mvaa 20,172,204
Ta 178,241,50560
Pk 40,0,1
Cvaa 24,172,220,124
Ex 2
Mvaa 24,168,216
Ta 180,241,51584
Mvaa 24,164,212
Ex 3
Mvaa 24,160,208
Ta 182,241,52608
Pk 41,2,3
Cvaa 28,160,224,128
Ex 4
Mvaa 28,164,228
Ta 184,241,49568
buffer_load_dwordx4 v248, s[40:43], s46 offen offset:2048 nt lds
buffer_load_dwordx4 v150, s[40:43], s46 offen offset:3072 nt lds
Mvaa 28,168,232
Ex 5
Mvaa 28,172,236
Ta 186,241,50592
Pk 48,4,5
Cvaa 32,172,252,132
Ex 6
Mvaa 32,168,248
Ta 188,241,51616
Mvaa 32,164,244
Ex 7
Mvaa 32,160,240
Wl 3
Ta 190,241,52640
Pk 49,6,7
Maav 0,176,80
Ex 8
Maav 0,180,84
Ra 160,240,2048
Maav 4,176,88
s_mov_b32 m0, 119712
Ex 9
Maav 4,180,92
Ra 164,240,2304
buffer_load_dwordx4 v249, s[40:43], s46 offen offset:0 nt lds
buffer_load_dwordx4 v151, s[40:43], s46 offen offset:1024 nt lds
Maav 8,176,96
Ex 10
Maav 8,180,100
Ra 168,240,2560
Pk 56,8,9
Maav 12,176,104
Ex 11
Maav 12,180,108
Wl 3
Ra 172,240,2816
Pk 57,10,11
Maav 20,184,80
Ex 12
Maav 20,188,84
Maav 24,184,88
Ex 13
Maav 24,188,92
buffer_load_dwordx4 v249, s[40:43], s46 offen offset:2048 nt lds
buffer_load_dwordx4 v151, s[40:43], s46 offen offset:3072 nt lds
Maav 28,184,96
Ex 14
Maav 28,188,100
Pk 64,12,13
Maav 32,184,104
Ex 15
Maav 32,188,108
Pk 65,14,15
Maav 16,156,84
Maav 36,156,92
Maav 56,156,100
Maav 76,156,108
Wl 0
Cvaa 0,160,192,120
s_mov_b32 m0, 123840
Ex 20
Mvaa 0,164,196
Ta 176,241,49792
buffer_load_dwordx4 v250, s[40:43], s46 offen offset:0 nt lds
buffer_load_dwordx4 v152, s[40:43], s46 offen offset:1024 nt lds
Mvaa 0,168,200
Ex 21
Mvaa 0,172,204
Ta 178,241,50816
Pk 42,20,21
Cvaa 4,172,220,124
Ex 22
Mvaa 4,168,216
Ta 180,241,51840
Mvaa 4,164,212
Ex 23
Mvaa 4,160,208
Ta 182,241,52864
Pk 43,22,23
Cvaa 8,160,224,128
Ex 24
Mvaa 8,164,228
Ta 184,241,49824
buffer_load_dwordx4 v250, s[40:43], s46 offen offset:2048 nt lds
buffer_load_dwordx4 v152, s[40:43], s46 offen offset:3072 nt lds
Mvaa 8,168,232
Ex 25
Mvaa 8,172,236
Ta 186,241,50848
Pk 50,24,25
Cvaa 12,172,252,132
Ex 26
Mvaa 12,168,248
Ta 188,241,51872
Mvaa 12,164,244
Ex 27
Mvaa 12,160,240
Wl 3
Ta 190,241,52896
Pk 51,26,27
Maav 40,176,80
s_mov_b32 m0, 127968
Ex 28
Maav 40,180,84
Ra 160,240,3072
buffer_load_dwordx4 v251, s[40:43], s46 offen offset:0 nt lds
buffer_load_dwordx4 v153, s[40:43], s46 offen offset:1024 nt lds
Maav 44,176,88
Ex 29
Maav 44,180,92
Ra 164,240,3328
Pk 58,28,29
Maav 48,176,96
Ex 30
Maav 48,180,100
Ra 168,240,3584
Maav 52,176,104
Ex 31
Maav 52,180,108
Wl 3
Ra 172,240,3840
Pk 59,30,31
Maav 60,184,80
Ex 32
Maav 60,188,84
buffer_load_dwordx4 v251, s[40:43], s46 offen offset:2048 nt lds
buffer_load_dwordx4 v153, s[40:43], s46 offen offset:3072 nt lds
Maav 64,184,88
Ex 33
Maav 64,188,92
Pk 66,32,33
Maav 68,184,96
Ex 34
Maav 68,188,100
Maav 72,184,104
Ex 35
Maav 72,188,108
Wl 0
Pk 67,34,35
s_mov_b64 exec, 0
s_swappc_b64 s[12:13], s[14:15]
s_add_u32 s14, s14, 4096
s_addc_u32 s15, s15, 0
Cvaa 20,160,192,120
s_add_u32 s46, s46, 1024
s_mov_b32 m0, 49536
Ex 0
Mvaa 20,164,196
Ta 176,241,50048
buffer_load_dwordx4 v248, s[36:39], s44 offen offset:2048 nt lds
buffer_load_dwordx4 v150, s[36:39], s44 offen offset:3072 nt lds
Mvaa 20,168,200
Ex 1
Mvaa 20,172,204
Ta 178,241,51072
Pk 44,0,1
Cvaa 24,172,220,124
Ex 2
Mvaa 24,168,216
Ta 180,241,52096
Mvaa 24,164,212
Ex 3
Mvaa 24,160,208
Ta 182,241,53120
Pk 45,2,3
Cvaa 28,160,224,128
s_mov_b32 m0, 53632
Ex 4
Mvaa 28,164,228
Ta 184,241,50080
buffer_load_dwordx4 v249, s[36:39], s44 offen offset:2048 nt lds
buffer_load_dwordx4 v151, s[36:39], s44 offen offset:3072 nt lds
Mvaa 28,168,232
Ex 5
Mvaa 28,172,236
Ta 186,241,51104
Pk 52,4,5
Cvaa 32,172,252,132
Ex 6
Mvaa 32,168,248
Ta 188,241,52128
Mvaa 32,164,244
Ex 7
Mvaa 32,160,240
Wl 3
Ta 190,241,53152
Pk 53,6,7
Maav 80,176,80
Ex 8
Maav 80,180,84
Ra 160,240,16512
Maav 84,176,88
s_mov_b32 m0, 57760
Ex 9
Maav 84,180,92
Ra 164,240,16768
buffer_load_dwordx4 v250, s[36:39], s44 offen offset:2048 nt lds
buffer_load_dwordx4 v152, s[36:39], s44 offen offset:3072 nt lds
Maav 88,176,96
Ex 10
Maav 88,180,100
Ra 168,240,17024
Pk 60,8,9
Maav 92,176,104
Ex 11
Maav 92,180,108
Wl 3
Ra 172,240,17280
Pk 61,10,11
Maav 100,184,80
Ex 12
Maav 100,188,84
Maav 104,184,88
s_mov_b32 m0, 61856
Ex 13
Maav 104,188,92
buffer_load_dwordx4 v251, s[36:39], s44 offen offset:2048 nt lds
buffer_load_dwordx4 v153, s[36:39], s44 offen offset:3072 nt lds
Maav 108,184,96
Ex 14
Maav 108,188,100
Pk 68,12,13
Maav 112,184,104
Ex 15
Maav 112,188,108
Pk 69,14,15
Maav 16,156,40
Maav 36,156,48
Maav 56,156,56
Maav 76,156,64
Wl 0
Cvaa 0,160,192,120
s_add_u32 s44, s44, s68
s_xor_b32 s68, s68, 114688
s_mov_b32 m0, 0
Ex 20
Mvaa 0,164,196
Ta 176,241,50304
buffer_load_dwordx4 v248, s[36:39], s44 offen offset:0 nt lds
buffer_load_dwordx4 v150, s[36:39], s44 offen offset:1024 nt lds
Mvaa 0,168,200
Ex 21
Mvaa 0,172,204
Ta 178,241,51328
Pk 46,20,21
Cvaa 4,172,220,124
Ex 22
Mvaa 4,168,216
Ta 180,241,52352
Mvaa 4,164,212
Ex 23
Mvaa 4,160,208
Ta 182,241,53376
Pk 47,22,23
Cvaa 8,160,224,128
s_mov_b32 m0, 4096
Ex 24
Mvaa 8,164,228
Ta 184,241,50336
buffer_load_dwordx4 v249, s[36:39], s44 offen offset:0 nt lds
buffer_load_dwordx4 v151, s[36:39], s44 offen offset:1024 nt lds
Mvaa 8,168,232
Ex 25
Mvaa 8,172,236
Ta 186,241,51360
Pk 54,24,25
Cvaa 12,172,252,132
Ex 26
Mvaa 12,168,248
Ta 188,241,52384
Mvaa 12,164,244
Ex 27
Mvaa 12,160,240
Wl 3
Ta 190,241,53408
Pk 55,26,27
Maav 120,176,80
s_mov_b32 m0, 8224
Ex 28
Maav 120,180,84
Ra 160,240,17536
buffer_load_dwordx4 v250, s[36:39], s44 offen offset:0 nt lds
buffer_load_dwordx4 v152, s[36:39], s44 offen offset:1024 nt lds
Maav 124,176,88
Ex 29
Maav 124,180,92
Ra 164,240,17792
Pk 62,28,29
Maav 128,176,96
Ex 30
Maav 128,180,100
Ra 168,240,18048
Maav 132,176,104
Ex 31
Maav 132,180,108
Wl 3
Ra 172,240,18304
Pk 63,30,31
Maav 140,184,80
s_mov_b32 m0, 12320
Ex 32
Maav 140,188,84
buffer_load_dwordx4 v251, s[36:39], s44 offen offset:0 nt lds
buffer_load_dwordx4 v153, s[36:39], s44 offen offset:1024 nt lds
Maav 144,184,88
Ex 33
Maav 144,188,92
Pk 70,32,33
Maav 148,184,96
)A"
R"A(Ex 34
Maav 148,188,100
Maav 152,184,104
Ex 35
Maav 152,188,108
Wl 0
Pk 71,34,35
Cvaa 20,160,192,120
s_waitcnt vmcnt(62)
s_barrier
s_mov_b32 m0, 132096
Ex 0
Mvaa 20,164,196
Ta 176,241,0
buffer_load_dwordx4 v248, s[40:43], s46 offen offset:0 nt lds
buffer_load_dwordx4 v150, s[40:43], s46 offen offset:1024 nt lds
Mvaa 20,168,200
Ex 1
Mvaa 20,172,204
Ta 178,241,1024
Pk 80,0,1
Cvaa 24,172,220,124
Ex 2
Mvaa 24,168,216
Ta 180,241,2048
Mvaa 24,164,212
Ex 3
Mvaa 24,160,208
Ta 182,241,3072
Pk 81,2,3
Cvaa 28,160,224,128
Ex 4
Mvaa 28,164,228
Ta 184,241,32
buffer_load_dwordx4 v248, s[40:43], s46 offen offset:2048 nt lds
buffer_load_dwordx4 v150, s[40:43], s46 offen offset:3072 nt lds
Mvaa 28,168,232
Ex 5
Mvaa 28,172,236
Ta 186,241,1056
Pk 88,4,5
Cvaa 32,172,252,132
Ex 6
Mvaa 32,168,248
Ta 188,241,2080
Mvaa 32,164,244
Ex 7
Mvaa 32,160,240
Wl 3
Ta 190,241,3104
Pk 89,6,7
Maav 0,176,40
Ex 8
Maav 0,180,44
Ra 160,240,18560
Maav 4,176,48
s_mov_b32 m0, 136224
Ex 9
Maav 4,180,52
Ra 164,240,18816
buffer_load_dwordx4 v249, s[40:43], s46 offen offset:0 nt lds
buffer_load_dwordx4 v151, s[40:43], s46 offen offset:1024 nt lds
Maav 8,176,56
Ex 10
Maav 8,180,60
Ra 168,240,19072
Pk 96,8,9
Maav 12,176,64
Ex 11
Maav 12,180,68
Wl 3
Ra 172,240,19328
Pk 97,10,11
Maav 20,184,40
Ex 12
Maav 20,188,44
Maav 24,184,48
Ex 13
Maav 24,188,52
buffer_load_dwordx4 v249, s[40:43], s46 offen offset:2048 nt lds
buffer_load_dwordx4 v151, s[40:43], s46 offen offset:3072 nt lds
Maav 28,184,56
Ex 14
Maav 28,188,60
Pk 104,12,13
Maav 32,184,64
Ex 15
Maav 32,188,68
Pk 105,14,15
Maav 16,156,44
Maav 36,156,52
Maav 56,156,60
Maav 76,156,68
Wl 0
Cvaa 0,160,192,120
s_mov_b32 m0, 140352
Ex 20
Mvaa 0,164,196
Ta 176,241,256
buffer_load_dwordx4 v250, s[40:43], s46 offen offset:0 nt lds
buffer_load_dwordx4 v152, s[40:43], s46 offen offset:1024 nt lds
Mvaa 0,168,200
Ex 21
Mvaa 0,172,204
Ta 178,241,1280
Pk 82,20,21
Cvaa 4,172,220,124
Ex 22
Mvaa 4,168,216
Ta 180,241,2304
Mvaa 4,164,212
Ex 23
Mvaa 4,160,208
Ta 182,241,3328
Pk 83,22,23
Cvaa 8,160,224,128
Ex 24
Mvaa 8,164,228
Ta 184,241,288
buffer_load_dwordx4 v250, s[40:43], s46 offen offset:2048 nt lds
buffer_load_dwordx4 v152, s[40:43], s46 offen offset:3072 nt lds
Mvaa 8,168,232
Ex 25
Mvaa 8,172,236
Ta 186,241,1312
Pk 90,24,25
Cvaa 12,172,252,132
Ex 26
Mvaa 12,168,248
Ta 188,241,2336
Mvaa 12,164,244
Ex 27
Mvaa 12,160,240
Wl 3
Ta 190,241,3360
Pk 91,26,27
Maav 40,176,40
s_mov_b32 m0, 144480
Ex 28
Maav 40,180,44
Ra 160,240,19584
buffer_load_dwordx4 v251, s[40:43], s46 offen offset:0 nt lds
buffer_load_dwordx4 v153, s[40:43], s46 offen offset:1024 nt lds
Maav 44,176,48
Ex 29
Maav 44,180,52
Ra 164,240,19840
Pk 98,28,29
Maav 48,176,56
Ex 30
Maav 48,180,60
Ra 168,240,20096
Maav 52,176,64
Ex 31
Maav 52,180,68
Wl 3
Ra 172,240,20352
Pk 99,30,31
Maav 60,184,40
Ex 32
Maav 60,188,44
buffer_load_dwordx4 v251, s[40:43], s46 offen offset:2048 nt lds
buffer_load_dwordx4 v153, s[40:43], s46 offen offset:3072 nt lds
Maav 64,184,48
Ex 33
Maav 64,188,52
Pk 106,32,33
Maav 68,184,56
Ex 34
Maav 68,188,60
Maav 72,184,64
Ex 35
Maav 72,188,68
Wl 0
Pk 107,34,35
Cvaa 20,160,192,120
s_add_u32 s46, s46, s69
s_xor_b32 s69, s69, 114688
s_mov_b32 m0, 0
Ex 0
Mvaa 20,164,196
Ta 176,241,512
buffer_load_dwordx4 v248, s[36:39], s44 offen offset:2048 nt lds
buffer_load_dwordx4 v150, s[36:39], s44 offen offset:3072 nt lds
Mvaa 20,168,200
Ex 1
Mvaa 20,172,204
Ta 178,241,1536
Pk 84,0,1
Cvaa 24,172,220,124
Ex 2
Mvaa 24,168,216
Ta 180,241,2560
Mvaa 24,164,212
Ex 3
Mvaa 24,160,208
Ta 182,241,3584
Pk 85,2,3
Cvaa 28,160,224,128
s_mov_b32 m0, 4096
Ex 4
Mvaa 28,164,228
Ta 184,241,544
buffer_load_dwordx4 v249, s[36:39], s44 offen offset:2048 nt lds
buffer_load_dwordx4 v151, s[36:39], s44 offen offset:3072 nt lds
Mvaa 28,168,232
Ex 5
Mvaa 28,172,236
Ta 186,241,1568
Pk 92,4,5
Cvaa 32,172,252,132
Ex 6
Mvaa 32,168,248
Ta 188,241,2592
Mvaa 32,164,244
Ex 7
Mvaa 32,160,240
Wl 3
Ta 190,241,3616
Pk 93,6,7
Maav 80,176,40
Ex 8
Maav 80,180,44
Ra 160,240,33024
Maav 84,176,48
s_mov_b32 m0, 8224
Ex 9
Maav 84,180,52
Ra 164,240,33280
buffer_load_dwordx4 v250, s[36:39], s44 offen offset:2048 nt lds
buffer_load_dwordx4 v152, s[36:39], s44 offen offset:3072 nt lds
Maav 88,176,56
Ex 10
Maav 88,180,60
Ra 168,240,33536
Pk 100,8,9
Maav 92,176,64
Ex 11
Maav 92,180,68
Wl 3
Ra 172,240,33792
Pk 101,10,11
Maav 100,184,40
Ex 12
Maav 100,188,44
Maav 104,184,48
s_mov_b32 m0, 12320
Ex 13
Maav 104,188,52
buffer_load_dwordx4 v251, s[36:39], s44 offen offset:2048 nt lds
buffer_load_dwordx4 v153, s[36:39], s44 offen offset:3072 nt lds
Maav 108,184,56
Ex 14
Maav 108,188,60
Pk 108,12,13
Maav 112,184,64
Ex 15
Maav 112,188,68
Pk 109,14,15
Maav 16,156,80
Maav 36,156,88
Maav 56,156,96
Maav 76,156,104
Wl 0
Cvaa 0,160,192,120
s_add_u32 s44, s44, 1024
s_mov_b32 m0, 16512
Ex 20
Mvaa 0,164,196
Ta 176,241,768
buffer_load_dwordx4 v248, s[36:39], s44 offen offset:0 nt lds
buffer_load_dwordx4 v150, s[36:39], s44 offen offset:1024 nt lds
Mvaa 0,168,200
Ex 21
Mvaa 0,172,204
Ta 178,241,1792
Pk 86,20,21
Cvaa 4,172,220,124
Ex 22
Mvaa 4,168,216
Ta 180,241,2816
Mvaa 4,164,212
Ex 23
Mvaa 4,160,208
Ta 182,241,3840
Pk 87,22,23
Cvaa 8,160,224,128
s_mov_b32 m0, 20608
Ex 24
Mvaa 8,164,228
Ta 184,241,800
buffer_load_dwordx4 v249, s[36:39], s44 offen offset:0 nt lds
buffer_load_dwordx4 v151, s[36:39], s44 offen offset:1024 nt lds
Mvaa 8,168,232
Ex 25
Mvaa 8,172,236
Ta 186,241,1824
Pk 94,24,25
Cvaa 12,172,252,132
Ex 26
Mvaa 12,168,248
Ta 188,241,2848
Mvaa 12,164,244
Ex 27
Mvaa 12,160,240
Wl 3
Ta 190,241,3872
Pk 95,26,27
Maav 120,176,40
s_mov_b32 m0, 24736
Ex 28
Maav 120,180,44
Ra 160,240,34048
buffer_load_dwordx4 v250, s[36:39], s44 offen offset:0 nt lds
buffer_load_dwordx4 v152, s[36:39], s44 offen offset:1024 nt lds
Maav 124,176,48
Ex 29
Maav 124,180,52
Ra 164,240,34304
Pk 102,28,29
Maav 128,176,56
Ex 30
Maav 128,180,60
Ra 168,240,34560
Maav 132,176,64
Ex 31
Maav 132,180,68
Wl 3
Ra 172,240,34816
Pk 103,30,31
Maav 140,184,40
s_mov_b32 m0, 28832
Ex 32
Maav 140,188,44
buffer_load_dwordx4 v251, s[36:39], s44 offen offset:0 nt lds
buffer_load_dwordx4 v153, s[36:39], s44 offen offset:1024 nt lds
Maav 144,184,48
Ex 33
Maav 144,188,52
Pk 110,32,33
Maav 148,184,56
Ex 34
Maav 148,188,60
Maav 152,184,64
Ex 35
Maav 152,188,68
Wl 0
Pk 111,34,35
Cvaa 20,160,192,120
s_waitcnt vmcnt(62)
s_barrier
s_mov_b32 m0, 82560
Ex 0
Mvaa 20,164,196
Ta 176,241,16512
buffer_load_dwordx4 v248, s[40:43], s46 offen offset:0 nt lds
buffer_load_dwordx4 v150, s[40:43], s46 offen offset:1024 nt lds
Mvaa 20,168,200
Ex 1
Mvaa 20,172,204
Ta 178,241,17536
Pk 40,0,1
Cvaa 24,172,220,124
Ex 2
Mvaa 24,168,216
Ta 180,241,18560
Mvaa 24,164,212
Ex 3
Mvaa 24,160,208
Ta 182,241,19584
Pk 41,2,3
Cvaa 28,160,224,128
Ex 4
Mvaa 28,164,228
Ta 184,241,16544
buffer_load_dwordx4 v248, s[40:43], s46 offen offset:2048 nt lds
buffer_load_dwordx4 v150, s[40:43], s46 offen offset:3072 nt lds
Mvaa 28,168,232
Ex 5
Mvaa 28,172,236
Ta 186,241,17568
Pk 48,4,5
Cvaa 32,172,252,132
Ex 6
Mvaa 32,168,248
Ta 188,241,18592
Mvaa 32,164,244
Ex 7
Mvaa 32,160,240
Wl 3
Ta 190,241,19616
Pk 49,6,7
Maav 0,176,80
Ex 8
Maav 0,180,84
Ra 160,240,35072
Maav 4,176,88
s_mov_b32 m0, 86688
Ex 9
Maav 4,180,92
)A"
R"A(Ra 164,240,35328
buffer_load_dwordx4 v249, s[40:43], s46 offen offset:0 nt lds
buffer_load_dwordx4 v151, s[40:43], s46 offen offset:1024 nt lds
Maav 8,176,96
Ex 10
Maav 8,180,100
Ra 168,240,35584
Pk 56,8,9
Maav 12,176,104
Ex 11
Maav 12,180,108
Wl 3
Ra 172,240,35840
Pk 57,10,11
Maav 20,184,80
Ex 12
Maav 20,188,84
Maav 24,184,88
Ex 13
Maav 24,188,92
buffer_load_dwordx4 v249, s[40:43], s46 offen offset:2048 nt lds
buffer_load_dwordx4 v151, s[40:43], s46 offen offset:3072 nt lds
Maav 28,184,96
Ex 14
Maav 28,188,100
Pk 64,12,13
Maav 32,184,104
Ex 15
Maav 32,188,108
Pk 65,14,15
Maav 16,156,84
Maav 36,156,92
Maav 56,156,100
Maav 76,156,108
Wl 0
Cvaa 0,160,192,120
s_mov_b32 m0, 90816
Ex 20
Mvaa 0,164,196
Ta 176,241,16768
buffer_load_dwordx4 v250, s[40:43], s46 offen offset:0 nt lds
buffer_load_dwordx4 v152, s[40:43], s46 offen offset:1024 nt lds
Mvaa 0,168,200
Ex 21
Mvaa 0,172,204
Ta 178,241,17792
Pk 42,20,21
Cvaa 4,172,220,124
Ex 22
Mvaa 4,168,216
Ta 180,241,18816
Mvaa 4,164,212
s_waitcnt vmcnt(62)
v_xor_b32 v158, s66, v158
Ex 23
Mvaa 4,160,208
Ta 182,241,19840
global_store_dword v159, v158, s[94:95] sc1
Cvaa 8,160,224,128
Ex 24
Mvaa 8,164,228
Ta 184,241,16800
Pk 43,22,23
Mvaa 8,168,232
Ex 25
Mvaa 8,172,236
Ta 186,241,17824
buffer_load_dwordx4 v250, s[40:43], s46 offen offset:2048 nt lds
buffer_load_dwordx4 v152, s[40:43], s46 offen offset:3072 nt lds
Cvaa 12,172,252,132
Ex 26
Mvaa 12,168,248
Ta 188,241,18848
Pk 50,24,25
Mvaa 12,164,244
Ex 27
Mvaa 12,160,240
Wl 3
Ta 190,241,19872
Pk 51,26,27
Maav 40,176,80
s_mov_b32 m0, 94944
Ex 28
Maav 40,180,84
Ra 160,240,36096
buffer_load_dwordx4 v251, s[40:43], s46 offen offset:0 nt lds
buffer_load_dwordx4 v153, s[40:43], s46 offen offset:1024 nt lds
Maav 44,176,88
Ex 29
Maav 44,180,92
Ra 164,240,36352
Pk 58,28,29
Maav 48,176,96
Ex 30
Maav 48,180,100
Ra 168,240,36608
Maav 52,176,104
Ex 31
Maav 52,180,108
Wl 3
Ra 172,240,36864
Pk 59,30,31
Maav 60,184,80
Ex 32
Maav 60,188,84
buffer_load_dwordx4 v251, s[40:43], s46 offen offset:2048 nt lds
buffer_load_dwordx4 v153, s[40:43], s46 offen offset:3072 nt lds
Maav 64,184,88
Ex 33
Maav 64,188,92
Pk 66,32,33
Maav 68,184,96
Ex 34
Maav 68,188,100
Maav 72,184,104
Ex 35
Maav 72,188,108
Wl 0
Pk 67,34,35
s_mov_b64 exec, 0
s_swappc_b64 s[12:13], s[14:15]
s_add_u32 s14, s14, 4096
s_addc_u32 s15, s15, 0
Cvaa 20,160,192,120
s_add_u32 s46, s46, 1024
s_mov_b32 m0, 16512
Ex 0
Mvaa 20,164,196
Ta 176,241,17024
buffer_load_dwordx4 v248, s[36:39], s44 offen offset:2048 nt lds
buffer_load_dwordx4 v150, s[36:39], s44 offen offset:3072 nt lds
Mvaa 20,168,200
Ex 1
Mvaa 20,172,204
Ta 178,241,18048
Pk 44,0,1
Cvaa 24,172,220,124
Ex 2
Mvaa 24,168,216
Ta 180,241,19072
Mvaa 24,164,212
Ex 3
Mvaa 24,160,208
Ta 182,241,20096
Pk 45,2,3
Cvaa 28,160,224,128
s_mov_b32 m0, 20608
Ex 4
Mvaa 28,164,228
Ta 184,241,17056
buffer_load_dwordx4 v249, s[36:39], s44 offen offset:2048 nt lds
buffer_load_dwordx4 v151, s[36:39], s44 offen offset:3072 nt lds
Mvaa 28,168,232
Ex 5
Mvaa 28,172,236
Ta 186,241,18080
Pk 52,4,5
Cvaa 32,172,252,132
Ex 6
Mvaa 32,168,248
Ta 188,241,19104
Mvaa 32,164,244
Ex 7
Mvaa 32,160,240
Wl 3
Ta 190,241,20128
Pk 53,6,7
Maav 80,176,80
Ex 8
Maav 80,180,84
Ra 160,240,49536
Maav 84,176,88
s_mov_b32 m0, 24736
Ex 9
Maav 84,180,92
Ra 164,240,49792
buffer_load_dwordx4 v250, s[36:39], s44 offen offset:2048 nt lds
buffer_load_dwordx4 v152, s[36:39], s44 offen offset:3072 nt lds
Maav 88,176,96
Ex 10
Maav 88,180,100
Ra 168,240,50048
Pk 60,8,9
Maav 92,176,104
Ex 11
Maav 92,180,108
Wl 3
Ra 172,240,50304
Pk 61,10,11
Maav 100,184,80
Ex 12
Maav 100,188,84
Maav 104,184,88
s_mov_b32 m0, 28832
Ex 13
Maav 104,188,92
buffer_load_dwordx4 v251, s[36:39], s44 offen offset:2048 nt lds
buffer_load_dwordx4 v153, s[36:39], s44 offen offset:3072 nt lds
Maav 108,184,96
Ex 14
Maav 108,188,100
Pk 68,12,13
Maav 112,184,104
Ex 15
Maav 112,188,108
Pk 69,14,15
Maav 16,156,40
Maav 36,156,48
Maav 56,156,56
Maav 76,156,64
Wl 0
Cvaa 0,160,192,120
s_add_u32 s44, s44, 3072
s_mov_b32 m0, 33024
Ex 20
Mvaa 0,164,196
Ta 176,241,17280
buffer_load_dwordx4 v248, s[36:39], s44 offen offset:0 nt lds
buffer_load_dwordx4 v150, s[36:39], s44 offen offset:1024 nt lds
Mvaa 0,168,200
Ex 21
Mvaa 0,172,204
Ta 178,241,18304
Pk 46,20,21
Cvaa 4,172,220,124
Ex 22
Mvaa 4,168,216
Ta 180,241,19328
Mvaa 4,164,212
Ex 23
Mvaa 4,160,208
Ta 182,241,20352
Pk 47,22,23
Cvaa 8,160,224,128
s_mov_b32 m0, 37120
Ex 24
Mvaa 8,164,228
Ta 184,241,17312
buffer_load_dwordx4 v249, s[36:39], s44 offen offset:0 nt lds
buffer_load_dwordx4 v151, s[36:39], s44 offen offset:1024 nt lds
Mvaa 8,168,232
Ex 25
Mvaa 8,172,236
Ta 186,241,18336
Pk 54,24,25
Cvaa 12,172,252,132
Ex 26
Mvaa 12,168,248
Ta 188,241,19360
Mvaa 12,164,244
Ex 27
Mvaa 12,160,240
Wl 3
Ta 190,241,20384
Pk 55,26,27
Maav 120,176,80
s_mov_b32 m0, 41248
Ex 28
Maav 120,180,84
Ra 160,240,50560
buffer_load_dwordx4 v250, s[36:39], s44 offen offset:0 nt lds
buffer_load_dwordx4 v152, s[36:39], s44 offen offset:1024 nt lds
Maav 124,176,88
Ex 29
Maav 124,180,92
Ra 164,240,50816
Pk 62,28,29
Maav 128,176,96
Ex 30
Maav 128,180,100
Ra 168,240,51072
Maav 132,176,104
Ex 31
Maav 132,180,108
Wl 3
Ra 172,240,51328
Pk 63,30,31
Maav 140,184,80
s_mov_b32 m0, 45344
Ex 32
Maav 140,188,84
buffer_load_dwordx4 v251, s[36:39], s44 offen offset:0 nt lds
buffer_load_dwordx4 v153, s[36:39], s44 offen offset:1024 nt lds
Maav 144,184,88
Ex 33
Maav 144,188,92
Pk 70,32,33
Maav 148,184,96
Ex 34
Maav 148,188,100
Maav 152,184,104
Ex 35
Maav 152,188,108
Wl 0
Pk 71,34,35
Cvaa 20,160,192,120
s_waitcnt vmcnt(62)
s_barrier
s_mov_b32 m0, 99072
Ex 0
Mvaa 20,164,196
Ta 176,241,33024
buffer_load_dwordx4 v248, s[40:43], s46 offen offset:0 nt lds
buffer_load_dwordx4 v150, s[40:43], s46 offen offset:1024 nt lds
Mvaa 20,168,200
Ex 1
Mvaa 20,172,204
Ta 178,241,34048
Pk 80,0,1
Cvaa 24,172,220,124
Ex 2
Mvaa 24,168,216
Ta 180,241,35072
Mvaa 24,164,212
Ex 3
Mvaa 24,160,208
Ta 182,241,36096
Pk 81,2,3
Cvaa 28,160,224,128
Ex 4
Mvaa 28,164,228
Ta 184,241,33056
buffer_load_dwordx4 v248, s[40:43], s46 offen offset:2048 nt lds
buffer_load_dwordx4 v150, s[40:43], s46 offen offset:3072 nt lds
Mvaa 28,168,232
Ex 5
Mvaa 28,172,236
Ta 186,241,34080
Pk 88,4,5
Cvaa 32,172,252,132
Ex 6
Mvaa 32,168,248
Ta 188,241,35104
Mvaa 32,164,244
Ex 7
Mvaa 32,160,240
Wl 3
Ta 190,241,36128
Pk 89,6,7
Maav 0,176,40
Ex 8
Maav 0,180,44
Ra 160,240,51584
Maav 4,176,48
s_mov_b32 m0, 103200
Ex 9
Maav 4,180,52
Ra 164,240,51840
buffer_load_dwordx4 v249, s[40:43], s46 offen offset:0 nt lds
buffer_load_dwordx4 v151, s[40:43], s46 offen offset:1024 nt lds
Maav 8,176,56
Ex 10
Maav 8,180,60
Ra 168,240,52096
Pk 96,8,9
Maav 12,176,64
Ex 11
Maav 12,180,68
Wl 3
Ra 172,240,52352
Pk 97,10,11
Maav 20,184,40
Ex 12
Maav 20,188,44
Maav 24,184,48
Ex 13
Maav 24,188,52
buffer_load_dwordx4 v249, s[40:43], s46 offen offset:2048 nt lds
buffer_load_dwordx4 v151, s[40:43], s46 offen offset:3072 nt lds
Maav 28,184,56
Ex 14
Maav 28,188,60
Pk 104,12,13
Maav 32,184,64
Ex 15
Maav 32,188,68
Pk 105,14,15
Maav 16,156,44
Maav 36,156,52
Maav 56,156,60
Maav 76,156,68
Wl 0
Cvaa 0,160,192,120
s_mov_b32 m0, 107328
Ex 20
Mvaa 0,164,196
Ta 176,241,33280
buffer_load_dwordx4 v250, s[40:43], s46 offen offset:0 nt lds
buffer_load_dwordx4 v152, s[40:43], s46 offen offset:1024 nt lds
Mvaa 0,168,200
Ex 21
Mvaa 0,172,204
Ta 178,241,34304
Pk 82,20,21
Cvaa 4,172,220,124
Ex 22
Mvaa 4,168,216
Ta 180,241,35328
Mvaa 4,164,212
Ex 23
)A"
R"A(Mvaa 4,160,208
Ta 182,241,36352
Pk 83,22,23
Cvaa 8,160,224,128
Ex 24
Mvaa 8,164,228
Ta 184,241,33312
buffer_load_dwordx4 v250, s[40:43], s46 offen offset:2048 nt lds
buffer_load_dwordx4 v152, s[40:43], s46 offen offset:3072 nt lds
Mvaa 8,168,232
Ex 25
Mvaa 8,172,236
Ta 186,241,34336
Pk 90,24,25
Cvaa 12,172,252,132
Ex 26
Mvaa 12,168,248
Ta 188,241,35360
Mvaa 12,164,244
Ex 27
Mvaa 12,160,240
Wl 3
Ta 190,241,36384
Pk 91,26,27
Maav 40,176,40
s_mov_b32 m0, 111456
Ex 28
Maav 40,180,44
Ra 160,240,52608
buffer_load_dwordx4 v251, s[40:43], s46 offen offset:0 nt lds
buffer_load_dwordx4 v153, s[40:43], s46 offen offset:1024 nt lds
Maav 44,176,48
Ex 29
Maav 44,180,52
Ra 164,240,52864
Pk 98,28,29
Maav 48,176,56
Ex 30
Maav 48,180,60
Ra 168,240,53120
Maav 52,176,64
Ex 31
Maav 52,180,68
Wl 3
Ra 172,240,53376
Pk 99,30,31
Maav 60,184,40
Ex 32
Maav 60,188,44
buffer_load_dwordx4 v251, s[40:43], s46 offen offset:2048 nt lds
buffer_load_dwordx4 v153, s[40:43], s46 offen offset:3072 nt lds
Maav 64,184,48
Ex 33
Maav 64,188,52
Pk 106,32,33
Maav 68,184,56
Ex 34
Maav 68,188,60
Maav 72,184,64
Ex 35
Maav 72,188,68
Wl 0
Pk 107,34,35
s_mov_b64 exec, 0
s_swappc_b64 s[12:13], s[14:15]
s_add_u32 s14, s14, 4096
s_addc_u32 s15, s15, 0
Cvaa 20,160,192,120
s_add_u32 s46, s46, 3072
s_mov_b32 m0, 33024
Ex 0
Mvaa 20,164,196
Ta 176,241,33536
buffer_load_dwordx4 v248, s[36:39], s44 offen offset:2048 nt lds
buffer_load_dwordx4 v150, s[36:39], s44 offen offset:3072 nt lds
Mvaa 20,168,200
Ex 1
Mvaa 20,172,204
Ta 178,241,34560
Pk 84,0,1
Cvaa 24,172,220,124
Ex 2
Mvaa 24,168,216
Ta 180,241,35584
Mvaa 24,164,212
Ex 3
Mvaa 24,160,208
Ta 182,241,36608
Pk 85,2,3
Cvaa 28,160,224,128
s_mov_b32 m0, 37120
Ex 4
Mvaa 28,164,228
Ta 184,241,33568
buffer_load_dwordx4 v249, s[36:39], s44 offen offset:2048 nt lds
buffer_load_dwordx4 v151, s[36:39], s44 offen offset:3072 nt lds
Mvaa 28,168,232
Ex 5
Mvaa 28,172,236
Ta 186,241,34592
Pk 92,4,5
Cvaa 32,172,252,132
Ex 6
Mvaa 32,168,248
Ta 188,241,35616
Mvaa 32,164,244
Ex 7
Mvaa 32,160,240
Wl 3
Ta 190,241,36640
Pk 93,6,7
Maav 80,176,40
Ex 8
Maav 80,180,44
Ra 160,240,0
Maav 84,176,48
s_mov_b32 m0, 41248
Ex 9
Maav 84,180,52
Ra 164,240,256
buffer_load_dwordx4 v250, s[36:39], s44 offen offset:2048 nt lds
buffer_load_dwordx4 v152, s[36:39], s44 offen offset:3072 nt lds
Maav 88,176,56
Ex 10
Maav 88,180,60
Ra 168,240,512
Pk 100,8,9
Maav 92,176,64
Ex 11
Maav 92,180,68
Wl 3
Ra 172,240,768
Pk 101,10,11
Maav 100,184,40
Ex 12
Maav 100,188,44
Maav 104,184,48
s_mov_b32 m0, 45344
Ex 13
Maav 104,188,52
buffer_load_dwordx4 v251, s[36:39], s44 offen offset:2048 nt lds
buffer_load_dwordx4 v153, s[36:39], s44 offen offset:3072 nt lds
Maav 108,184,56
Ex 14
Maav 108,188,60
Pk 108,12,13
Maav 112,184,64
Ex 15
Maav 112,188,68
Pk 109,14,15
Maav 16,156,80
Maav 36,156,88
Maav 56,156,96
Maav 76,156,104
Wl 0
Cvaa 0,160,192,120
s_add_u32 s44, s44, 1024
s_mov_b32 m0, 49536
Ex 20
Mvaa 0,164,196
Ta 176,241,33792
buffer_load_dwordx4 v248, s[36:39], s44 offen offset:0 nt lds
buffer_load_dwordx4 v150, s[36:39], s44 offen offset:1024 nt lds
Mvaa 0,168,200
Ex 21
Mvaa 0,172,204
Ta 178,241,34816
Pk 86,20,21
Cvaa 4,172,220,124
Ex 22
Mvaa 4,168,216
Ta 180,241,35840
Mvaa 4,164,212
Ex 23
Mvaa 4,160,208
Ta 182,241,36864
Pk 87,22,23
Cvaa 8,160,224,128
s_mov_b32 m0, 53632
Ex 24
Mvaa 8,164,228
Ta 184,241,33824
buffer_load_dwordx4 v249, s[36:39], s44 offen offset:0 nt lds
buffer_load_dwordx4 v151, s[36:39], s44 offen offset:1024 nt lds
Mvaa 8,168,232
Ex 25
Mvaa 8,172,236
Ta 186,241,34848
Pk 94,24,25
Cvaa 12,172,252,132
Ex 26
Mvaa 12,168,248
Ta 188,241,35872
Mvaa 12,164,244
Ex 27
Mvaa 12,160,240
Wl 3
Ta 190,241,36896
Pk 95,26,27
Maav 120,176,40
s_mov_b32 m0, 57760
Ex 28
Maav 120,180,44
Ra 160,240,1024
buffer_load_dwordx4 v250, s[36:39], s44 offen offset:0 nt lds
buffer_load_dwordx4 v152, s[36:39], s44 offen offset:1024 nt lds
Maav 124,176,48
Ex 29
Maav 124,180,52
Ra 164,240,1280
Pk 102,28,29
Maav 128,176,56
Ex 30
Maav 128,180,60
Ra 168,240,1536
Maav 132,176,64
Ex 31
Maav 132,180,68
Wl 3
Ra 172,240,1792
Pk 103,30,31
Maav 140,184,40
s_mov_b32 m0, 61856
Ex 32
Maav 140,188,44
buffer_load_dwordx4 v251, s[36:39], s44 offen offset:0 nt lds
buffer_load_dwordx4 v153, s[36:39], s44 offen offset:1024 nt lds
Maav 144,184,48
Ex 33
Maav 144,188,52
Pk 110,32,33
Maav 148,184,56
Ex 34
Maav 148,188,60
Maav 152,184,64
Ex 35
Maav 152,188,68
Wl 0
Pk 111,34,35
Cvaa 20,160,192,120
s_waitcnt vmcnt(62)
s_barrier
s_mov_b32 m0, 115584
Ex 0
Mvaa 20,164,196
Ta 176,241,49536
buffer_load_dwordx4 v248, s[40:43], s46 offen offset:0 nt lds
buffer_load_dwordx4 v150, s[40:43], s46 offen offset:1024 nt lds
Mvaa 20,168,200
Ex 1
Mvaa 20,172,204
Ta 178,241,50560
Pk 40,0,1
Cvaa 24,172,220,124
Ex 2
Mvaa 24,168,216
Ta 180,241,51584
Mvaa 24,164,212
Ex 3
Mvaa 24,160,208
Ta 182,241,52608
Pk 41,2,3
Cvaa 28,160,224,128
Ex 4
Mvaa 28,164,228
Ta 184,241,49568
buffer_load_dwordx4 v248, s[40:43], s46 offen offset:2048 nt lds
buffer_load_dwordx4 v150, s[40:43], s46 offen offset:3072 nt lds
Mvaa 28,168,232
Ex 5
Mvaa 28,172,236
Ta 186,241,50592
Pk 48,4,5
Cvaa 32,172,252,132
Ex 6
Mvaa 32,168,248
Ta 188,241,51616
Mvaa 32,164,244
Ex 7
Mvaa 32,160,240
Wl 3
Ta 190,241,52640
Pk 49,6,7
Maav 0,176,80
Ex 8
Maav 0,180,84
Ra 160,240,2048
Maav 4,176,88
s_mov_b32 m0, 119712
Ex 9
Maav 4,180,92
Ra 164,240,2304
buffer_load_dwordx4 v249, s[40:43], s46 offen offset:0 nt lds
buffer_load_dwordx4 v151, s[40:43], s46 offen offset:1024 nt lds
Maav 8,176,96
Ex 10
Maav 8,180,100
Ra 168,240,2560
Pk 56,8,9
Maav 12,176,104
Ex 11
Maav 12,180,108
Wl 3
Ra 172,240,2816
Pk 57,10,11
Maav 20,184,80
Ex 12
Maav 20,188,84
Maav 24,184,88
Ex 13
Maav 24,188,92
buffer_load_dwordx4 v249, s[40:43], s46 offen offset:2048 nt lds
buffer_load_dwordx4 v151, s[40:43], s46 offen offset:3072 nt lds
Maav 28,184,96
Ex 14
Maav 28,188,100
Pk 64,12,13
Maav 32,184,104
Ex 15
Maav 32,188,108
Pk 65,14,15
Maav 16,156,84
Maav 36,156,92
Maav 56,156,100
Maav 76,156,108
Wl 0
Cvaa 0,160,192,120
s_mov_b32 m0, 123840
Ex 20
Mvaa 0,164,196
Ta 176,241,49792
buffer_load_dwordx4 v250, s[40:43], s46 offen offset:0 nt lds
buffer_load_dwordx4 v152, s[40:43], s46 offen offset:1024 nt lds
Mvaa 0,168,200
Ex 21
Mvaa 0,172,204
Ta 178,241,50816
Pk 42,20,21
Cvaa 4,172,220,124
Ex 22
Mvaa 4,168,216
Ta 180,241,51840
Mvaa 4,164,212
Ex 23
Mvaa 4,160,208
Ta 182,241,52864
Pk 43,22,23
Cvaa 8,160,224,128
Ex 24
Mvaa 8,164,228
Ta 184,241,49824
buffer_load_dwordx4 v250, s[40:43], s46 offen offset:2048 nt lds
buffer_load_dwordx4 v152, s[40:43], s46 offen offset:3072 nt lds
Mvaa 8,168,232
Ex 25
Mvaa 8,172,236
Ta 186,241,50848
Pk 50,24,25
Cvaa 12,172,252,132
Ex 26
Mvaa 12,168,248
Ta 188,241,51872
Mvaa 12,164,244
Ex 27
Mvaa 12,160,240
Wl 3
Ta 190,241,52896
Pk 51,26,27
Maav 40,176,80
s_mov_b32 m0, 127968
Ex 28
Maav 40,180,84
Ra 160,240,3072
buffer_load_dwordx4 v251, s[40:43], s46 offen offset:0 nt lds
buffer_load_dwordx4 v153, s[40:43], s46 offen offset:1024 nt lds
Maav 44,176,88
Ex 29
Maav 44,180,92
Ra 164,240,3328
Pk 58,28,29
Maav 48,176,96
Ex 30
Maav 48,180,100
Ra 168,240,3584
Maav 52,176,104
Ex 31
Maav 52,180,108
Wl 3
Ra 172,240,3840
Pk 59,30,31
Maav 60,184,80
Ex 32
Maav 60,188,84
buffer_load_dwordx4 v251, s[40:43], s46 offen offset:2048 nt lds
buffer_load_dwordx4 v153, s[40:43], s46 offen offset:3072 nt lds
Maav 64,184,88
Ex 33
Maav 64,188,92
Pk 66,32,33
Maav 68,184,96
)A"
R"A(Ex 34
Maav 68,188,100
Maav 72,184,104
Ex 35
Maav 72,188,108
Wl 0
Pk 67,34,35
Cvaa 20,160,192,120
s_add_u32 s46, s46, 1024
s_mov_b32 m0, 49536
Ex 0
Mvaa 20,164,196
Ta 176,241,50048
buffer_load_dwordx4 v248, s[36:39], s44 offen offset:2048 nt lds
buffer_load_dwordx4 v150, s[36:39], s44 offen offset:3072 nt lds
Mvaa 20,168,200
Ex 1
Mvaa 20,172,204
Ta 178,241,51072
Pk 44,0,1
Cvaa 24,172,220,124
Ex 2
Mvaa 24,168,216
Ta 180,241,52096
Mvaa 24,164,212
Ex 3
Mvaa 24,160,208
Ta 182,241,53120
Pk 45,2,3
Cvaa 28,160,224,128
Ex 4
Mvaa 28,164,228
Ta 184,241,50080
global_load_dword v244, v243, s[50:51] sc1
Mvaa 28,168,232
Ex 5
Mvaa 28,172,236
Ta 186,241,51104
Pk 52,4,5
Cvaa 32,172,252,132
s_mov_b32 m0, 53632
Ex 6
Mvaa 32,168,248
Ta 188,241,52128
buffer_load_dwordx4 v249, s[36:39], s44 offen offset:2048 nt lds
buffer_load_dwordx4 v151, s[36:39], s44 offen offset:3072 nt lds
Mvaa 32,164,244
Ex 7
Mvaa 32,160,240
Wl 3
Ta 190,241,53152
Pk 53,6,7
Maav 80,176,80
Ex 8
Maav 80,180,84
Ra 160,240,16512
Maav 84,176,88
Ex 9
Maav 84,180,92
Ra 164,240,16768
Pk 60,8,9
Maav 88,176,96
s_mov_b32 m0, 57760
Ex 10
Maav 88,180,100
Ra 168,240,17024
buffer_load_dwordx4 v250, s[36:39], s44 offen offset:2048 nt lds
buffer_load_dwordx4 v152, s[36:39], s44 offen offset:3072 nt lds
Maav 92,176,104
Ex 11
Maav 92,180,108
Wl 3
Ra 172,240,17280
Pk 61,10,11
Maav 100,184,80
Ex 12
Maav 100,188,84
Maav 104,184,88
Ex 13
Maav 104,188,92
Pk 68,12,13
Maav 108,184,96
s_mov_b32 m0, 61856
Ex 14
Maav 108,188,100
buffer_load_dwordx4 v251, s[36:39], s44 offen offset:2048 nt lds
buffer_load_dwordx4 v153, s[36:39], s44 offen offset:3072 nt lds
Maav 112,184,104
Ex 15
Maav 112,188,108
Pk 69,14,15
Maav 16,156,40
Maav 36,156,48
Maav 56,156,56
Maav 76,156,64
Wl 0
Cvaa 0,160,192,120
s_add_u32 s44, s44, s68
s_xor_b32 s68, s68, 114688
s_mov_b32 m0, 66048
s_add_u32 s79, s49, 4294918144
Ex 20
Mvaa 0,164,196
Ta 176,241,50304
buffer_load_dwordx4 v72, s[72:75], s79 offen offset:0 lds
Mvaa 0,168,200
Ex 21
Mvaa 0,172,204
Ta 178,241,51328
Pk 46,20,21
Cvaa 4,172,220,124
Ex 22
Mvaa 4,168,216
Ta 180,241,52352
buffer_load_dwordx4 v73, s[72:75], s79 offen offset:1032 lds
Mvaa 4,164,212
Ex 23
Mvaa 4,160,208
Ta 182,241,53376
Pk 47,22,23
Cvaa 8,160,224,128
Ex 24
Mvaa 8,164,228
Ta 184,241,50336
buffer_load_dwordx4 v74, s[72:75], s79 offen offset:2064 lds
Mvaa 8,168,232
Ex 25
Mvaa 8,172,236
Ta 186,241,51360
Pk 54,24,25
Cvaa 12,172,252,132
Ex 26
Mvaa 12,168,248
Ta 188,241,52384
buffer_load_dwordx4 v75, s[72:75], s79 offen offset:3096 lds
Mvaa 12,164,244
Ex 27
Mvaa 12,160,240
Wl 3
Ta 190,241,53408
Pk 55,26,27
Maav 120,176,80
s_mov_b32 m0, 70176
s_add_u32 s79, s49, 4294934528
Ex 28
Maav 120,180,84
Ra 160,240,17536
buffer_load_dwordx4 v72, s[72:75], s79 offen offset:0 lds
Maav 124,176,88
Ex 29
Maav 124,180,92
Ra 164,240,17792
Pk 62,28,29
Maav 128,176,96
Ex 30
Maav 128,180,100
Ra 168,240,18048
buffer_load_dwordx4 v73, s[72:75], s79 offen offset:1032 lds
Maav 132,176,104
Ex 31
Maav 132,180,108
Wl 3
Ra 172,240,18304
Pk 63,30,31
Maav 140,184,80
Ex 32
Maav 140,188,84
buffer_load_dwordx4 v74, s[72:75], s79 offen offset:2064 lds
Maav 144,184,88
Ex 33
Maav 144,188,92
Pk 70,32,33
Maav 148,184,96
Ex 34
Maav 148,188,100
buffer_load_dwordx4 v75, s[72:75], s79 offen offset:3096 lds
Maav 152,184,104
Ex 35
Maav 152,188,108
Wl 0
Pk 71,34,35
Cvaa 20,160,192,120
s_waitcnt vmcnt(62)
s_barrier
s_mov_b32 m0, 132096
Ex 0
Mvaa 20,164,196
Ta 176,241,0
buffer_load_dwordx4 v248, s[40:43], s46 offen offset:0 nt lds
buffer_load_dwordx4 v150, s[40:43], s46 offen offset:1024 nt lds
Mvaa 20,168,200
Ex 1
Mvaa 20,172,204
Ta 178,241,1024
Pk 80,0,1
Cvaa 24,172,220,124
Ex 2
Mvaa 24,168,216
Ta 180,241,2048
Mvaa 24,164,212
Ex 3
Mvaa 24,160,208
Ta 182,241,3072
Pk 81,2,3
Cvaa 28,160,224,128
Ex 4
Mvaa 28,164,228
Ta 184,241,32
buffer_load_dwordx4 v248, s[40:43], s46 offen offset:2048 nt lds
buffer_load_dwordx4 v150, s[40:43], s46 offen offset:3072 nt lds
Mvaa 28,168,232
Ex 5
Mvaa 28,172,236
Ta 186,241,1056
Pk 88,4,5
Cvaa 32,172,252,132
Ex 6
Mvaa 32,168,248
Ta 188,241,2080
Mvaa 32,164,244
Ex 7
Mvaa 32,160,240
Wl 3
Ta 190,241,3104
Pk 89,6,7
Maav 0,176,40
Ex 8
Maav 0,180,44
Ra 160,240,18560
Maav 4,176,48
s_mov_b32 m0, 136224
Ex 9
Maav 4,180,52
Ra 164,240,18816
buffer_load_dwordx4 v249, s[40:43], s46 offen offset:0 nt lds
buffer_load_dwordx4 v151, s[40:43], s46 offen offset:1024 nt lds
Maav 8,176,56
Ex 10
Maav 8,180,60
Ra 168,240,19072
Pk 96,8,9
Maav 12,176,64
Ex 11
Maav 12,180,68
Wl 3
Ra 172,240,19328
Pk 97,10,11
Maav 20,184,40
Ex 12
Maav 20,188,44
Maav 24,184,48
Ex 13
Maav 24,188,52
buffer_load_dwordx4 v249, s[40:43], s46 offen offset:2048 nt lds
buffer_load_dwordx4 v151, s[40:43], s46 offen offset:3072 nt lds
Maav 28,184,56
Ex 14
Maav 28,188,60
Pk 104,12,13
Maav 32,184,64
Ex 15
Maav 32,188,68
Pk 105,14,15
Maav 16,156,44
Maav 36,156,52
Maav 56,156,60
Maav 76,156,68
Wl 0
Cvaa 0,160,192,120
s_mov_b32 m0, 140352
Ex 20
Mvaa 0,164,196
Ta 176,241,256
buffer_load_dwordx4 v250, s[40:43], s46 offen offset:0 nt lds
buffer_load_dwordx4 v152, s[40:43], s46 offen offset:1024 nt lds
Mvaa 0,168,200
Ex 21
Mvaa 0,172,204
Ta 178,241,1280
Pk 82,20,21
Cvaa 4,172,220,124
Ex 22
Mvaa 4,168,216
Ta 180,241,2304
Mvaa 4,164,212
Ex 23
Mvaa 4,160,208
Ta 182,241,3328
Pk 83,22,23
Cvaa 8,160,224,128
Ex 24
Mvaa 8,164,228
Ta 184,241,288
buffer_load_dwordx4 v250, s[40:43], s46 offen offset:2048 nt lds
buffer_load_dwordx4 v152, s[40:43], s46 offen offset:3072 nt lds
Mvaa 8,168,232
Ex 25
Mvaa 8,172,236
Ta 186,241,1312
Pk 90,24,25
Cvaa 12,172,252,132
Ex 26
Mvaa 12,168,248
Ta 188,241,2336
Mvaa 12,164,244
Ex 27
Mvaa 12,160,240
Wl 3
Ta 190,241,3360
Pk 91,26,27
Maav 40,176,40
s_mov_b32 m0, 144480
Ex 28
Maav 40,180,44
Ra 160,240,19584
buffer_load_dwordx4 v251, s[40:43], s46 offen offset:0 nt lds
buffer_load_dwordx4 v153, s[40:43], s46 offen offset:1024 nt lds
Maav 44,176,48
Ex 29
Maav 44,180,52
Ra 164,240,19840
Pk 98,28,29
Maav 48,176,56
Ex 30
Maav 48,180,60
Ra 168,240,20096
Maav 52,176,64
Ex 31
Maav 52,180,68
Wl 3
Ra 172,240,20352
Pk 99,30,31
Maav 60,184,40
Ex 32
Maav 60,188,44
buffer_load_dwordx4 v251, s[40:43], s46 offen offset:2048 nt lds
buffer_load_dwordx4 v153, s[40:43], s46 offen offset:3072 nt lds
Maav 64,184,48
Ex 33
Maav 64,188,52
Pk 106,32,33
Maav 68,184,56
Ex 34
Maav 68,188,60
Maav 72,184,64
Ex 35
Maav 72,188,68
Wl 0
Pk 107,34,35
s_bitcmp0_b32 s24, 3
s_cselect_b32 s19, 16384, 0
s_add_u32 s14, s14, s19
s_addc_u32 s15, s15, 0
s_mov_b64 exec, 0
s_swappc_b64 s[12:13], s[14:15]
s_add_u32 s14, s14, 4096
s_addc_u32 s15, s15, 0
Cvaa 20,160,192,120
s_add_u32 s46, s46, s69
s_xor_b32 s69, s69, 114688
s_mov_b32 m0, 74304
s_add_u32 s79, s49, 4294950912
Ex 0
Mvaa 20,164,196
Ta 176,241,512
buffer_load_dwordx4 v72, s[72:75], s79 offen offset:0 lds
Mvaa 20,168,200
Ex 1
Mvaa 20,172,204
Ta 178,241,1536
buffer_load_dwordx4 v73, s[72:75], s79 offen offset:1032 lds
Cvaa 24,172,220,124
Ex 2
Mvaa 24,168,216
Ta 180,241,2560
Pk 84,0,1
Mvaa 24,164,212
Ex 3
Mvaa 24,160,208
Ta 182,241,3584
buffer_load_dwordx4 v74, s[72:75], s79 offen offset:2064 lds
Cvaa 28,160,224,128
Ex 4
Mvaa 28,164,228
Ta 184,241,544
buffer_load_dwordx4 v75, s[72:75], s79 offen offset:3096 lds
Mvaa 28,168,232
Ex 5
Mvaa 28,172,236
Ta 186,241,1568
Pk 85,2,3
Cvaa 32,172,252,132
s_mov_b32 m0, 78432
s_add_u32 s79, s49, 0
Ex 6
Mvaa 32,168,248
Ta 188,241,2592
buffer_load_dwordx4 v72, s[72:75], s79 offen offset:0 lds
Mvaa 32,164,244
Ex 7
)A"
R"A(Mvaa 32,160,240
Wl 3
Ta 190,241,3616
buffer_load_dwordx4 v73, s[72:75], s79 offen offset:1032 lds
Maav 80,176,40
Ex 8
Maav 80,180,44
Ra 160,240,33024
Pk 92,4,5
Maav 84,176,48
Ex 9
Maav 84,180,52
Ra 164,240,33280
buffer_load_dwordx4 v74, s[72:75], s79 offen offset:2064 lds
Maav 88,176,56
Ex 10
Maav 88,180,60
Ra 168,240,33536
buffer_load_dwordx4 v75, s[72:75], s79 offen offset:3096 lds
Maav 92,176,64
Ex 11
Maav 92,180,68
Wl 3
Ra 172,240,33792
Pk 93,6,7
Maav 100,184,40
s_mov_b32 m0, 0
s_add_u32 s79, s49, 16384
Ex 12
Maav 100,188,44
buffer_load_dwordx4 v72, s[72:75], s79 offen offset:0 lds
Pk 100,8,9
Maav 104,184,48
Ex 13
Maav 104,188,52
buffer_load_dwordx4 v73, s[72:75], s79 offen offset:1032 lds
Pk 101,10,11
Maav 108,184,56
Ex 14
Maav 108,188,60
Pk 108,12,13
Maav 112,184,64
Ex 15
Maav 112,188,68
buffer_load_dwordx4 v74, s[72:75], s79 offen offset:2064 lds
Pk 109,14,15
Maav 16,156,80
Maav 36,156,88
buffer_load_dwordx4 v75, s[72:75], s79 offen offset:3096 lds
Maav 56,156,96
Maav 76,156,104
Wl 0
Cvaa 0,160,192,120
s_add_u32 s44, s44, 1024
s_mov_b32 m0, 4128
s_add_u32 s79, s49, 32768
Ex 20
Mvaa 0,164,196
Ta 176,241,768
buffer_load_dwordx4 v72, s[72:75], s79 offen offset:0 lds
Mvaa 0,168,200
Ex 21
Mvaa 0,172,204
Ta 178,241,1792
buffer_load_dwordx4 v73, s[72:75], s79 offen offset:1032 lds
Cvaa 4,172,220,124
Ex 22
Mvaa 4,168,216
Ta 180,241,2816
buffer_load_dwordx4 v74, s[72:75], s79 offen offset:2064 lds
Mvaa 4,164,212
Ex 23
Mvaa 4,160,208
Ta 182,241,3840
Pk 86,20,21
Cvaa 8,160,224,128
Ex 24
Mvaa 8,164,228
Ta 184,241,800
buffer_load_dwordx4 v75, s[72:75], s79 offen offset:3096 lds
Mvaa 8,168,232
s_mov_b32 m0, 8256
s_add_u32 s79, s49, 49152
Ex 25
Mvaa 8,172,236
Ta 186,241,1824
buffer_load_dwordx4 v72, s[72:75], s79 offen offset:0 lds
Cvaa 12,172,252,132
Ex 26
Mvaa 12,168,248
Ta 188,241,2848
buffer_load_dwordx4 v73, s[72:75], s79 offen offset:1032 lds
Mvaa 12,164,244
Ex 27
Mvaa 12,160,240
Wl 3
Ta 190,241,3872
Pk 87,22,23
Maav 120,176,40
Ex 28
Maav 120,180,44
Ra 160,240,34048
buffer_load_dwordx4 v74, s[72:75], s79 offen offset:2064 lds
Maav 124,176,48
Ex 29
Maav 124,180,52
Ra 164,240,34304
buffer_load_dwordx4 v75, s[72:75], s79 offen offset:3096 lds
Maav 128,176,56
s_mov_b32 m0, 12384
s_add_u32 s79, s49, 65536
Ex 30
Maav 128,180,60
Ra 168,240,34560
buffer_load_dwordx4 v72, s[72:75], s79 offen offset:0 lds
Maav 132,176,64
Ex 31
Maav 132,180,68
Wl 3
Ra 172,240,34816
Pk 94,24,25
Maav 140,184,40
Ex 32
Maav 140,188,44
buffer_load_dwordx4 v73, s[72:75], s79 offen offset:1032 lds
Pk 95,26,27
Maav 144,184,48
Ex 33
Maav 144,188,52
buffer_load_dwordx4 v74, s[72:75], s79 offen offset:2064 lds
Pk 102,28,29
Maav 148,184,56
Ex 34
Maav 148,188,60
buffer_load_dwordx4 v75, s[72:75], s79 offen offset:3096 lds
Pk 103,30,31
Maav 152,184,64
Ex 35
Maav 152,188,68
Wl 0
Pk 110,32,33
Pk 111,34,35
Cvaa 20,160,192,120
s_waitcnt vmcnt(62)
s_barrier
s_mov_b32 m0, 16512
s_add_u32 s79, s49, 81920
Ex 0
Mvaa 20,164,196
Ta 176,241,16512
buffer_load_dwordx4 v72, s[72:75], s79 offen offset:0 lds
Mvaa 20,168,200
Ex 1
Mvaa 20,172,204
Ta 178,241,17536
Pk 40,0,1
Cvaa 24,172,220,124
Ex 2
Mvaa 24,168,216
Ta 180,241,18560
buffer_load_dwordx4 v73, s[72:75], s79 offen offset:1032 lds
Mvaa 24,164,212
Ex 3
Mvaa 24,160,208
Ta 182,241,19584
Pk 41,2,3
Cvaa 28,160,224,128
Ex 4
Mvaa 28,164,228
Ta 184,241,16544
buffer_load_dwordx4 v74, s[72:75], s79 offen offset:2064 lds
Mvaa 28,168,232
Ex 5
Mvaa 28,172,236
Ta 186,241,17568
Pk 48,4,5
Cvaa 32,172,252,132
Ex 6
Mvaa 32,168,248
Ta 188,241,18592
buffer_load_dwordx4 v75, s[72:75], s79 offen offset:3096 lds
Mvaa 32,164,244
Ex 7
Mvaa 32,160,240
Wl 3
Ta 190,241,19616
Pk 49,6,7
Maav 0,176,80
Ex 8
Maav 0,180,84
Ra 160,240,35072
Maav 4,176,88
s_mov_b32 m0, 20640
s_add_u32 s79, s49, 98304
Ex 9
Maav 4,180,92
Ra 164,240,35328
buffer_load_dwordx4 v72, s[72:75], s79 offen offset:0 lds
Maav 8,176,96
Ex 10
Maav 8,180,100
Ra 168,240,35584
Pk 56,8,9
Maav 12,176,104
Ex 11
Maav 12,180,108
Wl 3
Ra 172,240,35840
buffer_load_dwordx4 v73, s[72:75], s79 offen offset:1032 lds
Maav 20,184,80
Ex 12
Maav 20,188,84
Pk 57,10,11
Maav 24,184,88
Ex 13
Maav 24,188,92
buffer_load_dwordx4 v74, s[72:75], s79 offen offset:2064 lds
Pk 64,12,13
Maav 28,184,96
Ex 14
Maav 28,188,100
Maav 32,184,104
Ex 15
Maav 32,188,108
buffer_load_dwordx4 v75, s[72:75], s79 offen offset:3096 lds
Pk 65,14,15
Maav 16,156,84
Maav 36,156,92
Maav 56,156,100
Maav 76,156,108
Wl 0
s_waitcnt vmcnt(60)
s_mov_b32 s58, 0
v_cmp_gt_i32 s[64:65], 0, v244
v_and_b32 v244, 0x7fffffff, v244
v_cmp_ne_u32 vcc, s81, v244
s_nop 0
s_and_b64 vcc, vcc, s[34:35]
s_cbranch_scc1 SPINP0B_%=
OKP0B_%=:
Cvaa 0,160,192,120
s_mov_b32 m0, 24768
s_add_u32 s79, s49, 114688
Ex 20
Mvaa 0,164,196
s_and_b32 s56, s24, 3
Ta 176,241,16768
buffer_load_dwordx4 v72, s[72:75], s79 offen offset:0 lds
Mvaa 0,168,200
s_cmp_le_u32 s56, 0
s_cselect_b32 s56, 1, 0
Ex 21
Mvaa 0,172,204
s_andn2_b32 s16, s49, 0xc0000
Ta 178,241,17792
Pk 42,20,21
Cvaa 4,172,220,124
s_lshl_b32 s17, s56, 18
Ex 22
Mvaa 4,168,216
s_add_u32 s16, s16, s17
Ta 180,241,18816
buffer_load_dwordx4 v73, s[72:75], s79 offen offset:1032 lds
Mvaa 4,164,212
s_lshr_b32 s17, s64, s56
Ex 23
Mvaa 4,160,208
s_and_b32 s17, s17, 16
Ta 182,241,19840
Pk 43,22,23
Cvaa 8,160,224,128
s_lshl_b32 s17, s17, 9
Ex 24
Mvaa 8,164,228
s_lshl_b32 s56, s56, 19
Ta 184,241,16800
buffer_load_dwordx4 v74, s[72:75], s79 offen offset:2064 lds
Mvaa 8,168,232
s_add_u32 s17, s17, s56
Ex 25
Mvaa 8,172,236
s_and_b32 s56, s24, 3
Ta 186,241,17824
Pk 50,24,25
Cvaa 12,172,252,132
s_cmp_le_u32 s56, 1
s_cselect_b32 s56, 2, 1
Ex 26
Mvaa 12,168,248
s_andn2_b32 s82, s49, 0xc0000
Ta 188,241,18848
buffer_load_dwordx4 v75, s[72:75], s79 offen offset:3096 lds
Mvaa 12,164,244
s_lshl_b32 s18, s56, 18
Ex 27
Mvaa 12,160,240
Wl 3
Ta 190,241,19872
Pk 51,26,27
Maav 40,176,80
s_mov_b32 m0, 28896
s_add_u32 s79, s49, 131072
Ex 28
Maav 40,180,84
s_add_u32 s82, s82, s18
Ra 160,240,36096
buffer_load_dwordx4 v72, s[72:75], s79 offen offset:0 lds
Maav 44,176,88
s_lshr_b32 s18, s64, s56
Ex 29
Maav 44,180,92
s_and_b32 s18, s18, 16
Ra 164,240,36352
Pk 58,28,29
Maav 48,176,96
s_lshl_b32 s18, s18, 9
Ex 30
Maav 48,180,100
s_lshl_b32 s56, s56, 19
Ra 168,240,36608
buffer_load_dwordx4 v73, s[72:75], s79 offen offset:1032 lds
Maav 52,176,104
s_add_u32 s18, s18, s56
Ex 31
Maav 52,180,108
Wl 3
Ra 172,240,36864
Pk 59,30,31
Maav 60,184,80
s_and_b32 s56, s24, 3
Ex 32
Maav 60,188,84
s_cmp_le_u32 s56, 2
s_cselect_b32 s56, 3, 2
buffer_load_dwordx4 v74, s[72:75], s79 offen offset:2064 lds
Maav 64,184,88
s_andn2_b32 s83, s49, 0xc0000
Ex 33
Maav 64,188,92
s_lshl_b32 s22, s56, 18
Pk 66,32,33
Maav 68,184,96
s_add_u32 s83, s83, s22
Ex 34
Maav 68,188,100
s_lshr_b32 s22, s64, s56
buffer_load_dwordx4 v75, s[72:75], s79 offen offset:3096 lds
Maav 72,184,104
s_and_b32 s22, s22, 16
Ex 35
Maav 72,188,108
Wl 0
Pk 67,34,35
s_mov_b64 exec, 0
s_swappc_b64 s[12:13], s[14:15]
s_add_u32 s14, s14, 4096
s_addc_u32 s15, s15, 0
Cvaa 20,160,192,120
s_add_u32 s46, s46, 1024
s_mov_b32 m0, 82560
s_add_u32 s79, s49, 147456
Ex 0
Mvaa 20,164,196
s_lshl_b32 s22, s22, 9
Ta 176,241,17024
buffer_load_dwordx4 v72, s[72:75], s79 offen offset:0 lds
Mvaa 20,168,200
s_lshl_b32 s56, s56, 19
Ex 1
Mvaa 20,172,204
s_add_u32 s22, s22, s56
Ta 178,241,18048
Pk 44,0,1
Cvaa 24,172,220,124
s_and_b64 s[64:65], s[64:65], s[34:35]
Ex 2
Mvaa 24,168,216
s_and_b32 s27, s64, 0xffff0000
s_cselect_b32 s27, 8192, 0
Ta 180,241,19072
buffer_load_dwordx4 v73, s[72:75], s79 offen offset:1032 lds
Mvaa 24,164,212
s_xor_b32 s56, s24, 4
Ex 3
Mvaa 24,160,208
s_lshl_b32 s56, s56, 19
Ta 182,241,20096
Pk 45,2,3
Cvaa 28,160,224,128
s_add_u32 s27, s27, s56
Ex 4
Mvaa 28,164,228
s_and_b32 s52, s65, 0xffff
s_cselect_b32 s52, 8192, 0
Ta 184,241,17056
buffer_load_dwordx4 v74, s[72:75], s79 offen offset:2064 lds
Mvaa 28,168,232
s_xor_b32 s56, s24, 8
Ex 5
Mvaa 28,172,236
)A"
R"A(s_lshl_b32 s56, s56, 19
Ta 186,241,18080
Pk 52,4,5
Cvaa 32,172,252,132
s_add_u32 s52, s52, s56
Ex 6
Mvaa 32,168,248
s_and_b32 s55, s65, 0xffff0000
s_cselect_b32 s55, 8192, 0
Ta 188,241,19104
buffer_load_dwordx4 v75, s[72:75], s79 offen offset:3096 lds
Mvaa 32,164,244
s_xor_b32 s56, s24, 12
Ex 7
Mvaa 32,160,240
Wl 3
Ta 190,241,20128
Pk 53,6,7
Maav 80,176,80
s_lshl_b32 s56, s56, 19
Ex 8
Maav 80,180,84
s_add_u32 s55, s55, s56
Ra 160,240,49536
Maav 84,176,88
s_mov_b32 m0, 86688
s_add_u32 s79, s49, 163840
Ex 9
Maav 84,180,92
Ra 164,240,49792
buffer_load_dwordx4 v72, s[72:75], s79 offen offset:0 lds
Maav 88,176,96
Ex 10
Maav 88,180,100
Ra 168,240,50048
Pk 60,8,9
Maav 92,176,104
Ex 11
Maav 92,180,108
Wl 3
Ra 172,240,50304
buffer_load_dwordx4 v73, s[72:75], s79 offen offset:1032 lds
Maav 100,184,80
Ex 12
Maav 100,188,84
Pk 61,10,11
Maav 104,184,88
Ex 13
Maav 104,188,92
buffer_load_dwordx4 v74, s[72:75], s79 offen offset:2064 lds
Pk 68,12,13
Maav 108,184,96
Ex 14
Maav 108,188,100
Maav 112,184,104
Ex 15
Maav 112,188,108
buffer_load_dwordx4 v75, s[72:75], s79 offen offset:3096 lds
Pk 69,14,15
Maav 16,156,40
Maav 36,156,48
Maav 56,156,56
Maav 76,156,64
Wl 0
Cvaa 0,160,192,120
s_add_u32 s44, s44, 3072
s_mov_b32 m0, 90816
s_add_u32 s79, s49, 180224
Ex 20
Mvaa 0,164,196
Ta 176,241,17280
buffer_load_dwordx4 v72, s[72:75], s79 offen offset:0 lds
Mvaa 0,168,200
Ex 21
Mvaa 0,172,204
Ta 178,241,18304
Pk 46,20,21
Cvaa 4,172,220,124
Ex 22
Mvaa 4,168,216
Ta 180,241,19328
buffer_load_dwordx4 v73, s[72:75], s79 offen offset:1032 lds
Mvaa 4,164,212
Ex 23
Mvaa 4,160,208
Ta 182,241,20352
Pk 47,22,23
Cvaa 8,160,224,128
Ex 24
Mvaa 8,164,228
Ta 184,241,17312
buffer_load_dwordx4 v74, s[72:75], s79 offen offset:2064 lds
Mvaa 8,168,232
Ex 25
Mvaa 8,172,236
Ta 186,241,18336
Pk 54,24,25
Cvaa 12,172,252,132
Ex 26
Mvaa 12,168,248
Ta 188,241,19360
buffer_load_dwordx4 v75, s[72:75], s79 offen offset:3096 lds
Mvaa 12,164,244
Ex 27
Mvaa 12,160,240
Wl 3
Ta 190,241,20384
Pk 55,26,27
Maav 120,176,80
s_mov_b32 m0, 94944
s_add_u32 s79, s49, 196608
Ex 28
Maav 120,180,84
Ra 160,240,50560
buffer_load_dwordx4 v72, s[72:75], s79 offen offset:0 lds
Maav 124,176,88
Ex 29
Maav 124,180,92
Ra 164,240,50816
Pk 62,28,29
Maav 128,176,96
Ex 30
Maav 128,180,100
Ra 168,240,51072
buffer_load_dwordx4 v73, s[72:75], s79 offen offset:1032 lds
Maav 132,176,104
Ex 31
Maav 132,180,108
Wl 3
Ra 172,240,51328
Pk 63,30,31
Maav 140,184,80
Ex 32
Maav 140,188,84
buffer_load_dwordx4 v74, s[72:75], s79 offen offset:2064 lds
Maav 144,184,88
Ex 33
Maav 144,188,92
Pk 70,32,33
Maav 148,184,96
Ex 34
Maav 148,188,100
buffer_load_dwordx4 v75, s[72:75], s79 offen offset:3096 lds
Maav 152,184,104
Ex 35
Maav 152,188,108
Wl 0
Pk 71,34,35
Cvaa 20,160,192,120
s_waitcnt vmcnt(62)
s_barrier
s_mov_b32 m0, 33024
s_add_u32 s79, s16, 4294918144
Ex 0
Mvaa 20,164,196
Ta 176,241,33024
buffer_load_dwordx4 v72, s[72:75], s79 offen offset:0 lds
Mvaa 20,168,200
Ex 1
Mvaa 20,172,204
Ta 178,241,34048
buffer_load_dwordx4 v73, s[72:75], s79 offen offset:1032 lds
Cvaa 24,172,220,124
Ex 2
Mvaa 24,168,216
Ta 180,241,35072
buffer_load_dwordx4 v74, s[72:75], s79 offen offset:2064 lds
Mvaa 24,164,212
Ex 3
Mvaa 24,160,208
Ta 182,241,36096
buffer_load_dwordx4 v75, s[72:75], s79 offen offset:3096 lds
Cvaa 28,160,224,128
s_mov_b32 m0, 37152
s_add_u32 s79, s16, 4294934528
Ex 4
Mvaa 28,164,228
Ta 184,241,33056
buffer_load_dwordx4 v72, s[72:75], s79 offen offset:0 lds
Mvaa 28,168,232
Ex 5
Mvaa 28,172,236
Ta 186,241,34080
buffer_load_dwordx4 v73, s[72:75], s79 offen offset:1032 lds
Cvaa 32,172,252,132
Ex 6
Mvaa 32,168,248
Ta 188,241,35104
buffer_load_dwordx4 v74, s[72:75], s79 offen offset:2064 lds
Mvaa 32,164,244
Ex 7
Mvaa 32,160,240
Wl 3
Ta 190,241,36128
buffer_load_dwordx4 v75, s[72:75], s79 offen offset:3096 lds
Maav 0,176,40
Ex 8
Maav 0,180,44
Ra 160,240,51584
Pk 80,0,1
Maav 4,176,48
s_mov_b32 m0, 41280
s_add_u32 s79, s16, 4294950912
Ex 9
Maav 4,180,52
Ra 164,240,51840
buffer_load_dwordx4 v72, s[72:75], s79 offen offset:0 lds
Maav 8,176,56
Ex 10
Maav 8,180,60
Ra 168,240,52096
buffer_load_dwordx4 v73, s[72:75], s79 offen offset:1032 lds
Maav 12,176,64
Ex 11
Maav 12,180,68
Wl 3
Ra 172,240,52352
buffer_load_dwordx4 v74, s[72:75], s79 offen offset:2064 lds
Maav 20,184,40
Ex 12
Maav 20,188,44
buffer_load_dwordx4 v75, s[72:75], s79 offen offset:3096 lds
Pk 81,2,3
Maav 24,184,48
s_mov_b32 m0, 45408
s_add_u32 s79, s16, 0
Ex 13
Maav 24,188,52
buffer_load_dwordx4 v72, s[72:75], s79 offen offset:0 lds
Pk 88,4,5
Maav 28,184,56
Ex 14
Maav 28,188,60
buffer_load_dwordx4 v73, s[72:75], s79 offen offset:1032 lds
Pk 89,6,7
Maav 32,184,64
Ex 15
Maav 32,188,68
buffer_load_dwordx4 v74, s[72:75], s79 offen offset:2064 lds
Pk 96,8,9
Maav 16,156,44
Maav 36,156,52
buffer_load_dwordx4 v75, s[72:75], s79 offen offset:3096 lds
Pk 97,10,11
Maav 56,156,60
Maav 76,156,68
Wl 0
Pk 104,12,13
Pk 105,14,15
Cvaa 0,160,192,120
s_mov_b32 m0, 99072
s_add_u32 s79, s16, 16384
Ex 20
Mvaa 0,164,196
Ta 176,241,33280
buffer_load_dwordx4 v72, s[72:75], s79 offen offset:0 lds
Mvaa 0,168,200
Ex 21
Mvaa 0,172,204
Ta 178,241,34304
buffer_load_dwordx4 v73, s[72:75], s79 offen offset:1032 lds
Cvaa 4,172,220,124
Ex 22
Mvaa 4,168,216
Ta 180,241,35328
buffer_load_dwordx4 v74, s[72:75], s79 offen offset:2064 lds
Mvaa 4,164,212
Ex 23
Mvaa 4,160,208
Ta 182,241,36352
buffer_load_dwordx4 v75, s[72:75], s79 offen offset:3096 lds
Pk 82,20,21
Cvaa 8,160,224,128
s_mov_b32 m0, 103200
s_add_u32 s79, s16, 32768
Ex 24
Mvaa 8,164,228
Ta 184,241,33312
buffer_load_dwordx4 v72, s[72:75], s79 offen offset:0 lds
Mvaa 8,168,232
Ex 25
Mvaa 8,172,236
Ta 186,241,34336
buffer_load_dwordx4 v73, s[72:75], s79 offen offset:1032 lds
Cvaa 12,172,252,132
Ex 26
Mvaa 12,168,248
Ta 188,241,35360
buffer_load_dwordx4 v74, s[72:75], s79 offen offset:2064 lds
Mvaa 12,164,244
Ex 27
Mvaa 12,160,240
Wl 3
Ta 190,241,36384
buffer_load_dwordx4 v75, s[72:75], s79 offen offset:3096 lds
Pk 83,22,23
Maav 40,176,40
s_mov_b32 m0, 107328
s_add_u32 s79, s16, 49152
Ex 28
Maav 40,180,44
Ra 160,240,52608
buffer_load_dwordx4 v72, s[72:75], s79 offen offset:0 lds
Maav 44,176,48
Ex 29
Maav 44,180,52
Ra 164,240,52864
buffer_load_dwordx4 v73, s[72:75], s79 offen offset:1032 lds
Maav 48,176,56
Ex 30
Maav 48,180,60
Ra 168,240,53120
buffer_load_dwordx4 v74, s[72:75], s79 offen offset:2064 lds
Maav 52,176,64
Ex 31
Maav 52,180,68
Wl 3
Ra 172,240,53376
buffer_load_dwordx4 v75, s[72:75], s79 offen offset:3096 lds
Pk 90,24,25
Maav 60,184,40
s_mov_b32 m0, 111456
s_add_u32 s79, s16, 65536
Ex 32
Maav 60,188,44
buffer_load_dwordx4 v72, s[72:75], s79 offen offset:0 lds
Pk 91,26,27
Maav 64,184,48
Ex 33
Maav 64,188,52
buffer_load_dwordx4 v73, s[72:75], s79 offen offset:1032 lds
Pk 98,28,29
Maav 68,184,56
Ex 34
Maav 68,188,60
buffer_load_dwordx4 v74, s[72:75], s79 offen offset:2064 lds
Pk 99,30,31
Maav 72,184,64
Ex 35
Maav 72,188,68
Wl 0
buffer_load_dwordx4 v75, s[72:75], s79 offen offset:3096 lds
Pk 106,32,33
Pk 107,34,35
Cvaa 20,160,192,120
s_add_u32 s46, s46, 3072
Ex 0
Mvaa 20,164,196
Ta 176,241,33536
Mvaa 20,168,200
Ex 1
Mvaa 20,172,204
Ta 178,241,34560
Pk 84,0,1
Cvaa 24,172,220,124
Ex 2
Mvaa 24,168,216
Ta 180,241,35584
Mvaa 24,164,212
Ex 3
Mvaa 24,160,208
Ta 182,241,36608
Pk 85,2,3
Cvaa 28,160,224,128
Ex 4
Mvaa 28,164,228
Ta 184,241,33568
Mvaa 28,168,232
Ex 5
Mvaa 28,172,236
Ta 186,241,34592
Pk 92,4,5
Cvaa 32,172,252,132
Ex 6
Mvaa 32,168,248
Ta 188,241,35616
Mvaa 32,164,244
Ex 7
Mvaa 32,160,240
Wl 3
Ta 190,241,36640
Pk 93,6,7
Maav 80,176,40
Ex 8
Maav 80,180,44
Ra 160,240,0
Maav 84,176,48
Ex 9
Maav 84,180,52
Ra 164,240,256
Pk 100,8,9
Maav 88,176,56
Ex 10
Maav 88,180,60
Ra 168,240,512
Maav 92,176,64
Ex 11
Maav 92,180,68
Wl 3
Ra 172,240,768
)A"
R"A(Pk 101,10,11
Maav 100,184,40
Ex 12
Maav 100,188,44
Maav 104,184,48
Ex 13
Maav 104,188,52
Pk 108,12,13
Maav 108,184,56
Ex 14
Maav 108,188,60
Maav 112,184,64
Ex 15
Maav 112,188,68
Pk 109,14,15
Maav 16,156,80
Maav 36,156,88
Maav 56,156,96
Maav 76,156,104
Wl 0
FINB_%=:
Ta 176,241,33792
Ta 178,241,34816
Ta 180,241,35840
Ta 182,241,36864
Ta 184,241,33824
Ta 186,241,34848
Ta 188,241,35872
Ta 190,241,36896
Ex 20
Ex 21
Ex 22
Pk 86,20,21
Ex 23
Ex 24
Pk 87,22,23
Ex 25
Ex 26
Wl 4
Maav 120,176,40
Pk 94,24,25
Maav 120,180,44
Ex 27
Maav 124,176,48
Ex 28
Maav 124,180,52
Pk 95,26,27
Maav 128,176,56
Ex 29
Maav 128,180,60
Ex 30
Maav 132,176,64
Pk 102,28,29
Maav 132,180,68
Wl 0
Ex 31
Maav 140,184,40
s_waitcnt vmcnt(0)
s_barrier
Ta 160,241,49536
Ex 32
Maav 140,188,44
Ta 162,241,50560
Pk 103,30,31
Maav 144,184,48
Ta 164,241,51584
Ex 33
Maav 144,188,52
Ta 166,241,52608
Ex 34
Maav 148,184,56
Ta 168,241,49568
Pk 110,32,33
Maav 148,188,60
Ta 170,241,50592
Ex 35
Maav 152,184,64
Ta 172,241,51616
s_nop 0
Maav 152,188,68
Ta 174,241,52640
Pk 111,34,35
s_nop 1
Maav 16,156,84
Maav 36,156,92
Maav 56,156,100
Maav 76,156,108
s_nop 7
Wl 4
Maav 0,160,80
v_lshlrev_b32 v124, 4, v245
Ar 140,16
Maav 0,164,84
Ta 176,241,49792
Ar 142,36
Maav 4,160,88
Ta 178,241,50816
Ar 144,56
Maav 4,164,92
Ta 180,241,51840
Ar 146,76
Maav 8,160,96
Ta 182,241,52864
v_add_f32 v141, v140, v142
Maav 8,164,100
Ta 184,241,49824
Ad 141,144
Maav 12,160,104
Ta 186,241,50848
Ad 141,146
Maav 12,164,108
Wl 6
Ta 188,241,51872
v_cmp_nge_f32 vcc, 0x5d800000, v141
Maav 20,168,80
Ta 190,241,52896
Maav 20,172,84
v_rcp_f32 v158, v140
Maav 24,168,88
v_rcp_f32 v159, v142
Maav 24,172,92
v_rcp_f32 v242, v144
Maav 28,168,96
v_rcp_f32 v243, v146
Maav 28,172,100
s_or_b32 s54, vcc_lo, vcc_hi
Maav 32,168,104
Maav 32,172,108
s_nop 0
s_cmp_lg_u32 s54, 0
s_cbranch_scc1 PREB_%=
Wl 4
Maav 40,176,80
v_bfe_u32 v120, v245, 2, 2
v_and_b32 v121, 3, v245
v_lshrrev_b32 v122, 4, v245
v_mul_u32_u24 v240, 1032, v122
v_lshl_add_u32 v240, v120, 6, v240
Maav 40,180,84
Ta 160,241,50048
v_lshl_add_u32 v240, v121, 4, v240
v_add_u32 v255, 66048, v240
v_mov_b32 v248, v72
v_mov_b32 v249, v73
v_mov_b32 v250, v74
v_mov_b32 v251, v75
Maav 44,176,88
Ta 162,241,51072
Ar 136,0
Ar 137,1
Ar 138,2
Ar 139,3
Ar 140,20
Ar 141,21
Maav 44,180,92
Ta 164,241,52096
Ar 142,22
Ar 143,23
v_mul_f32 v136, v158, v136
v_mul_f32 v137, v158, v137
v_mul_f32 v138, v158, v138
v_mul_f32 v139, v158, v139
Maav 48,176,96
Ta 166,241,53120
v_mul_f32 v140, v158, v140
v_mul_f32 v141, v158, v141
v_mul_f32 v142, v158, v142
v_mul_f32 v143, v158, v143
Pk 0,136,137
Pk 1,138,139
Maav 48,180,100
Ta 168,241,50080
Pk 2,140,141
Pk 3,142,143
Ar 144,4
Ar 145,5
Ar 146,6
Maav 52,176,104
Ta 170,241,51104
Ar 147,7
Ar 148,24
Ar 149,25
Ar 150,26
Ar 151,27
v_mul_f32 v144, v159, v144
Maav 52,180,108
Wl 6
Ta 172,241,52128
v_mul_f32 v145, v159, v145
v_mul_f32 v146, v159, v146
v_mul_f32 v147, v159, v147
v_mul_f32 v148, v159, v148
v_mul_f32 v149, v159, v149
v_mul_f32 v150, v159, v150
Maav 60,184,80
Ta 174,241,53152
v_mul_f32 v151, v159, v151
Pk 16,144,145
Pk 17,146,147
Pk 18,148,149
Pk 19,150,151
Maav 60,188,84
s_mov_b32 s57, 4096
buffer_store_dwordx4 v[0:3], v124, s[96:99], s57 offen offset:0 sc1
Ar 136,8
Ar 137,9
Ar 138,10
Ar 139,11
Ar 140,28
Ar 141,29
Maav 64,184,88
Ar 142,30
Ar 143,31
v_mul_f32 v136, v242, v136
v_mul_f32 v137, v242, v137
v_mul_f32 v138, v242, v138
Maav 64,188,92
v_mul_f32 v139, v242, v139
v_mul_f32 v140, v242, v140
v_mul_f32 v141, v242, v141
v_mul_f32 v142, v242, v142
v_mul_f32 v143, v242, v143
Pk 32,136,137
Maav 68,184,96
Pk 33,138,139
Pk 34,140,141
Pk 35,142,143
Ar 144,12
Ar 145,13
Ar 146,14
Maav 68,188,100
Ar 147,15
Ar 148,32
Ar 149,33
Ar 150,34
Ar 151,35
v_mul_f32 v144, v243, v144
Maav 72,184,104
v_mul_f32 v145, v243, v145
v_mul_f32 v146, v243, v146
v_mul_f32 v147, v243, v147
v_mul_f32 v148, v243, v148
v_mul_f32 v149, v243, v149
v_mul_f32 v150, v243, v150
Maav 72,188,108
Wl 4
v_mul_f32 v151, v243, v151
Pk 48,144,145
Pk 49,146,147
Pk 50,148,149
Pk 51,150,151
s_mov_b32 s57, 20480
buffer_store_dwordx4 v[16:19], v124, s[96:99], s57 offen offset:0 sc1
Maav 80,160,80
Maav 80,164,84
Ta 176,241,50304
Ar 136,40
Ar 137,41
Ar 138,42
Ar 139,43
Ar 140,60
Maav 84,160,88
Ta 178,241,51328
Ar 141,61
Ar 142,62
Ar 143,63
v_mul_f32 v136, v158, v136
v_mul_f32 v137, v158, v137
Maav 84,164,92
Ta 180,241,52352
v_mul_f32 v138, v158, v138
v_mul_f32 v139, v158, v139
v_mul_f32 v140, v158, v140
v_mul_f32 v141, v158, v141
v_mul_f32 v142, v158, v142
v_mul_f32 v143, v158, v143
Maav 88,160,96
Ta 182,241,53376
Pk 4,136,137
Pk 5,138,139
Pk 6,140,141
Pk 7,142,143
Ar 144,44
Maav 88,164,100
Ta 184,241,50336
Ar 145,45
Ar 146,46
Ar 147,47
Ar 148,64
Ar 149,65
Ar 150,66
Maav 92,160,104
Ta 186,241,51360
Ar 151,67
v_mul_f32 v144, v159, v144
v_mul_f32 v145, v159, v145
v_mul_f32 v146, v159, v146
v_mul_f32 v147, v159, v147
Maav 92,164,108
Wl 6
Ta 188,241,52384
v_mul_f32 v148, v159, v148
v_mul_f32 v149, v159, v149
v_mul_f32 v150, v159, v150
v_mul_f32 v151, v159, v151
Pk 20,144,145
Pk 21,146,147
Maav 100,168,80
Ta 190,241,53408
Pk 22,148,149
Pk 23,150,151
Ar 136,48
Ar 137,49
Maav 100,172,84
s_mov_b32 s57, 36864
buffer_store_dwordx4 v[32:35], v124, s[96:99], s57 offen offset:0 sc1
Ar 138,50
Ar 139,51
Ar 140,68
Ar 141,69
Ar 142,70
Ar 143,71
Maav 104,168,88
v_mul_f32 v136, v242, v136
v_mul_f32 v137, v242, v137
v_mul_f32 v138, v242, v138
v_mul_f32 v139, v242, v139
v_mul_f32 v140, v242, v140
Maav 104,172,92
v_mul_f32 v141, v242, v141
v_mul_f32 v142, v242, v142
v_mul_f32 v143, v242, v143
Pk 36,136,137
Pk 37,138,139
Pk 38,140,141
Maav 108,168,96
Pk 39,142,143
Ar 144,52
Ar 145,53
Ar 146,54
Ar 147,55
Maav 108,172,100
Ar 148,72
Ar 149,73
Ar 150,74
Ar 151,75
v_mul_f32 v144, v243, v144
v_mul_f32 v145, v243, v145
Maav 112,168,104
v_mul_f32 v146, v243, v146
v_mul_f32 v147, v243, v147
v_mul_f32 v148, v243, v148
v_mul_f32 v149, v243, v149
v_mul_f32 v150, v243, v150
Maav 112,172,108
Wl 4
v_mul_f32 v151, v243, v151
Pk 52,144,145
Pk 53,146,147
Pk 54,148,149
Pk 55,150,151
s_mov_b32 s57, 53248
buffer_store_dwordx4 v[48:51], v124, s[96:99], s57 offen offset:0 sc1
Maav 120,176,80
Ta 192,255,256
Maav 120,180,84
Ta 194,255,4384
Ar 136,80
Ar 137,81
Ar 138,82
Ar 139,83
Ar 140,100
Maav 124,176,88
Ta 196,255,8512
Ar 141,101
Ar 142,102
Ar 143,103
v_mul_f32 v136, v158, v136
v_mul_f32 v137, v158, v137
v_mul_f32 v138, v158, v138
Maav 124,180,92
Ta 198,255,12640
v_mul_f32 v139, v158, v139
v_mul_f32 v140, v158, v140
v_mul_f32 v141, v158, v141
v_mul_f32 v142, v158, v142
v_mul_f32 v143, v158, v143
Maav 128,176,96
Ta 200,240,256
Pk 8,136,137
Pk 9,138,139
Pk 10,140,141
Pk 11,142,143
Ar 144,84
Ar 145,85
Maav 128,180,100
Ta 202,240,4384
Ar 146,86
Ar 147,87
Ar 148,104
)A"
R"A(Ar 149,105
Ar 150,106
Maav 132,176,104
Ta 204,240,8512
Ar 151,107
v_mul_f32 v144, v159, v144
v_mul_f32 v145, v159, v145
v_mul_f32 v146, v159, v146
v_mul_f32 v147, v159, v147
v_mul_f32 v148, v159, v148
Maav 132,180,108
Wl 7
Ta 206,240,12640
v_mul_f32 v149, v159, v149
v_mul_f32 v150, v159, v150
v_mul_f32 v151, v159, v151
Pk 24,144,145
Pk 25,146,147
Maav 140,184,80
Ta 208,255,264
Pk 26,148,149
Pk 27,150,151
Ar 136,88
Ar 137,89
Ar 138,90
Maav 140,188,84
s_mov_b32 s57, 69632
Ta 210,255,4392
buffer_store_dwordx4 v[64:67], v124, s[96:99], s57 offen offset:0 sc1
Ar 139,91
Ar 140,108
Ar 141,109
Ar 142,110
Ar 143,111
Maav 144,184,88
Ta 212,255,8520
v_mul_f32 v136, v242, v136
v_mul_f32 v137, v242, v137
v_mul_f32 v138, v242, v138
v_mul_f32 v139, v242, v139
v_mul_f32 v140, v242, v140
v_mul_f32 v141, v242, v141
Maav 144,188,92
Ta 214,255,12648
v_mul_f32 v142, v242, v142
v_mul_f32 v143, v242, v143
Pk 40,136,137
Pk 41,138,139
Pk 42,140,141
Maav 148,184,96
Ta 216,240,264
Pk 43,142,143
Ar 144,92
Ar 145,93
Ar 146,94
Ar 147,95
Maav 148,188,100
s_mov_b32 s57, 4096
Ta 218,240,4392
buffer_store_dwordx4 v[4:7], v124, s[96:99], s57 offen offset:1024 sc1
Ar 148,112
Ar 149,113
Ar 150,114
Ar 151,115
v_mul_f32 v144, v243, v144
Maav 152,184,104
Ta 220,240,8520
v_mul_f32 v145, v243, v145
v_mul_f32 v146, v243, v146
v_mul_f32 v147, v243, v147
v_mul_f32 v148, v243, v148
v_mul_f32 v149, v243, v149
v_mul_f32 v150, v243, v150
Maav 152,188,108
Ta 222,240,12648
v_mul_f32 v151, v243, v151
Pk 56,144,145
Pk 57,146,147
Pk 58,148,149
Pk 59,150,151
s_mov_b32 s57, 20480
buffer_store_dwordx4 v[20:23], v124, s[96:99], s57 offen offset:1024 sc1
v_mov_b32 v241, v124
.p2align 3
Ar 136,120
Ar 137,121
Ar 138,122
Ar 139,123
Ar 140,140
Ar 141,141
Ar 142,142
Ar 143,143
v_pk_mul_f32 v[136:137], v[136:137], v[158:159] op_sel_hi:[1,0]
v_pk_mul_f32 v[138:139], v[138:139], v[158:159] op_sel_hi:[1,0]
v_pk_mul_f32 v[140:141], v[140:141], v[158:159] op_sel_hi:[1,0]
v_pk_mul_f32 v[142:143], v[142:143], v[158:159] op_sel_hi:[1,0]
Pk 12,136,137
Pk 13,138,139
Pk 14,140,141
Pk 15,142,143
Ar 144,124
Ar 145,125
Ar 146,126
Ar 147,127
Ar 148,144
Ar 149,145
Ar 150,146
Ar 151,147
v_pk_mul_f32 v[144:145], v[144:145], v[158:159] op_sel:[0,1] op_sel_hi:[1,1]
v_pk_mul_f32 v[146:147], v[146:147], v[158:159] op_sel:[0,1] op_sel_hi:[1,1]
v_pk_mul_f32 v[148:149], v[148:149], v[158:159] op_sel:[0,1] op_sel_hi:[1,1]
v_pk_mul_f32 v[150:151], v[150:151], v[158:159] op_sel:[0,1] op_sel_hi:[1,1]
Pk 28,144,145
Pk 29,146,147
Pk 30,148,149
Pk 31,150,151
Ar 136,128
Ar 137,129
Ar 138,130
Ar 139,131
Ar 140,148
Ar 141,149
Ar 142,150
Ar 143,151
v_pk_mul_f32 v[136:137], v[136:137], v[242:243] op_sel_hi:[1,0]
v_pk_mul_f32 v[138:139], v[138:139], v[242:243] op_sel_hi:[1,0]
v_pk_mul_f32 v[140:141], v[140:141], v[242:243] op_sel_hi:[1,0]
v_pk_mul_f32 v[142:143], v[142:143], v[242:243] op_sel_hi:[1,0]
Pk 44,136,137
Pk 45,138,139
Pk 46,140,141
Pk 47,142,143
Ar 144,132
Ar 145,133
Ar 146,134
Ar 147,135
Ar 148,152
Ar 149,153
Ar 150,154
Ar 151,155
s_mov_b32 s57, 36864
buffer_store_dwordx4 v[36:39], v124, s[96:99], s57 offen offset:1024 sc1
v_pk_mul_f32 v[144:145], v[144:145], v[242:243] op_sel:[0,1] op_sel_hi:[1,1]
v_pk_mul_f32 v[146:147], v[146:147], v[242:243] op_sel:[0,1] op_sel_hi:[1,1]
v_pk_mul_f32 v[148:149], v[148:149], v[242:243] op_sel:[0,1] op_sel_hi:[1,1]
v_pk_mul_f32 v[150:151], v[150:151], v[242:243] op_sel:[0,1] op_sel_hi:[1,1]
Pk 60,144,145
Pk 61,146,147
Pk 62,148,149
Pk 63,150,151
Wl 0
PPB_%=:
s_mov_b32 s19, PBO_%=-PC0_%=
s_bitcmp0_b32 s24, 3
s_cselect_b32 s19, PBE_%=-PC0_%=, s19
s_add_u32 s8, s8, s19
s_addc_u32 s9, s9, 0
s_setpc_b64 s[8:9]
SPINP0B_%=:
s_sleep 4
v_and_b32 v158, 3, v245
v_lshl_add_u32 v158, v158, 10, s59
v_writelane_b32 v158, s77, 4
v_writelane_b32 v158, s78, 5
v_writelane_b32 v158, s80, 6
global_load_dword v159, v158, s[92:93] sc1
v_lshlrev_b32 v244, 2, v245
global_load_dword v244, v244, s[50:51] sc1
s_waitcnt vmcnt(0)
v_xor_b32 v159, s66, v159
global_store_dword v158, v159, s[94:95] sc1
v_cmp_gt_i32 s[64:65], 0, v244
v_and_b32 v244, 0x7fffffff, v244
v_cmp_ne_u32 vcc, s81, v244
s_nop 0
s_and_b64 vcc, vcc, s[34:35]
s_cbranch_scc0 OKP0B_%=
s_add_u32 s58, s58, 1
s_cmp_lt_u32 s58, 0x80000
s_cbranch_scc1 SPINP0B_%=
s_branch OKP0B_%=
PREB_%=:
s_waitcnt vmcnt(0) lgkmcnt(0)
Ar 160,192
Ar 161,193
Ar 162,194
Ar 163,195
Ar 164,196
Ar 165,197
Ar 166,198
Ar 167,199
Ar 168,200
Ar 169,201
Ar 170,202
Ar 171,203
Ar 172,204
Ar 173,205
Ar 174,206
Ar 175,207
Ar 176,208
Ar 177,209
Ar 178,210
Ar 179,211
Ar 180,212
Ar 181,213
Ar 182,214
Ar 183,215
Ar 184,216
Ar 185,217
Ar 186,218
Ar 187,219
Ar 188,220
Ar 189,221
Ar 190,222
Ar 191,223
Ar 192,224
Ar 193,225
Ar 194,226
Ar 195,227
Ar 196,228
Ar 197,229
Ar 198,230
Ar 199,231
Ar 200,232
Ar 201,233
Ar 202,234
Ar 203,235
Ar 204,236
Ar 205,237
Ar 206,238
Ar 207,239
Ar 208,240
Ar 209,241
Ar 210,242
Ar 211,243
Ar 212,244
Ar 213,245
Ar 214,246
Ar 215,247
Ar 216,248
Ar 217,249
Ar 218,250
Ar 219,251
Ar 220,252
Ar 221,253
Ar 222,254
Ar 223,255
s_branch SLOWB_%=
RET1B_%=:
v_bfe_u32 v120, v245, 2, 2
v_and_b32 v121, 3, v245
v_lshrrev_b32 v122, 4, v245
v_mul_u32_u24 v240, 1032, v122
v_lshl_add_u32 v240, v120, 6, v240
v_lshl_add_u32 v240, v121, 4, v240
v_add_u32 v255, 66048, v240
v_mov_b32 v248, v72
v_mov_b32 v249, v73
v_mov_b32 v250, v74
v_mov_b32 v251, v75
v_mov_b32 v241, v124
s_mov_b32 s57, 0
buffer_load_dwordx4 v[160:163], v124, s[96:99], s57 offen offset:0 sc1
buffer_load_dwordx4 v[164:167], v124, s[96:99], s57 offen offset:1024 sc1
buffer_load_dwordx4 v[168:171], v124, s[96:99], s57 offen offset:2048 sc1
buffer_load_dwordx4 v[172:175], v124, s[96:99], s57 offen offset:3072 sc1
s_mov_b32 s57, 16384
buffer_load_dwordx4 v[176:179], v124, s[96:99], s57 offen offset:0 sc1
buffer_load_dwordx4 v[180:183], v124, s[96:99], s57 offen offset:1024 sc1
buffer_load_dwordx4 v[184:187], v124, s[96:99], s57 offen offset:2048 sc1
buffer_load_dwordx4 v[188:191], v124, s[96:99], s57 offen offset:3072 sc1
s_mov_b32 s57, 32768
buffer_load_dwordx4 v[192:195], v124, s[96:99], s57 offen offset:0 sc1
buffer_load_dwordx4 v[196:199], v124, s[96:99], s57 offen offset:1024 sc1
buffer_load_dwordx4 v[200:203], v124, s[96:99], s57 offen offset:2048 sc1
buffer_load_dwordx4 v[204:207], v124, s[96:99], s57 offen offset:3072 sc1
s_mov_b32 s57, 49152
buffer_load_dwordx4 v[208:211], v124, s[96:99], s57 offen offset:0 sc1
buffer_load_dwordx4 v[212:215], v124, s[96:99], s57 offen offset:1024 sc1
buffer_load_dwordx4 v[216:219], v124, s[96:99], s57 offen offset:2048 sc1
buffer_load_dwordx4 v[220:223], v124, s[96:99], s57 offen offset:3072 sc1
Ta 192,255,256
Ta 194,255,4384
Ta 196,255,8512
Ta 198,255,12640
Ta 200,240,256
Ta 202,240,4384
Ta 204,240,8512
Ta 206,240,12640
Ta 208,255,264
Ta 210,255,4392
Ta 212,255,8520
Ta 214,255,12648
Ta 216,240,264
Ta 218,240,4392
Ta 220,240,8520
Ta 222,240,12648
Ar 136,0
Ar 137,1
Ar 138,2
Ar 139,3
Ar 140,20
Ar 141,21
Ar 142,22
Ar 143,23
v_pk_mul_f32 v[136:137], v[136:137], v[158:159] op_sel_hi:[1,0]
v_pk_mul_f32 v[138:139], v[138:139], v[158:159] op_sel_hi:[1,0]
v_pk_mul_f32 v[140:141], v[140:141], v[158:159] op_sel_hi:[1,0]
v_pk_mul_f32 v[142:143], v[142:143], v[158:159] op_sel_hi:[1,0]
Pk 0,136,137
Pk 1,138,139
Pk 2,140,141
Pk 3,142,143
Ar 144,4
Ar 145,5
Ar 146,6
Ar 147,7
Ar 148,24
Ar 149,25
Ar 150,26
Ar 151,27
v_pk_mul_f32 v[144:145], v[144:145], v[158:159] op_sel:[0,1] op_sel_hi:[1,1]
v_pk_mul_f32 v[146:147], v[146:147], v[158:159] op_sel:[0,1] op_sel_hi:[1,1]
v_pk_mul_f32 v[148:149], v[148:149], v[158:159] op_sel:[0,1] op_sel_hi:[1,1]
v_pk_mul_f32 v[150:151], v[150:151], v[158:159] op_sel:[0,1] op_sel_hi:[1,1]
Pk 16,144,145
Pk 17,146,147
Pk 18,148,149
Pk 19,150,151
Ar 136,8
Ar 137,9
Ar 138,10
Ar 139,11
Ar 140,28
Ar 141,29
Ar 142,30
Ar 143,31
v_pk_mul_f32 v[136:137], v[136:137], v[242:243] op_sel_hi:[1,0]
v_pk_mul_f32 v[138:139], v[138:139], v[242:243] op_sel_hi:[1,0]
v_pk_mul_f32 v[140:141], v[140:141], v[242:243] op_sel_hi:[1,0]
v_pk_mul_f32 v[142:143], v[142:143], v[242:243] op_sel_hi:[1,0]
Pk 32,136,137
Pk 33,138,139
Pk 34,140,141
Pk 35,142,143
Ar 144,12
Ar 145,13
Ar 146,14
Ar 147,15
Ar 148,32
Ar 149,33
Ar 150,34
Ar 151,35
v_pk_mul_f32 v[144:145], v[144:145], v[242:243] op_sel:[0,1] op_sel_hi:[1,1]
v_pk_mul_f32 v[146:147], v[146:147], v[242:243] op_sel:[0,1] op_sel_hi:[1,1]
v_pk_mul_f32 v[148:149], v[148:149], v[242:243] op_sel:[0,1] op_sel_hi:[1,1]
v_pk_mul_f32 v[150:151], v[150:151], v[242:243] op_sel:[0,1] op_sel_hi:[1,1]
Pk 48,144,145
Pk 49,146,147
Pk 50,148,149
Pk 51,150,151
Ar 136,40
Ar 137,41
Ar 138,42
Ar 139,43
Ar 140,60
Ar 141,61
Ar 142,62
Ar 143,63
v_pk_mul_f32 v[136:137], v[136:137], v[158:159] op_sel_hi:[1,0]
v_pk_mul_f32 v[138:139], v[138:139], v[158:159] op_sel_hi:[1,0]
v_pk_mul_f32 v[140:141], v[140:141], v[158:159] op_sel_hi:[1,0]
v_pk_mul_f32 v[142:143], v[142:143], v[158:159] op_sel_hi:[1,0]
Pk 4,136,137
Pk 5,138,139
Pk 6,140,141
Pk 7,142,143
Ar 144,44
Ar 145,45
Ar 146,46
Ar 147,47
Ar 148,64
Ar 149,65
Ar 150,66
Ar 151,67
v_pk_mul_f32 v[144:145], v[144:145], v[158:159] op_sel:[0,1] op_sel_hi:[1,1]
v_pk_mul_f32 v[146:147], v[146:147], v[158:159] op_sel:[0,1] op_sel_hi:[1,1]
v_pk_mul_f32 v[148:149], v[148:149], v[158:159] op_sel:[0,1] op_sel_hi:[1,1]
v_pk_mul_f32 v[150:151], v[150:151], v[158:159] op_sel:[0,1] op_sel_hi:[1,1]
Pk 20,144,145
Pk 21,146,147
Pk 22,148,149
Pk 23,150,151
Ar 136,48
Ar 137,49
Ar 138,50
Ar 139,51
Ar 140,68
Ar 141,69
Ar 142,70
)A"
R"A(Ar 143,71
v_pk_mul_f32 v[136:137], v[136:137], v[242:243] op_sel_hi:[1,0]
v_pk_mul_f32 v[138:139], v[138:139], v[242:243] op_sel_hi:[1,0]
v_pk_mul_f32 v[140:141], v[140:141], v[242:243] op_sel_hi:[1,0]
v_pk_mul_f32 v[142:143], v[142:143], v[242:243] op_sel_hi:[1,0]
Pk 36,136,137
Pk 37,138,139
Pk 38,140,141
Pk 39,142,143
Ar 144,52
Ar 145,53
Ar 146,54
Ar 147,55
Ar 148,72
Ar 149,73
Ar 150,74
Ar 151,75
v_pk_mul_f32 v[144:145], v[144:145], v[242:243] op_sel:[0,1] op_sel_hi:[1,1]
v_pk_mul_f32 v[146:147], v[146:147], v[242:243] op_sel:[0,1] op_sel_hi:[1,1]
v_pk_mul_f32 v[148:149], v[148:149], v[242:243] op_sel:[0,1] op_sel_hi:[1,1]
v_pk_mul_f32 v[150:151], v[150:151], v[242:243] op_sel:[0,1] op_sel_hi:[1,1]
Pk 52,144,145
Pk 53,146,147
Pk 54,148,149
Pk 55,150,151
Ar 136,80
Ar 137,81
Ar 138,82
Ar 139,83
Ar 140,100
Ar 141,101
Ar 142,102
Ar 143,103
v_pk_mul_f32 v[136:137], v[136:137], v[158:159] op_sel_hi:[1,0]
v_pk_mul_f32 v[138:139], v[138:139], v[158:159] op_sel_hi:[1,0]
v_pk_mul_f32 v[140:141], v[140:141], v[158:159] op_sel_hi:[1,0]
v_pk_mul_f32 v[142:143], v[142:143], v[158:159] op_sel_hi:[1,0]
Pk 8,136,137
Pk 9,138,139
Pk 10,140,141
Pk 11,142,143
Ar 144,84
Ar 145,85
Ar 146,86
Ar 147,87
Ar 148,104
Ar 149,105
Ar 150,106
Ar 151,107
v_pk_mul_f32 v[144:145], v[144:145], v[158:159] op_sel:[0,1] op_sel_hi:[1,1]
v_pk_mul_f32 v[146:147], v[146:147], v[158:159] op_sel:[0,1] op_sel_hi:[1,1]
v_pk_mul_f32 v[148:149], v[148:149], v[158:159] op_sel:[0,1] op_sel_hi:[1,1]
v_pk_mul_f32 v[150:151], v[150:151], v[158:159] op_sel:[0,1] op_sel_hi:[1,1]
Pk 24,144,145
Pk 25,146,147
Pk 26,148,149
Pk 27,150,151
Ar 136,88
Ar 137,89
Ar 138,90
Ar 139,91
Ar 140,108
Ar 141,109
Ar 142,110
Ar 143,111
v_pk_mul_f32 v[136:137], v[136:137], v[242:243] op_sel_hi:[1,0]
v_pk_mul_f32 v[138:139], v[138:139], v[242:243] op_sel_hi:[1,0]
v_pk_mul_f32 v[140:141], v[140:141], v[242:243] op_sel_hi:[1,0]
v_pk_mul_f32 v[142:143], v[142:143], v[242:243] op_sel_hi:[1,0]
Pk 40,136,137
Pk 41,138,139
Pk 42,140,141
Pk 43,142,143
Ar 144,92
Ar 145,93
Ar 146,94
Ar 147,95
Ar 148,112
Ar 149,113
Ar 150,114
Ar 151,115
v_pk_mul_f32 v[144:145], v[144:145], v[242:243] op_sel:[0,1] op_sel_hi:[1,1]
v_pk_mul_f32 v[146:147], v[146:147], v[242:243] op_sel:[0,1] op_sel_hi:[1,1]
v_pk_mul_f32 v[148:149], v[148:149], v[242:243] op_sel:[0,1] op_sel_hi:[1,1]
v_pk_mul_f32 v[150:151], v[150:151], v[242:243] op_sel:[0,1] op_sel_hi:[1,1]
Pk 56,144,145
Pk 57,146,147
Pk 58,148,149
Pk 59,150,151
Ar 136,120
Ar 137,121
Ar 138,122
Ar 139,123
Ar 140,140
Ar 141,141
Ar 142,142
Ar 143,143
v_pk_mul_f32 v[136:137], v[136:137], v[158:159] op_sel_hi:[1,0]
v_pk_mul_f32 v[138:139], v[138:139], v[158:159] op_sel_hi:[1,0]
v_pk_mul_f32 v[140:141], v[140:141], v[158:159] op_sel_hi:[1,0]
v_pk_mul_f32 v[142:143], v[142:143], v[158:159] op_sel_hi:[1,0]
Pk 12,136,137
Pk 13,138,139
Pk 14,140,141
Pk 15,142,143
Ar 144,124
Ar 145,125
Ar 146,126
Ar 147,127
Ar 148,144
Ar 149,145
Ar 150,146
Ar 151,147
v_pk_mul_f32 v[144:145], v[144:145], v[158:159] op_sel:[0,1] op_sel_hi:[1,1]
v_pk_mul_f32 v[146:147], v[146:147], v[158:159] op_sel:[0,1] op_sel_hi:[1,1]
v_pk_mul_f32 v[148:149], v[148:149], v[158:159] op_sel:[0,1] op_sel_hi:[1,1]
v_pk_mul_f32 v[150:151], v[150:151], v[158:159] op_sel:[0,1] op_sel_hi:[1,1]
Pk 28,144,145
Pk 29,146,147
Pk 30,148,149
Pk 31,150,151
Ar 136,128
Ar 137,129
Ar 138,130
Ar 139,131
Ar 140,148
Ar 141,149
Ar 142,150
Ar 143,151
v_pk_mul_f32 v[136:137], v[136:137], v[242:243] op_sel_hi:[1,0]
v_pk_mul_f32 v[138:139], v[138:139], v[242:243] op_sel_hi:[1,0]
v_pk_mul_f32 v[140:141], v[140:141], v[242:243] op_sel_hi:[1,0]
v_pk_mul_f32 v[142:143], v[142:143], v[242:243] op_sel_hi:[1,0]
Pk 44,136,137
Pk 45,138,139
Pk 46,140,141
Pk 47,142,143
Ar 144,132
Ar 145,133
Ar 146,134
Ar 147,135
Ar 148,152
Ar 149,153
Ar 150,154
Ar 151,155
v_pk_mul_f32 v[144:145], v[144:145], v[242:243] op_sel:[0,1] op_sel_hi:[1,1]
v_pk_mul_f32 v[146:147], v[146:147], v[242:243] op_sel:[0,1] op_sel_hi:[1,1]
v_pk_mul_f32 v[148:149], v[148:149], v[242:243] op_sel:[0,1] op_sel_hi:[1,1]
v_pk_mul_f32 v[150:151], v[150:151], v[242:243] op_sel:[0,1] op_sel_hi:[1,1]
Pk 60,144,145
Pk 61,146,147
Pk 62,148,149
Pk 63,150,151
s_mov_b32 s57, 4096
buffer_store_dwordx4 v[0:3], v124, s[96:99], s57 offen offset:0 sc1
s_mov_b32 s57, 20480
buffer_store_dwordx4 v[16:19], v124, s[96:99], s57 offen offset:0 sc1
s_mov_b32 s57, 36864
buffer_store_dwordx4 v[32:35], v124, s[96:99], s57 offen offset:0 sc1
s_mov_b32 s57, 53248
buffer_store_dwordx4 v[48:51], v124, s[96:99], s57 offen offset:0 sc1
s_mov_b32 s57, 69632
buffer_store_dwordx4 v[64:67], v124, s[96:99], s57 offen offset:0 sc1
s_mov_b32 s57, 4096
buffer_store_dwordx4 v[4:7], v124, s[96:99], s57 offen offset:1024 sc1
s_mov_b32 s57, 20480
buffer_store_dwordx4 v[20:23], v124, s[96:99], s57 offen offset:1024 sc1
s_mov_b32 s57, 36864
buffer_store_dwordx4 v[36:39], v124, s[96:99], s57 offen offset:1024 sc1
s_waitcnt vmcnt(0) lgkmcnt(0)
s_branch PPB_%=
SLOWB_%=:
s_nop 7
v_and_b32 v105, 15, v245
v_lshrrev_b32 v107, 4, v245
v_lshlrev_b32 v106, 1, v105
v_lshl_add_u32 v106, v107, 10, v106
v_lshlrev_b32 v105, 8, v105
v_lshl_add_u32 v105, v107, 4, v105
v_add_u32 v107, 4096, v106
v_mov_b32 v100, 0xff800000
v_mov_b32 v101, 0xff800000
v_mov_b32 v102, 0xff800000
v_mov_b32 v103, 0xff800000
s_lshl_b32 s56, s48, 18
s_add_u32 s56, s56, 1024
s_mov_b32 s57, 0
SL1B_%=:
buffer_load_dwordx4 a[176:179], v105, s[36:39], s56 offen offset:0
buffer_load_dwordx4 a[180:183], v105, s[36:39], s56 offen offset:64
buffer_load_dwordx4 a[184:187], v105, s[36:39], s56 offen offset:128
buffer_load_dwordx4 a[188:191], v105, s[36:39], s56 offen offset:192
s_waitcnt vmcnt(0)
Zvav 20,176,160
Mvav 20,180,164
Mvav 20,184,168
Mvav 20,188,172
Zvav 24,176,176
Mvav 24,180,180
Mvav 24,184,184
Mvav 24,188,188
Zvav 28,176,192
Mvav 28,180,196
Mvav 28,184,200
Mvav 28,188,204
Zvav 32,176,208
Mvav 32,180,212
Mvav 32,184,216
Mvav 32,188,220
s_nop 7
v_max3_f32 v100, v100, v20, v21
v_max3_f32 v100, v100, v22, v23
v_max3_f32 v101, v101, v24, v25
v_max3_f32 v101, v101, v26, v27
v_max3_f32 v102, v102, v28, v29
v_max3_f32 v102, v102, v30, v31
v_max3_f32 v103, v103, v32, v33
v_max3_f32 v103, v103, v34, v35
s_add_u32 s56, s56, 4096
s_add_u32 s57, s57, 1
s_cmp_lt_u32 s57, 64
s_cbranch_scc1 SL1B_%=
v_mov_b32 v108, v100
v_mov_b32 v109, v101
v_mov_b32 v110, v102
v_mov_b32 v111, v103
s_nop 0
v_permlane32_swap_b32 v100, v108
v_permlane32_swap_b32 v101, v109
v_permlane32_swap_b32 v102, v110
v_permlane32_swap_b32 v103, v111
v_max_f32 v100, v100, v108
v_max_f32 v101, v101, v109
v_max_f32 v102, v102, v110
v_max_f32 v103, v103, v111
v_mov_b32 v108, v100
v_mov_b32 v109, v101
v_mov_b32 v110, v102
v_mov_b32 v111, v103
s_nop 0
v_permlane16_swap_b32 v100, v108
v_permlane16_swap_b32 v101, v109
v_permlane16_swap_b32 v102, v110
v_permlane16_swap_b32 v103, v111
v_max_f32 v100, v100, v108
v_max_f32 v101, v101, v109
v_max_f32 v102, v102, v110
v_max_f32 v103, v103, v111
v_mov_b32 v158, 0
v_mov_b32 v159, 0
v_mov_b32 v242, 0
v_mov_b32 v243, 0
v_accvgpr_write_b32 a0, 0
v_accvgpr_write_b32 a1, 0
v_accvgpr_write_b32 a2, 0
v_accvgpr_write_b32 a3, 0
v_accvgpr_write_b32 a4, 0
v_accvgpr_write_b32 a5, 0
v_accvgpr_write_b32 a6, 0
v_accvgpr_write_b32 a7, 0
v_accvgpr_write_b32 a8, 0
v_accvgpr_write_b32 a9, 0
v_accvgpr_write_b32 a10, 0
v_accvgpr_write_b32 a11, 0
v_accvgpr_write_b32 a12, 0
v_accvgpr_write_b32 a13, 0
v_accvgpr_write_b32 a14, 0
v_accvgpr_write_b32 a15, 0
v_accvgpr_write_b32 a20, 0
v_accvgpr_write_b32 a21, 0
v_accvgpr_write_b32 a22, 0
v_accvgpr_write_b32 a23, 0
v_accvgpr_write_b32 a24, 0
v_accvgpr_write_b32 a25, 0
v_accvgpr_write_b32 a26, 0
v_accvgpr_write_b32 a27, 0
v_accvgpr_write_b32 a28, 0
v_accvgpr_write_b32 a29, 0
v_accvgpr_write_b32 a30, 0
v_accvgpr_write_b32 a31, 0
v_accvgpr_write_b32 a32, 0
v_accvgpr_write_b32 a33, 0
v_accvgpr_write_b32 a34, 0
v_accvgpr_write_b32 a35, 0
v_accvgpr_write_b32 a40, 0
v_accvgpr_write_b32 a41, 0
v_accvgpr_write_b32 a42, 0
v_accvgpr_write_b32 a43, 0
v_accvgpr_write_b32 a44, 0
v_accvgpr_write_b32 a45, 0
v_accvgpr_write_b32 a46, 0
v_accvgpr_write_b32 a47, 0
v_accvgpr_write_b32 a48, 0
v_accvgpr_write_b32 a49, 0
v_accvgpr_write_b32 a50, 0
v_accvgpr_write_b32 a51, 0
v_accvgpr_write_b32 a52, 0
v_accvgpr_write_b32 a53, 0
v_accvgpr_write_b32 a54, 0
v_accvgpr_write_b32 a55, 0
v_accvgpr_write_b32 a60, 0
v_accvgpr_write_b32 a61, 0
v_accvgpr_write_b32 a62, 0
v_accvgpr_write_b32 a63, 0
v_accvgpr_write_b32 a64, 0
v_accvgpr_write_b32 a65, 0
v_accvgpr_write_b32 a66, 0
v_accvgpr_write_b32 a67, 0
v_accvgpr_write_b32 a68, 0
v_accvgpr_write_b32 a69, 0
v_accvgpr_write_b32 a70, 0
v_accvgpr_write_b32 a71, 0
v_accvgpr_write_b32 a72, 0
v_accvgpr_write_b32 a73, 0
v_accvgpr_write_b32 a74, 0
v_accvgpr_write_b32 a75, 0
v_accvgpr_write_b32 a80, 0
v_accvgpr_write_b32 a81, 0
v_accvgpr_write_b32 a82, 0
v_accvgpr_write_b32 a83, 0
v_accvgpr_write_b32 a84, 0
v_accvgpr_write_b32 a85, 0
v_accvgpr_write_b32 a86, 0
v_accvgpr_write_b32 a87, 0
v_accvgpr_write_b32 a88, 0
v_accvgpr_write_b32 a89, 0
v_accvgpr_write_b32 a90, 0
v_accvgpr_write_b32 a91, 0
v_accvgpr_write_b32 a92, 0
v_accvgpr_write_b32 a93, 0
v_accvgpr_write_b32 a94, 0
v_accvgpr_write_b32 a95, 0
v_accvgpr_write_b32 a100, 0
v_accvgpr_write_b32 a101, 0
v_accvgpr_write_b32 a102, 0
v_accvgpr_write_b32 a103, 0
v_accvgpr_write_b32 a104, 0
v_accvgpr_write_b32 a105, 0
v_accvgpr_write_b32 a106, 0
v_accvgpr_write_b32 a107, 0
v_accvgpr_write_b32 a108, 0
v_accvgpr_write_b32 a109, 0
v_accvgpr_write_b32 a110, 0
v_accvgpr_write_b32 a111, 0
v_accvgpr_write_b32 a112, 0
v_accvgpr_write_b32 a113, 0
v_accvgpr_write_b32 a114, 0
v_accvgpr_write_b32 a115, 0
v_accvgpr_write_b32 a120, 0
v_accvgpr_write_b32 a121, 0
v_accvgpr_write_b32 a122, 0
v_accvgpr_write_b32 a123, 0
v_accvgpr_write_b32 a124, 0
v_accvgpr_write_b32 a125, 0
v_accvgpr_write_b32 a126, 0
v_accvgpr_write_b32 a127, 0
v_accvgpr_write_b32 a128, 0
v_accvgpr_write_b32 a129, 0
v_accvgpr_write_b32 a130, 0
v_accvgpr_write_b32 a131, 0
v_accvgpr_write_b32 a132, 0
v_accvgpr_write_b32 a133, 0
v_accvgpr_write_b32 a134, 0
v_accvgpr_write_b32 a135, 0
v_accvgpr_write_b32 a140, 0
v_accvgpr_write_b32 a141, 0
v_accvgpr_write_b32 a142, 0
v_accvgpr_write_b32 a143, 0
v_accvgpr_write_b32 a144, 0
v_accvgpr_write_b32 a145, 0
v_accvgpr_write_b32 a146, 0
v_accvgpr_write_b32 a147, 0
v_accvgpr_write_b32 a148, 0
v_accvgpr_write_b32 a149, 0
v_accvgpr_write_b32 a150, 0
v_accvgpr_write_b32 a151, 0
v_accvgpr_write_b32 a152, 0
v_accvgpr_write_b32 a153, 0
v_accvgpr_write_b32 a154, 0
v_accvgpr_write_b32 a155, 0
s_lshl_b32 s56, s48, 18
s_add_u32 s56, s56, 1024
s_mov_b32 s57, 0
SL2B_%=:
s_mov_b32 s79, s56
buffer_load_dwordx4 a[176:179], v105, s[36:39], s56 offen offset:0
buffer_load_dwordx4 a[180:183], v105, s[36:39], s56 offen offset:64
buffer_load_dwordx4 a[184:187], v105, s[36:39], s56 offen offset:128
buffer_load_dwordx4 a[188:191], v105, s[36:39], s56 offen offset:192
s_waitcnt vmcnt(0)
Zvav 20,176,160
Mvav 20,180,164
Mvav 20,184,168
Mvav 20,188,172
Zvav 24,176,176
Mvav 24,180,180
Mvav 24,184,184
Mvav 24,188,188
Zvav 28,176,192
Mvav 28,180,196
)A"
R"A(Mvav 28,184,200
Mvav 28,188,204
Zvav 32,176,208
Mvav 32,180,212
Mvav 32,184,216
Mvav 32,188,220
s_nop 7
v_sub_f32 v20, v20, v100
v_sub_f32 v21, v21, v100
v_sub_f32 v22, v22, v100
v_sub_f32 v23, v23, v100
Ex 20
Ex 21
Ex 22
Ex 23
s_nop 0
Ad 158,20
Ad 158,21
Ad 158,22
Ad 158,23
Pk 80,20,21
Pk 81,22,23
v_sub_f32 v24, v24, v101
v_sub_f32 v25, v25, v101
v_sub_f32 v26, v26, v101
v_sub_f32 v27, v27, v101
Ex 24
Ex 25
Ex 26
Ex 27
s_nop 0
Ad 159,24
Ad 159,25
Ad 159,26
Ad 159,27
Pk 84,24,25
Pk 85,26,27
v_sub_f32 v28, v28, v102
v_sub_f32 v29, v29, v102
v_sub_f32 v30, v30, v102
v_sub_f32 v31, v31, v102
Ex 28
Ex 29
Ex 30
Ex 31
s_nop 0
Ad 242,28
Ad 242,29
Ad 242,30
Ad 242,31
Pk 88,28,29
Pk 89,30,31
v_sub_f32 v32, v32, v103
v_sub_f32 v33, v33, v103
v_sub_f32 v34, v34, v103
v_sub_f32 v35, v35, v103
Ex 32
Ex 33
Ex 34
Ex 35
s_nop 0
Ad 243,32
Ad 243,33
Ad 243,34
Ad 243,35
Pk 92,32,33
Pk 93,34,35
s_add_u32 s56, s56, 4096
buffer_load_dwordx4 a[176:179], v105, s[36:39], s56 offen offset:0
buffer_load_dwordx4 a[180:183], v105, s[36:39], s56 offen offset:64
buffer_load_dwordx4 a[184:187], v105, s[36:39], s56 offen offset:128
buffer_load_dwordx4 a[188:191], v105, s[36:39], s56 offen offset:192
s_waitcnt vmcnt(0)
Zvav 20,176,160
Mvav 20,180,164
Mvav 20,184,168
Mvav 20,188,172
Zvav 24,176,176
Mvav 24,180,180
Mvav 24,184,184
Mvav 24,188,188
Zvav 28,176,192
Mvav 28,180,196
Mvav 28,184,200
Mvav 28,188,204
Zvav 32,176,208
Mvav 32,180,212
Mvav 32,184,216
Mvav 32,188,220
s_nop 7
v_sub_f32 v20, v20, v100
v_sub_f32 v21, v21, v100
v_sub_f32 v22, v22, v100
v_sub_f32 v23, v23, v100
Ex 20
Ex 21
Ex 22
Ex 23
s_nop 0
Ad 158,20
Ad 158,21
Ad 158,22
Ad 158,23
Pk 82,20,21
Pk 83,22,23
v_sub_f32 v24, v24, v101
v_sub_f32 v25, v25, v101
v_sub_f32 v26, v26, v101
v_sub_f32 v27, v27, v101
Ex 24
Ex 25
Ex 26
Ex 27
s_nop 0
Ad 159,24
Ad 159,25
Ad 159,26
Ad 159,27
Pk 86,24,25
Pk 87,26,27
v_sub_f32 v28, v28, v102
v_sub_f32 v29, v29, v102
v_sub_f32 v30, v30, v102
v_sub_f32 v31, v31, v102
Ex 28
Ex 29
Ex 30
Ex 31
s_nop 0
Ad 242,28
Ad 242,29
Ad 242,30
Ad 242,31
Pk 90,28,29
Pk 91,30,31
v_sub_f32 v32, v32, v103
v_sub_f32 v33, v33, v103
v_sub_f32 v34, v34, v103
v_sub_f32 v35, v35, v103
Ex 32
Ex 33
Ex 34
Ex 35
s_nop 0
Ad 243,32
Ad 243,33
Ad 243,34
Ad 243,35
Pk 94,32,33
Pk 95,34,35
s_add_u32 s56, s56, 4096
buffer_load_ushort v108, v106, s[40:43], s79 offen offset:0
buffer_load_ushort v109, v106, s[40:43], s79 offen offset:256
buffer_load_ushort v110, v106, s[40:43], s79 offen offset:512
buffer_load_ushort v111, v106, s[40:43], s79 offen offset:768
buffer_load_ushort v112, v107, s[40:43], s79 offen offset:0
buffer_load_ushort v113, v107, s[40:43], s79 offen offset:256
buffer_load_ushort v114, v107, s[40:43], s79 offen offset:512
buffer_load_ushort v115, v107, s[40:43], s79 offen offset:768
s_waitcnt vmcnt(0)
v_lshl_or_b32 v116, v109, 16, v108
v_lshl_or_b32 v117, v111, 16, v110
v_lshl_or_b32 v118, v113, 16, v112
v_lshl_or_b32 v119, v115, 16, v114
s_nop 1
Mavv 0,116,80
Mavv 4,116,84
Mavv 8,116,88
Mavv 12,116,92
buffer_load_ushort v108, v106, s[40:43], s79 offen offset:32
buffer_load_ushort v109, v106, s[40:43], s79 offen offset:288
buffer_load_ushort v110, v106, s[40:43], s79 offen offset:544
buffer_load_ushort v111, v106, s[40:43], s79 offen offset:800
buffer_load_ushort v112, v107, s[40:43], s79 offen offset:32
buffer_load_ushort v113, v107, s[40:43], s79 offen offset:288
buffer_load_ushort v114, v107, s[40:43], s79 offen offset:544
buffer_load_ushort v115, v107, s[40:43], s79 offen offset:800
s_waitcnt vmcnt(0)
v_lshl_or_b32 v116, v109, 16, v108
v_lshl_or_b32 v117, v111, 16, v110
v_lshl_or_b32 v118, v113, 16, v112
v_lshl_or_b32 v119, v115, 16, v114
s_nop 1
Mavv 20,116,80
Mavv 24,116,84
Mavv 28,116,88
Mavv 32,116,92
buffer_load_ushort v108, v106, s[40:43], s79 offen offset:64
buffer_load_ushort v109, v106, s[40:43], s79 offen offset:320
buffer_load_ushort v110, v106, s[40:43], s79 offen offset:576
buffer_load_ushort v111, v106, s[40:43], s79 offen offset:832
buffer_load_ushort v112, v107, s[40:43], s79 offen offset:64
buffer_load_ushort v113, v107, s[40:43], s79 offen offset:320
buffer_load_ushort v114, v107, s[40:43], s79 offen offset:576
buffer_load_ushort v115, v107, s[40:43], s79 offen offset:832
s_waitcnt vmcnt(0)
v_lshl_or_b32 v116, v109, 16, v108
v_lshl_or_b32 v117, v111, 16, v110
v_lshl_or_b32 v118, v113, 16, v112
v_lshl_or_b32 v119, v115, 16, v114
s_nop 1
Mavv 40,116,80
Mavv 44,116,84
Mavv 48,116,88
Mavv 52,116,92
buffer_load_ushort v108, v106, s[40:43], s79 offen offset:96
buffer_load_ushort v109, v106, s[40:43], s79 offen offset:352
buffer_load_ushort v110, v106, s[40:43], s79 offen offset:608
buffer_load_ushort v111, v106, s[40:43], s79 offen offset:864
buffer_load_ushort v112, v107, s[40:43], s79 offen offset:96
buffer_load_ushort v113, v107, s[40:43], s79 offen offset:352
buffer_load_ushort v114, v107, s[40:43], s79 offen offset:608
buffer_load_ushort v115, v107, s[40:43], s79 offen offset:864
s_waitcnt vmcnt(0)
v_lshl_or_b32 v116, v109, 16, v108
v_lshl_or_b32 v117, v111, 16, v110
v_lshl_or_b32 v118, v113, 16, v112
v_lshl_or_b32 v119, v115, 16, v114
s_nop 1
Mavv 60,116,80
Mavv 64,116,84
Mavv 68,116,88
Mavv 72,116,92
buffer_load_ushort v108, v106, s[40:43], s79 offen offset:128
buffer_load_ushort v109, v106, s[40:43], s79 offen offset:384
buffer_load_ushort v110, v106, s[40:43], s79 offen offset:640
buffer_load_ushort v111, v106, s[40:43], s79 offen offset:896
buffer_load_ushort v112, v107, s[40:43], s79 offen offset:128
buffer_load_ushort v113, v107, s[40:43], s79 offen offset:384
buffer_load_ushort v114, v107, s[40:43], s79 offen offset:640
buffer_load_ushort v115, v107, s[40:43], s79 offen offset:896
s_waitcnt vmcnt(0)
v_lshl_or_b32 v116, v109, 16, v108
v_lshl_or_b32 v117, v111, 16, v110
v_lshl_or_b32 v118, v113, 16, v112
v_lshl_or_b32 v119, v115, 16, v114
s_nop 1
Mavv 80,116,80
Mavv 84,116,84
Mavv 88,116,88
Mavv 92,116,92
buffer_load_ushort v108, v106, s[40:43], s79 offen offset:160
buffer_load_ushort v109, v106, s[40:43], s79 offen offset:416
buffer_load_ushort v110, v106, s[40:43], s79 offen offset:672
buffer_load_ushort v111, v106, s[40:43], s79 offen offset:928
buffer_load_ushort v112, v107, s[40:43], s79 offen offset:160
buffer_load_ushort v113, v107, s[40:43], s79 offen offset:416
buffer_load_ushort v114, v107, s[40:43], s79 offen offset:672
buffer_load_ushort v115, v107, s[40:43], s79 offen offset:928
s_waitcnt vmcnt(0)
v_lshl_or_b32 v116, v109, 16, v108
v_lshl_or_b32 v117, v111, 16, v110
v_lshl_or_b32 v118, v113, 16, v112
v_lshl_or_b32 v119, v115, 16, v114
s_nop 1
Mavv 100,116,80
Mavv 104,116,84
Mavv 108,116,88
Mavv 112,116,92
buffer_load_ushort v108, v106, s[40:43], s79 offen offset:192
buffer_load_ushort v109, v106, s[40:43], s79 offen offset:448
buffer_load_ushort v110, v106, s[40:43], s79 offen offset:704
buffer_load_ushort v111, v106, s[40:43], s79 offen offset:960
buffer_load_ushort v112, v107, s[40:43], s79 offen offset:192
buffer_load_ushort v113, v107, s[40:43], s79 offen offset:448
buffer_load_ushort v114, v107, s[40:43], s79 offen offset:704
buffer_load_ushort v115, v107, s[40:43], s79 offen offset:960
s_waitcnt vmcnt(0)
v_lshl_or_b32 v116, v109, 16, v108
v_lshl_or_b32 v117, v111, 16, v110
v_lshl_or_b32 v118, v113, 16, v112
v_lshl_or_b32 v119, v115, 16, v114
s_nop 1
Mavv 120,116,80
Mavv 124,116,84
Mavv 128,116,88
Mavv 132,116,92
buffer_load_ushort v108, v106, s[40:43], s79 offen offset:224
buffer_load_ushort v109, v106, s[40:43], s79 offen offset:480
buffer_load_ushort v110, v106, s[40:43], s79 offen offset:736
buffer_load_ushort v111, v106, s[40:43], s79 offen offset:992
buffer_load_ushort v112, v107, s[40:43], s79 offen offset:224
buffer_load_ushort v113, v107, s[40:43], s79 offen offset:480
buffer_load_ushort v114, v107, s[40:43], s79 offen offset:736
buffer_load_ushort v115, v107, s[40:43], s79 offen offset:992
s_waitcnt vmcnt(0)
v_lshl_or_b32 v116, v109, 16, v108
v_lshl_or_b32 v117, v111, 16, v110
v_lshl_or_b32 v118, v113, 16, v112
v_lshl_or_b32 v119, v115, 16, v114
s_nop 1
Mavv 140,116,80
Mavv 144,116,84
Mavv 148,116,88
Mavv 152,116,92
s_add_u32 s57, s57, 1
s_cmp_lt_u32 s57, 32
s_cbranch_scc1 SL2B_%=
v_mov_b32 v108, v158
v_mov_b32 v109, v159
v_mov_b32 v110, v242
v_mov_b32 v111, v243
s_nop 0
v_permlane32_swap_b32 v158, v108
v_permlane32_swap_b32 v159, v109
v_permlane32_swap_b32 v242, v110
v_permlane32_swap_b32 v243, v111
Ad 158,108
Ad 159,109
Ad 242,110
Ad 243,111
v_mov_b32 v108, v158
v_mov_b32 v109, v159
v_mov_b32 v110, v242
v_mov_b32 v111, v243
s_nop 0
v_permlane16_swap_b32 v158, v108
v_permlane16_swap_b32 v159, v109
v_permlane16_swap_b32 v242, v110
v_permlane16_swap_b32 v243, v111
Ad 158,108
Ad 159,109
Ad 242,110
Ad 243,111
v_rcp_f32 v158, v158
v_rcp_f32 v159, v159
v_rcp_f32 v242, v242
v_rcp_f32 v243, v243
s_nop 7
s_cmp_eq_u32 s48, 0
s_cbranch_scc1 RET0B_%=
s_branch RET1B_%=
SPINP0A_%=:
s_sleep 4
v_and_b32 v158, 3, v245
v_lshl_add_u32 v158, v158, 10, s59
v_writelane_b32 v158, s77, 4
v_writelane_b32 v158, s78, 5
v_writelane_b32 v158, s80, 6
global_load_dword v159, v158, s[92:93] sc1
v_lshlrev_b32 v244, 2, v245
global_load_dword v244, v244, s[50:51] sc1
s_waitcnt vmcnt(0)
v_xor_b32 v159, s66, v159
global_store_dword v158, v159, s[94:95] sc1
v_cmp_gt_i32 s[64:65], 0, v244
v_and_b32 v244, 0x7fffffff, v244
v_cmp_ne_u32 vcc, s81, v244
s_nop 0
s_and_b64 vcc, vcc, s[34:35]
s_cbranch_scc0 OKP0A_%=
s_add_u32 s58, s58, 1
s_cmp_lt_u32 s58, 0x80000
s_cbranch_scc1 SPINP0A_%=
s_branch OKP0A_%=
PREA_%=:
s_waitcnt vmcnt(0) lgkmcnt(0)
Ar 160,192
Ar 161,193
Ar 162,194
Ar 163,195
Ar 164,196
Ar 165,197
Ar 166,198
Ar 167,199
Ar 168,200
Ar 169,201
Ar 170,202
Ar 171,203
Ar 172,204
Ar 173,205
Ar 174,206
Ar 175,207
Ar 176,208
Ar 177,209
Ar 178,210
Ar 179,211
Ar 180,212
Ar 181,213
Ar 182,214
Ar 183,215
Ar 184,216
Ar 185,217
Ar 186,218
Ar 187,219
Ar 188,220
Ar 189,221
Ar 190,222
Ar 191,223
Ar 192,224
Ar 193,225
Ar 194,226
Ar 195,227
Ar 196,228
Ar 197,229
Ar 198,230
Ar 199,231
Ar 200,232
Ar 201,233
Ar 202,234
)A"
R"A(Ar 203,235
Ar 204,236
Ar 205,237
Ar 206,238
Ar 207,239
Ar 208,240
Ar 209,241
Ar 210,242
Ar 211,243
Ar 212,244
Ar 213,245
Ar 214,246
Ar 215,247
Ar 216,248
Ar 217,249
Ar 218,250
Ar 219,251
Ar 220,252
Ar 221,253
Ar 222,254
Ar 223,255
v_mov_b32 v224, v248
v_mov_b32 v225, v249
v_mov_b32 v226, v250
v_mov_b32 v227, v251
v_mov_b32 v228, v252
v_mov_b32 v229, v253
v_mov_b32 v230, v254
v_mov_b32 v231, v255
v_mov_b32 v232, v150
v_mov_b32 v233, v151
v_mov_b32 v234, v152
v_mov_b32 v235, v153
v_mov_b32 v236, v154
v_mov_b32 v237, v155
v_mov_b32 v238, v156
v_mov_b32 v239, v157
s_branch SLOWA_%=
RET1A_%=:
v_bfe_u32 v120, v245, 2, 2
v_and_b32 v121, 3, v245
v_lshrrev_b32 v122, 4, v245
v_mul_u32_u24 v240, 1032, v122
v_lshl_add_u32 v240, v120, 6, v240
v_lshl_add_u32 v240, v121, 4, v240
v_add_u32 v255, 66048, v240
v_mov_b32 v241, v124
s_mov_b32 s57, 0
buffer_load_dwordx4 v[160:163], v124, s[96:99], s57 offen offset:0 sc1
buffer_load_dwordx4 v[164:167], v124, s[96:99], s57 offen offset:1024 sc1
buffer_load_dwordx4 v[168:171], v124, s[96:99], s57 offen offset:2048 sc1
buffer_load_dwordx4 v[172:175], v124, s[96:99], s57 offen offset:3072 sc1
s_mov_b32 s57, 16384
buffer_load_dwordx4 v[176:179], v124, s[96:99], s57 offen offset:0 sc1
buffer_load_dwordx4 v[180:183], v124, s[96:99], s57 offen offset:1024 sc1
buffer_load_dwordx4 v[184:187], v124, s[96:99], s57 offen offset:2048 sc1
buffer_load_dwordx4 v[188:191], v124, s[96:99], s57 offen offset:3072 sc1
s_mov_b32 s57, 32768
buffer_load_dwordx4 v[192:195], v124, s[96:99], s57 offen offset:0 sc1
buffer_load_dwordx4 v[196:199], v124, s[96:99], s57 offen offset:1024 sc1
buffer_load_dwordx4 v[200:203], v124, s[96:99], s57 offen offset:2048 sc1
buffer_load_dwordx4 v[204:207], v124, s[96:99], s57 offen offset:3072 sc1
s_mov_b32 s57, 49152
buffer_load_dwordx4 v[208:211], v124, s[96:99], s57 offen offset:0 sc1
buffer_load_dwordx4 v[212:215], v124, s[96:99], s57 offen offset:1024 sc1
buffer_load_dwordx4 v[216:219], v124, s[96:99], s57 offen offset:2048 sc1
buffer_load_dwordx4 v[220:223], v124, s[96:99], s57 offen offset:3072 sc1
s_mov_b32 s57, 65536
buffer_load_dwordx4 v[224:227], v124, s[96:99], s57 offen offset:0 sc1
buffer_load_dwordx4 v[228:231], v124, s[96:99], s57 offen offset:1024 sc1
buffer_load_dwordx4 v[232:235], v124, s[96:99], s57 offen offset:2048 sc1
buffer_load_dwordx4 v[236:239], v124, s[96:99], s57 offen offset:3072 sc1
Ta 192,255,256
Ta 194,255,4384
Ta 196,255,8512
Ta 198,255,12640
Ta 200,240,256
Ta 202,240,4384
Ta 204,240,8512
Ta 206,240,12640
Ta 208,255,264
Ta 210,255,4392
Ta 212,255,8520
Ta 214,255,12648
Ta 216,240,264
Ta 218,240,4392
Ta 220,240,8520
Ta 222,240,12648
Ar 136,0
Ar 137,1
Ar 138,2
Ar 139,3
Ar 140,20
Ar 141,21
Ar 142,22
Ar 143,23
v_pk_mul_f32 v[136:137], v[136:137], v[158:159] op_sel_hi:[1,0]
v_pk_mul_f32 v[138:139], v[138:139], v[158:159] op_sel_hi:[1,0]
v_pk_mul_f32 v[140:141], v[140:141], v[158:159] op_sel_hi:[1,0]
v_pk_mul_f32 v[142:143], v[142:143], v[158:159] op_sel_hi:[1,0]
Pk 0,136,137
Pk 1,138,139
Pk 2,140,141
Pk 3,142,143
Ar 144,4
Ar 145,5
Ar 146,6
Ar 147,7
Ar 148,24
Ar 149,25
Ar 150,26
Ar 151,27
v_pk_mul_f32 v[144:145], v[144:145], v[158:159] op_sel:[0,1] op_sel_hi:[1,1]
v_pk_mul_f32 v[146:147], v[146:147], v[158:159] op_sel:[0,1] op_sel_hi:[1,1]
v_pk_mul_f32 v[148:149], v[148:149], v[158:159] op_sel:[0,1] op_sel_hi:[1,1]
v_pk_mul_f32 v[150:151], v[150:151], v[158:159] op_sel:[0,1] op_sel_hi:[1,1]
Pk 16,144,145
Pk 17,146,147
Pk 18,148,149
Pk 19,150,151
Ar 136,8
Ar 137,9
Ar 138,10
Ar 139,11
Ar 140,28
Ar 141,29
Ar 142,30
Ar 143,31
v_pk_mul_f32 v[136:137], v[136:137], v[242:243] op_sel_hi:[1,0]
v_pk_mul_f32 v[138:139], v[138:139], v[242:243] op_sel_hi:[1,0]
v_pk_mul_f32 v[140:141], v[140:141], v[242:243] op_sel_hi:[1,0]
v_pk_mul_f32 v[142:143], v[142:143], v[242:243] op_sel_hi:[1,0]
Pk 32,136,137
Pk 33,138,139
Pk 34,140,141
Pk 35,142,143
Ar 144,12
Ar 145,13
Ar 146,14
Ar 147,15
Ar 148,32
Ar 149,33
Ar 150,34
Ar 151,35
v_pk_mul_f32 v[144:145], v[144:145], v[242:243] op_sel:[0,1] op_sel_hi:[1,1]
v_pk_mul_f32 v[146:147], v[146:147], v[242:243] op_sel:[0,1] op_sel_hi:[1,1]
v_pk_mul_f32 v[148:149], v[148:149], v[242:243] op_sel:[0,1] op_sel_hi:[1,1]
v_pk_mul_f32 v[150:151], v[150:151], v[242:243] op_sel:[0,1] op_sel_hi:[1,1]
Pk 48,144,145
Pk 49,146,147
Pk 50,148,149
Pk 51,150,151
Ar 136,16
Ar 137,17
Ar 138,18
Ar 139,19
Ar 140,36
Ar 141,37
Ar 142,38
Ar 143,39
v_pk_mul_f32 v[136:137], v[136:137], v[244:245] op_sel_hi:[1,0]
v_pk_mul_f32 v[138:139], v[138:139], v[244:245] op_sel_hi:[1,0]
v_pk_mul_f32 v[140:141], v[140:141], v[244:245] op_sel_hi:[1,0]
v_pk_mul_f32 v[142:143], v[142:143], v[244:245] op_sel_hi:[1,0]
Pk 64,136,137
Pk 65,138,139
Pk 66,140,141
Pk 67,142,143
Ar 136,40
Ar 137,41
Ar 138,42
Ar 139,43
Ar 140,60
Ar 141,61
Ar 142,62
Ar 143,63
v_pk_mul_f32 v[136:137], v[136:137], v[158:159] op_sel_hi:[1,0]
v_pk_mul_f32 v[138:139], v[138:139], v[158:159] op_sel_hi:[1,0]
v_pk_mul_f32 v[140:141], v[140:141], v[158:159] op_sel_hi:[1,0]
v_pk_mul_f32 v[142:143], v[142:143], v[158:159] op_sel_hi:[1,0]
Pk 4,136,137
Pk 5,138,139
Pk 6,140,141
Pk 7,142,143
Ar 144,44
Ar 145,45
Ar 146,46
Ar 147,47
Ar 148,64
Ar 149,65
Ar 150,66
Ar 151,67
v_pk_mul_f32 v[144:145], v[144:145], v[158:159] op_sel:[0,1] op_sel_hi:[1,1]
v_pk_mul_f32 v[146:147], v[146:147], v[158:159] op_sel:[0,1] op_sel_hi:[1,1]
v_pk_mul_f32 v[148:149], v[148:149], v[158:159] op_sel:[0,1] op_sel_hi:[1,1]
v_pk_mul_f32 v[150:151], v[150:151], v[158:159] op_sel:[0,1] op_sel_hi:[1,1]
Pk 20,144,145
Pk 21,146,147
Pk 22,148,149
Pk 23,150,151
Ar 136,48
Ar 137,49
Ar 138,50
Ar 139,51
Ar 140,68
Ar 141,69
Ar 142,70
Ar 143,71
v_pk_mul_f32 v[136:137], v[136:137], v[242:243] op_sel_hi:[1,0]
v_pk_mul_f32 v[138:139], v[138:139], v[242:243] op_sel_hi:[1,0]
v_pk_mul_f32 v[140:141], v[140:141], v[242:243] op_sel_hi:[1,0]
v_pk_mul_f32 v[142:143], v[142:143], v[242:243] op_sel_hi:[1,0]
Pk 36,136,137
Pk 37,138,139
Pk 38,140,141
Pk 39,142,143
Ar 144,52
Ar 145,53
Ar 146,54
Ar 147,55
Ar 148,72
Ar 149,73
Ar 150,74
Ar 151,75
v_pk_mul_f32 v[144:145], v[144:145], v[242:243] op_sel:[0,1] op_sel_hi:[1,1]
v_pk_mul_f32 v[146:147], v[146:147], v[242:243] op_sel:[0,1] op_sel_hi:[1,1]
v_pk_mul_f32 v[148:149], v[148:149], v[242:243] op_sel:[0,1] op_sel_hi:[1,1]
v_pk_mul_f32 v[150:151], v[150:151], v[242:243] op_sel:[0,1] op_sel_hi:[1,1]
Pk 52,144,145
Pk 53,146,147
Pk 54,148,149
Pk 55,150,151
Ar 136,56
Ar 137,57
Ar 138,58
Ar 139,59
Ar 140,76
Ar 141,77
Ar 142,78
Ar 143,79
v_pk_mul_f32 v[136:137], v[136:137], v[244:245] op_sel_hi:[1,0]
v_pk_mul_f32 v[138:139], v[138:139], v[244:245] op_sel_hi:[1,0]
v_pk_mul_f32 v[140:141], v[140:141], v[244:245] op_sel_hi:[1,0]
v_pk_mul_f32 v[142:143], v[142:143], v[244:245] op_sel_hi:[1,0]
Pk 68,136,137
Pk 69,138,139
Pk 70,140,141
Pk 71,142,143
Ar 136,80
Ar 137,81
Ar 138,82
Ar 139,83
Ar 140,100
Ar 141,101
Ar 142,102
Ar 143,103
v_pk_mul_f32 v[136:137], v[136:137], v[158:159] op_sel_hi:[1,0]
v_pk_mul_f32 v[138:139], v[138:139], v[158:159] op_sel_hi:[1,0]
v_pk_mul_f32 v[140:141], v[140:141], v[158:159] op_sel_hi:[1,0]
v_pk_mul_f32 v[142:143], v[142:143], v[158:159] op_sel_hi:[1,0]
Pk 8,136,137
Pk 9,138,139
Pk 10,140,141
Pk 11,142,143
Ar 144,84
Ar 145,85
Ar 146,86
Ar 147,87
Ar 148,104
Ar 149,105
Ar 150,106
Ar 151,107
v_pk_mul_f32 v[144:145], v[144:145], v[158:159] op_sel:[0,1] op_sel_hi:[1,1]
v_pk_mul_f32 v[146:147], v[146:147], v[158:159] op_sel:[0,1] op_sel_hi:[1,1]
v_pk_mul_f32 v[148:149], v[148:149], v[158:159] op_sel:[0,1] op_sel_hi:[1,1]
v_pk_mul_f32 v[150:151], v[150:151], v[158:159] op_sel:[0,1] op_sel_hi:[1,1]
Pk 24,144,145
Pk 25,146,147
Pk 26,148,149
Pk 27,150,151
Ar 136,88
Ar 137,89
Ar 138,90
Ar 139,91
Ar 140,108
Ar 141,109
Ar 142,110
Ar 143,111
v_pk_mul_f32 v[136:137], v[136:137], v[242:243] op_sel_hi:[1,0]
v_pk_mul_f32 v[138:139], v[138:139], v[242:243] op_sel_hi:[1,0]
v_pk_mul_f32 v[140:141], v[140:141], v[242:243] op_sel_hi:[1,0]
v_pk_mul_f32 v[142:143], v[142:143], v[242:243] op_sel_hi:[1,0]
Pk 40,136,137
Pk 41,138,139
Pk 42,140,141
Pk 43,142,143
Ar 144,92
Ar 145,93
Ar 146,94
Ar 147,95
Ar 148,112
Ar 149,113
Ar 150,114
Ar 151,115
v_pk_mul_f32 v[144:145], v[144:145], v[242:243] op_sel:[0,1] op_sel_hi:[1,1]
v_pk_mul_f32 v[146:147], v[146:147], v[242:243] op_sel:[0,1] op_sel_hi:[1,1]
v_pk_mul_f32 v[148:149], v[148:149], v[242:243] op_sel:[0,1] op_sel_hi:[1,1]
v_pk_mul_f32 v[150:151], v[150:151], v[242:243] op_sel:[0,1] op_sel_hi:[1,1]
Pk 56,144,145
Pk 57,146,147
Pk 58,148,149
Pk 59,150,151
Ar 136,96
Ar 137,97
Ar 138,98
Ar 139,99
Ar 140,116
Ar 141,117
Ar 142,118
Ar 143,119
v_pk_mul_f32 v[136:137], v[136:137], v[244:245] op_sel_hi:[1,0]
v_pk_mul_f32 v[138:139], v[138:139], v[244:245] op_sel_hi:[1,0]
v_pk_mul_f32 v[140:141], v[140:141], v[244:245] op_sel_hi:[1,0]
v_pk_mul_f32 v[142:143], v[142:143], v[244:245] op_sel_hi:[1,0]
Pk 72,136,137
Pk 73,138,139
Pk 74,140,141
Pk 75,142,143
Ar 136,120
Ar 137,121
Ar 138,122
Ar 139,123
Ar 140,140
Ar 141,141
Ar 142,142
Ar 143,143
v_pk_mul_f32 v[136:137], v[136:137], v[158:159] op_sel_hi:[1,0]
v_pk_mul_f32 v[138:139], v[138:139], v[158:159] op_sel_hi:[1,0]
v_pk_mul_f32 v[140:141], v[140:141], v[158:159] op_sel_hi:[1,0]
v_pk_mul_f32 v[142:143], v[142:143], v[158:159] op_sel_hi:[1,0]
Pk 12,136,137
Pk 13,138,139
Pk 14,140,141
Pk 15,142,143
Ar 144,124
Ar 145,125
Ar 146,126
Ar 147,127
Ar 148,144
Ar 149,145
Ar 150,146
Ar 151,147
v_pk_mul_f32 v[144:145], v[144:145], v[158:159] op_sel:[0,1] op_sel_hi:[1,1]
v_pk_mul_f32 v[146:147], v[146:147], v[158:159] op_sel:[0,1] op_sel_hi:[1,1]
v_pk_mul_f32 v[148:149], v[148:149], v[158:159] op_sel:[0,1] op_sel_hi:[1,1]
v_pk_mul_f32 v[150:151], v[150:151], v[158:159] op_sel:[0,1] op_sel_hi:[1,1]
Pk 28,144,145
Pk 29,146,147
Pk 30,148,149
Pk 31,150,151
Ar 136,128
Ar 137,129
Ar 138,130
Ar 139,131
Ar 140,148
Ar 141,149
Ar 142,150
Ar 143,151
v_pk_mul_f32 v[136:137], v[136:137], v[242:243] op_sel_hi:[1,0]
v_pk_mul_f32 v[138:139], v[138:139], v[242:243] op_sel_hi:[1,0]
v_pk_mul_f32 v[140:141], v[140:141], v[242:243] op_sel_hi:[1,0]
v_pk_mul_f32 v[142:143], v[142:143], v[242:243] op_sel_hi:[1,0]
Pk 44,136,137
Pk 45,138,139
Pk 46,140,141
Pk 47,142,143
Ar 144,132
Ar 145,133
Ar 146,134
Ar 147,135
Ar 148,152
Ar 149,153
Ar 150,154
Ar 151,155
v_pk_mul_f32 v[144:145], v[144:145], v[242:243] op_sel:[0,1] op_sel_hi:[1,1]
v_pk_mul_f32 v[146:147], v[146:147], v[242:243] op_sel:[0,1] op_sel_hi:[1,1]
v_pk_mul_f32 v[148:149], v[148:149], v[242:243] op_sel:[0,1] op_sel_hi:[1,1]
v_pk_mul_f32 v[150:151], v[150:151], v[242:243] op_sel:[0,1] op_sel_hi:[1,1]
Pk 60,144,145
Pk 61,146,147
Pk 62,148,149
Pk 63,150,151
Ar 136,136
Ar 137,137
Ar 138,138
Ar 139,139
Ar 140,156
Ar 141,157
Ar 142,158
Ar 143,159
)A"
R"A(v_pk_mul_f32 v[136:137], v[136:137], v[244:245] op_sel_hi:[1,0]
v_pk_mul_f32 v[138:139], v[138:139], v[244:245] op_sel_hi:[1,0]
v_pk_mul_f32 v[140:141], v[140:141], v[244:245] op_sel_hi:[1,0]
v_pk_mul_f32 v[142:143], v[142:143], v[244:245] op_sel_hi:[1,0]
Pk 76,136,137
Pk 77,138,139
Pk 78,140,141
Pk 79,142,143
s_mov_b32 s57, 4096
buffer_store_dwordx4 v[0:3], v124, s[96:99], s57 offen offset:0 sc1
s_mov_b32 s57, 20480
buffer_store_dwordx4 v[16:19], v124, s[96:99], s57 offen offset:0 sc1
s_mov_b32 s57, 36864
buffer_store_dwordx4 v[32:35], v124, s[96:99], s57 offen offset:0 sc1
s_mov_b32 s57, 53248
buffer_store_dwordx4 v[48:51], v124, s[96:99], s57 offen offset:0 sc1
s_mov_b32 s57, 69632
buffer_store_dwordx4 v[64:67], v124, s[96:99], s57 offen offset:0 sc1
s_mov_b32 s57, 4096
buffer_store_dwordx4 v[4:7], v124, s[96:99], s57 offen offset:1024 sc1
s_mov_b32 s57, 20480
buffer_store_dwordx4 v[20:23], v124, s[96:99], s57 offen offset:1024 sc1
s_mov_b32 s57, 36864
buffer_store_dwordx4 v[36:39], v124, s[96:99], s57 offen offset:1024 sc1
s_waitcnt vmcnt(0) lgkmcnt(0)
s_branch PPH_%=
SLOWA_%=:
s_nop 7
v_and_b32 v105, 15, v245
v_lshrrev_b32 v107, 4, v245
v_lshlrev_b32 v106, 1, v105
v_lshl_add_u32 v106, v107, 10, v106
v_lshlrev_b32 v105, 8, v105
v_lshl_add_u32 v105, v107, 4, v105
v_add_u32 v107, 4096, v106
v_mov_b32 v100, 0xff800000
v_mov_b32 v101, 0xff800000
v_mov_b32 v102, 0xff800000
v_mov_b32 v103, 0xff800000
v_mov_b32 v104, 0xff800000
s_lshl_b32 s56, s48, 18
s_add_u32 s56, s56, 1024
s_mov_b32 s57, 0
SL1A_%=:
buffer_load_dwordx4 a[176:179], v105, s[36:39], s56 offen offset:0
buffer_load_dwordx4 a[180:183], v105, s[36:39], s56 offen offset:64
buffer_load_dwordx4 a[184:187], v105, s[36:39], s56 offen offset:128
buffer_load_dwordx4 a[188:191], v105, s[36:39], s56 offen offset:192
s_waitcnt vmcnt(0)
Zvav 20,176,160
Mvav 20,180,164
Mvav 20,184,168
Mvav 20,188,172
Zvav 24,176,176
Mvav 24,180,180
Mvav 24,184,184
Mvav 24,188,188
Zvav 28,176,192
Mvav 28,180,196
Mvav 28,184,200
Mvav 28,188,204
Zvav 32,176,208
Mvav 32,180,212
Mvav 32,184,216
Mvav 32,188,220
Zvav 36,176,224
Mvav 36,180,228
Mvav 36,184,232
Mvav 36,188,236
s_nop 7
v_max3_f32 v100, v100, v20, v21
v_max3_f32 v100, v100, v22, v23
v_max3_f32 v101, v101, v24, v25
v_max3_f32 v101, v101, v26, v27
v_max3_f32 v102, v102, v28, v29
v_max3_f32 v102, v102, v30, v31
v_max3_f32 v103, v103, v32, v33
v_max3_f32 v103, v103, v34, v35
v_max3_f32 v104, v104, v36, v37
v_max3_f32 v104, v104, v38, v39
s_add_u32 s56, s56, 4096
s_add_u32 s57, s57, 1
s_cmp_lt_u32 s57, 64
s_cbranch_scc1 SL1A_%=
v_mov_b32 v108, v100
v_mov_b32 v109, v101
v_mov_b32 v110, v102
v_mov_b32 v111, v103
v_mov_b32 v112, v104
s_nop 0
v_permlane32_swap_b32 v100, v108
v_permlane32_swap_b32 v101, v109
v_permlane32_swap_b32 v102, v110
v_permlane32_swap_b32 v103, v111
v_permlane32_swap_b32 v104, v112
v_max_f32 v100, v100, v108
v_max_f32 v101, v101, v109
v_max_f32 v102, v102, v110
v_max_f32 v103, v103, v111
v_max_f32 v104, v104, v112
v_mov_b32 v108, v100
v_mov_b32 v109, v101
v_mov_b32 v110, v102
v_mov_b32 v111, v103
v_mov_b32 v112, v104
s_nop 0
v_permlane16_swap_b32 v100, v108
v_permlane16_swap_b32 v101, v109
v_permlane16_swap_b32 v102, v110
v_permlane16_swap_b32 v103, v111
v_permlane16_swap_b32 v104, v112
v_max_f32 v100, v100, v108
v_max_f32 v101, v101, v109
v_max_f32 v102, v102, v110
v_max_f32 v103, v103, v111
v_max_f32 v104, v104, v112
v_mov_b32 v158, 0
v_mov_b32 v159, 0
v_mov_b32 v242, 0
v_mov_b32 v243, 0
v_mov_b32 v244, 0
v_accvgpr_write_b32 a0, 0
v_accvgpr_write_b32 a1, 0
v_accvgpr_write_b32 a2, 0
v_accvgpr_write_b32 a3, 0
v_accvgpr_write_b32 a4, 0
v_accvgpr_write_b32 a5, 0
v_accvgpr_write_b32 a6, 0
v_accvgpr_write_b32 a7, 0
v_accvgpr_write_b32 a8, 0
v_accvgpr_write_b32 a9, 0
v_accvgpr_write_b32 a10, 0
v_accvgpr_write_b32 a11, 0
v_accvgpr_write_b32 a12, 0
v_accvgpr_write_b32 a13, 0
v_accvgpr_write_b32 a14, 0
v_accvgpr_write_b32 a15, 0
v_accvgpr_write_b32 a16, 0
v_accvgpr_write_b32 a17, 0
v_accvgpr_write_b32 a18, 0
v_accvgpr_write_b32 a19, 0
v_accvgpr_write_b32 a20, 0
v_accvgpr_write_b32 a21, 0
v_accvgpr_write_b32 a22, 0
v_accvgpr_write_b32 a23, 0
v_accvgpr_write_b32 a24, 0
v_accvgpr_write_b32 a25, 0
v_accvgpr_write_b32 a26, 0
v_accvgpr_write_b32 a27, 0
v_accvgpr_write_b32 a28, 0
v_accvgpr_write_b32 a29, 0
v_accvgpr_write_b32 a30, 0
v_accvgpr_write_b32 a31, 0
v_accvgpr_write_b32 a32, 0
v_accvgpr_write_b32 a33, 0
v_accvgpr_write_b32 a34, 0
v_accvgpr_write_b32 a35, 0
v_accvgpr_write_b32 a36, 0
v_accvgpr_write_b32 a37, 0
v_accvgpr_write_b32 a38, 0
v_accvgpr_write_b32 a39, 0
v_accvgpr_write_b32 a40, 0
v_accvgpr_write_b32 a41, 0
v_accvgpr_write_b32 a42, 0
v_accvgpr_write_b32 a43, 0
v_accvgpr_write_b32 a44, 0
v_accvgpr_write_b32 a45, 0
v_accvgpr_write_b32 a46, 0
v_accvgpr_write_b32 a47, 0
v_accvgpr_write_b32 a48, 0
v_accvgpr_write_b32 a49, 0
v_accvgpr_write_b32 a50, 0
v_accvgpr_write_b32 a51, 0
v_accvgpr_write_b32 a52, 0
v_accvgpr_write_b32 a53, 0
v_accvgpr_write_b32 a54, 0
v_accvgpr_write_b32 a55, 0
v_accvgpr_write_b32 a56, 0
v_accvgpr_write_b32 a57, 0
v_accvgpr_write_b32 a58, 0
v_accvgpr_write_b32 a59, 0
v_accvgpr_write_b32 a60, 0
v_accvgpr_write_b32 a61, 0
v_accvgpr_write_b32 a62, 0
v_accvgpr_write_b32 a63, 0
v_accvgpr_write_b32 a64, 0
v_accvgpr_write_b32 a65, 0
v_accvgpr_write_b32 a66, 0
v_accvgpr_write_b32 a67, 0
v_accvgpr_write_b32 a68, 0
v_accvgpr_write_b32 a69, 0
v_accvgpr_write_b32 a70, 0
v_accvgpr_write_b32 a71, 0
v_accvgpr_write_b32 a72, 0
v_accvgpr_write_b32 a73, 0
v_accvgpr_write_b32 a74, 0
v_accvgpr_write_b32 a75, 0
v_accvgpr_write_b32 a76, 0
v_accvgpr_write_b32 a77, 0
v_accvgpr_write_b32 a78, 0
v_accvgpr_write_b32 a79, 0
v_accvgpr_write_b32 a80, 0
v_accvgpr_write_b32 a81, 0
v_accvgpr_write_b32 a82, 0
v_accvgpr_write_b32 a83, 0
v_accvgpr_write_b32 a84, 0
v_accvgpr_write_b32 a85, 0
v_accvgpr_write_b32 a86, 0
v_accvgpr_write_b32 a87, 0
v_accvgpr_write_b32 a88, 0
v_accvgpr_write_b32 a89, 0
v_accvgpr_write_b32 a90, 0
v_accvgpr_write_b32 a91, 0
v_accvgpr_write_b32 a92, 0
v_accvgpr_write_b32 a93, 0
v_accvgpr_write_b32 a94, 0
v_accvgpr_write_b32 a95, 0
v_accvgpr_write_b32 a96, 0
v_accvgpr_write_b32 a97, 0
v_accvgpr_write_b32 a98, 0
v_accvgpr_write_b32 a99, 0
v_accvgpr_write_b32 a100, 0
v_accvgpr_write_b32 a101, 0
v_accvgpr_write_b32 a102, 0
v_accvgpr_write_b32 a103, 0
v_accvgpr_write_b32 a104, 0
v_accvgpr_write_b32 a105, 0
v_accvgpr_write_b32 a106, 0
v_accvgpr_write_b32 a107, 0
v_accvgpr_write_b32 a108, 0
v_accvgpr_write_b32 a109, 0
v_accvgpr_write_b32 a110, 0
v_accvgpr_write_b32 a111, 0
v_accvgpr_write_b32 a112, 0
v_accvgpr_write_b32 a113, 0
v_accvgpr_write_b32 a114, 0
v_accvgpr_write_b32 a115, 0
v_accvgpr_write_b32 a116, 0
v_accvgpr_write_b32 a117, 0
v_accvgpr_write_b32 a118, 0
v_accvgpr_write_b32 a119, 0
v_accvgpr_write_b32 a120, 0
v_accvgpr_write_b32 a121, 0
v_accvgpr_write_b32 a122, 0
v_accvgpr_write_b32 a123, 0
v_accvgpr_write_b32 a124, 0
v_accvgpr_write_b32 a125, 0
v_accvgpr_write_b32 a126, 0
v_accvgpr_write_b32 a127, 0
v_accvgpr_write_b32 a128, 0
v_accvgpr_write_b32 a129, 0
v_accvgpr_write_b32 a130, 0
v_accvgpr_write_b32 a131, 0
v_accvgpr_write_b32 a132, 0
v_accvgpr_write_b32 a133, 0
v_accvgpr_write_b32 a134, 0
v_accvgpr_write_b32 a135, 0
v_accvgpr_write_b32 a136, 0
v_accvgpr_write_b32 a137, 0
v_accvgpr_write_b32 a138, 0
v_accvgpr_write_b32 a139, 0
v_accvgpr_write_b32 a140, 0
v_accvgpr_write_b32 a141, 0
v_accvgpr_write_b32 a142, 0
v_accvgpr_write_b32 a143, 0
v_accvgpr_write_b32 a144, 0
v_accvgpr_write_b32 a145, 0
v_accvgpr_write_b32 a146, 0
v_accvgpr_write_b32 a147, 0
v_accvgpr_write_b32 a148, 0
v_accvgpr_write_b32 a149, 0
v_accvgpr_write_b32 a150, 0
v_accvgpr_write_b32 a151, 0
v_accvgpr_write_b32 a152, 0
v_accvgpr_write_b32 a153, 0
v_accvgpr_write_b32 a154, 0
v_accvgpr_write_b32 a155, 0
v_accvgpr_write_b32 a156, 0
v_accvgpr_write_b32 a157, 0
v_accvgpr_write_b32 a158, 0
v_accvgpr_write_b32 a159, 0
s_lshl_b32 s56, s48, 18
s_add_u32 s56, s56, 1024
s_mov_b32 s57, 0
SL2A_%=:
s_mov_b32 s79, s56
buffer_load_dwordx4 a[176:179], v105, s[36:39], s56 offen offset:0
buffer_load_dwordx4 a[180:183], v105, s[36:39], s56 offen offset:64
buffer_load_dwordx4 a[184:187], v105, s[36:39], s56 offen offset:128
buffer_load_dwordx4 a[188:191], v105, s[36:39], s56 offen offset:192
s_waitcnt vmcnt(0)
Zvav 20,176,160
Mvav 20,180,164
Mvav 20,184,168
Mvav 20,188,172
Zvav 24,176,176
Mvav 24,180,180
Mvav 24,184,184
Mvav 24,188,188
Zvav 28,176,192
Mvav 28,180,196
Mvav 28,184,200
Mvav 28,188,204
Zvav 32,176,208
Mvav 32,180,212
Mvav 32,184,216
Mvav 32,188,220
Zvav 36,176,224
Mvav 36,180,228
Mvav 36,184,232
Mvav 36,188,236
s_nop 7
v_sub_f32 v20, v20, v100
v_sub_f32 v21, v21, v100
v_sub_f32 v22, v22, v100
v_sub_f32 v23, v23, v100
Ex 20
Ex 21
Ex 22
Ex 23
s_nop 0
Ad 158,20
Ad 158,21
Ad 158,22
Ad 158,23
Pk 80,20,21
Pk 81,22,23
v_sub_f32 v24, v24, v101
v_sub_f32 v25, v25, v101
v_sub_f32 v26, v26, v101
v_sub_f32 v27, v27, v101
Ex 24
Ex 25
Ex 26
Ex 27
s_nop 0
Ad 159,24
Ad 159,25
Ad 159,26
Ad 159,27
Pk 84,24,25
Pk 85,26,27
v_sub_f32 v28, v28, v102
v_sub_f32 v29, v29, v102
v_sub_f32 v30, v30, v102
v_sub_f32 v31, v31, v102
Ex 28
Ex 29
Ex 30
Ex 31
s_nop 0
Ad 242,28
Ad 242,29
Ad 242,30
Ad 242,31
Pk 88,28,29
Pk 89,30,31
v_sub_f32 v32, v32, v103
v_sub_f32 v33, v33, v103
v_sub_f32 v34, v34, v103
v_sub_f32 v35, v35, v103
Ex 32
Ex 33
Ex 34
Ex 35
s_nop 0
Ad 243,32
Ad 243,33
Ad 243,34
Ad 243,35
Pk 92,32,33
Pk 93,34,35
v_sub_f32 v36, v36, v104
v_sub_f32 v37, v37, v104
v_sub_f32 v38, v38, v104
v_sub_f32 v39, v39, v104
Ex 36
Ex 37
Ex 38
Ex 39
s_nop 0
Ad 244,36
Ad 244,37
Ad 244,38
Ad 244,39
Pk 96,36,37
Pk 97,38,39
s_add_u32 s56, s56, 4096
buffer_load_dwordx4 a[176:179], v105, s[36:39], s56 offen offset:0
buffer_load_dwordx4 a[180:183], v105, s[36:39], s56 offen offset:64
buffer_load_dwordx4 a[184:187], v105, s[36:39], s56 offen offset:128
buffer_load_dwordx4 a[188:191], v105, s[36:39], s56 offen offset:192
s_waitcnt vmcnt(0)
Zvav 20,176,160
Mvav 20,180,164
Mvav 20,184,168
Mvav 20,188,172
Zvav 24,176,176
Mvav 24,180,180
Mvav 24,184,184
)A"
R"A(Mvav 24,188,188
Zvav 28,176,192
Mvav 28,180,196
Mvav 28,184,200
Mvav 28,188,204
Zvav 32,176,208
Mvav 32,180,212
Mvav 32,184,216
Mvav 32,188,220
Zvav 36,176,224
Mvav 36,180,228
Mvav 36,184,232
Mvav 36,188,236
s_nop 7
v_sub_f32 v20, v20, v100
v_sub_f32 v21, v21, v100
v_sub_f32 v22, v22, v100
v_sub_f32 v23, v23, v100
Ex 20
Ex 21
Ex 22
Ex 23
s_nop 0
Ad 158,20
Ad 158,21
Ad 158,22
Ad 158,23
Pk 82,20,21
Pk 83,22,23
v_sub_f32 v24, v24, v101
v_sub_f32 v25, v25, v101
v_sub_f32 v26, v26, v101
v_sub_f32 v27, v27, v101
Ex 24
Ex 25
Ex 26
Ex 27
s_nop 0
Ad 159,24
Ad 159,25
Ad 159,26
Ad 159,27
Pk 86,24,25
Pk 87,26,27
v_sub_f32 v28, v28, v102
v_sub_f32 v29, v29, v102
v_sub_f32 v30, v30, v102
v_sub_f32 v31, v31, v102
Ex 28
Ex 29
Ex 30
Ex 31
s_nop 0
Ad 242,28
Ad 242,29
Ad 242,30
Ad 242,31
Pk 90,28,29
Pk 91,30,31
v_sub_f32 v32, v32, v103
v_sub_f32 v33, v33, v103
v_sub_f32 v34, v34, v103
v_sub_f32 v35, v35, v103
Ex 32
Ex 33
Ex 34
Ex 35
s_nop 0
Ad 243,32
Ad 243,33
Ad 243,34
Ad 243,35
Pk 94,32,33
Pk 95,34,35
v_sub_f32 v36, v36, v104
v_sub_f32 v37, v37, v104
v_sub_f32 v38, v38, v104
v_sub_f32 v39, v39, v104
Ex 36
Ex 37
Ex 38
Ex 39
s_nop 0
Ad 244,36
Ad 244,37
Ad 244,38
Ad 244,39
Pk 98,36,37
Pk 99,38,39
s_add_u32 s56, s56, 4096
buffer_load_ushort v108, v106, s[40:43], s79 offen offset:0
buffer_load_ushort v109, v106, s[40:43], s79 offen offset:256
buffer_load_ushort v110, v106, s[40:43], s79 offen offset:512
buffer_load_ushort v111, v106, s[40:43], s79 offen offset:768
buffer_load_ushort v112, v107, s[40:43], s79 offen offset:0
buffer_load_ushort v113, v107, s[40:43], s79 offen offset:256
buffer_load_ushort v114, v107, s[40:43], s79 offen offset:512
buffer_load_ushort v115, v107, s[40:43], s79 offen offset:768
s_waitcnt vmcnt(0)
v_lshl_or_b32 v116, v109, 16, v108
v_lshl_or_b32 v117, v111, 16, v110
v_lshl_or_b32 v118, v113, 16, v112
v_lshl_or_b32 v119, v115, 16, v114
s_nop 1
Mavv 0,116,80
Mavv 4,116,84
Mavv 8,116,88
Mavv 12,116,92
Mavv 16,116,96
buffer_load_ushort v108, v106, s[40:43], s79 offen offset:32
buffer_load_ushort v109, v106, s[40:43], s79 offen offset:288
buffer_load_ushort v110, v106, s[40:43], s79 offen offset:544
buffer_load_ushort v111, v106, s[40:43], s79 offen offset:800
buffer_load_ushort v112, v107, s[40:43], s79 offen offset:32
buffer_load_ushort v113, v107, s[40:43], s79 offen offset:288
buffer_load_ushort v114, v107, s[40:43], s79 offen offset:544
buffer_load_ushort v115, v107, s[40:43], s79 offen offset:800
s_waitcnt vmcnt(0)
v_lshl_or_b32 v116, v109, 16, v108
v_lshl_or_b32 v117, v111, 16, v110
v_lshl_or_b32 v118, v113, 16, v112
v_lshl_or_b32 v119, v115, 16, v114
s_nop 1
Mavv 20,116,80
Mavv 24,116,84
Mavv 28,116,88
Mavv 32,116,92
Mavv 36,116,96
buffer_load_ushort v108, v106, s[40:43], s79 offen offset:64
buffer_load_ushort v109, v106, s[40:43], s79 offen offset:320
buffer_load_ushort v110, v106, s[40:43], s79 offen offset:576
buffer_load_ushort v111, v106, s[40:43], s79 offen offset:832
buffer_load_ushort v112, v107, s[40:43], s79 offen offset:64
buffer_load_ushort v113, v107, s[40:43], s79 offen offset:320
buffer_load_ushort v114, v107, s[40:43], s79 offen offset:576
buffer_load_ushort v115, v107, s[40:43], s79 offen offset:832
s_waitcnt vmcnt(0)
v_lshl_or_b32 v116, v109, 16, v108
v_lshl_or_b32 v117, v111, 16, v110
v_lshl_or_b32 v118, v113, 16, v112
v_lshl_or_b32 v119, v115, 16, v114
s_nop 1
Mavv 40,116,80
Mavv 44,116,84
Mavv 48,116,88
Mavv 52,116,92
Mavv 56,116,96
buffer_load_ushort v108, v106, s[40:43], s79 offen offset:96
buffer_load_ushort v109, v106, s[40:43], s79 offen offset:352
buffer_load_ushort v110, v106, s[40:43], s79 offen offset:608
buffer_load_ushort v111, v106, s[40:43], s79 offen offset:864
buffer_load_ushort v112, v107, s[40:43], s79 offen offset:96
buffer_load_ushort v113, v107, s[40:43], s79 offen offset:352
buffer_load_ushort v114, v107, s[40:43], s79 offen offset:608
buffer_load_ushort v115, v107, s[40:43], s79 offen offset:864
s_waitcnt vmcnt(0)
v_lshl_or_b32 v116, v109, 16, v108
v_lshl_or_b32 v117, v111, 16, v110
v_lshl_or_b32 v118, v113, 16, v112
v_lshl_or_b32 v119, v115, 16, v114
s_nop 1
Mavv 60,116,80
Mavv 64,116,84
Mavv 68,116,88
Mavv 72,116,92
Mavv 76,116,96
buffer_load_ushort v108, v106, s[40:43], s79 offen offset:128
buffer_load_ushort v109, v106, s[40:43], s79 offen offset:384
buffer_load_ushort v110, v106, s[40:43], s79 offen offset:640
buffer_load_ushort v111, v106, s[40:43], s79 offen offset:896
buffer_load_ushort v112, v107, s[40:43], s79 offen offset:128
buffer_load_ushort v113, v107, s[40:43], s79 offen offset:384
buffer_load_ushort v114, v107, s[40:43], s79 offen offset:640
buffer_load_ushort v115, v107, s[40:43], s79 offen offset:896
s_waitcnt vmcnt(0)
v_lshl_or_b32 v116, v109, 16, v108
v_lshl_or_b32 v117, v111, 16, v110
v_lshl_or_b32 v118, v113, 16, v112
v_lshl_or_b32 v119, v115, 16, v114
s_nop 1
Mavv 80,116,80
Mavv 84,116,84
Mavv 88,116,88
Mavv 92,116,92
Mavv 96,116,96
buffer_load_ushort v108, v106, s[40:43], s79 offen offset:160
buffer_load_ushort v109, v106, s[40:43], s79 offen offset:416
buffer_load_ushort v110, v106, s[40:43], s79 offen offset:672
buffer_load_ushort v111, v106, s[40:43], s79 offen offset:928
buffer_load_ushort v112, v107, s[40:43], s79 offen offset:160
buffer_load_ushort v113, v107, s[40:43], s79 offen offset:416
buffer_load_ushort v114, v107, s[40:43], s79 offen offset:672
buffer_load_ushort v115, v107, s[40:43], s79 offen offset:928
s_waitcnt vmcnt(0)
v_lshl_or_b32 v116, v109, 16, v108
v_lshl_or_b32 v117, v111, 16, v110
v_lshl_or_b32 v118, v113, 16, v112
v_lshl_or_b32 v119, v115, 16, v114
s_nop 1
Mavv 100,116,80
Mavv 104,116,84
Mavv 108,116,88
Mavv 112,116,92
Mavv 116,116,96
buffer_load_ushort v108, v106, s[40:43], s79 offen offset:192
buffer_load_ushort v109, v106, s[40:43], s79 offen offset:448
buffer_load_ushort v110, v106, s[40:43], s79 offen offset:704
buffer_load_ushort v111, v106, s[40:43], s79 offen offset:960
buffer_load_ushort v112, v107, s[40:43], s79 offen offset:192
buffer_load_ushort v113, v107, s[40:43], s79 offen offset:448
buffer_load_ushort v114, v107, s[40:43], s79 offen offset:704
buffer_load_ushort v115, v107, s[40:43], s79 offen offset:960
s_waitcnt vmcnt(0)
v_lshl_or_b32 v116, v109, 16, v108
v_lshl_or_b32 v117, v111, 16, v110
v_lshl_or_b32 v118, v113, 16, v112
v_lshl_or_b32 v119, v115, 16, v114
s_nop 1
Mavv 120,116,80
Mavv 124,116,84
Mavv 128,116,88
Mavv 132,116,92
Mavv 136,116,96
buffer_load_ushort v108, v106, s[40:43], s79 offen offset:224
buffer_load_ushort v109, v106, s[40:43], s79 offen offset:480
buffer_load_ushort v110, v106, s[40:43], s79 offen offset:736
buffer_load_ushort v111, v106, s[40:43], s79 offen offset:992
buffer_load_ushort v112, v107, s[40:43], s79 offen offset:224
buffer_load_ushort v113, v107, s[40:43], s79 offen offset:480
buffer_load_ushort v114, v107, s[40:43], s79 offen offset:736
buffer_load_ushort v115, v107, s[40:43], s79 offen offset:992
s_waitcnt vmcnt(0)
v_lshl_or_b32 v116, v109, 16, v108
v_lshl_or_b32 v117, v111, 16, v110
v_lshl_or_b32 v118, v113, 16, v112
v_lshl_or_b32 v119, v115, 16, v114
s_nop 1
Mavv 140,116,80
Mavv 144,116,84
Mavv 148,116,88
Mavv 152,116,92
Mavv 156,116,96
s_add_u32 s57, s57, 1
s_cmp_lt_u32 s57, 32
s_cbranch_scc1 SL2A_%=
v_mov_b32 v108, v158
v_mov_b32 v109, v159
v_mov_b32 v110, v242
v_mov_b32 v111, v243
v_mov_b32 v112, v244
s_nop 0
v_permlane32_swap_b32 v158, v108
v_permlane32_swap_b32 v159, v109
v_permlane32_swap_b32 v242, v110
v_permlane32_swap_b32 v243, v111
v_permlane32_swap_b32 v244, v112
Ad 158,108
Ad 159,109
Ad 242,110
Ad 243,111
Ad 244,112
v_mov_b32 v108, v158
v_mov_b32 v109, v159
v_mov_b32 v110, v242
v_mov_b32 v111, v243
v_mov_b32 v112, v244
s_nop 0
v_permlane16_swap_b32 v158, v108
v_permlane16_swap_b32 v159, v109
v_permlane16_swap_b32 v242, v110
v_permlane16_swap_b32 v243, v111
v_permlane16_swap_b32 v244, v112
Ad 158,108
Ad 159,109
Ad 242,110
Ad 243,111
Ad 244,112
v_rcp_f32 v158, v158
v_rcp_f32 v159, v159
v_rcp_f32 v242, v242
v_rcp_f32 v243, v243
v_rcp_f32 v244, v244
s_nop 7
s_cmp_eq_u32 s48, 0
s_cbranch_scc1 RET0A_%=
s_branch RET1A_%=
.p2align 12
PG0_%=:
s_cbranch_execz VRET_%=
s_nop 0
WA_%=:
s_waitcnt vmcnt(9)
s_barrier
Ta 0,243,0
Ta 2,243,1024
Ta 4,243,2048
Ta 6,243,3072
Ta 8,244,0
Ta 10,244,1024
Ta 12,244,2048
Ta 14,244,3072
Ra 160,216,0
Ra 164,216,256
Ra 168,216,512
Ra 172,216,768
Ra 176,216,16512
Ra 180,216,16768
Ra 184,216,17024
Ra 188,216,17280
Wl 4
Ta 16,243,8
Ta 18,243,1032
Ta 20,243,2056
Ta 22,243,3080
Ta 24,244,8
Ta 26,244,1032
Ta 28,244,2056
Ta 30,244,3080
Zvaa 0,0,160
s_add_u32 m0, s77, 99072
s_mov_b32 s79, 65536
Ta 32,243,256
Mvaa 0,4,164
s_add_u32 s20, s20, 0x1fffff
s_addc_u32 s21, s21, 0
Ta 34,243,1280
buffer_load_dwordx4 v212, s[64:67], s79 offen offset:0 lds
Mvaa 0,8,168
s_and_b32 s20, s20, 0xffe00000
s_lshl_b32 s53, s25, 4
Ta 36,243,2304
Mvaa 0,12,172
Wl 11
s_add_u32 s56, s53, s24
Ta 38,243,3328
Zvaa 4,12,188
s_lshl_b32 s56, s56, 2
Ta 40,244,256
buffer_load_dwordx4 v213, s[64:67], s79 offen offset:1024 lds
Mvaa 4,8,184
s_add_u32 s52, s56, s76
s_lshl_b32 s56, s52, 8
Ta 42,244,1280
Mvaa 4,4,180
s_add_u32 s56, s56, 134217728
s_mov_b64 s[50:51], s[20:21]
Ta 44,244,2304
Mvaa 4,0,176
Wl 7
Ta 46,244,3328
buffer_load_dwordx4 v214, s[64:67], s79 offen offset:2080 lds
Zvaa 20,16,160
s_add_u32 s50, s50, s56
s_addc_u32 s51, s51, 0
Ta 48,243,264
Mvaa 20,20,164
s_lshl_b32 s56, s52, 17
s_mov_b64 s[96:97], s[20:21]
Ta 50,243,1288
Mvaa 20,24,168
s_add_u32 s96, s96, s56
s_addc_u32 s97, s97, 0
Ta 52,243,2312
buffer_load_dwordx4 v215, s[64:67], s79 offen offset:3104 lds
Mvaa 20,28,172
Ta 54,243,3336
v_lshlrev_b32 v200, 2, v245
Zvaa 24,28,188
s_add_u32 m0, s78, 66048
s_mov_b32 s79, 256
Ta 56,244,264
Mvaa 24,24,184
s_lshl_b32 s29, s27, 4
Ta 58,244,1288
buffer_load_dwordx4 v208, s[68:71], s79 offen offset:0 lds
Mvaa 24,20,180
s_add_u32 s29, s29, s24
s_lshl_b32 s29, s29, 19
Ta 60,244,2312
Mvaa 24,16,176
Wl 7
Ta 62,244,3336
Zvaa 40,32,160
s_add_u32 s36, s10, s29
s_addc_u32 s37, s11, 0
Ta 64,243,512
buffer_load_dwordx4 v209, s[68:71], s79 offen offset:1024 lds
Mvaa 40,36,164
s_mov_b32 s38, 525312
s_mov_b32 s39, 0x20000
Ta 66,243,1536
Mvaa 40,40,168
s_add_u32 s40, s18, s29
s_addc_u32 s41, s19, 0
Ta 68,243,2560
Mvaa 40,44,172
s_mov_b32 s42, 525312
Ta 70,243,3584
buffer_load_dwordx4 v210, s[68:71], s79 offen offset:2048 lds
Zvaa 44,44,188
)A"
R"A(s_mov_b32 s43, 0x20000
Ta 72,244,512
Mvaa 44,40,184
s_sub_u32 s36, s36, 1024
s_subb_u32 s37, s37, 0
Ta 74,244,1536
Mvaa 44,36,180
s_sub_u32 s40, s40, 1024
s_subb_u32 s41, s41, 0
Ta 76,244,2560
buffer_load_dwordx4 v211, s[68:71], s79 offen offset:3072 lds
Mvaa 44,32,176
Wl 7
s_mov_b64 s[10:11], s[90:91]
Ta 78,244,3584
Zvaa 60,48,160
s_add_u32 m0, s78, 132096
s_mov_b32 s79, 524544
Ta 80,243,520
Mvaa 60,52,164
s_and_b32 s29, s24, 3
Ta 82,243,1544
buffer_load_dwordx4 v208, s[68:71], s79 offen offset:0 lds
Mvaa 60,56,168
s_lshl_b32 s29, s29, 8
Ta 84,243,2568
Mvaa 60,60,172
s_add_u32 s72, s16, s29
s_addc_u32 s73, s17, 0
Ta 86,243,3592
Zvaa 64,60,188
s_mov_b32 s74, 4193536
Ta 88,244,520
buffer_load_dwordx4 v209, s[68:71], s79 offen offset:1024 lds
Mvaa 64,56,184
s_mov_b32 s75, 0x20000
s_lshr_b32 s44, s24, 2
Ta 90,244,1544
Mvaa 64,52,180
s_and_b32 s46, s24, 3
s_and_b32 s52, s52, 511
Ta 92,244,2568
Mvaa 64,48,176
Wl 7
Ta 94,244,3592
buffer_load_dwordx4 v210, s[68:71], s79 offen offset:2048 lds
Zvaa 80,64,160
s_lshl_b32 s52, s52, 13
s_lshl_b32 s56, s46, 8
Ta 96,243,768
Mvaa 80,68,164
s_sub_u32 s52, s52, s56
s_lshl_b32 s56, s25, 6
Ta 98,243,1792
Mvaa 80,72,168
s_add_u32 s56, s56, s76
Ta 100,243,2816
buffer_load_dwordx4 v211, s[68:71], s79 offen offset:3072 lds
Mvaa 80,76,172
s_lshl_b32 s56, s56, 8
s_add_u32 s56, s56, 134217728
Ta 102,243,3840
Zvaa 84,76,188
s_mov_b64 s[92:93], s[20:21]
Ta 104,244,768
Mvaa 84,72,184
s_add_u32 s92, s92, s56
s_addc_u32 s93, s93, 0
Ta 106,244,1792
global_load_dword v201, v200, s[50:51] sc1
Mvaa 84,68,180
s_lshl_b32 s57, s46, 2
s_add_u32 s56, s56, s57
Ta 108,244,2816
Mvaa 84,64,176
Wl 7
s_add_u32 s56, s56, 16
Ta 110,244,3840
Zvaa 100,80,160
s_mov_b64 s[94:95], s[20:21]
Ta 112,243,776
Mvaa 100,84,164
s_add_u32 s94, s94, s56
s_addc_u32 s95, s95, 0
Ta 114,243,1800
Mvaa 100,88,168
s_lshl_b32 s34, 0x100000, s46
s_or_b32 s34, s34, 0xf0
Ta 116,243,2824
Mvaa 100,92,172
s_lshl_b32 s35, 0x100010, s46
s_mul_i32 s45, s76, 4128
Ta 118,243,3848
Zvaa 104,92,188
s_lshl_b32 s49, s24, 18
s_lshl_b32 s56, s76, 14
Ta 120,244,776
Mvaa 104,88,184
s_add_u32 s49, s49, s56
s_lshl_b32 s59, s44, 12
Ta 122,244,1800
Mvaa 104,84,180
s_lshl_b32 s53, s25, 6
s_lshl_b32 s56, s44, 4
Ta 124,244,2824
Mvaa 104,80,176
Wl 7
s_add_u32 s56, s56, s76
Ta 126,244,3848
Zvaa 120,96,160
s_add_u32 s56, s56, s53
s_lshl_b32 s56, s56, 17
s_mov_b64 s[84:85], s[20:21]
Mvaa 120,100,164
s_add_u32 s84, s84, s56
s_addc_u32 s85, s85, 0
s_mov_b32 s98, 73728
Mvaa 120,104,168
s_mov_b32 s99, 0x20000
s_mov_b32 s86, 5898240
s_mov_b32 s87, 0x20000
Mvaa 120,108,172
s_mul_i32 s30, s27, 304
s_mul_i32 s56, s76, 80
s_add_u32 s30, s30, s56
Zvaa 124,108,188
s_waitcnt vmcnt(9)
s_lshl_b32 s30, s30, 11
Ra 192,217,0
Mvaa 124,104,184
s_lshl_b32 s56, s28, 10
s_add_u32 s30, s30, s56
Ra 196,217,256
Mvaa 124,100,180
s_add_u32 s30, s30, s29
s_lshl_b32 s56, s44, 6
Ra 200,217,512
Mvaa 124,96,176
Wl 3
s_add_u32 s30, s30, s56
Ra 204,217,768
Zvaa 140,112,160
s_add_u32 s88, s22, s30
s_addc_u32 s89, s23, 0
Ra 208,216,33024
Mvaa 140,116,164
s_mov_b32 s90, 0
s_mov_b32 s91, 0x20000
Ra 212,216,33280
Mvaa 140,120,168
s_cmp_eq_u32 s76, 3
s_cselect_b32 s29, 0x40000000, 0
s_mov_b32 s56, 161856
s_cselect_b32 s90, 129088, s56
Ra 216,216,33536
Mvaa 140,124,172
s_mov_b32 s82, 0x3e0293ee
s_mov_b32 s83, 0x3e0293ee
Ra 220,216,33792
Zvaa 144,124,188
s_lshl_b32 s80, s76, 12
s_lshr_b32 s56, s76, 1
Ra 224,217,16512
Mvaa 144,120,184
s_lshl_b32 s56, s56, 5
s_add_u32 s80, s80, s56
Ra 228,217,16768
v_lshl_add_u32 v203, v204, 15, v207
Mvaa 144,116,180
Ra 232,217,17024
v_lshrrev_b32 v202, 1, v204
Mvaa 144,112,176
Wl 7
Ra 236,217,17280
v_lshl_add_u32 v203, v202, 9, v203
Zvaa 8,0,192
s_add_u32 m0, s77, 0
s_mov_b32 s79, 256
v_xor_b32 v248, 0, v203
v_add_u32 v248, 1024, v248
Mvaa 8,4,196
buffer_load_dwordx4 v212, s[64:67], s79 offen offset:0 lds
v_xor_b32 v249, 512, v203
Mvaa 8,8,200
v_add_u32 v249, 1024, v249
v_xor_b32 v250, 16384, v203
Mvaa 8,12,204
Wl 4
v_add_u32 v250, 1024, v250
v_xor_b32 v251, 16896, v203
Zvaa 12,12,220
buffer_load_dwordx4 v213, s[64:67], s79 offen offset:1024 lds
v_add_u32 v251, 1024, v251
Mvaa 12,8,216
s_and_b32 s56, s76, 1
s_lshl_b32 s56, s56, 9
s_lshr_b32 s57, s76, 1
Mvaa 12,4,212
s_lshl_b32 s57, s57, 14
s_add_u32 s56, s56, s57
v_xor_b32 v242, s56, v203
v_add_u32 v241, 256, v242
Mvaa 12,0,208
Wl 0
buffer_load_dwordx4 v214, s[64:67], s79 offen offset:2080 lds
v_add_u32 v242, 1024, v242
Zvaa 16,0,224
v_lshlrev_b32 v219, 4, v245
Mvaa 16,4,228
s_lshr_b32 s26, s26, 9
s_lshl_b32 s26, s26, 7
Mvaa 16,8,232
buffer_load_dwordx4 v215, s[64:67], s79 offen offset:3104 lds
Mvaa 16,12,236
Zvaa 28,16,192
Mvaa 28,20,196
Mvaa 28,24,200
Mvaa 28,28,204
Zvaa 32,28,220
Mvaa 32,24,216
Mvaa 32,20,212
Mvaa 32,16,208
Zvaa 36,16,224
Mvaa 36,20,228
Mvaa 36,24,232
Mvaa 36,28,236
Zvaa 48,32,192
s_add_u32 m0, s77, 16512
s_mov_b32 s79, 16640
Mvaa 48,36,196
buffer_load_dwordx4 v212, s[64:67], s79 offen offset:0 lds
Mvaa 48,40,200
Mvaa 48,44,204
Zvaa 52,44,220
buffer_load_dwordx4 v213, s[64:67], s79 offen offset:1024 lds
Mvaa 52,40,216
Mvaa 52,36,212
Mvaa 52,32,208
buffer_load_dwordx4 v214, s[64:67], s79 offen offset:2080 lds
Zvaa 56,32,224
Mvaa 56,36,228
Mvaa 56,40,232
buffer_load_dwordx4 v215, s[64:67], s79 offen offset:3104 lds
Mvaa 56,44,236
Zvaa 68,48,192
Mvaa 68,52,196
Mvaa 68,56,200
Mvaa 68,60,204
Zvaa 72,60,220
Mvaa 72,56,216
Mvaa 72,52,212
Mvaa 72,48,208
Zvaa 76,48,224
Mvaa 76,52,228
Mvaa 76,56,232
Mvaa 76,60,236
Zvaa 88,64,192
s_add_u32 m0, s77, 82560
s_mov_b32 s79, 33024
Mvaa 88,68,196
buffer_load_dwordx4 v212, s[64:67], s79 offen offset:0 lds
Mvaa 88,72,200
Mvaa 88,76,204
Zvaa 92,76,220
buffer_load_dwordx4 v213, s[64:67], s79 offen offset:1024 lds
Mvaa 92,72,216
Mvaa 92,68,212
Mvaa 92,64,208
buffer_load_dwordx4 v214, s[64:67], s79 offen offset:2080 lds
Zvaa 96,64,224
Mvaa 96,68,228
Mvaa 96,72,232
buffer_load_dwordx4 v215, s[64:67], s79 offen offset:3104 lds
Mvaa 96,76,236
Zvaa 108,80,192
Mvaa 108,84,196
Mvaa 108,88,200
Mvaa 108,92,204
Zvaa 112,92,220
Mvaa 112,88,216
Mvaa 112,84,212
Mvaa 112,80,208
Zvaa 116,80,224
Mvaa 116,84,228
Mvaa 116,88,232
Mvaa 116,92,236
Zvaa 128,96,192
s_waitcnt vmcnt(12)
s_barrier
s_add_u32 m0, s78, 49536
s_mov_b32 s79, 1048576
Ta 240,243,16512
Mvaa 128,100,196
Ta 242,243,17536
buffer_load_dwordx4 v208, s[68:71], s79 offen offset:0 lds
Mvaa 128,104,200
Ta 244,243,18560
Mvaa 128,108,204
Ta 246,243,19584
Zvaa 132,108,220
Ta 248,244,16512
buffer_load_dwordx4 v209, s[68:71], s79 offen offset:1024 lds
Mvaa 132,104,216
Ta 250,244,17536
Mvaa 132,100,212
Ta 252,244,18560
Mvaa 132,96,208
Ta 254,244,19584
buffer_load_dwordx4 v210, s[68:71], s79 offen offset:2048 lds
Zvaa 136,96,224
Mvaa 136,100,228
Mvaa 136,104,232
buffer_load_dwordx4 v211, s[68:71], s79 offen offset:3072 lds
Mvaa 136,108,236
Zvaa 148,112,192
s_add_u32 m0, s78, 115584
s_mov_b32 s79, 1572864
Mvaa 148,116,196
buffer_load_dwordx4 v208, s[68:71], s79 offen offset:0 lds
Mvaa 148,120,200
Mvaa 148,124,204
Zvaa 152,124,220
buffer_load_dwordx4 v209, s[68:71], s79 offen offset:1024 lds
Mvaa 152,120,216
Mvaa 152,116,212
Mvaa 152,112,208
buffer_load_dwordx4 v210, s[68:71], s79 offen offset:2048 lds
Zvaa 156,112,224
Mvaa 156,116,228
Mvaa 156,120,232
buffer_load_dwordx4 v211, s[68:71], s79 offen offset:3072 lds
Mvaa 156,124,236
Wl 0
v_readfirstlane_b32 s81, v201
v_and_b32 v200, 0xc0, v200
v_and_b32 v201, 0x7fffffff, v201
NONCEA_%=:
s_add_u32 s81, s81, 1
s_bitset0_b32 s81, 31
v_cmp_eq_u32 vcc, s81, v201
s_nop 0
s_cmp_lg_u64 vcc, 0
s_cbranch_scc1 NONCEA_%=
v_mov_b32 v201, s81
global_store_dword v200, v201, s[50:51] sc1
Zaaa 0,240,160
Maaa 0,244,164
s_add_u32 m0, s77, 33024
Tv 224,243,16520
Maaa 0,248,168
s_mov_b32 s79, 49408
Tv 226,243,17544
Maaa 0,252,172
Tv 228,243,18568
buffer_load_dwordx4 v212, s[64:67], s79 offen offset:0 lds
Zaaa 4,252,188
Tv 230,243,19592
Maaa 4,248,184
Tv 232,244,16520
Maaa 4,244,180
Tv 234,244,17544
Maaa 4,240,176
Tv 236,244,18568
Zaaa 8,240,192
Tv 238,244,19592
buffer_load_dwordx4 v213, s[64:67], s79 offen offset:1024 lds
Maaa 8,244,196
Maaa 8,248,200
Maaa 8,252,204
Zaaa 12,252,220
Maaa 12,248,216
buffer_load_dwordx4 v214, s[64:67], s79 offen offset:2080 lds
Maaa 12,244,212
Maaa 12,240,208
Zaaa 16,240,224
Maaa 16,244,228
Maaa 16,248,232
buffer_load_dwordx4 v215, s[64:67], s79 offen offset:3104 lds
Maaa 16,252,236
Wl 0
Zava 20,224,160
Mava 20,228,164
s_add_u32 m0, s77, 99072
Ta 240,243,16768
Mava 20,232,168
s_mov_b32 s79, 65792
Ta 242,243,17792
Mava 20,236,172
Ta 244,243,18816
buffer_load_dwordx4 v212, s[64:67], s79 offen offset:0 lds
Zava 24,236,188
Ta 246,243,19840
Mava 24,232,184
Ta 248,244,16768
Mava 24,228,180
Ta 250,244,17792
Mava 24,224,176
Ta 252,244,18816
Zava 28,224,192
Ta 254,244,19840
)A"
R"A(buffer_load_dwordx4 v213, s[64:67], s79 offen offset:1024 lds
Mava 28,228,196
Mava 28,232,200
Mava 28,236,204
Zava 32,236,220
Mava 32,232,216
buffer_load_dwordx4 v214, s[64:67], s79 offen offset:2080 lds
Mava 32,228,212
Mava 32,224,208
Zava 36,224,224
Mava 36,228,228
Mava 36,232,232
buffer_load_dwordx4 v215, s[64:67], s79 offen offset:3104 lds
Mava 36,236,236
Wl 0
Zaaa 40,240,160
Maaa 40,244,164
Tv 224,243,16776
Maaa 40,248,168
Tv 226,243,17800
Maaa 40,252,172
Tv 228,243,18824
Zaaa 44,252,188
Tv 230,243,19848
Maaa 44,248,184
Tv 232,244,16776
Maaa 44,244,180
Tv 234,244,17800
Maaa 44,240,176
Tv 236,244,18824
Zaaa 48,240,192
Tv 238,244,19848
Maaa 48,244,196
Maaa 48,248,200
buffer_load_dword v220, v219, s[72:75], s52 offen
Maaa 48,252,204
s_add_u32 s52, s52, 1024
Zaaa 52,252,220
Maaa 52,248,216
Maaa 52,244,212
Maaa 52,240,208
Zaaa 56,240,224
Maaa 56,244,228
Maaa 56,248,232
Maaa 56,252,236
Wl 0
Zava 60,224,160
Mava 60,228,164
Ta 240,243,17024
Mava 60,232,168
Ta 242,243,18048
Mava 60,236,172
Ta 244,243,19072
Zava 64,236,188
Ta 246,243,20096
Mava 64,232,184
Ta 248,244,17024
Mava 64,228,180
Ta 250,244,18048
Mava 64,224,176
.p2align 12
.org PG0_%=+4096
s_cbranch_execz VRET_%=
s_nop 0
Ta 252,244,19072
Zava 68,224,192
Ta 254,244,20096
Mava 68,228,196
Mava 68,232,200
Mava 68,236,204
Zava 72,236,220
Mava 72,232,216
Mava 72,228,212
Mava 72,224,208
Zava 76,224,224
Mava 76,228,228
Mava 76,232,232
Mava 76,236,236
Wl 0
Zaaa 80,240,160
Maaa 80,244,164
Tv 224,243,17032
Maaa 80,248,168
Tv 226,243,18056
Maaa 80,252,172
Tv 228,243,19080
Zaaa 84,252,188
Tv 230,243,20104
Maaa 84,248,184
Tv 232,244,17032
Maaa 84,244,180
Tv 234,244,18056
Maaa 84,240,176
Tv 236,244,19080
Zaaa 88,240,192
Tv 238,244,20104
Maaa 88,244,196
Maaa 88,248,200
Maaa 88,252,204
Zaaa 92,252,220
Maaa 92,248,216
Maaa 92,244,212
Maaa 92,240,208
Zaaa 96,240,224
Maaa 96,244,228
Maaa 96,248,232
Maaa 96,252,236
Wl 0
Zava 100,224,160
Mava 100,228,164
Ta 240,243,17280
Mava 100,232,168
Ta 242,243,18304
Mava 100,236,172
Ta 244,243,19328
Zava 104,236,188
Ta 246,243,20352
Mava 104,232,184
Ta 248,244,17280
Mava 104,228,180
Ta 250,244,18304
Mava 104,224,176
Ta 252,244,19328
Zava 108,224,192
Ta 254,244,20352
Mava 108,228,196
Mava 108,232,200
Mava 108,236,204
Zava 112,236,220
Mava 112,232,216
Mava 112,228,212
Mava 112,224,208
Zava 116,224,224
Mava 116,228,228
Mava 116,232,232
Mava 116,236,236
Wl 0
Zaaa 120,240,160
Maaa 120,244,164
Tv 224,243,17288
Maaa 120,248,168
Tv 226,243,18312
Maaa 120,252,172
Tv 228,243,19336
Zaaa 124,252,188
Tv 230,243,20360
Maaa 124,248,184
Tv 232,244,17288
Maaa 124,244,180
Tv 234,244,18312
Maaa 124,240,176
Tv 236,244,19336
Zaaa 128,240,192
Tv 238,244,20360
Maaa 128,244,196
Maaa 128,248,200
buffer_load_dword v220, v219, s[72:75], s52 offen
Maaa 128,252,204
s_add_u32 s52, s52, 1024
Zaaa 132,252,220
Maaa 132,248,216
Maaa 132,244,212
Maaa 132,240,208
Zaaa 136,240,224
Maaa 136,244,228
Maaa 136,248,232
Maaa 136,252,236
Wl 0
Zava 140,224,160
s_waitcnt vmcnt(2)
s_barrier
Mava 140,228,164
s_add_u32 m0, s78, 66048
Ta 240,243,0
Mava 140,232,168
s_mov_b32 s79, 1048832
Ta 242,243,1024
Mava 140,236,172
Ta 244,243,2048
buffer_load_dwordx4 v208, s[68:71], s79 offen offset:0 lds
Zava 144,236,188
Ta 246,243,3072
Ra 160,216,0
Mava 144,232,184
Ta 248,244,0
Ra 164,216,256
Mava 144,228,180
Ta 250,244,1024
Ra 168,216,512
Mava 144,224,176
Ta 252,244,2048
Ra 172,216,768
Zava 148,224,192
Ta 254,244,3072
Ra 176,216,16512
buffer_load_dwordx4 v209, s[68:71], s79 offen offset:1024 lds
Mava 148,228,196
Ra 180,216,16768
Mava 148,232,200
Ra 184,216,17024
Mava 148,236,204
Ra 188,216,17280
Zava 152,236,220
Ra 192,217,0
Mava 152,232,216
Ra 196,217,256
buffer_load_dwordx4 v210, s[68:71], s79 offen offset:2048 lds
Mava 152,228,212
Ra 200,217,512
Mava 152,224,208
Ra 204,217,768
Zava 156,224,224
Ra 208,216,33024
Mava 156,228,228
Ra 212,216,33280
Mava 156,232,232
Ra 216,216,33536
buffer_load_dwordx4 v211, s[68:71], s79 offen offset:3072 lds
Mava 156,236,236
Wl 11
Ra 220,216,33792
Mvaa 0,240,160
Ra 224,217,16512
Mvaa 0,244,164
s_add_u32 m0, s78, 132096
Tv 224,243,8
Ra 228,217,16768
Mvaa 0,248,168
s_mov_b32 s79, 1573120
Tv 226,243,1032
Ra 232,217,17024
Mvaa 0,252,172
Wl 13
Tv 228,243,2056
Ra 236,217,17280
buffer_load_dwordx4 v208, s[68:71], s79 offen offset:0 lds
Mvaa 4,252,188
Tv 230,243,3080
Mvaa 4,248,184
Tv 232,244,8
Mvaa 4,244,180
Tv 234,244,1032
Mvaa 4,240,176
Wl 14
Tv 236,244,2056
Mvaa 8,240,192
Tv 238,244,3080
buffer_load_dwordx4 v209, s[68:71], s79 offen offset:1024 lds
Mvaa 8,244,196
Mvaa 8,248,200
Mvaa 8,252,204
Wl 12
Mvaa 12,252,220
Mvaa 12,248,216
buffer_load_dwordx4 v210, s[68:71], s79 offen offset:2048 lds
Mvaa 12,244,212
Mvaa 12,240,208
Wl 5
Mvaa 16,240,224
Mvaa 16,244,228
Mvaa 16,248,232
buffer_load_dwordx4 v211, s[68:71], s79 offen offset:3072 lds
Mvaa 16,252,236
Wl 0
Mvva 20,224,160
Mvva 20,228,164
s_add_u32 m0, s77, 0
Ta 240,243,256
Mvva 20,232,168
s_mov_b32 s79, 512
Ta 242,243,1280
Mvva 20,236,172
Ta 244,243,2304
buffer_load_dwordx4 v212, s[64:67], s79 offen offset:0 lds
Mvva 24,236,188
Ta 246,243,3328
Mvva 24,232,184
Ta 248,244,256
Mvva 24,228,180
Ta 250,244,1280
Mvva 24,224,176
Ta 252,244,2304
Mvva 28,224,192
Ta 254,244,3328
buffer_load_dwordx4 v213, s[64:67], s79 offen offset:1024 lds
Mvva 28,228,196
Mvva 28,232,200
Mvva 28,236,204
Mvva 32,236,220
Mvva 32,232,216
buffer_load_dwordx4 v214, s[64:67], s79 offen offset:2080 lds
Mvva 32,228,212
Mvva 32,224,208
Mvva 36,224,224
Mvva 36,228,228
Mvva 36,232,232
buffer_load_dwordx4 v215, s[64:67], s79 offen offset:3104 lds
Mvva 36,236,236
Wl 0
Mvaa 40,240,160
Mvaa 40,244,164
Tv 224,243,264
Mvaa 40,248,168
Tv 226,243,1288
Mvaa 40,252,172
Tv 228,243,2312
Mvaa 44,252,188
Tv 230,243,3336
Mvaa 44,248,184
Tv 232,244,264
Mvaa 44,244,180
Tv 234,244,1288
Mvaa 44,240,176
Tv 236,244,2312
Mvaa 48,240,192
Tv 238,244,3336
Mvaa 48,244,196
Mvaa 48,248,200
Mvaa 48,252,204
Mvaa 52,252,220
Mvaa 52,248,216
Mvaa 52,244,212
Mvaa 52,240,208
Mvaa 56,240,224
Mvaa 56,244,228
Mvaa 56,248,232
Mvaa 56,252,236
Wl 0
Mvva 60,224,160
Mvva 60,228,164
s_add_u32 m0, s77, 16512
Ta 240,243,512
Mvva 60,232,168
s_mov_b32 s79, 16896
Ta 242,243,1536
Mvva 60,236,172
Ta 244,243,2560
buffer_load_dwordx4 v212, s[64:67], s79 offen offset:0 lds
Mvva 64,236,188
Ta 246,243,3584
Mvva 64,232,184
Ta 248,244,512
Mvva 64,228,180
Ta 250,244,1536
Mvva 64,224,176
Ta 252,244,2560
Mvva 68,224,192
Ta 254,244,3584
buffer_load_dwordx4 v213, s[64:67], s79 offen offset:1024 lds
Mvva 68,228,196
Mvva 68,232,200
Mvva 68,236,204
Mvva 72,236,220
Mvva 72,232,216
buffer_load_dwordx4 v214, s[64:67], s79 offen offset:2080 lds
Mvva 72,228,212
Mvva 72,224,208
Mvva 76,224,224
Mvva 76,228,228
Mvva 76,232,232
buffer_load_dwordx4 v215, s[64:67], s79 offen offset:3104 lds
Mvva 76,236,236
Wl 0
Mvaa 80,240,160
Mvaa 80,244,164
Tv 224,243,520
Mvaa 80,248,168
Tv 226,243,1544
Mvaa 80,252,172
Tv 228,243,2568
Mvaa 84,252,188
Tv 230,243,3592
Mvaa 84,248,184
Tv 232,244,520
Mvaa 84,244,180
Tv 234,244,1544
Mvaa 84,240,176
Tv 236,244,2568
Mvaa 88,240,192
Tv 238,244,3592
Mvaa 88,244,196
Mvaa 88,248,200
Mvaa 88,252,204
Mvaa 92,252,220
Mvaa 92,248,216
Mvaa 92,244,212
Mvaa 92,240,208
Mvaa 96,240,224
Mvaa 96,244,228
Mvaa 96,248,232
Mvaa 96,252,236
Wl 0
Mvva 100,224,160
Mvva 100,228,164
s_add_u32 m0, s77, 82560
Ta 240,243,768
Mvva 100,232,168
s_mov_b32 s79, 33280
Ta 242,243,1792
Mvva 100,236,172
)A"
R"A(Ta 244,243,2816
buffer_load_dwordx4 v212, s[64:67], s79 offen offset:0 lds
Mvva 104,236,188
Ta 246,243,3840
Mvva 104,232,184
Ta 248,244,768
Mvva 104,228,180
Ta 250,244,1792
Mvva 104,224,176
Ta 252,244,2816
Mvva 108,224,192
Ta 254,244,3840
buffer_load_dwordx4 v213, s[64:67], s79 offen offset:1024 lds
Mvva 108,228,196
Mvva 108,232,200
Mvva 108,236,204
Mvva 112,236,220
Mvva 112,232,216
buffer_load_dwordx4 v214, s[64:67], s79 offen offset:2080 lds
Mvva 112,228,212
Mvva 112,224,208
Mvva 116,224,224
Mvva 116,228,228
Mvva 116,232,232
buffer_load_dwordx4 v215, s[64:67], s79 offen offset:3104 lds
Mvva 116,236,236
Wl 0
Mvaa 120,240,160
Mvaa 120,244,164
Tv 224,243,776
Mvaa 120,248,168
Tv 226,243,1800
Mvaa 120,252,172
Tv 228,243,2824
Mvaa 124,252,188
Tv 230,243,3848
Mvaa 124,248,184
Tv 232,244,776
Mvaa 124,244,180
Tv 234,244,1800
Mvaa 124,240,176
Tv 236,244,2824
Mvaa 128,240,192
Tv 238,244,3848
Mvaa 128,244,196
Mvaa 128,248,200
buffer_load_dword v220, v219, s[72:75], s52 offen
Mvaa 128,252,204
s_add_u32 s52, s52, 1024
Mvaa 132,252,220
Mvaa 132,248,216
Mvaa 132,244,212
Mvaa 132,240,208
Mvaa 136,240,224
Mvaa 136,244,228
Mvaa 136,248,232
Mvaa 136,252,236
Wl 0
Mvva 140,224,160
s_waitcnt vmcnt(13)
s_barrier
Mvva 140,228,164
s_add_u32 m0, s78, 49536
Ta 240,243,16512
Mvva 140,232,168
s_mov_b32 s79, 2097152
Ta 242,243,17536
Mvva 140,236,172
Ta 244,243,18560
buffer_load_dwordx4 v208, s[68:71], s79 offen offset:0 lds
Mvva 144,236,188
Ta 246,243,19584
Mvva 144,232,184
Ta 248,244,16512
Mvva 144,228,180
Ta 250,244,17536
Mvva 144,224,176
Ta 252,244,18560
Mvva 148,224,192
Ta 254,244,19584
buffer_load_dwordx4 v209, s[68:71], s79 offen offset:1024 lds
Mvva 148,228,196
Mvva 148,232,200
Mvva 148,236,204
Mvva 152,236,220
Mvva 152,232,216
buffer_load_dwordx4 v210, s[68:71], s79 offen offset:2048 lds
Mvva 152,228,212
Mvva 152,224,208
Mvva 156,224,224
Mvva 156,228,228
Mvva 156,232,232
buffer_load_dwordx4 v211, s[68:71], s79 offen offset:3072 lds
Mvva 156,236,236
Wl 0
Maaa 0,240,160
Maaa 0,244,164
s_add_u32 m0, s78, 115584
Tv 224,243,16520
Maaa 0,248,168
s_mov_b32 s79, 2621440
Tv 226,243,17544
Maaa 0,252,172
Tv 228,243,18568
buffer_load_dwordx4 v208, s[68:71], s79 offen offset:0 lds
Maaa 4,252,188
Tv 230,243,19592
Maaa 4,248,184
Tv 232,244,16520
Maaa 4,244,180
Tv 234,244,17544
Maaa 4,240,176
Tv 236,244,18568
Maaa 8,240,192
Tv 238,244,19592
buffer_load_dwordx4 v209, s[68:71], s79 offen offset:1024 lds
Maaa 8,244,196
Maaa 8,248,200
Maaa 8,252,204
Maaa 12,252,220
Maaa 12,248,216
buffer_load_dwordx4 v210, s[68:71], s79 offen offset:2048 lds
Maaa 12,244,212
Maaa 12,240,208
Maaa 16,240,224
Maaa 16,244,228
Maaa 16,248,232
buffer_load_dwordx4 v211, s[68:71], s79 offen offset:3072 lds
Maaa 16,252,236
Wl 0
Mava 20,224,160
Mava 20,228,164
s_add_u32 m0, s77, 33024
Ta 240,243,16768
Mava 20,232,168
s_mov_b32 s79, 49664
Ta 242,243,17792
Mava 20,236,172
Ta 244,243,18816
buffer_load_dwordx4 v212, s[64:67], s79 offen offset:0 lds
Mava 24,236,188
Ta 246,243,19840
Mava 24,232,184
Ta 248,244,16768
Mava 24,228,180
Ta 250,244,17792
Mava 24,224,176
Ta 252,244,18816
Mava 28,224,192
Ta 254,244,19840
buffer_load_dwordx4 v213, s[64:67], s79 offen offset:1024 lds
Mava 28,228,196
Mava 28,232,200
Mava 28,236,204
Mava 32,236,220
Mava 32,232,216
buffer_load_dwordx4 v214, s[64:67], s79 offen offset:2080 lds
Mava 32,228,212
Mava 32,224,208
Mava 36,224,224
Mava 36,228,228
Mava 36,232,232
buffer_load_dwordx4 v215, s[64:67], s79 offen offset:3104 lds
Mava 36,236,236
Wl 0
Maaa 40,240,160
Maaa 40,244,164
s_add_u32 m0, s77, 99072
Tv 224,243,16776
Maaa 40,248,168
s_mov_b32 s79, 66048
Tv 226,243,17800
Maaa 40,252,172
Tv 228,243,18824
buffer_load_dwordx4 v212, s[64:67], s79 offen offset:0 lds
Maaa 44,252,188
Tv 230,243,19848
Maaa 44,248,184
Tv 232,244,16776
Maaa 44,244,180
Tv 234,244,17800
Maaa 44,240,176
Tv 236,244,18824
Maaa 48,240,192
Tv 238,244,19848
buffer_load_dwordx4 v213, s[64:67], s79 offen offset:1024 lds
Maaa 48,244,196
.p2align 12
.org PG0_%=+8192
s_cbranch_execz VRET_%=
s_nop 0
Maaa 48,248,200
buffer_load_dword v220, v219, s[72:75], s52 offen
Maaa 48,252,204
s_add_u32 s52, s52, 1024
Maaa 52,252,220
Maaa 52,248,216
buffer_load_dwordx4 v214, s[64:67], s79 offen offset:2080 lds
Maaa 52,244,212
Maaa 52,240,208
Maaa 56,240,224
Maaa 56,244,228
Maaa 56,248,232
buffer_load_dwordx4 v215, s[64:67], s79 offen offset:3104 lds
Maaa 56,252,236
Wl 0
Mava 60,224,160
Mava 60,228,164
Ta 240,243,17024
Mava 60,232,168
Ta 242,243,18048
Mava 60,236,172
Ta 244,243,19072
Mava 64,236,188
Ta 246,243,20096
Mava 64,232,184
Ta 248,244,17024
Mava 64,228,180
Ta 250,244,18048
Mava 64,224,176
Ta 252,244,19072
Mava 68,224,192
Ta 254,244,20096
Mava 68,228,196
Mava 68,232,200
Mava 68,236,204
Mava 72,236,220
Mava 72,232,216
Mava 72,228,212
Mava 72,224,208
Mava 76,224,224
Mava 76,228,228
Mava 76,232,232
Mava 76,236,236
Wl 0
Maaa 80,240,160
Maaa 80,244,164
Tv 224,243,17032
Maaa 80,248,168
Tv 226,243,18056
Maaa 80,252,172
Tv 228,243,19080
Maaa 84,252,188
Tv 230,243,20104
Maaa 84,248,184
Tv 232,244,17032
Maaa 84,244,180
Tv 234,244,18056
Maaa 84,240,176
Tv 236,244,19080
Maaa 88,240,192
Tv 238,244,20104
Maaa 88,244,196
Maaa 88,248,200
Maaa 88,252,204
Maaa 92,252,220
Maaa 92,248,216
Maaa 92,244,212
Maaa 92,240,208
Maaa 96,240,224
Maaa 96,244,228
Maaa 96,248,232
Maaa 96,252,236
Wl 0
Mava 100,224,160
Mava 100,228,164
Ta 240,243,17280
Mava 100,232,168
Ta 242,243,18304
Mava 100,236,172
Ta 244,243,19328
Mava 104,236,188
Ta 246,243,20352
Mava 104,232,184
Ta 248,244,17280
Mava 104,228,180
Ta 250,244,18304
Mava 104,224,176
Ta 252,244,19328
Mava 108,224,192
Ta 254,244,20352
Mava 108,228,196
Mava 108,232,200
Mava 108,236,204
Mava 112,236,220
Mava 112,232,216
Mava 112,228,212
Mava 112,224,208
Mava 116,224,224
Mava 116,228,228
Mava 116,232,232
Mava 116,236,236
Wl 0
Maaa 120,240,160
Maaa 120,244,164
Tv 224,243,17288
Maaa 120,248,168
Tv 226,243,18312
Maaa 120,252,172
Tv 228,243,19336
Maaa 124,252,188
Tv 230,243,20360
Maaa 124,248,184
Tv 232,244,17288
Maaa 124,244,180
Tv 234,244,18312
Maaa 124,240,176
Tv 236,244,19336
Maaa 128,240,192
Tv 238,244,20360
Maaa 128,244,196
Maaa 128,248,200
buffer_load_dword v220, v219, s[72:75], s52 offen
Maaa 128,252,204
s_add_u32 s52, s52, 1024
Maaa 132,252,220
Maaa 132,248,216
Maaa 132,244,212
Maaa 132,240,208
Maaa 136,240,224
Maaa 136,244,228
Maaa 136,248,232
Maaa 136,252,236
Wl 0
Mava 140,224,160
s_waitcnt vmcnt(1)
s_barrier
Mava 140,228,164
s_add_u32 m0, s78, 66048
Ta 240,243,0
Mava 140,232,168
s_mov_b32 s79, 2097408
Ta 242,243,1024
Mava 140,236,172
Ta 244,243,2048
buffer_load_dwordx4 v208, s[68:71], s79 offen offset:0 lds
Mava 144,236,188
Ta 246,243,3072
Ra 160,216,0
Mava 144,232,184
Ta 248,244,0
Ra 164,216,256
Mava 144,228,180
Ta 250,244,1024
Ra 168,216,512
Mava 144,224,176
Ta 252,244,2048
Ra 172,216,768
Mava 148,224,192
Ta 254,244,3072
Ra 176,216,16512
buffer_load_dwordx4 v209, s[68:71], s79 offen offset:1024 lds
Mava 148,228,196
Ra 180,216,16768
Mava 148,232,200
Ra 184,216,17024
Mava 148,236,204
Ra 188,216,17280
Mava 152,236,220
Ra 192,217,0
Mava 152,232,216
Ra 196,217,256
buffer_load_dwordx4 v210, s[68:71], s79 offen offset:2048 lds
Mava 152,228,212
Ra 200,217,512
Mava 152,224,208
Ra 204,217,768
Mava 156,224,224
Ra 208,216,33024
Mava 156,228,228
Ra 212,216,33280
Mava 156,232,232
Ra 216,216,33536
buffer_load_dwordx4 v211, s[68:71], s79 offen offset:3072 lds
Mava 156,236,236
Wl 11
Ra 220,216,33792
Mvaa 0,240,160
Ra 224,217,16512
Mvaa 0,244,164
s_add_u32 m0, s78, 132096
Tv 224,243,8
Ra 228,217,16768
Mvaa 0,248,168
s_mov_b32 s79, 2621696
Tv 226,243,1032
Ra 232,217,17024
Mvaa 0,252,172
Wl 13
Tv 228,243,2056
Ra 236,217,17280
buffer_load_dwordx4 v208, s[68:71], s79 offen offset:0 lds
Mvaa 4,252,188
Tv 230,243,3080
Mvaa 4,248,184
Tv 232,244,8
Mvaa 4,244,180
Tv 234,244,1032
Mvaa 4,240,176
Wl 14
)A"
R"A(Tv 236,244,2056
Mvaa 8,240,192
Tv 238,244,3080
buffer_load_dwordx4 v209, s[68:71], s79 offen offset:1024 lds
Mvaa 8,244,196
Mvaa 8,248,200
Mvaa 8,252,204
Wl 12
Mvaa 12,252,220
Mvaa 12,248,216
buffer_load_dwordx4 v210, s[68:71], s79 offen offset:2048 lds
Mvaa 12,244,212
Mvaa 12,240,208
Wl 5
Mvaa 16,240,224
Mvaa 16,244,228
Mvaa 16,248,232
buffer_load_dwordx4 v211, s[68:71], s79 offen offset:3072 lds
Mvaa 16,252,236
Wl 0
Mvva 20,224,160
Mvva 20,228,164
s_add_u32 m0, s77, 0
Ta 240,243,256
Mvva 20,232,168
s_mov_b32 s79, 768
Ta 242,243,1280
Mvva 20,236,172
Ta 244,243,2304
buffer_load_dwordx4 v212, s[64:67], s79 offen offset:0 lds
Mvva 24,236,188
Ta 246,243,3328
Mvva 24,232,184
Ta 248,244,256
Mvva 24,228,180
Ta 250,244,1280
Mvva 24,224,176
Ta 252,244,2304
Mvva 28,224,192
Ta 254,244,3328
buffer_load_dwordx4 v213, s[64:67], s79 offen offset:1024 lds
Mvva 28,228,196
Mvva 28,232,200
Mvva 28,236,204
Mvva 32,236,220
Mvva 32,232,216
buffer_load_dwordx4 v214, s[64:67], s79 offen offset:2080 lds
Mvva 32,228,212
Mvva 32,224,208
Mvva 36,224,224
Mvva 36,228,228
Mvva 36,232,232
buffer_load_dwordx4 v215, s[64:67], s79 offen offset:3104 lds
Mvva 36,236,236
Wl 0
Mvaa 40,240,160
Mvaa 40,244,164
Tv 224,243,264
Mvaa 40,248,168
Tv 226,243,1288
Mvaa 40,252,172
Tv 228,243,2312
Mvaa 44,252,188
Tv 230,243,3336
Mvaa 44,248,184
Tv 232,244,264
Mvaa 44,244,180
Tv 234,244,1288
Mvaa 44,240,176
Tv 236,244,2312
Mvaa 48,240,192
Tv 238,244,3336
Mvaa 48,244,196
Mvaa 48,248,200
Mvaa 48,252,204
Mvaa 52,252,220
Mvaa 52,248,216
Mvaa 52,244,212
Mvaa 52,240,208
Mvaa 56,240,224
Mvaa 56,244,228
Mvaa 56,248,232
Mvaa 56,252,236
Wl 0
Mvva 60,224,160
Mvva 60,228,164
s_add_u32 m0, s77, 16512
Ta 240,243,512
Mvva 60,232,168
s_mov_b32 s79, 17152
Ta 242,243,1536
Mvva 60,236,172
Ta 244,243,2560
buffer_load_dwordx4 v212, s[64:67], s79 offen offset:0 lds
Mvva 64,236,188
Ta 246,243,3584
Mvva 64,232,184
Ta 248,244,512
Mvva 64,228,180
Ta 250,244,1536
Mvva 64,224,176
Ta 252,244,2560
Mvva 68,224,192
Ta 254,244,3584
buffer_load_dwordx4 v213, s[64:67], s79 offen offset:1024 lds
Mvva 68,228,196
Mvva 68,232,200
Mvva 68,236,204
Mvva 72,236,220
Mvva 72,232,216
buffer_load_dwordx4 v214, s[64:67], s79 offen offset:2080 lds
Mvva 72,228,212
Mvva 72,224,208
Mvva 76,224,224
Mvva 76,228,228
Mvva 76,232,232
buffer_load_dwordx4 v215, s[64:67], s79 offen offset:3104 lds
Mvva 76,236,236
Wl 0
Mvaa 80,240,160
Mvaa 80,244,164
Tv 224,243,520
Mvaa 80,248,168
Tv 226,243,1544
Mvaa 80,252,172
Tv 228,243,2568
Mvaa 84,252,188
Tv 230,243,3592
Mvaa 84,248,184
Tv 232,244,520
Mvaa 84,244,180
Tv 234,244,1544
Mvaa 84,240,176
Tv 236,244,2568
Mvaa 88,240,192
Tv 238,244,3592
Mvaa 88,244,196
Mvaa 88,248,200
Mvaa 88,252,204
Mvaa 92,252,220
Mvaa 92,248,216
Mvaa 92,244,212
Mvaa 92,240,208
Mvaa 96,240,224
Mvaa 96,244,228
Mvaa 96,248,232
Mvaa 96,252,236
Wl 0
Mvva 100,224,160
Mvva 100,228,164
s_add_u32 m0, s77, 82560
Ta 240,243,768
Mvva 100,232,168
s_mov_b32 s79, 33536
Ta 242,243,1792
Mvva 100,236,172
Ta 244,243,2816
buffer_load_dwordx4 v212, s[64:67], s79 offen offset:0 lds
Mvva 104,236,188
Ta 246,243,3840
Mvva 104,232,184
Ta 248,244,768
Mvva 104,228,180
Ta 250,244,1792
Mvva 104,224,176
Ta 252,244,2816
Mvva 108,224,192
Ta 254,244,3840
buffer_load_dwordx4 v213, s[64:67], s79 offen offset:1024 lds
Mvva 108,228,196
Mvva 108,232,200
Mvva 108,236,204
Mvva 112,236,220
Mvva 112,232,216
buffer_load_dwordx4 v214, s[64:67], s79 offen offset:2080 lds
Mvva 112,228,212
Mvva 112,224,208
Mvva 116,224,224
Mvva 116,228,228
Mvva 116,232,232
buffer_load_dwordx4 v215, s[64:67], s79 offen offset:3104 lds
Mvva 116,236,236
Wl 0
Mvaa 120,240,160
Mvaa 120,244,164
Tv 224,243,776
Mvaa 120,248,168
Tv 226,243,1800
Mvaa 120,252,172
Tv 228,243,2824
Mvaa 124,252,188
Tv 230,243,3848
Mvaa 124,248,184
Tv 232,244,776
Mvaa 124,244,180
Tv 234,244,1800
Mvaa 124,240,176
Tv 236,244,2824
Mvaa 128,240,192
Tv 238,244,3848
Mvaa 128,244,196
Mvaa 128,248,200
buffer_load_dword v220, v219, s[72:75], s52 offen
Mvaa 128,252,204
s_add_u32 s52, s52, 1024
Mvaa 132,252,220
Mvaa 132,248,216
Mvaa 132,244,212
Mvaa 132,240,208
Mvaa 136,240,224
Mvaa 136,244,228
Mvaa 136,248,232
Mvaa 136,252,236
Wl 0
Mvva 140,224,160
s_waitcnt vmcnt(13)
s_barrier
Mvva 140,228,164
s_add_u32 m0, s78, 49536
Ta 240,243,16512
Mvva 140,232,168
s_mov_b32 s79, 3145728
Ta 242,243,17536
Mvva 140,236,172
Ta 244,243,18560
buffer_load_dwordx4 v208, s[68:71], s79 offen offset:0 lds
Mvva 144,236,188
Ta 246,243,19584
Mvva 144,232,184
Ta 248,244,16512
Mvva 144,228,180
Ta 250,244,17536
Mvva 144,224,176
Ta 252,244,18560
Mvva 148,224,192
Ta 254,244,19584
buffer_load_dwordx4 v209, s[68:71], s79 offen offset:1024 lds
Mvva 148,228,196
Mvva 148,232,200
Mvva 148,236,204
Mvva 152,236,220
Mvva 152,232,216
buffer_load_dwordx4 v210, s[68:71], s79 offen offset:2048 lds
Mvva 152,228,212
Mvva 152,224,208
Mvva 156,224,224
Mvva 156,228,228
Mvva 156,232,232
buffer_load_dwordx4 v211, s[68:71], s79 offen offset:3072 lds
Mvva 156,236,236
Wl 0
Maaa 0,240,160
Maaa 0,244,164
s_add_u32 m0, s78, 115584
Tv 224,243,16520
Maaa 0,248,168
s_mov_b32 s79, 3670016
Tv 226,243,17544
Maaa 0,252,172
Tv 228,243,18568
buffer_load_dwordx4 v208, s[68:71], s79 offen offset:0 lds
Maaa 4,252,188
Tv 230,243,19592
Maaa 4,248,184
Tv 232,244,16520
Maaa 4,244,180
Tv 234,244,17544
Maaa 4,240,176
Tv 236,244,18568
Maaa 8,240,192
Tv 238,244,19592
buffer_load_dwordx4 v209, s[68:71], s79 offen offset:1024 lds
Maaa 8,244,196
Maaa 8,248,200
Maaa 8,252,204
Maaa 12,252,220
Maaa 12,248,216
buffer_load_dwordx4 v210, s[68:71], s79 offen offset:2048 lds
Maaa 12,244,212
Maaa 12,240,208
Maaa 16,240,224
Maaa 16,244,228
Maaa 16,248,232
buffer_load_dwordx4 v211, s[68:71], s79 offen offset:3072 lds
Maaa 16,252,236
Wl 0
Mava 20,224,160
Mava 20,228,164
s_add_u32 m0, s77, 33024
Ta 240,243,16768
Mava 20,232,168
s_mov_b32 s79, 49920
Ta 242,243,17792
Mava 20,236,172
Ta 244,243,18816
buffer_load_dwordx4 v212, s[64:67], s79 offen offset:0 lds
Mava 24,236,188
Ta 246,243,19840
Mava 24,232,184
Ta 248,244,16768
Mava 24,228,180
Ta 250,244,17792
Mava 24,224,176
Ta 252,244,18816
Mava 28,224,192
Ta 254,244,19840
buffer_load_dwordx4 v213, s[64:67], s79 offen offset:1024 lds
Mava 28,228,196
Mava 28,232,200
Mava 28,236,204
Mava 32,236,220
Mava 32,232,216
buffer_load_dwordx4 v214, s[64:67], s79 offen offset:2080 lds
Mava 32,228,212
.p2align 12
.org PG0_%=+12288
s_cbranch_execz VRET_%=
s_nop 0
Mava 32,224,208
Mava 36,224,224
Mava 36,228,228
Mava 36,232,232
buffer_load_dwordx4 v215, s[64:67], s79 offen offset:3104 lds
Mava 36,236,236
Wl 0
Maaa 40,240,160
Maaa 40,244,164
s_add_u32 m0, s77, 99072
Tv 224,243,16776
Maaa 40,248,168
s_mov_b32 s79, 66304
Tv 226,243,17800
Maaa 40,252,172
Tv 228,243,18824
buffer_load_dwordx4 v212, s[64:67], s79 offen offset:0 lds
Maaa 44,252,188
Tv 230,243,19848
Maaa 44,248,184
Tv 232,244,16776
Maaa 44,244,180
Tv 234,244,17800
Maaa 44,240,176
Tv 236,244,18824
Maaa 48,240,192
Tv 238,244,19848
buffer_load_dwordx4 v213, s[64:67], s79 offen offset:1024 lds
Maaa 48,244,196
Maaa 48,248,200
buffer_load_dword v220, v219, s[72:75], s52 offen
Maaa 48,252,204
s_add_u32 s52, s52, 1024
Maaa 52,252,220
Maaa 52,248,216
buffer_load_dwordx4 v214, s[64:67], s79 offen offset:2080 lds
Maaa 52,244,212
Maaa 52,240,208
Maaa 56,240,224
Maaa 56,244,228
Maaa 56,248,232
buffer_load_dwordx4 v215, s[64:67], s79 offen offset:3104 lds
Maaa 56,252,236
Wl 0
Mava 60,224,160
Mava 60,228,164
Ta 240,243,17024
Mava 60,232,168
Ta 242,243,18048
Mava 60,236,172
Ta 244,243,19072
Mava 64,236,188
Ta 246,243,20096
Mava 64,232,184
Ta 248,244,17024
Mava 64,228,180
Ta 250,244,18048
Mava 64,224,176
Ta 252,244,19072
Mava 68,224,192
Ta 254,244,20096
Mava 68,228,196
Mava 68,232,200
Mava 68,236,204
Mava 72,236,220
Mava 72,232,216
Mava 72,228,212
Mava 72,224,208
Mava 76,224,224
Mava 76,228,228
Mava 76,232,232
Mava 76,236,236
Wl 0
Maaa 80,240,160
Maaa 80,244,164
Tv 224,243,17032
Maaa 80,248,168
Tv 226,243,18056
Maaa 80,252,172
Tv 228,243,19080
Maaa 84,252,188
Tv 230,243,20104
)A"
R"A(Maaa 84,248,184
Tv 232,244,17032
Maaa 84,244,180
Tv 234,244,18056
Maaa 84,240,176
Tv 236,244,19080
Maaa 88,240,192
Tv 238,244,20104
Maaa 88,244,196
Maaa 88,248,200
Maaa 88,252,204
Maaa 92,252,220
Maaa 92,248,216
Maaa 92,244,212
Maaa 92,240,208
Maaa 96,240,224
Maaa 96,244,228
Maaa 96,248,232
Maaa 96,252,236
Wl 0
Mava 100,224,160
Mava 100,228,164
Ta 240,243,17280
Mava 100,232,168
Ta 242,243,18304
Mava 100,236,172
Ta 244,243,19328
Mava 104,236,188
Ta 246,243,20352
Mava 104,232,184
Ta 248,244,17280
Mava 104,228,180
Ta 250,244,18304
Mava 104,224,176
Ta 252,244,19328
Mava 108,224,192
Ta 254,244,20352
Mava 108,228,196
Mava 108,232,200
Mava 108,236,204
Mava 112,236,220
Mava 112,232,216
Mava 112,228,212
Mava 112,224,208
Mava 116,224,224
Mava 116,228,228
Mava 116,232,232
Mava 116,236,236
Wl 0
Maaa 120,240,160
Maaa 120,244,164
Tv 224,243,17288
Maaa 120,248,168
Tv 226,243,18312
Maaa 120,252,172
Tv 228,243,19336
Maaa 124,252,188
Tv 230,243,20360
Maaa 124,248,184
Tv 232,244,17288
Maaa 124,244,180
Tv 234,244,18312
Maaa 124,240,176
Tv 236,244,19336
Maaa 128,240,192
Tv 238,244,20360
Maaa 128,244,196
Maaa 128,248,200
buffer_load_dword v220, v219, s[72:75], s52 offen
Maaa 128,252,204
s_add_u32 s52, s52, 1024
Maaa 132,252,220
Maaa 132,248,216
Maaa 132,244,212
Maaa 132,240,208
Maaa 136,240,224
Maaa 136,244,228
Maaa 136,248,232
Maaa 136,252,236
Wl 0
Mava 140,224,160
s_waitcnt vmcnt(1)
s_barrier
Mava 140,228,164
s_add_u32 m0, s78, 66048
Ta 240,243,0
Mava 140,232,168
s_mov_b32 s79, 3145984
Ta 242,243,1024
Mava 140,236,172
Ta 244,243,2048
buffer_load_dwordx4 v208, s[68:71], s79 offen offset:0 lds
Mava 144,236,188
Ta 246,243,3072
Ra 160,216,0
Mava 144,232,184
Ta 248,244,0
Ra 164,216,256
Mava 144,228,180
Ta 250,244,1024
Ra 168,216,512
Mava 144,224,176
Ta 252,244,2048
Ra 172,216,768
Mava 148,224,192
Ta 254,244,3072
Ra 176,216,16512
buffer_load_dwordx4 v209, s[68:71], s79 offen offset:1024 lds
Mava 148,228,196
Ra 180,216,16768
Mava 148,232,200
Ra 184,216,17024
Mava 148,236,204
Ra 188,216,17280
Mava 152,236,220
Ra 192,217,0
Mava 152,232,216
Ra 196,217,256
buffer_load_dwordx4 v210, s[68:71], s79 offen offset:2048 lds
Mava 152,228,212
Ra 200,217,512
Mava 152,224,208
Ra 204,217,768
Mava 156,224,224
Ra 208,216,33024
Mava 156,228,228
Ra 212,216,33280
Mava 156,232,232
Ra 216,216,33536
buffer_load_dwordx4 v211, s[68:71], s79 offen offset:3072 lds
Mava 156,236,236
Wl 11
Ra 220,216,33792
Mvaa 0,240,160
Ra 224,217,16512
Mvaa 0,244,164
s_add_u32 m0, s78, 132096
Tv 224,243,8
Ra 228,217,16768
Mvaa 0,248,168
s_mov_b32 s79, 3670272
Tv 226,243,1032
Ra 232,217,17024
Mvaa 0,252,172
Wl 13
Tv 228,243,2056
Ra 236,217,17280
buffer_load_dwordx4 v208, s[68:71], s79 offen offset:0 lds
Mvaa 4,252,188
Tv 230,243,3080
Mvaa 4,248,184
Tv 232,244,8
Mvaa 4,244,180
Tv 234,244,1032
Mvaa 4,240,176
Wl 14
Tv 236,244,2056
Mvaa 8,240,192
Tv 238,244,3080
buffer_load_dwordx4 v209, s[68:71], s79 offen offset:1024 lds
Mvaa 8,244,196
Mvaa 8,248,200
Mvaa 8,252,204
Wl 12
Mvaa 12,252,220
Mvaa 12,248,216
buffer_load_dwordx4 v210, s[68:71], s79 offen offset:2048 lds
Mvaa 12,244,212
Mvaa 12,240,208
Wl 5
Mvaa 16,240,224
Mvaa 16,244,228
Mvaa 16,248,232
buffer_load_dwordx4 v211, s[68:71], s79 offen offset:3072 lds
Mvaa 16,252,236
Wl 0
Mvva 20,224,160
s_barrier
Mvva 20,228,164
Ta 240,243,256
Mvva 20,232,168
Ta 242,243,1280
Mvva 20,236,172
Ta 244,243,2304
Mvva 24,236,188
Ta 246,243,3328
Mvva 24,232,184
Ta 248,244,256
Mvva 24,228,180
Ta 250,244,1280
Mvva 24,224,176
Ta 252,244,2304
Mvva 28,224,192
Ta 254,244,3328
Mvva 28,228,196
s_add_u32 m0, s80, 0
Mvva 28,232,200
s_mov_b32 s79, 0
Mvva 28,236,204
buffer_load_dwordx4 v242, s[36:39], s79 offen offset:0 nt lds
buffer_load_dwordx4 v241, s[36:39], s79 offen offset:1024 nt lds
Mvva 32,236,220
Mvva 32,232,216
Mvva 32,228,212
Mvva 32,224,208
Mvva 36,224,224
Mvva 36,228,228
Mvva 36,232,232
Mvva 36,236,236
Wl 0
Mvaa 40,240,160
v_mul_f32 v0, s82, v0
v_mul_f32 v1, s82, v1
Mvaa 40,244,164
Tv 224,243,264
v_mul_f32 v2, s82, v2
Mvaa 40,248,168
Tv 226,243,1288
v_mul_f32 v3, s82, v3
Mvaa 40,252,172
Tv 228,243,2312
Pk 160,0,1
Mvaa 44,252,188
Tv 230,243,3336
Pk 161,2,3
Mvaa 44,248,184
Tv 232,244,264
v_mul_f32 v20, s82, v20
Mvaa 44,244,180
Tv 234,244,1288
v_mul_f32 v21, s82, v21
Mvaa 44,240,176
Tv 236,244,2312
v_mul_f32 v22, s82, v22
Mvaa 48,240,192
Tv 238,244,3336
v_mul_f32 v23, s82, v23
Mvaa 48,244,196
Pk 162,20,21
Pk 163,22,23
Mvaa 48,248,200
v_mul_f32 v4, s82, v4
v_mul_f32 v5, s82, v5
Mvaa 48,252,204
v_mul_f32 v6, s82, v6
v_mul_f32 v7, s82, v7
Mvaa 52,252,220
Pk 176,4,5
Pk 177,6,7
Mvaa 52,248,216
v_mul_f32 v24, s82, v24
v_mul_f32 v25, s82, v25
Mvaa 52,244,212
v_mul_f32 v26, s82, v26
v_mul_f32 v27, s82, v27
Mvaa 52,240,208
Pk 178,24,25
Pk 179,26,27
Mvaa 56,240,224
v_mul_f32 v8, s82, v8
v_mul_f32 v9, s82, v9
Mvaa 56,244,228
v_mul_f32 v10, s82, v10
v_mul_f32 v11, s82, v11
Mvaa 56,248,232
Pk 192,8,9
Pk 193,10,11
Mvaa 56,252,236
Wl 0
v_mul_f32 v28, s82, v28
v_mul_f32 v29, s82, v29
Mvva 60,224,160
v_mul_f32 v30, s82, v30
v_mul_f32 v31, s82, v31
Mvva 60,228,164
Ta 240,243,512
Pk 194,28,29
Mvva 60,232,168
Ta 242,243,1536
Pk 195,30,31
Mvva 60,236,172
Ta 244,243,2560
v_mul_f32 v12, s82, v12
Mvva 64,236,188
Ta 246,243,3584
v_mul_f32 v13, s82, v13
Mvva 64,232,184
Ta 248,244,512
v_mul_f32 v14, s82, v14
Mvva 64,228,180
Ta 250,244,1536
v_mul_f32 v15, s82, v15
Mvva 64,224,176
Ta 252,244,2560
Pk 208,12,13
Mvva 68,224,192
Ta 254,244,3584
Pk 209,14,15
Mvva 68,228,196
v_mul_f32 v32, s82, v32
v_mul_f32 v33, s82, v33
Mvva 68,232,200
v_mul_f32 v34, s82, v34
v_mul_f32 v35, s82, v35
Mvva 68,236,204
buffer_load_dwordx4 v242, s[36:39], s79 offen offset:2048 nt lds
buffer_load_dwordx4 v241, s[36:39], s79 offen offset:3072 nt lds
Mvva 72,236,220
Pk 210,32,33
Pk 211,34,35
Mvva 72,232,216
Mvva 72,228,212
Mvva 72,224,208
Mvva 76,224,224
Mvva 76,228,228
Mvva 76,232,232
Mvva 76,236,236
Wl 0
Mvaa 80,240,160
v_mul_f32 v40, s82, v40
v_mul_f32 v41, s82, v41
Mvaa 80,244,164
Tv 224,243,520
v_mul_f32 v42, s82, v42
Mvaa 80,248,168
Tv 226,243,1544
v_mul_f32 v43, s82, v43
Mvaa 80,252,172
Tv 228,243,2568
Pk 164,40,41
Mvaa 84,252,188
Tv 230,243,3592
Pk 165,42,43
Mvaa 84,248,184
Tv 232,244,520
v_mul_f32 v60, s82, v60
Mvaa 84,244,180
Tv 234,244,1544
v_mul_f32 v61, s82, v61
Mvaa 84,240,176
Tv 236,244,2568
v_mul_f32 v62, s82, v62
Mvaa 88,240,192
Tv 238,244,3592
v_mul_f32 v63, s82, v63
Mvaa 88,244,196
s_add_u32 m0, s80, 16512
Pk 166,60,61
Pk 167,62,63
Mvaa 88,248,200
s_mov_b32 s79, 1024
v_mul_f32 v44, s82, v44
v_mul_f32 v45, s82, v45
Mvaa 88,252,204
buffer_load_dwordx4 v242, s[36:39], s79 offen offset:0 nt lds
buffer_load_dwordx4 v241, s[36:39], s79 offen offset:1024 nt lds
Mvaa 92,252,220
v_mul_f32 v46, s82, v46
v_mul_f32 v47, s82, v47
Mvaa 92,248,216
Pk 180,44,45
Pk 181,46,47
Mvaa 92,244,212
v_mul_f32 v64, s82, v64
v_mul_f32 v65, s82, v65
Mvaa 92,240,208
v_mul_f32 v66, s82, v66
v_mul_f32 v67, s82, v67
Mvaa 96,240,224
Pk 182,64,65
Pk 183,66,67
Mvaa 96,244,228
v_mul_f32 v48, s82, v48
v_mul_f32 v49, s82, v49
Mvaa 96,248,232
v_mul_f32 v50, s82, v50
v_mul_f32 v51, s82, v51
Mvaa 96,252,236
Wl 0
Pk 196,48,49
Pk 197,50,51
Mvva 100,224,160
v_mul_f32 v68, s82, v68
v_mul_f32 v69, s82, v69
Mvva 100,228,164
Ta 240,243,768
v_mul_f32 v70, s82, v70
Mvva 100,232,168
Ta 242,243,1792
v_mul_f32 v71, s82, v71
Mvva 100,236,172
Ta 244,243,2816
Pk 198,68,69
Mvva 104,236,188
Ta 246,243,3840
Pk 199,70,71
Mvva 104,232,184
Ta 248,244,768
v_mul_f32 v52, s82, v52
Mvva 104,228,180
)A"
R"A(Ta 250,244,1792
v_mul_f32 v53, s82, v53
Mvva 104,224,176
Ta 252,244,2816
v_mul_f32 v54, s82, v54
Mvva 108,224,192
Ta 254,244,3840
v_mul_f32 v55, s82, v55
Mvva 108,228,196
Pk 212,52,53
Pk 213,54,55
Mvva 108,232,200
v_mul_f32 v72, s82, v72
v_mul_f32 v73, s82, v73
Mvva 108,236,204
v_mul_f32 v74, s82, v74
v_mul_f32 v75, s82, v75
Mvva 112,236,220
Pk 214,72,73
Pk 215,74,75
Mvva 112,232,216
Mvva 112,228,212
Mvva 112,224,208
Mvva 116,224,224
Mvva 116,228,228
Mvva 116,232,232
Mvva 116,236,236
Wl 0
Mvaa 120,240,160
v_mul_f32 v80, s82, v80
v_mul_f32 v81, s82, v81
Mvaa 120,244,164
Tv 224,243,776
v_mul_f32 v82, s82, v82
Mvaa 120,248,168
Tv 226,243,1800
v_mul_f32 v83, s82, v83
Mvaa 120,252,172
Tv 228,243,2824
Pk 168,80,81
Mvaa 124,252,188
Tv 230,243,3848
Pk 169,82,83
Mvaa 124,248,184
Tv 232,244,776
v_mul_f32 v100, s82, v100
Mvaa 124,244,180
Tv 234,244,1800
v_mul_f32 v101, s82, v101
Mvaa 124,240,176
Tv 236,244,2824
v_mul_f32 v102, s82, v102
Mvaa 128,240,192
Tv 238,244,3848
v_mul_f32 v103, s82, v103
Mvaa 128,244,196
Pk 170,100,101
Pk 171,102,103
Mvaa 128,248,200
v_mul_f32 v84, s82, v84
v_mul_f32 v85, s82, v85
Mvaa 128,252,204
buffer_load_dwordx4 v242, s[36:39], s79 offen offset:2048 nt lds
buffer_load_dwordx4 v241, s[36:39], s79 offen offset:3072 nt lds
Mvaa 132,252,220
v_mul_f32 v86, s82, v86
v_mul_f32 v87, s82, v87
Mvaa 132,248,216
Pk 184,84,85
Pk 185,86,87
Mvaa 132,244,212
v_mul_f32 v104, s82, v104
v_mul_f32 v105, s82, v105
Mvaa 132,240,208
v_mul_f32 v106, s82, v106
v_mul_f32 v107, s82, v107
Mvaa 136,240,224
Pk 186,104,105
Pk 187,106,107
Mvaa 136,244,228
.p2align 12
.org PG0_%=+16384
s_cbranch_execz VRET_%=
s_nop 0
v_mul_f32 v88, s82, v88
v_mul_f32 v89, s82, v89
Mvaa 136,248,232
v_mul_f32 v90, s82, v90
v_mul_f32 v91, s82, v91
Mvaa 136,252,236
Wl 0
Pk 200,88,89
Pk 201,90,91
Mvva 140,224,160
s_waitcnt vmcnt(8)
s_barrier
v_mul_f32 v108, s82, v108
v_mul_f32 v109, s82, v109
Mvva 140,228,164
Ta 240,243,16512
v_mul_f32 v110, s82, v110
Mvva 140,232,168
Ta 242,243,17536
v_mul_f32 v111, s82, v111
Mvva 140,236,172
Ta 244,243,18560
Pk 202,108,109
Mvva 144,236,188
Ta 246,243,19584
Pk 203,110,111
Mvva 144,232,184
Ta 248,244,16512
v_mul_f32 v92, s82, v92
Mvva 144,228,180
Ta 250,244,17536
v_mul_f32 v93, s82, v93
Mvva 144,224,176
Ta 252,244,18560
v_mul_f32 v94, s82, v94
Mvva 148,224,192
Ta 254,244,19584
v_mul_f32 v95, s82, v95
Mvva 148,228,196
Pk 216,92,93
Pk 217,94,95
Mvva 148,232,200
v_mul_f32 v112, s82, v112
v_mul_f32 v113, s82, v113
Mvva 148,236,204
v_mul_f32 v114, s82, v114
v_mul_f32 v115, s82, v115
Mvva 152,236,220
Pk 218,112,113
Pk 219,114,115
Mvva 152,232,216
Mvva 152,228,212
Mvva 152,224,208
Mvva 156,224,224
Mvva 156,228,228
Mvva 156,232,232
Mvva 156,236,236
Wl 0
Maaa 0,240,160
v_mul_f32 v120, s82, v120
v_mul_f32 v121, s82, v121
Maaa 0,244,164
Tv 224,243,16520
v_mul_f32 v122, s82, v122
Maaa 0,248,168
Tv 226,243,17544
v_mul_f32 v123, s82, v123
Maaa 0,252,172
Tv 228,243,18568
Pk 172,120,121
Maaa 4,252,188
Tv 230,243,19592
Pk 173,122,123
Maaa 4,248,184
Tv 232,244,16520
v_mul_f32 v140, s82, v140
Maaa 4,244,180
Tv 234,244,17544
v_mul_f32 v141, s82, v141
Maaa 4,240,176
Tv 236,244,18568
v_mul_f32 v142, s82, v142
Maaa 8,240,192
Tv 238,244,19592
v_mul_f32 v143, s82, v143
Maaa 8,244,196
s_add_u32 m0, s77, 82560
Pk 174,140,141
Pk 175,142,143
Maaa 8,248,200
s_mov_b32 s79, 0
v_mul_f32 v124, s82, v124
v_mul_f32 v125, s82, v125
Maaa 8,252,204
buffer_load_dwordx4 v242, s[40:43], s79 offen offset:0 nt lds
buffer_load_dwordx4 v241, s[40:43], s79 offen offset:1024 nt lds
Maaa 12,252,220
v_mul_f32 v126, s82, v126
v_mul_f32 v127, s82, v127
Maaa 12,248,216
Pk 188,124,125
Pk 189,126,127
Maaa 12,244,212
v_mul_f32 v144, s82, v144
v_mul_f32 v145, s82, v145
Maaa 12,240,208
v_mul_f32 v146, s82, v146
v_mul_f32 v147, s82, v147
Maaa 16,240,224
Pk 190,144,145
Pk 191,146,147
Maaa 16,244,228
v_mul_f32 v128, s82, v128
v_mul_f32 v129, s82, v129
Maaa 16,248,232
v_mul_f32 v130, s82, v130
v_mul_f32 v131, s82, v131
Maaa 16,252,236
Wl 0
Pk 204,128,129
Pk 205,130,131
Mava 20,224,160
v_mul_f32 v148, s82, v148
v_mul_f32 v149, s82, v149
Mava 20,228,164
Ta 240,243,16768
v_mul_f32 v150, s82, v150
Mava 20,232,168
Ta 242,243,17792
v_mul_f32 v151, s82, v151
Mava 20,236,172
Ta 244,243,18816
Pk 206,148,149
Mava 24,236,188
Ta 246,243,19840
Pk 207,150,151
Mava 24,232,184
Ta 248,244,16768
v_mul_f32 v132, s82, v132
Mava 24,228,180
Ta 250,244,17792
v_mul_f32 v133, s82, v133
Mava 24,224,176
Ta 252,244,18816
v_mul_f32 v134, s82, v134
Mava 28,224,192
Ta 254,244,19840
v_mul_f32 v135, s82, v135
Mava 28,228,196
Pk 220,132,133
Pk 221,134,135
Mava 28,232,200
v_mul_f32 v152, s82, v152
v_mul_f32 v153, s82, v153
Mava 28,236,204
buffer_load_dwordx4 v242, s[40:43], s79 offen offset:2048 nt lds
buffer_load_dwordx4 v241, s[40:43], s79 offen offset:3072 nt lds
Mava 32,236,220
v_mul_f32 v154, s82, v154
v_mul_f32 v155, s82, v155
Mava 32,232,216
Pk 222,152,153
Pk 223,154,155
Mava 32,228,212
Mava 32,224,208
Mava 36,224,224
Mava 36,228,228
Mava 36,232,232
Mava 36,236,236
Wl 0
Maaa 40,240,160
Ar 0,0
Ar 1,1
Maaa 40,244,164
Tv 224,243,16776
Ar 2,2
Maaa 40,248,168
Tv 226,243,17800
Ar 3,3
Maaa 40,252,172
Tv 228,243,18824
Ar 4,20
Maaa 44,252,188
Tv 230,243,19848
Ar 5,21
Maaa 44,248,184
Tv 232,244,16776
Ar 6,22
Maaa 44,244,180
Tv 234,244,17800
Ar 7,23
Maaa 44,240,176
Tv 236,244,18824
v_mul_f32 v0, s82, v0
Maaa 48,240,192
Tv 238,244,19848
v_mul_f32 v1, s82, v1
Maaa 48,244,196
v_mul_f32 v2, s82, v2
v_mul_f32 v3, s82, v3
Maaa 48,248,200
v_mul_f32 v4, s82, v4
v_mul_f32 v5, s82, v5
Maaa 48,252,204
v_mul_f32 v6, s82, v6
v_mul_f32 v7, s82, v7
Maaa 52,252,220
Pk 80,0,1
Pk 81,2,3
Maaa 52,248,216
Pk 82,4,5
Pk 83,6,7
Maaa 52,244,212
Ar 8,4
Ar 9,5
Maaa 52,240,208
Ar 10,6
Ar 11,7
Maaa 56,240,224
Ar 12,24
Ar 13,25
Maaa 56,244,228
Ar 14,26
Ar 15,27
Maaa 56,248,232
v_mul_f32 v8, s82, v8
v_mul_f32 v9, s82, v9
Maaa 56,252,236
Wl 0
v_mul_f32 v10, s82, v10
v_mul_f32 v11, s82, v11
Mava 60,224,160
v_mul_f32 v12, s82, v12
v_mul_f32 v13, s82, v13
Mava 60,228,164
Ta 240,243,17024
v_mul_f32 v14, s82, v14
Mava 60,232,168
Ta 242,243,18048
v_mul_f32 v15, s82, v15
Mava 60,236,172
Ta 244,243,19072
Pk 84,8,9
Mava 64,236,188
Ta 246,243,20096
Pk 85,10,11
Mava 64,232,184
Ta 248,244,17024
Pk 86,12,13
Mava 64,228,180
Ta 250,244,18048
Pk 87,14,15
Mava 64,224,176
Ta 252,244,19072
Ar 0,8
Mava 68,224,192
Ta 254,244,20096
Ar 1,9
Mava 68,228,196
s_add_u32 m0, s80, 33024
Ar 2,10
Ar 3,11
Mava 68,232,200
s_mov_b32 s79, 4096
Ar 4,28
Ar 5,29
Mava 68,236,204
buffer_load_dwordx4 v242, s[36:39], s79 offen offset:0 nt lds
buffer_load_dwordx4 v241, s[36:39], s79 offen offset:1024 nt lds
Mava 72,236,220
Ar 6,30
Ar 7,31
Mava 72,232,216
v_mul_f32 v0, s82, v0
v_mul_f32 v1, s82, v1
Mava 72,228,212
v_mul_f32 v2, s82, v2
v_mul_f32 v3, s82, v3
Mava 72,224,208
v_mul_f32 v4, s82, v4
v_mul_f32 v5, s82, v5
Mava 76,224,224
v_mul_f32 v6, s82, v6
v_mul_f32 v7, s82, v7
Mava 76,228,228
Pk 88,0,1
Pk 89,2,3
Mava 76,232,232
Pk 90,4,5
Pk 91,6,7
Mava 76,236,236
Wl 0
Ar 8,12
Ar 9,13
Maaa 80,240,160
Ar 10,14
Ar 11,15
Maaa 80,244,164
Tv 224,243,17032
Ar 12,32
Maaa 80,248,168
Tv 226,243,18056
Ar 13,33
Maaa 80,252,172
Tv 228,243,19080
Ar 14,34
Maaa 84,252,188
Tv 230,243,20104
Ar 15,35
Maaa 84,248,184
Tv 232,244,17032
v_mul_f32 v8, s82, v8
Maaa 84,244,180
Tv 234,244,18056
v_mul_f32 v9, s82, v9
Maaa 84,240,176
)A"
R"A(Tv 236,244,19080
v_mul_f32 v10, s82, v10
Maaa 88,240,192
Tv 238,244,20104
v_mul_f32 v11, s82, v11
Maaa 88,244,196
v_mul_f32 v12, s82, v12
v_mul_f32 v13, s82, v13
Maaa 88,248,200
v_mul_f32 v14, s82, v14
v_mul_f32 v15, s82, v15
Maaa 88,252,204
buffer_load_dwordx4 v242, s[36:39], s79 offen offset:2048 nt lds
buffer_load_dwordx4 v241, s[36:39], s79 offen offset:3072 nt lds
Maaa 92,252,220
Pk 92,8,9
Pk 93,10,11
Maaa 92,248,216
Pk 94,12,13
Pk 95,14,15
Maaa 92,244,212
Ar 0,16
Ar 1,17
Maaa 92,240,208
Ar 2,18
Ar 3,19
Maaa 96,240,224
Ar 4,36
Ar 5,37
Maaa 96,244,228
Ar 6,38
Ar 7,39
Maaa 96,248,232
v_mul_f32 v0, s82, v0
v_mul_f32 v1, s82, v1
Maaa 96,252,236
Wl 0
v_mul_f32 v2, s82, v2
v_mul_f32 v3, s82, v3
Mava 100,224,160
v_mul_f32 v4, s82, v4
v_mul_f32 v5, s82, v5
Mava 100,228,164
Ta 240,243,17280
v_mul_f32 v6, s82, v6
Mava 100,232,168
Ta 242,243,18304
v_mul_f32 v7, s82, v7
Mava 100,236,172
Ta 244,243,19328
Pk 248,0,1
Mava 104,236,188
Ta 246,243,20352
Pk 249,2,3
Mava 104,232,184
Ta 248,244,17280
Pk 250,4,5
Mava 104,228,180
Ta 250,244,18304
Pk 251,6,7
Mava 104,224,176
Ta 252,244,19328
Ar 8,40
Mava 108,224,192
Ta 254,244,20352
Ar 9,41
Mava 108,228,196
Ar 10,42
Ar 11,43
Mava 108,232,200
Ar 12,60
Ar 13,61
Mava 108,236,204
Ar 14,62
Ar 15,63
Mava 112,236,220
v_mul_f32 v8, s82, v8
v_mul_f32 v9, s82, v9
Mava 112,232,216
v_mul_f32 v10, s82, v10
v_mul_f32 v11, s82, v11
Mava 112,228,212
v_mul_f32 v12, s82, v12
v_mul_f32 v13, s82, v13
Mava 112,224,208
v_mul_f32 v14, s82, v14
v_mul_f32 v15, s82, v15
Mava 116,224,224
Pk 100,8,9
Pk 101,10,11
Mava 116,228,228
Pk 102,12,13
Pk 103,14,15
Mava 116,232,232
Ar 0,44
Ar 1,45
Mava 116,236,236
Wl 0
Ar 2,46
Ar 3,47
Maaa 120,240,160
Ar 4,64
Ar 5,65
Maaa 120,244,164
Tv 224,243,17288
Ar 6,66
Maaa 120,248,168
Tv 226,243,18312
Ar 7,67
Maaa 120,252,172
Tv 228,243,19336
v_mul_f32 v0, s82, v0
Maaa 124,252,188
Tv 230,243,20360
v_mul_f32 v1, s82, v1
Maaa 124,248,184
Tv 232,244,17288
v_mul_f32 v2, s82, v2
Maaa 124,244,180
Tv 234,244,18312
v_mul_f32 v3, s82, v3
Maaa 124,240,176
Tv 236,244,19336
v_mul_f32 v4, s82, v4
Maaa 128,240,192
Tv 238,244,20360
v_mul_f32 v5, s82, v5
Maaa 128,244,196
s_add_u32 m0, s77, 99072
v_mul_f32 v6, s82, v6
v_mul_f32 v7, s82, v7
Maaa 128,248,200
s_mov_b32 s79, 1024
Pk 104,0,1
Pk 105,2,3
Maaa 128,252,204
buffer_load_dwordx4 v242, s[40:43], s79 offen offset:0 nt lds
buffer_load_dwordx4 v241, s[40:43], s79 offen offset:1024 nt lds
Maaa 132,252,220
Pk 106,4,5
Pk 107,6,7
Maaa 132,248,216
Ar 8,48
Ar 9,49
Maaa 132,244,212
Ar 10,50
Ar 11,51
Maaa 132,240,208
Ar 12,68
Ar 13,69
Maaa 136,240,224
Ar 14,70
Ar 15,71
Maaa 136,244,228
v_mul_f32 v8, s82, v8
v_mul_f32 v9, s82, v9
Maaa 136,248,232
v_mul_f32 v10, s82, v10
v_mul_f32 v11, s82, v11
Maaa 136,252,236
Wl 0
v_mul_f32 v12, s82, v12
v_mul_f32 v13, s82, v13
Mava 140,224,160
v_mul_f32 v14, s82, v14
v_mul_f32 v15, s82, v15
Mava 140,228,164
Pk 108,8,9
Pk 109,10,11
Mava 140,232,168
Pk 110,12,13
Pk 111,14,15
Mava 140,236,172
Ar 0,52
Ar 1,53
Mava 144,236,188
Ar 2,54
Ar 3,55
Mava 144,232,184
Ar 4,72
Ar 5,73
Mava 144,228,180
Ar 6,74
Ar 7,75
Mava 144,224,176
v_mul_f32 v0, s82, v0
v_mul_f32 v1, s82, v1
Mava 148,224,192
v_mul_f32 v2, s82, v2
v_mul_f32 v3, s82, v3
Mava 148,228,196
v_mul_f32 v4, s82, v4
v_mul_f32 v5, s82, v5
Mava 148,232,200
v_mul_f32 v6, s82, v6
v_mul_f32 v7, s82, v7
Mava 148,236,204
Pk 112,0,1
Pk 113,2,3
Mava 152,236,220
Pk 114,4,5
Pk 115,6,7
Mava 152,232,216
Ar 8,56
Ar 9,57
Mava 152,228,212
Ar 10,58
Ar 11,59
Mava 152,224,208
Ar 12,76
Ar 13,77
Mava 156,224,224
Ar 14,78
Ar 15,79
Mava 156,228,228
v_mul_f32 v8, s82, v8
v_mul_f32 v9, s82, v9
Mava 156,232,232
v_mul_f32 v10, s82, v10
v_mul_f32 v11, s82, v11
Mava 156,236,236
v_mul_f32 v12, s82, v12
v_mul_f32 v13, s82, v13
.p2align 3
v_mul_f32 v14, s82, v14
v_mul_f32 v15, s82, v15
Pk 252,8,9
Pk 253,10,11
Pk 254,12,13
Pk 255,14,15
v_pk_mul_f32 v[16:17], v[16:17], s[82:83]
v_pk_mul_f32 v[18:19], v[18:19], s[82:83]
Pk 224,16,17
Pk 225,18,19
v_pk_mul_f32 v[36:37], v[36:37], s[82:83]
v_pk_mul_f32 v[38:39], v[38:39], s[82:83]
Pk 226,36,37
Pk 227,38,39
v_pk_mul_f32 v[56:57], v[56:57], s[82:83]
v_pk_mul_f32 v[58:59], v[58:59], s[82:83]
Pk 228,56,57
Pk 229,58,59
v_pk_mul_f32 v[76:77], v[76:77], s[82:83]
v_pk_mul_f32 v[78:79], v[78:79], s[82:83]
Pk 230,76,77
Pk 231,78,79
v_pk_mul_f32 v[96:97], v[96:97], s[82:83]
v_pk_mul_f32 v[98:99], v[98:99], s[82:83]
Pk 232,96,97
Pk 233,98,99
v_pk_mul_f32 v[116:117], v[116:117], s[82:83]
v_pk_mul_f32 v[118:119], v[118:119], s[82:83]
Pk 234,116,117
Pk 235,118,119
v_pk_mul_f32 v[136:137], v[136:137], s[82:83]
v_pk_mul_f32 v[138:139], v[138:139], s[82:83]
Pk 236,136,137
Pk 237,138,139
buffer_load_dwordx4 v242, s[40:43], s79 offen offset:2048 nt lds
buffer_load_dwordx4 v241, s[40:43], s79 offen offset:3072 nt lds
v_pk_mul_f32 v[156:157], v[156:157], s[82:83]
v_pk_mul_f32 v[158:159], v[158:159], s[82:83]
Pk 238,156,157
Pk 239,158,159
Ar 20,80
Ar 21,81
.p2align 12
.org PG0_%=+20480
s_cbranch_execz VRET_%=
s_nop 0
Ar 22,82
Ar 23,83
Ar 24,100
Ar 25,101
Ar 26,102
Ar 27,103
v_pk_mul_f32 v[20:21], v[20:21], s[82:83]
v_pk_mul_f32 v[22:23], v[22:23], s[82:83]
v_pk_mul_f32 v[24:25], v[24:25], s[82:83]
v_pk_mul_f32 v[26:27], v[26:27], s[82:83]
Pk 40,20,21
Pk 41,22,23
Pk 42,24,25
Pk 43,26,27
Ar 28,84
Ar 29,85
Ar 30,86
Ar 31,87
Ar 32,104
Ar 33,105
Ar 34,106
Ar 35,107
v_pk_mul_f32 v[28:29], v[28:29], s[82:83]
v_pk_mul_f32 v[30:31], v[30:31], s[82:83]
v_pk_mul_f32 v[32:33], v[32:33], s[82:83]
v_pk_mul_f32 v[34:35], v[34:35], s[82:83]
Pk 44,28,29
s_add_u32 m0, s80, 49536
s_mov_b32 s79, 5120
buffer_load_dwordx4 v242, s[36:39], s79 offen offset:0 nt lds
buffer_load_dwordx4 v241, s[36:39], s79 offen offset:1024 nt lds
Pk 45,30,31
Pk 46,32,33
Pk 47,34,35
Ar 20,88
Ar 21,89
Ar 22,90
Ar 23,91
Ar 24,108
Ar 25,109
Ar 26,110
Ar 27,111
v_pk_mul_f32 v[20:21], v[20:21], s[82:83]
v_pk_mul_f32 v[22:23], v[22:23], s[82:83]
v_pk_mul_f32 v[24:25], v[24:25], s[82:83]
v_pk_mul_f32 v[26:27], v[26:27], s[82:83]
Pk 48,20,21
Pk 49,22,23
Pk 50,24,25
Pk 51,26,27
Ar 28,92
Ar 29,93
Ar 30,94
Ar 31,95
Ar 32,112
Ar 33,113
Ar 34,114
Ar 35,115
v_pk_mul_f32 v[28:29], v[28:29], s[82:83]
v_pk_mul_f32 v[30:31], v[30:31], s[82:83]
v_pk_mul_f32 v[32:33], v[32:33], s[82:83]
v_pk_mul_f32 v[34:35], v[34:35], s[82:83]
Pk 52,28,29
Pk 53,30,31
buffer_load_dwordx4 v242, s[36:39], s79 offen offset:2048 nt lds
buffer_load_dwordx4 v241, s[36:39], s79 offen offset:3072 nt lds
Pk 54,32,33
Pk 55,34,35
Ar 20,96
Ar 21,97
Ar 22,98
Ar 23,99
Ar 24,116
Ar 25,117
Ar 26,118
Ar 27,119
v_pk_mul_f32 v[20:21], v[20:21], s[82:83]
v_pk_mul_f32 v[22:23], v[22:23], s[82:83]
v_pk_mul_f32 v[24:25], v[24:25], s[82:83]
v_pk_mul_f32 v[26:27], v[26:27], s[82:83]
Pk 150,20,21
Pk 151,22,23
Pk 152,24,25
Pk 153,26,27
Ar 28,120
Ar 29,121
Ar 30,122
Ar 31,123
Ar 32,140
Ar 33,141
Ar 34,142
Ar 35,143
v_pk_mul_f32 v[28:29], v[28:29], s[82:83]
v_pk_mul_f32 v[30:31], v[30:31], s[82:83]
v_pk_mul_f32 v[32:33], v[32:33], s[82:83]
v_pk_mul_f32 v[34:35], v[34:35], s[82:83]
Pk 60,28,29
Pk 61,30,31
Pk 62,32,33
s_add_u32 m0, s77, 115584
s_mov_b32 s79, 4096
buffer_load_dwordx4 v242, s[40:43], s79 offen offset:0 nt lds
buffer_load_dwordx4 v241, s[40:43], s79 offen offset:1024 nt lds
Pk 63,34,35
Ar 20,124
Ar 21,125
Ar 22,126
Ar 23,127
Ar 24,144
Ar 25,145
Ar 26,146
Ar 27,147
v_pk_mul_f32 v[20:21], v[20:21], s[82:83]
v_pk_mul_f32 v[22:23], v[22:23], s[82:83]
v_pk_mul_f32 v[24:25], v[24:25], s[82:83]
v_pk_mul_f32 v[26:27], v[26:27], s[82:83]
Pk 64,20,21
Pk 65,22,23
Pk 66,24,25
Pk 67,26,27
Ar 28,128
Ar 29,129
Ar 30,130
Ar 31,131
Ar 32,148
Ar 33,149
Ar 34,150
Ar 35,151
v_pk_mul_f32 v[28:29], v[28:29], s[82:83]
v_pk_mul_f32 v[30:31], v[30:31], s[82:83]
)A"
R"A(v_pk_mul_f32 v[32:33], v[32:33], s[82:83]
v_pk_mul_f32 v[34:35], v[34:35], s[82:83]
Pk 68,28,29
Pk 69,30,31
Pk 70,32,33
Pk 71,34,35
buffer_load_dwordx4 v242, s[40:43], s79 offen offset:2048 nt lds
buffer_load_dwordx4 v241, s[40:43], s79 offen offset:3072 nt lds
s_waitcnt vmcnt(20)
s_barrier
Ra 160,240,0
Ra 164,240,256
Ra 168,240,512
Ra 172,240,768
ds_read_b32 v36, v244 offset:33024
Ar 20,132
Ar 21,133
Ar 22,134
Ar 23,135
Ar 24,152
Ar 25,153
Ar 26,154
Ar 27,155
v_pk_mul_f32 v[20:21], v[20:21], s[82:83]
v_pk_mul_f32 v[22:23], v[22:23], s[82:83]
v_pk_mul_f32 v[24:25], v[24:25], s[82:83]
v_pk_mul_f32 v[26:27], v[26:27], s[82:83]
Pk 72,20,21
Pk 73,22,23
Pk 74,24,25
Pk 75,26,27
Ar 28,136
Ar 29,137
Ar 30,138
Ar 31,139
Ar 32,156
Ar 33,157
Ar 34,158
Ar 35,159
v_pk_mul_f32 v[28:29], v[28:29], s[82:83]
v_pk_mul_f32 v[30:31], v[30:31], s[82:83]
v_pk_mul_f32 v[32:33], v[32:33], s[82:83]
v_pk_mul_f32 v[34:35], v[34:35], s[82:83]
Pk 154,28,29
Pk 155,30,31
Pk 156,32,33
Pk 157,34,35
Wl 0
Zvav 0,160,160
v_readfirstlane_b32 s28, v36
v_accvgpr_write_b32 a192, v80
Mvav 0,164,164
v_accvgpr_write_b32 a193, v81
v_accvgpr_write_b32 a194, v82
Mvav 0,168,168
s_add_u32 s96, s96, s28
v_accvgpr_write_b32 a195, v83
v_accvgpr_write_b32 a208, v84
Mvav 0,172,172
s_lshl_b32 s66, s28, 18
v_accvgpr_write_b32 a209, v85
v_accvgpr_write_b32 a210, v86
Zvav 4,172,188
s_xor_b32 s77, s24, 4
v_accvgpr_write_b32 a211, v87
v_accvgpr_write_b32 a224, v88
Mvav 4,168,184
s_lshl_b32 s77, s77, 10
v_accvgpr_write_b32 a225, v89
v_accvgpr_write_b32 a226, v90
Mvav 4,164,180
s_add_u32 s77, s77, 64
v_accvgpr_write_b32 a227, v91
v_accvgpr_write_b32 a240, v92
Mvav 4,160,176
s_xor_b32 s78, s24, 8
v_accvgpr_write_b32 a241, v93
v_accvgpr_write_b32 a242, v94
Zvav 8,160,192
s_lshl_b32 s78, s78, 10
v_accvgpr_write_b32 a243, v95
v_accvgpr_write_b32 a196, v100
Mvav 8,164,196
s_add_u32 s78, s78, 128
v_accvgpr_write_b32 a197, v101
v_accvgpr_write_b32 a198, v102
Mvav 8,168,200
s_xor_b32 s80, s24, 12
v_accvgpr_write_b32 a199, v103
v_accvgpr_write_b32 a212, v104
Mvav 8,172,204
s_lshl_b32 s80, s80, 10
v_accvgpr_write_b32 a213, v105
v_accvgpr_write_b32 a214, v106
Zvav 12,172,220
s_add_u32 s80, s80, 192
v_accvgpr_write_b32 a215, v107
v_accvgpr_write_b32 a228, v108
Mvav 12,168,216
v_accvgpr_write_b32 a229, v109
v_accvgpr_write_b32 a230, v110
Mvav 12,164,212
v_accvgpr_write_b32 a231, v111
v_accvgpr_write_b32 a244, v112
Mvav 12,160,208
v_accvgpr_write_b32 a245, v113
v_accvgpr_write_b32 a246, v114
Zvav 16,160,224
Ra 160,240,1024
v_accvgpr_write_b32 a247, v115
Mvav 16,164,228
Ra 164,240,1280
v_accvgpr_write_b32 a200, v40
Mvav 16,168,232
Ra 168,240,1536
v_accvgpr_write_b32 a201, v41
Mvav 16,172,236
Ra 172,240,1792
v_accvgpr_write_b32 a202, v42
.p2align 3
v_accvgpr_write_b32 a203, v43
v_accvgpr_write_b32 a216, v44
v_accvgpr_write_b32 a217, v45
v_accvgpr_write_b32 a218, v46
v_accvgpr_write_b32 a219, v47
v_accvgpr_write_b32 a232, v48
v_accvgpr_write_b32 a233, v49
v_accvgpr_write_b32 a234, v50
v_accvgpr_write_b32 a235, v51
v_accvgpr_write_b32 a248, v52
v_accvgpr_write_b32 a249, v53
v_accvgpr_write_b32 a250, v54
v_accvgpr_write_b32 a251, v55
v_accvgpr_write_b32 a204, v60
v_accvgpr_write_b32 a205, v61
v_accvgpr_write_b32 a206, v62
v_accvgpr_write_b32 a207, v63
v_accvgpr_write_b32 a220, v64
v_accvgpr_write_b32 a221, v65
v_accvgpr_write_b32 a222, v66
v_accvgpr_write_b32 a223, v67
v_accvgpr_write_b32 a236, v68
v_accvgpr_write_b32 a237, v69
v_accvgpr_write_b32 a238, v70
v_accvgpr_write_b32 a239, v71
v_accvgpr_write_b32 a252, v72
v_accvgpr_write_b32 a253, v73
v_accvgpr_write_b32 a254, v74
v_accvgpr_write_b32 a255, v75
v_lshrrev_b32 v243, 4, v245
.p2align 3
v_mul_u32_u24 v241, 4128, v243
v_bfe_u32 v243, v245, 2, 2
v_lshl_add_u32 v241, v243, 6, v241
v_and_b32 v243, 3, v245
.p2align 3
v_lshl_add_u32 v241, v243, 3, v241
v_add_u32 v241, 82560, v241
v_max3_f32 v120, v0, v1, v2
v_max_f32 v120, v120, v3
v_max3_f32 v124, v4, v5, v6
v_max_f32 v124, v124, v7
v_max3_f32 v128, v8, v9, v10
v_max_f32 v128, v128, v11
v_max3_f32 v132, v12, v13, v14
v_max_f32 v132, v132, v15
v_max3_f32 v136, v16, v17, v18
v_max_f32 v136, v136, v19
v_mov_b32 v121, v120
v_mov_b32 v125, v124
v_mov_b32 v129, v128
v_mov_b32 v133, v132
v_mov_b32 v137, v136
s_nop 0
v_permlane32_swap_b32 v120, v121
v_permlane32_swap_b32 v124, v125
v_permlane32_swap_b32 v128, v129
v_permlane32_swap_b32 v132, v133
v_permlane32_swap_b32 v136, v137
v_max_f32 v120, v120, v121
v_max_f32 v124, v124, v125
v_max_f32 v128, v128, v129
v_max_f32 v132, v132, v133
v_max_f32 v136, v136, v137
v_mov_b32 v121, v120
v_mov_b32 v125, v124
v_mov_b32 v129, v128
v_mov_b32 v133, v132
v_mov_b32 v137, v136
s_nop 0
v_permlane16_swap_b32 v120, v121
v_permlane16_swap_b32 v124, v125
v_permlane16_swap_b32 v128, v129
v_permlane16_swap_b32 v132, v133
v_permlane16_swap_b32 v136, v137
v_max_f32 v120, v120, v121
v_max_f32 v124, v124, v125
v_max_f32 v128, v128, v129
v_max_f32 v132, v132, v133
v_max_f32 v136, v136, v137
v_pk_mul_f32 v[122:123], v[120:121], -1.0 op_sel_hi:[0,0]
v_pk_mul_f32 v[120:121], v[120:121], -1.0 op_sel_hi:[0,0]
v_pk_add_f32 v[0:1], v[0:1], v[122:123]
v_pk_add_f32 v[2:3], v[2:3], v[122:123]
v_pk_mov_b32 v[140:141], 0, 0
v_pk_mul_f32 v[126:127], v[124:125], -1.0 op_sel_hi:[0,0]
v_pk_mul_f32 v[124:125], v[124:125], -1.0 op_sel_hi:[0,0]
v_pk_add_f32 v[4:5], v[4:5], v[126:127]
v_pk_add_f32 v[6:7], v[6:7], v[126:127]
v_pk_mov_b32 v[142:143], 0, 0
v_pk_mul_f32 v[130:131], v[128:129], -1.0 op_sel_hi:[0,0]
v_pk_mul_f32 v[128:129], v[128:129], -1.0 op_sel_hi:[0,0]
v_pk_add_f32 v[8:9], v[8:9], v[130:131]
v_pk_add_f32 v[10:11], v[10:11], v[130:131]
v_pk_mov_b32 v[144:145], 0, 0
v_pk_mul_f32 v[134:135], v[132:133], -1.0 op_sel_hi:[0,0]
v_pk_mul_f32 v[132:133], v[132:133], -1.0 op_sel_hi:[0,0]
v_pk_add_f32 v[12:13], v[12:13], v[134:135]
v_pk_add_f32 v[14:15], v[14:15], v[134:135]
v_pk_mov_b32 v[146:147], 0, 0
v_pk_mul_f32 v[138:139], v[136:137], -1.0 op_sel_hi:[0,0]
v_pk_mul_f32 v[136:137], v[136:137], -1.0 op_sel_hi:[0,0]
v_pk_add_f32 v[16:17], v[16:17], v[138:139]
v_pk_add_f32 v[18:19], v[18:19], v[138:139]
v_pk_mov_b32 v[148:149], 0, 0
Wl 0
s_mov_b32 s48, 0
Cvav 20,160,160,120
s_barrier
Ex 0
Ex 1
Mvav 20,164,164
Ad 140,0
Ex 2
Mvav 20,168,168
Ad 141,1
Pk 40,0,1
Mvav 20,172,172
Ex 3
Ad 140,2
Cvav 24,172,188,124
Ex 4
Ad 141,3
Mvav 24,168,184
Pk 41,2,3
Ex 5
Ad 142,4
Mvav 24,164,180
Ex 6
Ad 143,5
Mvav 24,160,176
Pk 48,4,5
Ex 7
Cvav 28,160,192,128
Ad 142,6
Ex 8
Mvav 28,164,196
Ad 143,7
Pk 49,6,7
Mvav 28,168,200
Ex 9
Ad 144,8
Mvav 28,172,204
Ex 10
Ad 145,9
Pk 56,8,9
Cvav 32,172,220,132
Ex 11
Ad 144,10
Mvav 32,168,216
Ex 12
Ad 145,11
Mvav 32,164,212
Pk 57,10,11
Ex 13
Mvav 32,160,208
Ad 146,12
Ex 14
Cvav 36,160,224,136
Ra 160,240,2048
Ad 147,13
Pk 64,12,13
Ex 15
Mvav 36,164,228
Ra 164,240,2304
Mvav 36,168,232
Ra 168,240,2560
Mvav 36,172,236
Ra 172,240,2816
Ad 146,14
Ex 16
Ad 147,15
Pk 65,14,15
Ex 17
Ad 148,16
Ex 18
Ad 149,17
Pk 72,16,17
Ex 19
Ad 148,18
s_nop 0
Ad 149,19
Pk 73,18,19
Wl 0
Cvav 0,160,160,120
Ex 20
Ex 21
Mvav 0,164,164
Ad 140,20
Ex 22
Mvav 0,168,168
Ad 141,21
Pk 42,20,21
Mvav 0,172,172
Ex 23
Ad 140,22
Cvav 4,172,188,124
Ex 24
Ad 141,23
Mvav 4,168,184
Pk 43,22,23
Ex 25
Ad 142,24
Mvav 4,164,180
Ex 26
Ad 143,25
Mvav 4,160,176
Pk 50,24,25
Ex 27
Cvav 8,160,192,128
Ad 142,26
Ex 28
Mvav 8,164,196
Ad 143,27
Pk 51,26,27
Mvav 8,168,200
Ex 29
Ad 144,28
Mvav 8,172,204
Ex 30
Ad 145,29
Pk 58,28,29
Cvav 12,172,220,132
Ex 31
Ad 144,30
Mvav 12,168,216
Ex 32
Ad 145,31
Mvav 12,164,212
Pk 59,30,31
Ex 33
Mvav 12,160,208
Ad 146,32
Ex 34
Cvav 16,160,224,136
Ra 160,240,3072
Ad 147,33
Pk 66,32,33
Ex 35
Mvav 16,164,228
Ra 164,240,3328
Mvav 16,168,232
Ra 168,240,3584
Mvav 16,172,236
Ra 172,240,3840
Ad 146,34
Ex 36
Ad 147,35
Pk 67,34,35
Ex 37
Ad 148,36
Ex 38
Ad 149,37
Pk 74,36,37
Ex 39
Ad 148,38
s_nop 0
Ad 149,39
Pk 75,38,39
Wl 0
Cvav 20,160,160,120
Ex 0
Ex 1
Mvav 20,164,164
Ad 140,0
Ex 2
Mvav 20,168,168
Ad 141,1
Pk 44,0,1
Mvav 20,172,172
Ex 3
Ad 140,2
Cvav 24,172,188,124
Ex 4
Ad 141,3
Mvav 24,168,184
Pk 45,2,3
Ex 5
Ad 142,4
Mvav 24,164,180
)A"
R"A(Ex 6
Ad 143,5
Mvav 24,160,176
Pk 52,4,5
Ex 7
Cvav 28,160,192,128
Ad 142,6
Ex 8
Mvav 28,164,196
Ad 143,7
Pk 53,6,7
Mvav 28,168,200
Ex 9
Ad 144,8
Mvav 28,172,204
Ex 10
Ad 145,9
Pk 60,8,9
Cvav 32,172,220,132
Ex 11
Ad 144,10
Mvav 32,168,216
Ex 12
Ad 145,11
Mvav 32,164,212
Pk 61,10,11
Ex 13
Mvav 32,160,208
Ad 146,12
Ex 14
Cvav 36,160,224,136
Ra 160,240,16512
Ad 147,13
Pk 68,12,13
Ex 15
Mvav 36,164,228
Ra 164,240,16768
Mvav 36,168,232
Ra 168,240,17024
Mvav 36,172,236
Ra 172,240,17280
Ad 146,14
Ex 16
Ad 147,15
Pk 69,14,15
Ex 17
Ad 148,16
Ex 18
Ad 149,17
Pk 76,16,17
Ex 19
Ad 148,18
s_nop 0
Ad 149,19
Pk 77,18,19
Wl 0
Cvav 0,160,160,120
Ex 20
Ex 21
Mvav 0,164,164
Ad 140,20
Ex 22
Mvav 0,168,168
Ad 141,21
Pk 46,20,21
Mvav 0,172,172
Ex 23
Ad 140,22
Cvav 4,172,188,124
Ex 24
Ad 141,23
Mvav 4,168,184
Pk 47,22,23
Ex 25
Ad 142,24
Mvav 4,164,180
Ex 26
Ad 143,25
Mvav 4,160,176
.p2align 12
.org PG0_%=+24576
s_cbranch_execz VRET_%=
Pk 54,24,25
Ex 27
Cvav 8,160,192,128
Ad 142,26
Ex 28
Mvav 8,164,196
Ad 143,27
Pk 55,26,27
Mvav 8,168,200
Ex 29
Ad 144,28
Mvav 8,172,204
Ex 30
Ad 145,29
Pk 62,28,29
Cvav 12,172,220,132
Ex 31
Ad 144,30
Mvav 12,168,216
Ex 32
Ad 145,31
Mvav 12,164,212
Pk 63,30,31
Ex 33
Mvav 12,160,208
Ad 146,32
Ex 34
Cvav 16,160,224,136
Ra 160,240,17536
Ad 147,33
Pk 70,32,33
Ex 35
Mvav 16,164,228
Ra 164,240,17792
Mvav 16,168,232
Ra 168,240,18048
Mvav 16,172,236
Ra 172,240,18304
Ad 146,34
Ex 36
Ad 147,35
Pk 71,34,35
Ex 37
Ad 148,36
Ex 38
Ad 149,37
Pk 78,36,37
Ex 39
Ad 148,38
s_nop 0
Ad 149,39
Pk 79,38,39
Wl 0
s_waitcnt vmcnt(0)
s_mov_b32 s47, 0
Cvav 20,160,160,120
s_barrier
Ex 0
Mvav 20,164,164
Ta 176,241,0
Ad 140,0
Mvav 20,168,168
Ex 1
Mvav 20,172,172
Ta 178,241,1024
Ad 141,1
Cvav 24,172,188,124
Ex 2
Mvav 24,168,184
Ta 180,241,2048
Pk 80,0,1
Mvav 24,164,180
Ex 3
Mvav 24,160,176
Ta 182,241,3072
Ad 140,2
Cvav 28,160,192,128
Ex 4
Mvav 28,164,196
Ta 184,241,32
Ad 141,3
Mvav 28,168,200
Ex 5
Mvav 28,172,204
Ta 186,241,1056
Pk 81,2,3
Cvav 32,172,220,132
Ex 6
Mvav 32,168,216
Ta 188,241,2080
Ad 142,4
Mvav 32,164,212
Ex 7
Mvav 32,160,208
Ta 190,241,3104
Ad 143,5
Cvav 36,160,224,136
Ex 8
Mvav 36,164,228
Pk 88,4,5
Ad 142,6
Mvav 36,168,232
Ex 9
Mvav 36,172,236
Wl 4
Ad 143,7
Pk 89,6,7
Ad 144,8
Zaav 0,176,40
Ex 10
Maav 0,180,44
Ra 160,240,18560
Ad 145,9
Zaav 4,176,48
Ex 11
Maav 4,180,52
Ra 164,240,18816
Pk 96,8,9
Zaav 8,176,56
Ex 12
Maav 8,180,60
Ra 168,240,19072
Ad 144,10
Zaav 12,176,64
Ex 13
Maav 12,180,68
Ra 172,240,19328
Ad 145,11
Zaav 16,176,72
Ex 14
Maav 16,180,76
Wl 4
Pk 97,10,11
Ad 146,12
Zaav 20,184,40
Ex 15
Maav 20,188,44
Ad 147,13
Pk 104,12,13
Zaav 24,184,48
Ex 16
Maav 24,188,52
Ad 146,14
Ad 147,15
Zaav 28,184,56
Ex 17
Maav 28,188,60
Pk 105,14,15
Ad 148,16
Zaav 32,184,64
Ex 18
Maav 32,188,68
Ad 149,17
Pk 112,16,17
Zaav 36,184,72
Ex 19
Maav 36,188,76
Wl 0
Ad 148,18
Ad 149,19
Pk 113,18,19
Cvav 0,160,160,120
Ex 20
Mvav 0,164,164
Ta 176,241,256
Ad 140,20
Mvav 0,168,168
Ex 21
Mvav 0,172,172
Ta 178,241,1280
Ad 141,21
Cvav 4,172,188,124
Ex 22
Mvav 4,168,184
Ta 180,241,2304
Pk 82,20,21
Mvav 4,164,180
Ex 23
Mvav 4,160,176
Ta 182,241,3328
Ad 140,22
Cvav 8,160,192,128
Ex 24
Mvav 8,164,196
Ta 184,241,288
Ad 141,23
Mvav 8,168,200
Ex 25
Mvav 8,172,204
Ta 186,241,1312
Pk 83,22,23
Cvav 12,172,220,132
Ex 26
Mvav 12,168,216
Ta 188,241,2336
Ad 142,24
Mvav 12,164,212
Ex 27
Mvav 12,160,208
Ta 190,241,3360
Ad 143,25
Cvav 16,160,224,136
Ex 28
Mvav 16,164,228
Pk 90,24,25
Ad 142,26
Mvav 16,168,232
Ex 29
Mvav 16,172,236
Wl 4
Ad 143,27
Pk 91,26,27
Ad 144,28
Zaav 40,176,40
Ex 30
Maav 40,180,44
Ra 160,240,19584
Ad 145,29
Zaav 44,176,48
Ex 31
Maav 44,180,52
Ra 164,240,19840
Pk 98,28,29
Zaav 48,176,56
Ex 32
Maav 48,180,60
Ra 168,240,20096
Ad 144,30
Zaav 52,176,64
Ex 33
Maav 52,180,68
Ra 172,240,20352
Ad 145,31
Zaav 56,176,72
Ex 34
Maav 56,180,76
Wl 4
Pk 99,30,31
Ad 146,32
Zaav 60,184,40
Ex 35
Maav 60,188,44
Ad 147,33
Pk 106,32,33
Zaav 64,184,48
Ex 36
Maav 64,188,52
Ad 146,34
Ad 147,35
Zaav 68,184,56
Ex 37
Maav 68,188,60
Pk 107,34,35
Ad 148,36
Zaav 72,184,64
Ex 38
Maav 72,188,68
Ad 149,37
Pk 114,36,37
Zaav 76,184,72
Ex 39
Maav 76,188,76
Wl 0
Ad 148,38
Ad 149,39
Pk 115,38,39
Cvav 20,160,160,120
Ex 0
Mvav 20,164,164
Ta 176,241,512
Ad 140,0
Mvav 20,168,168
Ex 1
Mvav 20,172,172
Ta 178,241,1536
Ad 141,1
Cvav 24,172,188,124
Ex 2
Mvav 24,168,184
Ta 180,241,2560
Pk 84,0,1
Mvav 24,164,180
Ex 3
Mvav 24,160,176
Ta 182,241,3584
Ad 140,2
Cvav 28,160,192,128
Ex 4
Mvav 28,164,196
Ta 184,241,544
Ad 141,3
Mvav 28,168,200
Ex 5
Mvav 28,172,204
Ta 186,241,1568
Pk 85,2,3
Cvav 32,172,220,132
Ex 6
Mvav 32,168,216
Ta 188,241,2592
Ad 142,4
Mvav 32,164,212
Ex 7
Mvav 32,160,208
Ta 190,241,3616
Ad 143,5
Cvav 36,160,224,136
Ex 8
Mvav 36,164,228
Pk 92,4,5
Ad 142,6
Mvav 36,168,232
Ex 9
Mvav 36,172,236
Wl 4
Ad 143,7
Pk 93,6,7
Ad 144,8
)A"
R"A(Zaav 80,176,40
Ex 10
Maav 80,180,44
Ra 160,240,33024
Ad 145,9
Zaav 84,176,48
Ex 11
Maav 84,180,52
Ra 164,240,33280
Pk 100,8,9
Zaav 88,176,56
Ex 12
Maav 88,180,60
Ra 168,240,33536
Ad 144,10
Zaav 92,176,64
Ex 13
Maav 92,180,68
Ra 172,240,33792
Ad 145,11
Zaav 96,176,72
Ex 14
Maav 96,180,76
Wl 4
Pk 101,10,11
Ad 146,12
Zaav 100,184,40
Ex 15
Maav 100,188,44
Ad 147,13
Pk 108,12,13
Zaav 104,184,48
Ex 16
Maav 104,188,52
Ad 146,14
Ad 147,15
Zaav 108,184,56
Ex 17
Maav 108,188,60
Pk 109,14,15
Ad 148,16
Zaav 112,184,64
Ex 18
Maav 112,188,68
Ad 149,17
Pk 116,16,17
Zaav 116,184,72
Ex 19
Maav 116,188,76
Wl 0
Ad 148,18
Ad 149,19
Pk 117,18,19
Cvav 0,160,160,120
Ex 20
Mvav 0,164,164
Ta 176,241,768
Ad 140,20
Mvav 0,168,168
Ex 21
Mvav 0,172,172
Ta 178,241,1792
Ad 141,21
Cvav 4,172,188,124
Ex 22
Mvav 4,168,184
Ta 180,241,2816
Pk 86,20,21
Mvav 4,164,180
Ex 23
Mvav 4,160,176
Ta 182,241,3840
Ad 140,22
Cvav 8,160,192,128
Ex 24
Mvav 8,164,196
Ta 184,241,800
Ad 141,23
Mvav 8,168,200
Ex 25
Mvav 8,172,204
Ta 186,241,1824
Pk 87,22,23
Cvav 12,172,220,132
Ex 26
Mvav 12,168,216
Ta 188,241,2848
Ad 142,24
Mvav 12,164,212
Ex 27
Mvav 12,160,208
Ta 190,241,3872
Ad 143,25
Cvav 16,160,224,136
Ex 28
Mvav 16,164,228
Pk 94,24,25
Ad 142,26
Mvav 16,168,232
Ex 29
Mvav 16,172,236
Wl 4
Ad 143,27
Pk 95,26,27
Ad 144,28
Zaav 120,176,40
Ex 30
Maav 120,180,44
Ra 160,240,34048
Ad 145,29
Zaav 124,176,48
Ex 31
Maav 124,180,52
Ra 164,240,34304
Pk 102,28,29
Zaav 128,176,56
Ex 32
Maav 128,180,60
Ra 168,240,34560
Ad 144,30
Zaav 132,176,64
Ex 33
Maav 132,180,68
Ra 172,240,34816
Ad 145,31
Zaav 136,176,72
Ex 34
Maav 136,180,76
Wl 4
Pk 103,30,31
Ad 146,32
Zaav 140,184,40
Ex 35
Maav 140,188,44
Ad 147,33
Pk 110,32,33
Zaav 144,184,48
Ex 36
Maav 144,188,52
Ad 146,34
Ad 147,35
Zaav 148,184,56
Ex 37
Maav 148,188,60
Pk 111,34,35
Ad 148,36
Zaav 152,184,64
Ex 38
Maav 152,188,68
Ad 149,37
Pk 118,36,37
Zaav 156,184,72
Ex 39
Maav 156,188,76
Wl 0
Ad 148,38
Ad 149,39
Pk 119,38,39
LOOPA0_%=:
Cvav 20,160,160,120
s_barrier
Ex 0
Mvav 20,164,164
Ta 176,241,16512
Ad 140,0
Mvav 20,168,168
Ex 1
Mvav 20,172,172
Ta 178,241,17536
Ad 141,1
Cvav 24,172,188,124
Ex 2
Mvav 24,168,184
Ta 180,241,18560
Pk 40,0,1
Mvav 24,164,180
Ex 3
Mvav 24,160,176
Ta 182,241,19584
Ad 140,2
Cvav 28,160,192,128
Ex 4
Mvav 28,164,196
Ta 184,241,16544
Ad 141,3
Mvav 28,168,200
Ex 5
Mvav 28,172,204
Ta 186,241,17568
Pk 41,2,3
Cvav 32,172,220,132
Ex 6
Mvav 32,168,216
Ta 188,241,18592
Ad 142,4
Mvav 32,164,212
Ex 7
Mvav 32,160,208
Ta 190,241,19616
Ad 143,5
Cvav 36,160,224,136
Ex 8
Mvav 36,164,228
Pk 48,4,5
Ad 142,6
Mvav 36,168,232
Ex 9
Mvav 36,172,236
Wl 4
Ad 143,7
Pk 49,6,7
Ad 144,8
Maav 0,176,80
Ex 10
Maav 0,180,84
Ra 160,240,35072
Ad 145,9
Maav 4,176,88
Ex 11
Maav 4,180,92
Ra 164,240,35328
Pk 56,8,9
Maav 8,176,96
Ex 12
Maav 8,180,100
Ra 168,240,35584
Ad 144,10
Maav 12,176,104
Ex 13
Maav 12,180,108
Ra 172,240,35840
Ad 145,11
Maav 16,176,112
Ex 14
Maav 16,180,116
Wl 4
Pk 57,10,11
Ad 146,12
Maav 20,184,80
Ex 15
Maav 20,188,84
Ad 147,13
Pk 64,12,13
Maav 24,184,88
Ex 16
Maav 24,188,92
Ad 146,14
Ad 147,15
Maav 28,184,96
Ex 17
Maav 28,188,100
Pk 65,14,15
Ad 148,16
Maav 32,184,104
Ex 18
Maav 32,188,108
Ad 149,17
Pk 72,16,17
Maav 36,184,112
Ex 19
Maav 36,188,116
Wl 0
Ad 148,18
Ad 149,19
Pk 73,18,19
Cvav 0,160,160,120
Ex 20
Mvav 0,164,164
Ta 176,241,16768
Ad 140,20
Mvav 0,168,168
Ex 21
Mvav 0,172,172
Ta 178,241,17792
Ad 141,21
Cvav 4,172,188,124
Ex 22
Mvav 4,168,184
Ta 180,241,18816
Pk 42,20,21
Mvav 4,164,180
Ex 23
Mvav 4,160,176
Ta 182,241,19840
Ad 140,22
Cvav 8,160,192,128
Ex 24
Mvav 8,164,196
Ta 184,241,16800
Ad 141,23
Mvav 8,168,200
Ex 25
Mvav 8,172,204
Ta 186,241,17824
Pk 43,22,23
Cvav 12,172,220,132
Ex 26
Mvav 12,168,216
Ta 188,241,18848
Ad 142,24
Mvav 12,164,212
Ex 27
Mvav 12,160,208
Ta 190,241,19872
Ad 143,25
Cvav 16,160,224,136
Ex 28
Mvav 16,164,228
Pk 50,24,25
Ad 142,26
Mvav 16,168,232
Ex 29
Mvav 16,172,236
Wl 4
Ad 143,27
Pk 51,26,27
Ad 144,28
Maav 40,176,80
Ex 30
Maav 40,180,84
Ra 160,240,36096
Ad 145,29
Maav 44,176,88
Ex 31
Maav 44,180,92
Ra 164,240,36352
Pk 58,28,29
Maav 48,176,96
Ex 32
Maav 48,180,100
.p2align 12
.org PG0_%=+28672
s_cbranch_execz VRET_%=
Ra 168,240,36608
Ad 144,30
Maav 52,176,104
Ex 33
Maav 52,180,108
Ra 172,240,36864
Ad 145,31
Maav 56,176,112
Ex 34
Maav 56,180,116
Wl 4
Pk 59,30,31
Ad 146,32
Maav 60,184,80
Ex 35
Maav 60,188,84
Ad 147,33
Pk 66,32,33
Maav 64,184,88
Ex 36
Maav 64,188,92
Ad 146,34
Ad 147,35
Maav 68,184,96
Ex 37
Maav 68,188,100
Pk 67,34,35
Ad 148,36
Maav 72,184,104
Ex 38
Maav 72,188,108
Ad 149,37
Pk 74,36,37
Maav 76,184,112
Ex 39
Maav 76,188,116
Wl 0
Ad 148,38
Ad 149,39
Pk 75,38,39
Cvav 20,160,160,120
Ex 0
Mvav 20,164,164
Ta 176,241,17024
Ad 140,0
Mvav 20,168,168
Ex 1
Mvav 20,172,172
Ta 178,241,18048
Ad 141,1
Cvav 24,172,188,124
Ex 2
Mvav 24,168,184
Ta 180,241,19072
Pk 44,0,1
Mvav 24,164,180
Ex 3
Mvav 24,160,176
Ta 182,241,20096
Ad 140,2
Cvav 28,160,192,128
Ex 4
Mvav 28,164,196
Ta 184,241,17056
Ad 141,3
Mvav 28,168,200
Ex 5
)A"
R"A(Mvav 28,172,204
Ta 186,241,18080
Pk 45,2,3
Cvav 32,172,220,132
Ex 6
Mvav 32,168,216
Ta 188,241,19104
Ad 142,4
Mvav 32,164,212
Ex 7
Mvav 32,160,208
Ta 190,241,20128
Ad 143,5
Cvav 36,160,224,136
Ex 8
Mvav 36,164,228
Pk 52,4,5
Ad 142,6
Mvav 36,168,232
Ex 9
Mvav 36,172,236
Wl 4
Ad 143,7
Pk 53,6,7
Ad 144,8
Maav 80,176,80
Ex 10
Maav 80,180,84
Ra 160,240,49536
Ad 145,9
Maav 84,176,88
Ex 11
Maav 84,180,92
Ra 164,240,49792
Pk 60,8,9
Maav 88,176,96
Ex 12
Maav 88,180,100
Ra 168,240,50048
Ad 144,10
Maav 92,176,104
Ex 13
Maav 92,180,108
Ra 172,240,50304
Ad 145,11
Maav 96,176,112
Ex 14
Maav 96,180,116
Wl 4
Pk 61,10,11
Ad 146,12
Maav 100,184,80
Ex 15
Maav 100,188,84
Ad 147,13
Pk 68,12,13
Maav 104,184,88
Ex 16
Maav 104,188,92
Ad 146,14
Ad 147,15
Maav 108,184,96
Ex 17
Maav 108,188,100
Pk 69,14,15
Ad 148,16
Maav 112,184,104
Ex 18
Maav 112,188,108
Ad 149,17
Pk 76,16,17
Maav 116,184,112
Ex 19
Maav 116,188,116
Wl 0
Ad 148,18
Ad 149,19
Pk 77,18,19
Cvav 0,160,160,120
Ex 20
Mvav 0,164,164
Ta 176,241,17280
Ad 140,20
Mvav 0,168,168
Ex 21
Mvav 0,172,172
Ta 178,241,18304
Ad 141,21
Cvav 4,172,188,124
Ex 22
Mvav 4,168,184
Ta 180,241,19328
Pk 46,20,21
Mvav 4,164,180
Ex 23
Mvav 4,160,176
Ta 182,241,20352
Ad 140,22
Cvav 8,160,192,128
Ex 24
Mvav 8,164,196
Ta 184,241,17312
Ad 141,23
Mvav 8,168,200
Ex 25
Mvav 8,172,204
Ta 186,241,18336
Pk 47,22,23
Cvav 12,172,220,132
Ex 26
Mvav 12,168,216
Ta 188,241,19360
Ad 142,24
Mvav 12,164,212
Ex 27
Mvav 12,160,208
Ta 190,241,20384
Ad 143,25
Cvav 16,160,224,136
Ex 28
Mvav 16,164,228
Pk 54,24,25
Ad 142,26
Mvav 16,168,232
Ex 29
Mvav 16,172,236
Wl 4
Ad 143,27
Pk 55,26,27
Ad 144,28
Maav 120,176,80
Ex 30
Maav 120,180,84
Ra 160,240,50560
Ad 145,29
Maav 124,176,88
Ex 31
Maav 124,180,92
Ra 164,240,50816
Pk 62,28,29
Maav 128,176,96
Ex 32
Maav 128,180,100
Ra 168,240,51072
Ad 144,30
Maav 132,176,104
Ex 33
Maav 132,180,108
Ra 172,240,51328
Ad 145,31
Maav 136,176,112
Ex 34
Maav 136,180,116
Wl 4
Pk 63,30,31
Ad 146,32
Maav 140,184,80
Ex 35
Maav 140,188,84
Ad 147,33
Pk 70,32,33
Maav 144,184,88
Ex 36
Maav 144,188,92
Ad 146,34
Ad 147,35
Maav 148,184,96
Ex 37
Maav 148,188,100
Pk 71,34,35
Ad 148,36
Maav 152,184,104
Ex 38
Maav 152,188,108
Ad 149,37
Pk 78,36,37
Maav 156,184,112
Ex 39
Maav 156,188,116
Wl 0
Ad 148,38
Ad 149,39
Pk 79,38,39
Cvav 20,160,160,120
s_barrier
Ex 0
Mvav 20,164,164
Ta 176,241,33024
Ad 140,0
Mvav 20,168,168
Ex 1
Mvav 20,172,172
Ta 178,241,34048
Ad 141,1
Cvav 24,172,188,124
Ex 2
Mvav 24,168,184
Ta 180,241,35072
Pk 80,0,1
Mvav 24,164,180
Ex 3
Mvav 24,160,176
Ta 182,241,36096
Ad 140,2
Cvav 28,160,192,128
Ex 4
Mvav 28,164,196
Ta 184,241,33056
Ad 141,3
Mvav 28,168,200
Ex 5
Mvav 28,172,204
Ta 186,241,34080
Pk 81,2,3
Cvav 32,172,220,132
Ex 6
Mvav 32,168,216
Ta 188,241,35104
Ad 142,4
Mvav 32,164,212
Ex 7
Mvav 32,160,208
Ta 190,241,36128
Ad 143,5
Cvav 36,160,224,136
Ex 8
Mvav 36,164,228
Pk 88,4,5
Ad 142,6
Mvav 36,168,232
Ex 9
Mvav 36,172,236
Wl 4
Ad 143,7
Pk 89,6,7
Ad 144,8
Maav 0,176,40
Ex 10
Maav 0,180,44
Ra 160,240,51584
Ad 145,9
Maav 4,176,48
Ex 11
Maav 4,180,52
Ra 164,240,51840
Pk 96,8,9
Maav 8,176,56
Ex 12
Maav 8,180,60
Ra 168,240,52096
Ad 144,10
Maav 12,176,64
Ex 13
Maav 12,180,68
Ra 172,240,52352
Ad 145,11
Maav 16,176,72
Ex 14
Maav 16,180,76
Wl 4
Pk 97,10,11
Ad 146,12
Maav 20,184,40
Ex 15
Maav 20,188,44
Ad 147,13
Pk 104,12,13
Maav 24,184,48
Ex 16
Maav 24,188,52
Ad 146,14
Ad 147,15
Maav 28,184,56
Ex 17
Maav 28,188,60
Pk 105,14,15
Ad 148,16
Maav 32,184,64
Ex 18
Maav 32,188,68
Ad 149,17
Pk 112,16,17
Maav 36,184,72
Ex 19
Maav 36,188,76
Wl 0
Ad 148,18
Ad 149,19
Pk 113,18,19
Cvav 0,160,160,120
Ex 20
Mvav 0,164,164
Ta 176,241,33280
Ad 140,20
Mvav 0,168,168
Ex 21
Mvav 0,172,172
Ta 178,241,34304
Ad 141,21
Cvav 4,172,188,124
Ex 22
Mvav 4,168,184
Ta 180,241,35328
Pk 82,20,21
Mvav 4,164,180
Ex 23
Mvav 4,160,176
Ta 182,241,36352
Ad 140,22
Cvav 8,160,192,128
Ex 24
Mvav 8,164,196
Ta 184,241,33312
Ad 141,23
Mvav 8,168,200
Ex 25
Mvav 8,172,204
Ta 186,241,34336
Pk 83,22,23
Cvav 12,172,220,132
Ex 26
Mvav 12,168,216
Ta 188,241,35360
Ad 142,24
Mvav 12,164,212
Ex 27
Mvav 12,160,208
Ta 190,241,36384
Ad 143,25
Cvav 16,160,224,136
Ex 28
Mvav 16,164,228
Pk 90,24,25
Ad 142,26
Mvav 16,168,232
Ex 29
Mvav 16,172,236
Wl 4
Ad 143,27
Pk 91,26,27
Ad 144,28
Maav 40,176,40
Ex 30
Maav 40,180,44
Ra 160,240,52608
Ad 145,29
Maav 44,176,48
Ex 31
Maav 44,180,52
Ra 164,240,52864
Pk 98,28,29
Maav 48,176,56
Ex 32
Maav 48,180,60
Ra 168,240,53120
Ad 144,30
Maav 52,176,64
Ex 33
Maav 52,180,68
Ra 172,240,53376
Ad 145,31
Maav 56,176,72
Ex 34
Maav 56,180,76
Wl 4
Pk 99,30,31
Ad 146,32
Maav 60,184,40
Ex 35
Maav 60,188,44
Ad 147,33
Pk 106,32,33
Maav 64,184,48
Ex 36
Maav 64,188,52
Ad 146,34
Ad 147,35
Maav 68,184,56
Ex 37
Maav 68,188,60
Pk 107,34,35
Ad 148,36
Maav 72,184,64
Ex 38
Maav 72,188,68
Ad 149,37
Pk 114,36,37
Maav 76,184,72
Ex 39
Maav 76,188,76
Wl 0
Ad 148,38
Ad 149,39
Pk 115,38,39
Cvav 20,160,160,120
Ex 0
Mvav 20,164,164
Ta 176,241,33536
Ad 140,0
Mvav 20,168,168
)A"
R"A(Ex 1
Mvav 20,172,172
Ta 178,241,34560
Ad 141,1
Cvav 24,172,188,124
Ex 2
Mvav 24,168,184
Ta 180,241,35584
Pk 84,0,1
Mvav 24,164,180
Ex 3
Mvav 24,160,176
Ta 182,241,36608
Ad 140,2
Cvav 28,160,192,128
Ex 4
Mvav 28,164,196
Ta 184,241,33568
Ad 141,3
Mvav 28,168,200
Ex 5
Mvav 28,172,204
Ta 186,241,34592
Pk 85,2,3
Cvav 32,172,220,132
Ex 6
Mvav 32,168,216
Ta 188,241,35616
Ad 142,4
Mvav 32,164,212
Ex 7
Mvav 32,160,208
Ta 190,241,36640
Ad 143,5
Cvav 36,160,224,136
Ex 8
Mvav 36,164,228
Pk 92,4,5
Ad 142,6
Mvav 36,168,232
Ex 9
Mvav 36,172,236
Wl 4
Ad 143,7
Pk 93,6,7
Ad 144,8
Maav 80,176,40
Ex 10
Maav 80,180,44
Ra 160,240,0
Ad 145,9
Maav 84,176,48
Ex 11
Maav 84,180,52
Ra 164,240,256
Pk 100,8,9
Maav 88,176,56
Ex 12
Maav 88,180,60
Ra 168,240,512
Ad 144,10
Maav 92,176,64
Ex 13
Maav 92,180,68
Ra 172,240,768
Ad 145,11
Maav 96,176,72
Ex 14
Maav 96,180,76
Wl 4
Pk 101,10,11
Ad 146,12
Maav 100,184,40
Ex 15
Maav 100,188,44
Ad 147,13
Pk 108,12,13
Maav 104,184,48
Ex 16
Maav 104,188,52
Ad 146,14
Ad 147,15
Maav 108,184,56
Ex 17
Maav 108,188,60
Pk 109,14,15
Ad 148,16
Maav 112,184,64
Ex 18
Maav 112,188,68
Ad 149,17
Pk 116,16,17
Maav 116,184,72
s_cmp_eq_u32 s47, 3
Ex 19
Maav 116,188,76
Wl 0
Ad 148,18
Ad 149,19
Pk 117,18,19
s_cbranch_scc1 EXITA0_%=
Cvav 0,160,160,120
Ex 20
Mvav 0,164,164
Ta 176,241,33792
Ad 140,20
Mvav 0,168,168
Ex 21
Mvav 0,172,172
Ta 178,241,34816
Ad 141,21
Cvav 4,172,188,124
Ex 22
Mvav 4,168,184
Ta 180,241,35840
Pk 86,20,21
Mvav 4,164,180
Ex 23
Mvav 4,160,176
Ta 182,241,36864
Ad 140,22
Cvav 8,160,192,128
Ex 24
Mvav 8,164,196
Ta 184,241,33824
Ad 141,23
Mvav 8,168,200
Ex 25
Mvav 8,172,204
Ta 186,241,34848
Pk 87,22,23
Cvav 12,172,220,132
Ex 26
Mvav 12,168,216
Ta 188,241,35872
Ad 142,24
Mvav 12,164,212
Ex 27
Mvav 12,160,208
Ta 190,241,36896
Ad 143,25
Cvav 16,160,224,136
Ex 28
Mvav 16,164,228
Pk 94,24,25
Ad 142,26
Mvav 16,168,232
Ex 29
Mvav 16,172,236
Wl 4
Ad 143,27
Pk 95,26,27
Ad 144,28
Maav 120,176,40
Ex 30
Maav 120,180,44
Ra 160,240,1024
Ad 145,29
Maav 124,176,48
Ex 31
Maav 124,180,52
Ra 164,240,1280
Pk 102,28,29
Maav 128,176,56
Ex 32
Maav 128,180,60
Ra 168,240,1536
Ad 144,30
Maav 132,176,64
Ex 33
Maav 132,180,68
Ra 172,240,1792
Ad 145,31
Maav 136,176,72
Ex 34
Maav 136,180,76
Wl 4
.p2align 12
.org PG0_%=+32768
s_cbranch_execz VRET_%=
Pk 103,30,31
Ad 146,32
Maav 140,184,40
Ex 35
Maav 140,188,44
Ad 147,33
Pk 110,32,33
Maav 144,184,48
Ex 36
Maav 144,188,52
Ad 146,34
Ad 147,35
Maav 148,184,56
Ex 37
Maav 148,188,60
Pk 111,34,35
Ad 148,36
Maav 152,184,64
Ex 38
Maav 152,188,68
Ad 149,37
Pk 118,36,37
Maav 156,184,72
Ex 39
Maav 156,188,76
Wl 0
Ad 148,38
Ad 149,39
Pk 119,38,39
Cvav 20,160,160,120
s_barrier
Ex 0
Mvav 20,164,164
Ta 176,241,49536
Ad 140,0
Mvav 20,168,168
Ex 1
Mvav 20,172,172
Ta 178,241,50560
Ad 141,1
Cvav 24,172,188,124
Ex 2
Mvav 24,168,184
Ta 180,241,51584
Pk 40,0,1
Mvav 24,164,180
Ex 3
Mvav 24,160,176
Ta 182,241,52608
Ad 140,2
Cvav 28,160,192,128
Ex 4
Mvav 28,164,196
Ta 184,241,49568
Ad 141,3
Mvav 28,168,200
Ex 5
Mvav 28,172,204
Ta 186,241,50592
Pk 41,2,3
Cvav 32,172,220,132
Ex 6
Mvav 32,168,216
Ta 188,241,51616
Ad 142,4
Mvav 32,164,212
Ex 7
Mvav 32,160,208
Ta 190,241,52640
Ad 143,5
Cvav 36,160,224,136
Ex 8
Mvav 36,164,228
Pk 48,4,5
Ad 142,6
Mvav 36,168,232
Ex 9
Mvav 36,172,236
Wl 4
Ad 143,7
Pk 49,6,7
Ad 144,8
Maav 0,176,80
Ex 10
Maav 0,180,84
Ra 160,240,2048
Ad 145,9
Maav 4,176,88
Ex 11
Maav 4,180,92
Ra 164,240,2304
Pk 56,8,9
Maav 8,176,96
Ex 12
Maav 8,180,100
Ra 168,240,2560
Ad 144,10
Maav 12,176,104
Ex 13
Maav 12,180,108
Ra 172,240,2816
Ad 145,11
Maav 16,176,112
Ex 14
Maav 16,180,116
Wl 4
Pk 57,10,11
Ad 146,12
Maav 20,184,80
Ex 15
Maav 20,188,84
Ad 147,13
Pk 64,12,13
Maav 24,184,88
Ex 16
Maav 24,188,92
Ad 146,14
Ad 147,15
Maav 28,184,96
Ex 17
Maav 28,188,100
Pk 65,14,15
Ad 148,16
Maav 32,184,104
Ex 18
Maav 32,188,108
Ad 149,17
Pk 72,16,17
Maav 36,184,112
Ex 19
Maav 36,188,116
Wl 0
Ad 148,18
Ad 149,19
Pk 73,18,19
Cvav 0,160,160,120
Ex 20
Mvav 0,164,164
Ta 176,241,49792
Ad 140,20
Mvav 0,168,168
Ex 21
Mvav 0,172,172
Ta 178,241,50816
Ad 141,21
Cvav 4,172,188,124
Ex 22
Mvav 4,168,184
Ta 180,241,51840
Pk 42,20,21
Mvav 4,164,180
Ex 23
Mvav 4,160,176
Ta 182,241,52864
Ad 140,22
Cvav 8,160,192,128
Ex 24
Mvav 8,164,196
Ta 184,241,49824
Ad 141,23
Mvav 8,168,200
Ex 25
Mvav 8,172,204
Ta 186,241,50848
Pk 43,22,23
Cvav 12,172,220,132
Ex 26
Mvav 12,168,216
Ta 188,241,51872
Ad 142,24
Mvav 12,164,212
Ex 27
Mvav 12,160,208
Ta 190,241,52896
Ad 143,25
Cvav 16,160,224,136
Ex 28
Mvav 16,164,228
Pk 50,24,25
Ad 142,26
Mvav 16,168,232
Ex 29
Mvav 16,172,236
Wl 4
Ad 143,27
Pk 51,26,27
Ad 144,28
Maav 40,176,80
Ex 30
Maav 40,180,84
Ra 160,240,3072
Ad 145,29
Maav 44,176,88
Ex 31
Maav 44,180,92
Ra 164,240,3328
Pk 58,28,29
Maav 48,176,96
Ex 32
Maav 48,180,100
Ra 168,240,3584
Ad 144,30
Maav 52,176,104
Ex 33
Maav 52,180,108
Ra 172,240,3840
Ad 145,31
Maav 56,176,112
Ex 34
Maav 56,180,116
Wl 4
Pk 59,30,31
Ad 146,32
Maav 60,184,80
Ex 35
Maav 60,188,84
Ad 147,33
Pk 66,32,33
Maav 64,184,88
Ex 36
)A"
R"A(Maav 64,188,92
Ad 146,34
Ad 147,35
Maav 68,184,96
Ex 37
Maav 68,188,100
Pk 67,34,35
Ad 148,36
Maav 72,184,104
Ex 38
Maav 72,188,108
Ad 149,37
Pk 74,36,37
Maav 76,184,112
Ex 39
Maav 76,188,116
Wl 0
Ad 148,38
Ad 149,39
Pk 75,38,39
Cvav 20,160,160,120
Ex 0
Mvav 20,164,164
Ta 176,241,50048
Ad 140,0
Mvav 20,168,168
Ex 1
Mvav 20,172,172
Ta 178,241,51072
Ad 141,1
Cvav 24,172,188,124
Ex 2
Mvav 24,168,184
Ta 180,241,52096
Pk 44,0,1
Mvav 24,164,180
Ex 3
Mvav 24,160,176
Ta 182,241,53120
Ad 140,2
Cvav 28,160,192,128
Ex 4
Mvav 28,164,196
Ta 184,241,50080
Ad 141,3
Mvav 28,168,200
Ex 5
Mvav 28,172,204
Ta 186,241,51104
Pk 45,2,3
Cvav 32,172,220,132
Ex 6
Mvav 32,168,216
Ta 188,241,52128
Ad 142,4
Mvav 32,164,212
Ex 7
Mvav 32,160,208
Ta 190,241,53152
Ad 143,5
Cvav 36,160,224,136
Ex 8
Mvav 36,164,228
Pk 52,4,5
Ad 142,6
Mvav 36,168,232
Ex 9
Mvav 36,172,236
Wl 4
Ad 143,7
Pk 53,6,7
Ad 144,8
Maav 80,176,80
Ex 10
Maav 80,180,84
Ra 160,240,16512
Ad 145,9
Maav 84,176,88
Ex 11
Maav 84,180,92
Ra 164,240,16768
Pk 60,8,9
Maav 88,176,96
Ex 12
Maav 88,180,100
Ra 168,240,17024
Ad 144,10
Maav 92,176,104
Ex 13
Maav 92,180,108
Ra 172,240,17280
Ad 145,11
Maav 96,176,112
Ex 14
Maav 96,180,116
Wl 4
Pk 61,10,11
Ad 146,12
Maav 100,184,80
Ex 15
Maav 100,188,84
Ad 147,13
Pk 68,12,13
Maav 104,184,88
Ex 16
Maav 104,188,92
Ad 146,14
Ad 147,15
Maav 108,184,96
Ex 17
Maav 108,188,100
Pk 69,14,15
Ad 148,16
Maav 112,184,104
Ex 18
Maav 112,188,108
Ad 149,17
Pk 76,16,17
Maav 116,184,112
Ex 19
Maav 116,188,116
Wl 0
Ad 148,18
Ad 149,19
Pk 77,18,19
Cvav 0,160,160,120
Ex 20
Mvav 0,164,164
Ta 176,241,50304
Ad 140,20
Mvav 0,168,168
Ex 21
Mvav 0,172,172
Ta 178,241,51328
Ad 141,21
Cvav 4,172,188,124
Ex 22
Mvav 4,168,184
Ta 180,241,52352
Pk 46,20,21
Mvav 4,164,180
Ex 23
Mvav 4,160,176
Ta 182,241,53376
Ad 140,22
Cvav 8,160,192,128
Ex 24
Mvav 8,164,196
Ta 184,241,50336
Ad 141,23
Mvav 8,168,200
Ex 25
Mvav 8,172,204
Ta 186,241,51360
Pk 47,22,23
Cvav 12,172,220,132
Ex 26
Mvav 12,168,216
Ta 188,241,52384
Ad 142,24
Mvav 12,164,212
Ex 27
Mvav 12,160,208
Ta 190,241,53408
Ad 143,25
Cvav 16,160,224,136
Ex 28
Mvav 16,164,228
Pk 54,24,25
Ad 142,26
Mvav 16,168,232
Ex 29
Mvav 16,172,236
Wl 4
Ad 143,27
Pk 55,26,27
Ad 144,28
Maav 120,176,80
Ex 30
Maav 120,180,84
Ra 160,240,17536
Ad 145,29
Maav 124,176,88
Ex 31
Maav 124,180,92
Ra 164,240,17792
Pk 62,28,29
Maav 128,176,96
Ex 32
Maav 128,180,100
Ra 168,240,18048
Ad 144,30
Maav 132,176,104
Ex 33
Maav 132,180,108
Ra 172,240,18304
Ad 145,31
Maav 136,176,112
Ex 34
Maav 136,180,116
Wl 4
Pk 63,30,31
Ad 146,32
Maav 140,184,80
Ex 35
Maav 140,188,84
Ad 147,33
Pk 70,32,33
Maav 144,184,88
Ex 36
Maav 144,188,92
Ad 146,34
Ad 147,35
Maav 148,184,96
Ex 37
Maav 148,188,100
Pk 71,34,35
Ad 148,36
Maav 152,184,104
Ex 38
Maav 152,188,108
Ad 149,37
Pk 78,36,37
Maav 156,184,112
Ex 39
Maav 156,188,116
Wl 0
Ad 148,38
Ad 149,39
Pk 79,38,39
Cvav 20,160,160,120
s_barrier
Ex 0
Mvav 20,164,164
Ta 176,241,0
Ad 140,0
Mvav 20,168,168
Ex 1
Mvav 20,172,172
Ta 178,241,1024
Ad 141,1
Cvav 24,172,188,124
Ex 2
Mvav 24,168,184
Ta 180,241,2048
Pk 80,0,1
Mvav 24,164,180
Ex 3
Mvav 24,160,176
Ta 182,241,3072
Ad 140,2
Cvav 28,160,192,128
Ex 4
Mvav 28,164,196
Ta 184,241,32
Ad 141,3
Mvav 28,168,200
Ex 5
Mvav 28,172,204
Ta 186,241,1056
Pk 81,2,3
Cvav 32,172,220,132
Ex 6
Mvav 32,168,216
Ta 188,241,2080
Ad 142,4
Mvav 32,164,212
Ex 7
Mvav 32,160,208
Ta 190,241,3104
Ad 143,5
Cvav 36,160,224,136
Ex 8
Mvav 36,164,228
Pk 88,4,5
Ad 142,6
Mvav 36,168,232
Ex 9
Mvav 36,172,236
Wl 4
Ad 143,7
Pk 89,6,7
Ad 144,8
Maav 0,176,40
Ex 10
Maav 0,180,44
Ra 160,240,18560
Ad 145,9
Maav 4,176,48
Ex 11
Maav 4,180,52
Ra 164,240,18816
Pk 96,8,9
Maav 8,176,56
Ex 12
Maav 8,180,60
Ra 168,240,19072
Ad 144,10
Maav 12,176,64
Ex 13
Maav 12,180,68
Ra 172,240,19328
Ad 145,11
Maav 16,176,72
Ex 14
Maav 16,180,76
Wl 4
Pk 97,10,11
Ad 146,12
Maav 20,184,40
Ex 15
Maav 20,188,44
Ad 147,13
Pk 104,12,13
Maav 24,184,48
Ex 16
Maav 24,188,52
Ad 146,14
Ad 147,15
Maav 28,184,56
Ex 17
Maav 28,188,60
Pk 105,14,15
Ad 148,16
Maav 32,184,64
Ex 18
Maav 32,188,68
Ad 149,17
Pk 112,16,17
Maav 36,184,72
Ex 19
Maav 36,188,76
Wl 0
Ad 148,18
Ad 149,19
Pk 113,18,19
Cvav 0,160,160,120
Ex 20
Mvav 0,164,164
Ta 176,241,256
Ad 140,20
Mvav 0,168,168
Ex 21
Mvav 0,172,172
Ta 178,241,1280
Ad 141,21
Cvav 4,172,188,124
Ex 22
Mvav 4,168,184
Ta 180,241,2304
Pk 82,20,21
Mvav 4,164,180
Ex 23
Mvav 4,160,176
Ta 182,241,3328
Ad 140,22
Cvav 8,160,192,128
Ex 24
Mvav 8,164,196
Ta 184,241,288
Ad 141,23
Mvav 8,168,200
Ex 25
Mvav 8,172,204
Ta 186,241,1312
Pk 83,22,23
Cvav 12,172,220,132
Ex 26
Mvav 12,168,216
Ta 188,241,2336
Ad 142,24
Mvav 12,164,212
Ex 27
Mvav 12,160,208
Ta 190,241,3360
Ad 143,25
Cvav 16,160,224,136
Ex 28
Mvav 16,164,228
Pk 90,24,25
Ad 142,26
Mvav 16,168,232
Ex 29
Mvav 16,172,236
Wl 4
Ad 143,27
Pk 91,26,27
Ad 144,28
Maav 40,176,40
Ex 30
Maav 40,180,44
Ra 160,240,19584
Ad 145,29
Maav 44,176,48
Ex 31
Maav 44,180,52
Ra 164,240,19840
Pk 98,28,29
Maav 48,176,56
Ex 32
)A"
R"A(Maav 48,180,60
Ra 168,240,20096
Ad 144,30
Maav 52,176,64
Ex 33
Maav 52,180,68
Ra 172,240,20352
Ad 145,31
Maav 56,176,72
Ex 34
Maav 56,180,76
Wl 4
Pk 99,30,31
Ad 146,32
Maav 60,184,40
Ex 35
Maav 60,188,44
Ad 147,33
Pk 106,32,33
Maav 64,184,48
Ex 36
Maav 64,188,52
Ad 146,34
Ad 147,35
.p2align 12
.org PG0_%=+36864
s_cbranch_execz VRET_%=
Maav 68,184,56
Ex 37
Maav 68,188,60
Pk 107,34,35
Ad 148,36
Maav 72,184,64
Ex 38
Maav 72,188,68
Ad 149,37
Pk 114,36,37
Maav 76,184,72
Ex 39
Maav 76,188,76
Wl 0
Ad 148,38
Ad 149,39
Pk 115,38,39
Cvav 20,160,160,120
Ex 0
Mvav 20,164,164
Ta 176,241,512
Ad 140,0
Mvav 20,168,168
Ex 1
Mvav 20,172,172
Ta 178,241,1536
Ad 141,1
Cvav 24,172,188,124
Ex 2
Mvav 24,168,184
Ta 180,241,2560
Pk 84,0,1
Mvav 24,164,180
Ex 3
Mvav 24,160,176
Ta 182,241,3584
Ad 140,2
Cvav 28,160,192,128
Ex 4
Mvav 28,164,196
Ta 184,241,544
Ad 141,3
Mvav 28,168,200
Ex 5
Mvav 28,172,204
Ta 186,241,1568
Pk 85,2,3
Cvav 32,172,220,132
Ex 6
Mvav 32,168,216
Ta 188,241,2592
Ad 142,4
Mvav 32,164,212
Ex 7
Mvav 32,160,208
Ta 190,241,3616
Ad 143,5
Cvav 36,160,224,136
Ex 8
Mvav 36,164,228
Pk 92,4,5
Ad 142,6
Mvav 36,168,232
Ex 9
Mvav 36,172,236
Wl 4
Ad 143,7
Pk 93,6,7
Ad 144,8
Maav 80,176,40
Ex 10
Maav 80,180,44
Ra 160,240,33024
Ad 145,9
Maav 84,176,48
Ex 11
Maav 84,180,52
Ra 164,240,33280
Pk 100,8,9
Maav 88,176,56
Ex 12
Maav 88,180,60
Ra 168,240,33536
Ad 144,10
Maav 92,176,64
Ex 13
Maav 92,180,68
Ra 172,240,33792
Ad 145,11
Maav 96,176,72
Ex 14
Maav 96,180,76
Wl 4
Pk 101,10,11
Ad 146,12
Maav 100,184,40
Ex 15
Maav 100,188,44
Ad 147,13
Pk 108,12,13
Maav 104,184,48
Ex 16
Maav 104,188,52
Ad 146,14
Ad 147,15
Maav 108,184,56
Ex 17
Maav 108,188,60
Pk 109,14,15
Ad 148,16
Maav 112,184,64
Ex 18
Maav 112,188,68
Ad 149,17
Pk 116,16,17
Maav 116,184,72
Ex 19
Maav 116,188,76
Wl 0
Ad 148,18
Ad 149,19
Pk 117,18,19
Cvav 0,160,160,120
Ex 20
Mvav 0,164,164
Ta 176,241,768
Ad 140,20
Mvav 0,168,168
Ex 21
Mvav 0,172,172
Ta 178,241,1792
Ad 141,21
Cvav 4,172,188,124
Ex 22
Mvav 4,168,184
Ta 180,241,2816
Pk 86,20,21
Mvav 4,164,180
Ex 23
Mvav 4,160,176
Ta 182,241,3840
Ad 140,22
Cvav 8,160,192,128
Ex 24
Mvav 8,164,196
Ta 184,241,800
Ad 141,23
Mvav 8,168,200
Ex 25
Mvav 8,172,204
Ta 186,241,1824
Pk 87,22,23
Cvav 12,172,220,132
Ex 26
Mvav 12,168,216
Ta 188,241,2848
Ad 142,24
Mvav 12,164,212
Ex 27
Mvav 12,160,208
Ta 190,241,3872
Ad 143,25
Cvav 16,160,224,136
Ex 28
Mvav 16,164,228
Pk 94,24,25
Ad 142,26
Mvav 16,168,232
Ex 29
Mvav 16,172,236
Wl 4
Ad 143,27
Pk 95,26,27
Ad 144,28
Maav 120,176,40
Ex 30
Maav 120,180,44
Ra 160,240,34048
Ad 145,29
Maav 124,176,48
Ex 31
Maav 124,180,52
Ra 164,240,34304
Pk 102,28,29
Maav 128,176,56
Ex 32
Maav 128,180,60
Ra 168,240,34560
Ad 144,30
Maav 132,176,64
Ex 33
Maav 132,180,68
Ra 172,240,34816
Ad 145,31
Maav 136,176,72
Ex 34
Maav 136,180,76
Wl 4
Pk 103,30,31
Ad 146,32
Maav 140,184,40
Ex 35
Maav 140,188,44
Ad 147,33
Pk 110,32,33
Maav 144,184,48
Ex 36
Maav 144,188,52
Ad 146,34
Ad 147,35
Maav 148,184,56
Ex 37
Maav 148,188,60
Pk 111,34,35
Ad 148,36
Maav 152,184,64
Ex 38
Maav 152,188,68
Ad 149,37
Pk 118,36,37
Maav 156,184,72
s_add_u32 s47, s47, 1
Ex 39
Maav 156,188,76
Wl 0
Ad 148,38
Ad 149,39
Pk 119,38,39
s_branch LOOPA0_%=
EXITA0_%=:
Zvaa 0,160,192
Ex 20
Mvaa 0,164,196
Ta 176,241,33792
Ad 140,20
Mvaa 0,168,200
Ex 21
Mvaa 0,172,204
Ta 178,241,34816
Ad 141,21
Zvaa 4,172,220
Ex 22
Mvaa 4,168,216
Ta 180,241,35840
Pk 86,20,21
Mvaa 4,164,212
Ex 23
Mvaa 4,160,208
Ta 182,241,36864
Ad 140,22
Zvaa 8,160,224
Ex 24
Mvaa 8,164,228
Ta 184,241,33824
Ad 141,23
Mvaa 8,168,232
Ex 25
Mvaa 8,172,236
Ta 186,241,34848
Pk 87,22,23
Zvaa 12,172,252
Ex 26
Mvaa 12,168,248
Ta 188,241,35872
Ad 142,24
Mvaa 12,164,244
Ex 27
Mvaa 12,160,240
Ta 190,241,36896
Ad 143,25
Zvav 16,160,248
Ex 28
Mvav 16,164,252
Pk 94,24,25
Ad 142,26
Mvav 16,168,150
Ex 29
Mvav 16,172,154
Wl 4
Ad 143,27
Pk 95,26,27
Ad 144,28
Maav 120,176,40
Ex 30
Maav 120,180,44
Ra 160,240,1024
Ad 145,29
Maav 124,176,48
Ex 31
Maav 124,180,52
Ra 164,240,1280
Pk 102,28,29
Maav 128,176,56
Ex 32
Maav 128,180,60
Ra 168,240,1536
Ad 144,30
Maav 132,176,64
Ex 33
Maav 132,180,68
Ra 172,240,1792
Ad 145,31
Maav 136,176,72
Ex 34
Maav 136,180,76
Wl 4
Pk 103,30,31
Ad 146,32
Maav 140,184,40
Ex 35
Maav 140,188,44
Ad 147,33
Pk 110,32,33
Maav 144,184,48
Ex 36
Maav 144,188,52
Ad 146,34
Ad 147,35
Maav 148,184,56
Ex 37
Maav 148,188,60
Pk 111,34,35
Ad 148,36
Maav 152,184,64
Ex 38
Maav 152,188,68
Ad 149,37
Pk 118,36,37
Maav 156,184,72
Ex 39
Maav 156,188,76
Wl 0
Ad 148,38
Ad 149,39
Pk 119,38,39
Ad 140,141
Ad 142,143
Ad 144,145
Ad 146,147
Ad 148,149
v_mov_b32 v141, v140
v_mov_b32 v143, v142
v_mov_b32 v145, v144
v_mov_b32 v147, v146
v_mov_b32 v149, v148
s_nop 0
v_permlane32_swap_b32 v140, v141
v_permlane32_swap_b32 v142, v143
v_permlane32_swap_b32 v144, v145
v_permlane32_swap_b32 v146, v147
v_permlane32_swap_b32 v148, v149
Ad 140,141
Ad 142,143
Ad 144,145
Ad 146,147
Ad 148,149
v_mov_b32 v141, v140
v_mov_b32 v143, v142
v_mov_b32 v145, v144
v_mov_b32 v147, v146
v_mov_b32 v149, v148
s_nop 0
v_permlane16_swap_b32 v140, v141
v_permlane16_swap_b32 v142, v143
v_permlane16_swap_b32 v144, v145
v_permlane16_swap_b32 v146, v147
v_permlane16_swap_b32 v148, v149
Ad 140,141
Ad 142,143
Ad 144,145
Ad 146,147
Ad 148,149
v_add_f32 v141, v140, v142
)A"
R"A(Ad 141,144
Ad 141,146
Ad 141,148
v_cmp_nge_f32 vcc, 0x5d800000, v141
v_rcp_f32 v158, v140
v_rcp_f32 v159, v142
v_rcp_f32 v242, v144
v_rcp_f32 v243, v146
v_rcp_f32 v244, v148
s_or_b32 s54, vcc_lo, vcc_hi
v_max3_f32 v120, v0, v1, v2
v_max_f32 v120, v120, v3
v_max3_f32 v124, v4, v5, v6
v_max_f32 v124, v124, v7
v_max3_f32 v128, v8, v9, v10
v_max_f32 v128, v128, v11
v_max3_f32 v132, v12, v13, v14
v_max_f32 v132, v132, v15
v_max3_f32 v136, v16, v17, v18
v_max_f32 v136, v136, v19
v_mov_b32 v121, v120
v_mov_b32 v125, v124
v_mov_b32 v129, v128
v_mov_b32 v133, v132
v_mov_b32 v137, v136
s_nop 0
v_permlane32_swap_b32 v120, v121
v_permlane32_swap_b32 v124, v125
v_permlane32_swap_b32 v128, v129
v_permlane32_swap_b32 v132, v133
v_permlane32_swap_b32 v136, v137
v_max_f32 v120, v120, v121
v_max_f32 v124, v124, v125
v_max_f32 v128, v128, v129
v_max_f32 v132, v132, v133
v_max_f32 v136, v136, v137
v_mov_b32 v121, v120
v_mov_b32 v125, v124
v_mov_b32 v129, v128
v_mov_b32 v133, v132
v_mov_b32 v137, v136
s_nop 0
v_permlane16_swap_b32 v120, v121
v_permlane16_swap_b32 v124, v125
v_permlane16_swap_b32 v128, v129
v_permlane16_swap_b32 v132, v133
v_permlane16_swap_b32 v136, v137
v_max_f32 v120, v120, v121
v_max_f32 v124, v124, v125
v_max_f32 v128, v128, v129
v_max_f32 v132, v132, v133
v_max_f32 v136, v136, v137
v_pk_mul_f32 v[122:123], v[120:121], -1.0 op_sel_hi:[0,0]
v_pk_mul_f32 v[120:121], v[120:121], -1.0 op_sel_hi:[0,0]
v_pk_add_f32 v[0:1], v[0:1], v[122:123]
v_pk_add_f32 v[2:3], v[2:3], v[122:123]
v_pk_mov_b32 v[140:141], 0, 0
v_pk_mul_f32 v[126:127], v[124:125], -1.0 op_sel_hi:[0,0]
v_pk_mul_f32 v[124:125], v[124:125], -1.0 op_sel_hi:[0,0]
v_pk_add_f32 v[4:5], v[4:5], v[126:127]
v_pk_add_f32 v[6:7], v[6:7], v[126:127]
v_pk_mov_b32 v[142:143], 0, 0
v_pk_mul_f32 v[130:131], v[128:129], -1.0 op_sel_hi:[0,0]
v_pk_mul_f32 v[128:129], v[128:129], -1.0 op_sel_hi:[0,0]
v_pk_add_f32 v[8:9], v[8:9], v[130:131]
v_pk_add_f32 v[10:11], v[10:11], v[130:131]
v_pk_mov_b32 v[144:145], 0, 0
v_pk_mul_f32 v[134:135], v[132:133], -1.0 op_sel_hi:[0,0]
v_pk_mul_f32 v[132:133], v[132:133], -1.0 op_sel_hi:[0,0]
v_pk_add_f32 v[12:13], v[12:13], v[134:135]
v_pk_add_f32 v[14:15], v[14:15], v[134:135]
v_pk_mov_b32 v[146:147], 0, 0
v_pk_mul_f32 v[138:139], v[136:137], -1.0 op_sel_hi:[0,0]
v_pk_mul_f32 v[136:137], v[136:137], -1.0 op_sel_hi:[0,0]
v_pk_add_f32 v[16:17], v[16:17], v[138:139]
v_pk_add_f32 v[18:19], v[18:19], v[138:139]
v_pk_mov_b32 v[148:149], 0, 0
s_nop 0
Cvaa 20,160,192,120
s_barrier
Ex 0
Mvaa 20,164,196
Ta 176,241,49536
Ad 140,0
Mvaa 20,168,200
Ex 1
Mvaa 20,172,204
Ta 178,241,50560
Ad 141,1
Cvaa 24,172,220,124
Ex 2
Mvaa 24,168,216
Ta 180,241,51584
Pk 40,0,1
Mvaa 24,164,212
Ex 3
Mvaa 24,160,208
Ta 182,241,52608
Ad 140,2
Cvaa 28,160,224,128
Ex 4
Mvaa 28,164,228
Ta 184,241,49568
Ad 141,3
Mvaa 28,168,232
Ex 5
Mvaa 28,172,236
Ta 186,241,50592
Pk 41,2,3
Cvaa 32,172,252,132
Ex 6
Mvaa 32,168,248
Ta 188,241,51616
Ad 142,4
Mvaa 32,164,244
Ex 7
Mvaa 32,160,240
Ta 190,241,52640
Ad 143,5
Cvav 36,160,248,136
Ex 8
Mvav 36,164,252
Pk 48,4,5
Ad 142,6
Mvav 36,168,150
Ex 9
Mvav 36,172,154
Wl 4
Ad 143,7
Pk 49,6,7
Ad 144,8
Maav 0,176,80
Ex 10
Maav 0,180,84
Ra 160,240,2048
Ad 145,9
Maav 4,176,88
Ex 11
Maav 4,180,92
Ra 164,240,2304
Pk 56,8,9
Maav 8,176,96
Ex 12
Maav 8,180,100
Ra 168,240,2560
Ad 144,10
Maav 12,176,104
Ex 13
Maav 12,180,108
Ra 172,240,2816
Ad 145,11
Maav 16,176,112
Ex 14
Maav 16,180,116
Wl 4
Pk 57,10,11
Ad 146,12
Maav 20,184,80
Ex 15
Maav 20,188,84
Ad 147,13
Pk 64,12,13
Maav 24,184,88
Ex 16
Maav 24,188,92
Ad 146,14
Ad 147,15
Maav 28,184,96
Ex 17
Maav 28,188,100
Pk 65,14,15
Ad 148,16
Maav 32,184,104
Ex 18
Maav 32,188,108
Ad 149,17
Pk 72,16,17
Maav 36,184,112
Ex 19
Maav 36,188,116
Wl 0
Ad 148,18
Ad 149,19
Pk 73,18,19
Cvaa 0,160,192,120
Ex 20
Mvaa 0,164,196
Ta 176,241,49792
Ad 140,20
Mvaa 0,168,200
Ex 21
Mvaa 0,172,204
Ta 178,241,50816
Ad 141,21
Cvaa 4,172,220,124
Ex 22
Mvaa 4,168,216
Ta 180,241,51840
Pk 42,20,21
Mvaa 4,164,212
Ex 23
Mvaa 4,160,208
Ta 182,241,52864
Ad 140,22
Cvaa 8,160,224,128
Ex 24
Mvaa 8,164,228
Ta 184,241,49824
Ad 141,23
Mvaa 8,168,232
Ex 25
Mvaa 8,172,236
Ta 186,241,50848
Pk 43,22,23
Cvaa 12,172,252,132
Ex 26
Mvaa 12,168,248
Ta 188,241,51872
Ad 142,24
Mvaa 12,164,244
Ex 27
Mvaa 12,160,240
Ta 190,241,52896
Ad 143,25
Cvav 16,160,248,136
Ex 28
Mvav 16,164,252
Pk 50,24,25
Ad 142,26
Mvav 16,168,150
Ex 29
Mvav 16,172,154
Wl 4
Ad 143,27
Pk 51,26,27
Ad 144,28
Maav 40,176,80
Ex 30
Maav 40,180,84
Ra 160,240,3072
Ad 145,29
Maav 44,176,88
Ex 31
Maav 44,180,92
Ra 164,240,3328
Pk 58,28,29
Maav 48,176,96
Ex 32
Maav 48,180,100
Ra 168,240,3584
Ad 144,30
Maav 52,176,104
Ex 33
Maav 52,180,108
Ra 172,240,3840
Ad 145,31
Maav 56,176,112
Ex 34
Maav 56,180,116
Wl 4
Pk 59,30,31
Ad 146,32
Maav 60,184,80
Ex 35
Maav 60,188,84
Ad 147,33
Pk 66,32,33
Maav 64,184,88
Ex 36
Maav 64,188,92
Ad 146,34
Ad 147,35
Maav 68,184,96
Ex 37
Maav 68,188,100
Pk 67,34,35
Ad 148,36
Maav 72,184,104
Ex 38
Maav 72,188,108
Ad 149,37
Pk 74,36,37
Maav 76,184,112
Ex 39
Maav 76,188,116
Wl 0
Ad 148,38
Ad 149,39
Pk 75,38,39
Cvaa 20,160,192,120
Ex 0
Mvaa 20,164,196
Ta 176,241,50048
Ad 140,0
Mvaa 20,168,200
Ex 1
.p2align 12
.org PG0_%=+40960
s_cbranch_execz VRET_%=
Mvaa 20,172,204
Ta 178,241,51072
Ad 141,1
Cvaa 24,172,220,124
Ex 2
Mvaa 24,168,216
Ta 180,241,52096
Pk 44,0,1
Mvaa 24,164,212
Ex 3
Mvaa 24,160,208
Ta 182,241,53120
Ad 140,2
Cvaa 28,160,224,128
Ex 4
Mvaa 28,164,228
Ta 184,241,50080
Ad 141,3
Mvaa 28,168,232
Ex 5
Mvaa 28,172,236
Ta 186,241,51104
Pk 45,2,3
Cvaa 32,172,252,132
Ex 6
Mvaa 32,168,248
Ta 188,241,52128
Ad 142,4
Mvaa 32,164,244
Ex 7
Mvaa 32,160,240
Ta 190,241,53152
Ad 143,5
Cvav 36,160,248,136
Ex 8
Mvav 36,164,252
Pk 52,4,5
Ad 142,6
Mvav 36,168,150
Ex 9
Mvav 36,172,154
Wl 4
Ad 143,7
Pk 53,6,7
Ad 144,8
Maav 80,176,80
Ex 10
Maav 80,180,84
Ra 160,240,16512
Ad 145,9
Maav 84,176,88
Ex 11
Maav 84,180,92
Ra 164,240,16768
Pk 60,8,9
Maav 88,176,96
Ex 12
Maav 88,180,100
Ra 168,240,17024
Ad 144,10
Maav 92,176,104
Ex 13
Maav 92,180,108
Ra 172,240,17280
Ad 145,11
Maav 96,176,112
Ex 14
Maav 96,180,116
Wl 4
Pk 61,10,11
Ad 146,12
Maav 100,184,80
Ex 15
Maav 100,188,84
Ad 147,13
Pk 68,12,13
Maav 104,184,88
Ex 16
Maav 104,188,92
Ad 146,14
Ad 147,15
Maav 108,184,96
Ex 17
Maav 108,188,100
Pk 69,14,15
Ad 148,16
Maav 112,184,104
Ex 18
Maav 112,188,108
Ad 149,17
Pk 76,16,17
Maav 116,184,112
Ex 19
Maav 116,188,116
Wl 0
Ad 148,18
Ad 149,19
Pk 77,18,19
Cvaa 0,160,192,120
Ex 20
Mvaa 0,164,196
)A"
R"A(Ta 176,241,50304
Ad 140,20
Mvaa 0,168,200
Ex 21
Mvaa 0,172,204
Ta 178,241,51328
Ad 141,21
Cvaa 4,172,220,124
Ex 22
Mvaa 4,168,216
Ta 180,241,52352
Pk 46,20,21
Mvaa 4,164,212
Ex 23
Mvaa 4,160,208
Ta 182,241,53376
Ad 140,22
Cvaa 8,160,224,128
Ex 24
Mvaa 8,164,228
Ta 184,241,50336
Ad 141,23
Mvaa 8,168,232
Ex 25
Mvaa 8,172,236
Ta 186,241,51360
Pk 47,22,23
Cvaa 12,172,252,132
Ex 26
Mvaa 12,168,248
Ta 188,241,52384
Ad 142,24
Mvaa 12,164,244
Ex 27
Mvaa 12,160,240
Ta 190,241,53408
Ad 143,25
Cvav 16,160,248,136
Ex 28
Mvav 16,164,252
Pk 54,24,25
Ad 142,26
Mvav 16,168,150
Ex 29
Mvav 16,172,154
Wl 4
Ad 143,27
Pk 55,26,27
Ad 144,28
Maav 120,176,80
Ex 30
Maav 120,180,84
Ra 160,240,17536
Ad 145,29
Maav 124,176,88
Ex 31
Maav 124,180,92
Ra 164,240,17792
Pk 62,28,29
Maav 128,176,96
Ex 32
Maav 128,180,100
Ra 168,240,18048
Ad 144,30
Maav 132,176,104
Ex 33
Maav 132,180,108
Ra 172,240,18304
Ad 145,31
Maav 136,176,112
Ex 34
Maav 136,180,116
Wl 4
Pk 63,30,31
Ad 146,32
Maav 140,184,80
Ex 35
Maav 140,188,84
Ad 147,33
Pk 70,32,33
Maav 144,184,88
Ex 36
Maav 144,188,92
Ad 146,34
Ad 147,35
Maav 148,184,96
Ex 37
Maav 148,188,100
Pk 71,34,35
Ad 148,36
Maav 152,184,104
Ex 38
Maav 152,188,108
Ad 149,37
Pk 78,36,37
Maav 156,184,112
Ex 39
Maav 156,188,116
Wl 0
Ad 148,38
Ad 149,39
Pk 79,38,39
s_cmp_lg_u32 s54, 0
s_cbranch_scc1 SLOWA_%=
RET0A_%=:
.p2align 3
Ar 80,0
Ar 81,1
Ar 82,2
Ar 83,3
Ar 84,20
Ar 85,21
Ar 86,22
Ar 87,23
v_pk_mul_f32 v[80:81], v[80:81], v[158:159] op_sel_hi:[1,0]
v_pk_mul_f32 v[82:83], v[82:83], v[158:159] op_sel_hi:[1,0]
v_pk_mul_f32 v[84:85], v[84:85], v[158:159] op_sel_hi:[1,0]
v_pk_mul_f32 v[86:87], v[86:87], v[158:159] op_sel_hi:[1,0]
Pk 160,80,81
Pk 161,82,83
Pk 162,84,85
Pk 163,86,87
Ar 88,4
Ar 89,5
Ar 90,6
Ar 91,7
Ar 92,24
Ar 93,25
Ar 94,26
Ar 95,27
v_pk_mul_f32 v[88:89], v[88:89], v[158:159] op_sel:[0,1] op_sel_hi:[1,1]
v_pk_mul_f32 v[90:91], v[90:91], v[158:159] op_sel:[0,1] op_sel_hi:[1,1]
v_pk_mul_f32 v[92:93], v[92:93], v[158:159] op_sel:[0,1] op_sel_hi:[1,1]
v_pk_mul_f32 v[94:95], v[94:95], v[158:159] op_sel:[0,1] op_sel_hi:[1,1]
Pk 176,88,89
Pk 177,90,91
Pk 178,92,93
Pk 179,94,95
Ar 80,8
Ar 81,9
Ar 82,10
Ar 83,11
Ar 84,28
Ar 85,29
Ar 86,30
Ar 87,31
v_pk_mul_f32 v[80:81], v[80:81], v[242:243] op_sel_hi:[1,0]
v_pk_mul_f32 v[82:83], v[82:83], v[242:243] op_sel_hi:[1,0]
v_pk_mul_f32 v[84:85], v[84:85], v[242:243] op_sel_hi:[1,0]
v_pk_mul_f32 v[86:87], v[86:87], v[242:243] op_sel_hi:[1,0]
Pk 192,80,81
Pk 193,82,83
Pk 194,84,85
Pk 195,86,87
Ar 88,12
Ar 89,13
Ar 90,14
Ar 91,15
Ar 92,32
Ar 93,33
Ar 94,34
Ar 95,35
v_pk_mul_f32 v[88:89], v[88:89], v[242:243] op_sel:[0,1] op_sel_hi:[1,1]
v_pk_mul_f32 v[90:91], v[90:91], v[242:243] op_sel:[0,1] op_sel_hi:[1,1]
v_pk_mul_f32 v[92:93], v[92:93], v[242:243] op_sel:[0,1] op_sel_hi:[1,1]
v_pk_mul_f32 v[94:95], v[94:95], v[242:243] op_sel:[0,1] op_sel_hi:[1,1]
Pk 208,88,89
Pk 209,90,91
Pk 210,92,93
Pk 211,94,95
Ar 80,16
Ar 81,17
Ar 82,18
Ar 83,19
Ar 84,36
Ar 85,37
Ar 86,38
Ar 87,39
v_pk_mul_f32 v[80:81], v[80:81], v[244:245] op_sel_hi:[1,0]
v_pk_mul_f32 v[82:83], v[82:83], v[244:245] op_sel_hi:[1,0]
v_pk_mul_f32 v[84:85], v[84:85], v[244:245] op_sel_hi:[1,0]
v_pk_mul_f32 v[86:87], v[86:87], v[244:245] op_sel_hi:[1,0]
Pk 224,80,81
Pk 225,82,83
Pk 226,84,85
Pk 227,86,87
Ar 88,40
Ar 89,41
Ar 90,42
Ar 91,43
Ar 92,60
Ar 93,61
Ar 94,62
Ar 95,63
v_pk_mul_f32 v[88:89], v[88:89], v[158:159] op_sel_hi:[1,0]
v_pk_mul_f32 v[90:91], v[90:91], v[158:159] op_sel_hi:[1,0]
v_pk_mul_f32 v[92:93], v[92:93], v[158:159] op_sel_hi:[1,0]
v_pk_mul_f32 v[94:95], v[94:95], v[158:159] op_sel_hi:[1,0]
Pk 164,88,89
Pk 165,90,91
Pk 166,92,93
Pk 167,94,95
Ar 80,44
Ar 81,45
Ar 82,46
Ar 83,47
Ar 84,64
Ar 85,65
Ar 86,66
Ar 87,67
v_pk_mul_f32 v[80:81], v[80:81], v[158:159] op_sel:[0,1] op_sel_hi:[1,1]
v_pk_mul_f32 v[82:83], v[82:83], v[158:159] op_sel:[0,1] op_sel_hi:[1,1]
v_pk_mul_f32 v[84:85], v[84:85], v[158:159] op_sel:[0,1] op_sel_hi:[1,1]
v_pk_mul_f32 v[86:87], v[86:87], v[158:159] op_sel:[0,1] op_sel_hi:[1,1]
Pk 180,80,81
Pk 181,82,83
Pk 182,84,85
Pk 183,86,87
Ar 88,48
Ar 89,49
Ar 90,50
Ar 91,51
Ar 92,68
Ar 93,69
Ar 94,70
Ar 95,71
v_pk_mul_f32 v[88:89], v[88:89], v[242:243] op_sel_hi:[1,0]
v_pk_mul_f32 v[90:91], v[90:91], v[242:243] op_sel_hi:[1,0]
v_pk_mul_f32 v[92:93], v[92:93], v[242:243] op_sel_hi:[1,0]
v_pk_mul_f32 v[94:95], v[94:95], v[242:243] op_sel_hi:[1,0]
Pk 196,88,89
Pk 197,90,91
Pk 198,92,93
Pk 199,94,95
Ar 80,52
Ar 81,53
Ar 82,54
Ar 83,55
Ar 84,72
Ar 85,73
Ar 86,74
Ar 87,75
v_pk_mul_f32 v[80:81], v[80:81], v[242:243] op_sel:[0,1] op_sel_hi:[1,1]
v_pk_mul_f32 v[82:83], v[82:83], v[242:243] op_sel:[0,1] op_sel_hi:[1,1]
v_pk_mul_f32 v[84:85], v[84:85], v[242:243] op_sel:[0,1] op_sel_hi:[1,1]
v_pk_mul_f32 v[86:87], v[86:87], v[242:243] op_sel:[0,1] op_sel_hi:[1,1]
Pk 212,80,81
Pk 213,82,83
Pk 214,84,85
Pk 215,86,87
Ar 88,56
Ar 89,57
Ar 90,58
Ar 91,59
Ar 92,76
Ar 93,77
Ar 94,78
Ar 95,79
v_pk_mul_f32 v[88:89], v[88:89], v[244:245] op_sel_hi:[1,0]
v_pk_mul_f32 v[90:91], v[90:91], v[244:245] op_sel_hi:[1,0]
v_pk_mul_f32 v[92:93], v[92:93], v[244:245] op_sel_hi:[1,0]
v_pk_mul_f32 v[94:95], v[94:95], v[244:245] op_sel_hi:[1,0]
Pk 228,88,89
Pk 229,90,91
Pk 230,92,93
Pk 231,94,95
Ar 80,80
Ar 81,81
Ar 82,82
Ar 83,83
Ar 84,100
Ar 85,101
Ar 86,102
Ar 87,103
v_pk_mul_f32 v[80:81], v[80:81], v[158:159] op_sel_hi:[1,0]
v_pk_mul_f32 v[82:83], v[82:83], v[158:159] op_sel_hi:[1,0]
v_pk_mul_f32 v[84:85], v[84:85], v[158:159] op_sel_hi:[1,0]
v_pk_mul_f32 v[86:87], v[86:87], v[158:159] op_sel_hi:[1,0]
Pk 168,80,81
Pk 169,82,83
Pk 170,84,85
Pk 171,86,87
Ar 88,84
Ar 89,85
Ar 90,86
Ar 91,87
Ar 92,104
Ar 93,105
Ar 94,106
Ar 95,107
v_pk_mul_f32 v[88:89], v[88:89], v[158:159] op_sel:[0,1] op_sel_hi:[1,1]
v_pk_mul_f32 v[90:91], v[90:91], v[158:159] op_sel:[0,1] op_sel_hi:[1,1]
v_pk_mul_f32 v[92:93], v[92:93], v[158:159] op_sel:[0,1] op_sel_hi:[1,1]
v_pk_mul_f32 v[94:95], v[94:95], v[158:159] op_sel:[0,1] op_sel_hi:[1,1]
Pk 184,88,89
Pk 185,90,91
Pk 186,92,93
Pk 187,94,95
Ar 80,88
Ar 81,89
Ar 82,90
Ar 83,91
Ar 84,108
Ar 85,109
Ar 86,110
Ar 87,111
v_pk_mul_f32 v[80:81], v[80:81], v[242:243] op_sel_hi:[1,0]
v_pk_mul_f32 v[82:83], v[82:83], v[242:243] op_sel_hi:[1,0]
v_pk_mul_f32 v[84:85], v[84:85], v[242:243] op_sel_hi:[1,0]
v_pk_mul_f32 v[86:87], v[86:87], v[242:243] op_sel_hi:[1,0]
Pk 200,80,81
Pk 201,82,83
Pk 202,84,85
Pk 203,86,87
Ar 88,92
Ar 89,93
Ar 90,94
Ar 91,95
Ar 92,112
Ar 93,113
Ar 94,114
Ar 95,115
v_pk_mul_f32 v[88:89], v[88:89], v[242:243] op_sel:[0,1] op_sel_hi:[1,1]
v_pk_mul_f32 v[90:91], v[90:91], v[242:243] op_sel:[0,1] op_sel_hi:[1,1]
v_pk_mul_f32 v[92:93], v[92:93], v[242:243] op_sel:[0,1] op_sel_hi:[1,1]
v_pk_mul_f32 v[94:95], v[94:95], v[242:243] op_sel:[0,1] op_sel_hi:[1,1]
Pk 216,88,89
Pk 217,90,91
Pk 218,92,93
Pk 219,94,95
Ar 80,96
Ar 81,97
Ar 82,98
Ar 83,99
Ar 84,116
Ar 85,117
Ar 86,118
Ar 87,119
v_pk_mul_f32 v[80:81], v[80:81], v[244:245] op_sel_hi:[1,0]
v_pk_mul_f32 v[82:83], v[82:83], v[244:245] op_sel_hi:[1,0]
v_pk_mul_f32 v[84:85], v[84:85], v[244:245] op_sel_hi:[1,0]
v_pk_mul_f32 v[86:87], v[86:87], v[244:245] op_sel_hi:[1,0]
Pk 232,80,81
Pk 233,82,83
Pk 234,84,85
Pk 235,86,87
Ar 88,120
Ar 89,121
Ar 90,122
Ar 91,123
Ar 92,140
Ar 93,141
Ar 94,142
Ar 95,143
v_pk_mul_f32 v[88:89], v[88:89], v[158:159] op_sel_hi:[1,0]
v_pk_mul_f32 v[90:91], v[90:91], v[158:159] op_sel_hi:[1,0]
v_pk_mul_f32 v[92:93], v[92:93], v[158:159] op_sel_hi:[1,0]
v_pk_mul_f32 v[94:95], v[94:95], v[158:159] op_sel_hi:[1,0]
Pk 172,88,89
Pk 173,90,91
Pk 174,92,93
Pk 175,94,95
Ar 80,124
Ar 81,125
Ar 82,126
Ar 83,127
Ar 84,144
Ar 85,145
Ar 86,146
Ar 87,147
v_pk_mul_f32 v[80:81], v[80:81], v[158:159] op_sel:[0,1] op_sel_hi:[1,1]
v_pk_mul_f32 v[82:83], v[82:83], v[158:159] op_sel:[0,1] op_sel_hi:[1,1]
v_pk_mul_f32 v[84:85], v[84:85], v[158:159] op_sel:[0,1] op_sel_hi:[1,1]
v_pk_mul_f32 v[86:87], v[86:87], v[158:159] op_sel:[0,1] op_sel_hi:[1,1]
Pk 188,80,81
Pk 189,82,83
Pk 190,84,85
Pk 191,86,87
Ar 88,128
Ar 89,129
Ar 90,130
Ar 91,131
Ar 92,148
Ar 93,149
Ar 94,150
Ar 95,151
v_pk_mul_f32 v[88:89], v[88:89], v[242:243] op_sel_hi:[1,0]
v_pk_mul_f32 v[90:91], v[90:91], v[242:243] op_sel_hi:[1,0]
v_pk_mul_f32 v[92:93], v[92:93], v[242:243] op_sel_hi:[1,0]
v_pk_mul_f32 v[94:95], v[94:95], v[242:243] op_sel_hi:[1,0]
Pk 204,88,89
Pk 205,90,91
Pk 206,92,93
Pk 207,94,95
Ar 80,132
Ar 81,133
Ar 82,134
Ar 83,135
Ar 84,152
Ar 85,153
)A"
R"A(Ar 86,154
Ar 87,155
v_pk_mul_f32 v[80:81], v[80:81], v[242:243] op_sel:[0,1] op_sel_hi:[1,1]
v_pk_mul_f32 v[82:83], v[82:83], v[242:243] op_sel:[0,1] op_sel_hi:[1,1]
v_pk_mul_f32 v[84:85], v[84:85], v[242:243] op_sel:[0,1] op_sel_hi:[1,1]
v_pk_mul_f32 v[86:87], v[86:87], v[242:243] op_sel:[0,1] op_sel_hi:[1,1]
Pk 220,80,81
Pk 221,82,83
Pk 222,84,85
Pk 223,86,87
Ar 88,136
Ar 89,137
Ar 90,138
Ar 91,139
Ar 92,156
Ar 93,157
Ar 94,158
Ar 95,159
v_pk_mul_f32 v[88:89], v[88:89], v[244:245] op_sel_hi:[1,0]
v_pk_mul_f32 v[90:91], v[90:91], v[244:245] op_sel_hi:[1,0]
v_pk_mul_f32 v[92:93], v[92:93], v[244:245] op_sel_hi:[1,0]
v_pk_mul_f32 v[94:95], v[94:95], v[244:245] op_sel_hi:[1,0]
Pk 236,88,89
Pk 237,90,91
Pk 238,92,93
Pk 239,94,95
v_and_b32 v159, 3, v245
v_lshl_add_u32 v159, v159, 10, s59
v_writelane_b32 v159, s77, 4
v_writelane_b32 v159, s78, 5
v_writelane_b32 v159, s80, 6
global_load_dword v158, v159, s[92:93] sc1
v_lshlrev_b32 v242, 4, v245
s_mov_b32 s48, 1
s_mov_b32 s47, 0
v_lshlrev_b32 v243, 2, v245
Cvaa 20,160,192,120
s_barrier
s_mov_b32 s57, 0
Ex 0
Mvaa 20,164,196
Ta 176,241,0
buffer_store_dwordx4 v[160:163], v242, s[96:99], s57 offen offset:0 sc1
Mvaa 20,168,200
Ex 1
Mvaa 20,172,204
Ta 178,241,1024
Ad 140,0
Cvaa 24,172,220,124
Ex 2
Mvaa 24,168,216
Ta 180,241,2048
Ad 141,1
Mvaa 24,164,212
Ex 3
Mvaa 24,160,208
Ta 182,241,3072
Pk 80,0,1
Cvaa 28,160,224,128
Ex 4
Mvaa 28,164,228
Ta 184,241,32
.p2align 12
.org PG0_%=+45056
s_cbranch_execz VRET_%=
s_nop 0
Ad 140,2
Mvaa 28,168,232
Ex 5
Mvaa 28,172,236
Ta 186,241,1056
Ad 141,3
Cvaa 32,172,252,132
Ex 6
Mvaa 32,168,248
Ta 188,241,2080
Pk 81,2,3
Ad 142,4
Mvaa 32,164,244
Ex 7
Mvaa 32,160,240
Ta 190,241,3104
Ad 143,5
Cvav 36,160,248,136
Ex 8
Mvav 36,164,252
Pk 88,4,5
Ad 142,6
Mvav 36,168,150
Ex 9
Mvav 36,172,154
Wl 4
Ad 143,7
Pk 89,6,7
Zaav 0,176,40
Ex 10
Maav 0,180,44
Ra 160,240,18560
Ad 144,8
Zaav 4,176,48
Ex 11
Maav 4,180,52
Ra 164,240,18816
Ad 145,9
Zaav 8,176,56
Ex 12
Maav 8,180,60
Ra 168,240,19072
Pk 96,8,9
Zaav 12,176,64
Ex 13
Maav 12,180,68
Ra 172,240,19328
Ad 144,10
Ad 145,11
Zaav 16,176,72
Ex 14
Maav 16,180,76
Wl 4
Pk 97,10,11
Ad 146,12
Zaav 20,184,40
Ex 15
Maav 20,188,44
Ad 147,13
Pk 104,12,13
Zaav 24,184,48
Ex 16
Maav 24,188,52
Ad 146,14
Ad 147,15
Zaav 28,184,56
Ex 17
Maav 28,188,60
Pk 105,14,15
Ad 148,16
Zaav 32,184,64
Ex 18
Maav 32,188,68
Ad 149,17
Pk 112,16,17
Zaav 36,184,72
Ex 19
Maav 36,188,76
Wl 0
Ad 148,18
Ad 149,19
Pk 113,18,19
Cvaa 0,160,192,120
s_mov_b32 s57, 0
Ex 20
Mvaa 0,164,196
Ta 176,241,256
buffer_store_dwordx4 v[164:167], v242, s[96:99], s57 offen offset:1024 sc1
Mvaa 0,168,200
Ex 21
Mvaa 0,172,204
Ta 178,241,1280
Ad 140,20
Cvaa 4,172,220,124
Ex 22
Mvaa 4,168,216
Ta 180,241,2304
Ad 141,21
Mvaa 4,164,212
Ex 23
Mvaa 4,160,208
Ta 182,241,3328
Pk 82,20,21
Cvaa 8,160,224,128
Ex 24
Mvaa 8,164,228
Ta 184,241,288
Ad 140,22
Mvaa 8,168,232
Ex 25
Mvaa 8,172,236
Ta 186,241,1312
Ad 141,23
Cvaa 12,172,252,132
Ex 26
Mvaa 12,168,248
Ta 188,241,2336
Pk 83,22,23
Ad 142,24
Mvaa 12,164,244
Ex 27
Mvaa 12,160,240
Ta 190,241,3360
Ad 143,25
Cvav 16,160,248,136
Ex 28
Mvav 16,164,252
Pk 90,24,25
Ad 142,26
Mvav 16,168,150
Ex 29
Mvav 16,172,154
Wl 4
Ad 143,27
Pk 91,26,27
Zaav 40,176,40
Ex 30
Maav 40,180,44
Ra 160,240,19584
Ad 144,28
Zaav 44,176,48
Ex 31
Maav 44,180,52
Ra 164,240,19840
Ad 145,29
Zaav 48,176,56
Ex 32
Maav 48,180,60
Ra 168,240,20096
Pk 98,28,29
Zaav 52,176,64
Ex 33
Maav 52,180,68
Ra 172,240,20352
Ad 144,30
Ad 145,31
Zaav 56,176,72
Ex 34
Maav 56,180,76
Wl 4
Pk 99,30,31
Ad 146,32
Zaav 60,184,40
Ex 35
Maav 60,188,44
Ad 147,33
Pk 106,32,33
Zaav 64,184,48
Ex 36
Maav 64,188,52
Ad 146,34
Ad 147,35
Zaav 68,184,56
Ex 37
Maav 68,188,60
Pk 107,34,35
Ad 148,36
Zaav 72,184,64
Ex 38
Maav 72,188,68
Ad 149,37
Pk 114,36,37
Zaav 76,184,72
Ex 39
Maav 76,188,76
Wl 0
Ad 148,38
Ad 149,39
Pk 115,38,39
Cvaa 20,160,192,120
Ex 0
Mvaa 20,164,196
Ta 176,241,512
Ad 140,0
Mvaa 20,168,200
Ex 1
Mvaa 20,172,204
Ta 178,241,1536
Ad 141,1
Cvaa 24,172,220,124
Ex 2
Mvaa 24,168,216
Ta 180,241,2560
Pk 84,0,1
Mvaa 24,164,212
Ex 3
Mvaa 24,160,208
Ta 182,241,3584
Ad 140,2
Cvaa 28,160,224,128
Ex 4
Mvaa 28,164,228
Ta 184,241,544
Ad 141,3
Mvaa 28,168,232
Ex 5
Mvaa 28,172,236
Ta 186,241,1568
Pk 85,2,3
Cvaa 32,172,252,132
Ex 6
Mvaa 32,168,248
Ta 188,241,2592
Ad 142,4
Mvaa 32,164,244
Ex 7
Mvaa 32,160,240
Ta 190,241,3616
Ad 143,5
Cvav 36,160,248,136
Ex 8
Mvav 36,164,252
Pk 92,4,5
Ad 142,6
Mvav 36,168,150
Ex 9
Mvav 36,172,154
Wl 4
Ad 143,7
Pk 93,6,7
Ad 144,8
Zaav 80,176,40
Ex 10
Maav 80,180,44
Ra 160,240,33024
Ad 145,9
Zaav 84,176,48
Ex 11
Maav 84,180,52
Ra 164,240,33280
Pk 100,8,9
Zaav 88,176,56
Ex 12
Maav 88,180,60
Ra 168,240,33536
Ad 144,10
Zaav 92,176,64
Ex 13
Maav 92,180,68
Ra 172,240,33792
Ad 145,11
Zaav 96,176,72
Ex 14
Maav 96,180,76
Wl 4
Pk 101,10,11
Ad 146,12
Zaav 100,184,40
Ex 15
Maav 100,188,44
Ad 147,13
Pk 108,12,13
Zaav 104,184,48
Ex 16
Maav 104,188,52
Ad 146,14
Ad 147,15
Zaav 108,184,56
Ex 17
Maav 108,188,60
Pk 109,14,15
Ad 148,16
Zaav 112,184,64
Ex 18
Maav 112,188,68
Ad 149,17
Pk 116,16,17
Zaav 116,184,72
Ex 19
Maav 116,188,76
Wl 0
Ad 148,18
Ad 149,19
Pk 117,18,19
Cvaa 0,160,192,120
s_mov_b32 s57, 0
Ex 20
Mvaa 0,164,196
Ta 176,241,768
buffer_store_dwordx4 v[168:171], v242, s[96:99], s57 offen offset:2048 sc1
Mvaa 0,168,200
Ex 21
Mvaa 0,172,204
Ta 178,241,1792
Ad 140,20
Cvaa 4,172,220,124
Ex 22
Mvaa 4,168,216
Ta 180,241,2816
Ad 141,21
Mvaa 4,164,212
Ex 23
Mvaa 4,160,208
Ta 182,241,3840
Pk 86,20,21
Cvaa 8,160,224,128
Ex 24
Mvaa 8,164,228
Ta 184,241,800
Ad 140,22
Mvaa 8,168,232
Ex 25
Mvaa 8,172,236
Ta 186,241,1824
Ad 141,23
Cvaa 12,172,252,132
Ex 26
Mvaa 12,168,248
Ta 188,241,2848
Pk 87,22,23
Ad 142,24
Mvaa 12,164,244
Ex 27
Mvaa 12,160,240
)A"
R"A(Ta 190,241,3872
Ad 143,25
Cvav 16,160,248,136
Ex 28
Mvav 16,164,252
Pk 94,24,25
Ad 142,26
Mvav 16,168,150
Ex 29
Mvav 16,172,154
Wl 4
Ad 143,27
Pk 95,26,27
Zaav 120,176,40
Ex 30
Maav 120,180,44
Ra 160,240,34048
Ad 144,28
Zaav 124,176,48
Ex 31
Maav 124,180,52
Ra 164,240,34304
Ad 145,29
Zaav 128,176,56
Ex 32
Maav 128,180,60
Ra 168,240,34560
Pk 102,28,29
Zaav 132,176,64
Ex 33
Maav 132,180,68
Ra 172,240,34816
Ad 144,30
Ad 145,31
Zaav 136,176,72
Ex 34
Maav 136,180,76
Wl 4
Pk 103,30,31
Ad 146,32
Zaav 140,184,40
Ex 35
Maav 140,188,44
Ad 147,33
Pk 110,32,33
Zaav 144,184,48
Ex 36
Maav 144,188,52
Ad 146,34
Ad 147,35
Zaav 148,184,56
Ex 37
Maav 148,188,60
Pk 111,34,35
Ad 148,36
Zaav 152,184,64
Ex 38
Maav 152,188,68
Ad 149,37
Pk 118,36,37
Zaav 156,184,72
Ex 39
Maav 156,188,76
Wl 0
Ad 148,38
Ad 149,39
Pk 119,38,39
Cvaa 20,160,192,120
s_barrier
s_mov_b32 s57, 0
Ex 0
Mvaa 20,164,196
Ta 176,241,16512
buffer_store_dwordx4 v[172:175], v242, s[96:99], s57 offen offset:3072 sc1
Mvaa 20,168,200
Ex 1
Mvaa 20,172,204
Ta 178,241,17536
Ad 140,0
Cvaa 24,172,220,124
Ex 2
Mvaa 24,168,216
Ta 180,241,18560
Ad 141,1
Mvaa 24,164,212
Ex 3
Mvaa 24,160,208
Ta 182,241,19584
Pk 40,0,1
Cvaa 28,160,224,128
Ex 4
Mvaa 28,164,228
Ta 184,241,16544
Ad 140,2
Mvaa 28,168,232
Ex 5
Mvaa 28,172,236
Ta 186,241,17568
Ad 141,3
Cvaa 32,172,252,132
Ex 6
Mvaa 32,168,248
Ta 188,241,18592
Pk 41,2,3
Ad 142,4
Mvaa 32,164,244
Ex 7
Mvaa 32,160,240
Ta 190,241,19616
Ad 143,5
Cvav 36,160,248,136
Ex 8
Mvav 36,164,252
Pk 48,4,5
Ad 142,6
Mvav 36,168,150
Ex 9
Mvav 36,172,154
Wl 4
Ad 143,7
Pk 49,6,7
Maav 0,176,80
Ex 10
Maav 0,180,84
Ra 160,240,35072
Ad 144,8
Maav 4,176,88
Ex 11
Maav 4,180,92
Ra 164,240,35328
Ad 145,9
Maav 8,176,96
Ex 12
Maav 8,180,100
Ra 168,240,35584
Pk 56,8,9
Maav 12,176,104
Ex 13
Maav 12,180,108
Ra 172,240,35840
Ad 144,10
Ad 145,11
Maav 16,176,112
Ex 14
Maav 16,180,116
Wl 4
Pk 57,10,11
Ad 146,12
Maav 20,184,80
Ex 15
Maav 20,188,84
Ad 147,13
Pk 64,12,13
Maav 24,184,88
Ex 16
Maav 24,188,92
Ad 146,14
Ad 147,15
Maav 28,184,96
Ex 17
Maav 28,188,100
Pk 65,14,15
Ad 148,16
Maav 32,184,104
Ex 18
Maav 32,188,108
Ad 149,17
Pk 72,16,17
Maav 36,184,112
Ex 19
Maav 36,188,116
Wl 0
Ad 148,18
Ad 149,19
Pk 73,18,19
Cvaa 0,160,192,120
Ex 20
Mvaa 0,164,196
Ta 176,241,16768
Ad 140,20
Mvaa 0,168,200
Ex 21
Mvaa 0,172,204
Ta 178,241,17792
Ad 141,21
Cvaa 4,172,220,124
Ex 22
Mvaa 4,168,216
Ta 180,241,18816
Pk 42,20,21
Mvaa 4,164,212
Ex 23
Mvaa 4,160,208
Ta 182,241,19840
Ad 140,22
Cvaa 8,160,224,128
Ex 24
Mvaa 8,164,228
Ta 184,241,16800
Ad 141,23
Mvaa 8,168,232
Ex 25
Mvaa 8,172,236
Ta 186,241,17824
Pk 43,22,23
Cvaa 12,172,252,132
Ex 26
Mvaa 12,168,248
Ta 188,241,18848
Ad 142,24
Mvaa 12,164,244
Ex 27
Mvaa 12,160,240
Ta 190,241,19872
Ad 143,25
Cvav 16,160,248,136
Ex 28
Mvav 16,164,252
Pk 50,24,25
Ad 142,26
Mvav 16,168,150
Ex 29
Mvav 16,172,154
Wl 4
Ad 143,27
Pk 51,26,27
Ad 144,28
Maav 40,176,80
Ex 30
Maav 40,180,84
Ra 160,240,36096
Ad 145,29
Maav 44,176,88
Ex 31
Maav 44,180,92
Ra 164,240,36352
Pk 58,28,29
Maav 48,176,96
Ex 32
Maav 48,180,100
Ra 168,240,36608
Ad 144,30
Maav 52,176,104
Ex 33
Maav 52,180,108
Ra 172,240,36864
Ad 145,31
Maav 56,176,112
Ex 34
Maav 56,180,116
Wl 4
Pk 59,30,31
Ad 146,32
Maav 60,184,80
Ex 35
Maav 60,188,84
Ad 147,33
Pk 66,32,33
Maav 64,184,88
Ex 36
Maav 64,188,92
Ad 146,34
Ad 147,35
Maav 68,184,96
Ex 37
Maav 68,188,100
Pk 67,34,35
Ad 148,36
Maav 72,184,104
Ex 38
Maav 72,188,108
Ad 149,37
Pk 74,36,37
Maav 76,184,112
Ex 39
Maav 76,188,116
Wl 0
Ad 148,38
Ad 149,39
Pk 75,38,39
Cvaa 20,160,192,120
s_mov_b32 s57, 16384
Ex 0
Mvaa 20,164,196
Ta 176,241,17024
buffer_store_dwordx4 v[176:179], v242, s[96:99], s57 offen offset:0 sc1
Mvaa 20,168,200
Ex 1
Mvaa 20,172,204
Ta 178,241,18048
Ad 140,0
Cvaa 24,172,220,124
Ex 2
Mvaa 24,168,216
Ta 180,241,19072
Ad 141,1
Mvaa 24,164,212
Ex 3
Mvaa 24,160,208
Ta 182,241,20096
Pk 44,0,1
Cvaa 28,160,224,128
Ex 4
Mvaa 28,164,228
Ta 184,241,17056
Ad 140,2
Mvaa 28,168,232
Ex 5
Mvaa 28,172,236
.p2align 12
.org PG0_%=+49152
s_cbranch_execz VRET_%=
s_nop 0
Ta 186,241,18080
Ad 141,3
Cvaa 32,172,252,132
Ex 6
Mvaa 32,168,248
Ta 188,241,19104
Pk 45,2,3
Ad 142,4
Mvaa 32,164,244
Ex 7
Mvaa 32,160,240
Ta 190,241,20128
Ad 143,5
Cvav 36,160,248,136
Ex 8
Mvav 36,164,252
Pk 52,4,5
Ad 142,6
Mvav 36,168,150
Ex 9
Mvav 36,172,154
Wl 4
Ad 143,7
Pk 53,6,7
Maav 80,176,80
Ex 10
Maav 80,180,84
Ra 160,240,49536
Ad 144,8
Maav 84,176,88
Ex 11
Maav 84,180,92
Ra 164,240,49792
Ad 145,9
Maav 88,176,96
Ex 12
Maav 88,180,100
Ra 168,240,50048
Pk 60,8,9
Maav 92,176,104
Ex 13
Maav 92,180,108
Ra 172,240,50304
Ad 144,10
Ad 145,11
Maav 96,176,112
Ex 14
Maav 96,180,116
Wl 4
Pk 61,10,11
Ad 146,12
Maav 100,184,80
Ex 15
Maav 100,188,84
Ad 147,13
Pk 68,12,13
Maav 104,184,88
Ex 16
Maav 104,188,92
Ad 146,14
Ad 147,15
Maav 108,184,96
Ex 17
Maav 108,188,100
Pk 69,14,15
Ad 148,16
Maav 112,184,104
Ex 18
Maav 112,188,108
Ad 149,17
Pk 76,16,17
Maav 116,184,112
Ex 19
Maav 116,188,116
Wl 0
Ad 148,18
Ad 149,19
Pk 77,18,19
Cvaa 0,160,192,120
Ex 20
Mvaa 0,164,196
Ta 176,241,17280
Ad 140,20
Mvaa 0,168,200
Ex 21
Mvaa 0,172,204
Ta 178,241,18304
)A"
R"A(Ad 141,21
Cvaa 4,172,220,124
Ex 22
Mvaa 4,168,216
Ta 180,241,19328
Pk 46,20,21
Mvaa 4,164,212
Ex 23
Mvaa 4,160,208
Ta 182,241,20352
Ad 140,22
Cvaa 8,160,224,128
Ex 24
Mvaa 8,164,228
Ta 184,241,17312
Ad 141,23
Mvaa 8,168,232
Ex 25
Mvaa 8,172,236
Ta 186,241,18336
Pk 47,22,23
Cvaa 12,172,252,132
Ex 26
Mvaa 12,168,248
Ta 188,241,19360
Ad 142,24
Mvaa 12,164,244
Ex 27
Mvaa 12,160,240
Ta 190,241,20384
Ad 143,25
Cvav 16,160,248,136
Ex 28
Mvav 16,164,252
Pk 54,24,25
Ad 142,26
Mvav 16,168,150
Ex 29
Mvav 16,172,154
Wl 4
Ad 143,27
Pk 55,26,27
Ad 144,28
Maav 120,176,80
Ex 30
Maav 120,180,84
Ra 160,240,50560
Ad 145,29
Maav 124,176,88
Ex 31
Maav 124,180,92
Ra 164,240,50816
Pk 62,28,29
Maav 128,176,96
Ex 32
Maav 128,180,100
Ra 168,240,51072
Ad 144,30
Maav 132,176,104
Ex 33
Maav 132,180,108
Ra 172,240,51328
Ad 145,31
Maav 136,176,112
Ex 34
Maav 136,180,116
Wl 4
Pk 63,30,31
Ad 146,32
Maav 140,184,80
Ex 35
Maav 140,188,84
Ad 147,33
Pk 70,32,33
Maav 144,184,88
Ex 36
Maav 144,188,92
Ad 146,34
Ad 147,35
Maav 148,184,96
Ex 37
Maav 148,188,100
Pk 71,34,35
Ad 148,36
Maav 152,184,104
Ex 38
Maav 152,188,108
Ad 149,37
Pk 78,36,37
Maav 156,184,112
Ex 39
Maav 156,188,116
Wl 0
Ad 148,38
Ad 149,39
Pk 79,38,39
Cvaa 20,160,192,120
s_barrier
s_mov_b32 s57, 16384
Ex 0
Mvaa 20,164,196
Ta 176,241,33024
buffer_store_dwordx4 v[180:183], v242, s[96:99], s57 offen offset:1024 sc1
Mvaa 20,168,200
Ex 1
Mvaa 20,172,204
Ta 178,241,34048
Ad 140,0
Cvaa 24,172,220,124
Ex 2
Mvaa 24,168,216
Ta 180,241,35072
Ad 141,1
Mvaa 24,164,212
Ex 3
Mvaa 24,160,208
Ta 182,241,36096
Pk 80,0,1
Cvaa 28,160,224,128
Ex 4
Mvaa 28,164,228
Ta 184,241,33056
Ad 140,2
Mvaa 28,168,232
Ex 5
Mvaa 28,172,236
Ta 186,241,34080
Ad 141,3
Cvaa 32,172,252,132
Ex 6
Mvaa 32,168,248
Ta 188,241,35104
Pk 81,2,3
Ad 142,4
Mvaa 32,164,244
Ex 7
Mvaa 32,160,240
Ta 190,241,36128
Ad 143,5
Cvav 36,160,248,136
Ex 8
Mvav 36,164,252
Pk 88,4,5
Ad 142,6
Mvav 36,168,150
Ex 9
Mvav 36,172,154
Wl 4
Ad 143,7
Pk 89,6,7
Maav 0,176,40
Ex 10
Maav 0,180,44
Ra 160,240,51584
Ad 144,8
Maav 4,176,48
Ex 11
Maav 4,180,52
Ra 164,240,51840
Ad 145,9
Maav 8,176,56
Ex 12
Maav 8,180,60
Ra 168,240,52096
Pk 96,8,9
Maav 12,176,64
Ex 13
Maav 12,180,68
Ra 172,240,52352
Ad 144,10
Ad 145,11
Maav 16,176,72
Ex 14
Maav 16,180,76
Wl 4
Pk 97,10,11
Ad 146,12
Maav 20,184,40
Ex 15
Maav 20,188,44
Ad 147,13
Pk 104,12,13
Maav 24,184,48
Ex 16
Maav 24,188,52
Ad 146,14
Ad 147,15
Maav 28,184,56
Ex 17
Maav 28,188,60
Pk 105,14,15
Ad 148,16
Maav 32,184,64
Ex 18
Maav 32,188,68
Ad 149,17
Pk 112,16,17
Maav 36,184,72
Ex 19
Maav 36,188,76
Wl 0
Ad 148,18
Ad 149,19
Pk 113,18,19
Cvaa 0,160,192,120
s_mov_b32 s57, 16384
Ex 20
Mvaa 0,164,196
Ta 176,241,33280
buffer_store_dwordx4 v[184:187], v242, s[96:99], s57 offen offset:2048 sc1
Mvaa 0,168,200
Ex 21
Mvaa 0,172,204
Ta 178,241,34304
Ad 140,20
Cvaa 4,172,220,124
Ex 22
Mvaa 4,168,216
Ta 180,241,35328
Ad 141,21
Mvaa 4,164,212
Ex 23
Mvaa 4,160,208
Ta 182,241,36352
Pk 82,20,21
Cvaa 8,160,224,128
Ex 24
Mvaa 8,164,228
Ta 184,241,33312
Ad 140,22
Mvaa 8,168,232
Ex 25
Mvaa 8,172,236
Ta 186,241,34336
Ad 141,23
Cvaa 12,172,252,132
Ex 26
Mvaa 12,168,248
Ta 188,241,35360
Pk 83,22,23
Ad 142,24
Mvaa 12,164,244
Ex 27
Mvaa 12,160,240
Ta 190,241,36384
Ad 143,25
Cvav 16,160,248,136
Ex 28
Mvav 16,164,252
Pk 90,24,25
Ad 142,26
Mvav 16,168,150
Ex 29
Mvav 16,172,154
Wl 4
Ad 143,27
Pk 91,26,27
Maav 40,176,40
Ex 30
Maav 40,180,44
Ra 160,240,52608
Ad 144,28
Maav 44,176,48
Ex 31
Maav 44,180,52
Ra 164,240,52864
Ad 145,29
Maav 48,176,56
Ex 32
Maav 48,180,60
Ra 168,240,53120
Pk 98,28,29
Maav 52,176,64
Ex 33
Maav 52,180,68
Ra 172,240,53376
Ad 144,30
Ad 145,31
Maav 56,176,72
Ex 34
Maav 56,180,76
Wl 4
Pk 99,30,31
Ad 146,32
Maav 60,184,40
Ex 35
Maav 60,188,44
Ad 147,33
Pk 106,32,33
Maav 64,184,48
Ex 36
Maav 64,188,52
Ad 146,34
Ad 147,35
Maav 68,184,56
Ex 37
Maav 68,188,60
Pk 107,34,35
Ad 148,36
Maav 72,184,64
Ex 38
Maav 72,188,68
Ad 149,37
Pk 114,36,37
Maav 76,184,72
Ex 39
Maav 76,188,76
Wl 0
Ad 148,38
Ad 149,39
Pk 115,38,39
Cvaa 20,160,192,120
Ex 0
Mvaa 20,164,196
Ta 176,241,33536
Ad 140,0
Mvaa 20,168,200
Ex 1
Mvaa 20,172,204
Ta 178,241,34560
Ad 141,1
Cvaa 24,172,220,124
Ex 2
Mvaa 24,168,216
Ta 180,241,35584
Pk 84,0,1
Mvaa 24,164,212
Ex 3
Mvaa 24,160,208
Ta 182,241,36608
Ad 140,2
Cvaa 28,160,224,128
Ex 4
Mvaa 28,164,228
Ta 184,241,33568
Ad 141,3
Mvaa 28,168,232
Ex 5
Mvaa 28,172,236
Ta 186,241,34592
Pk 85,2,3
Cvaa 32,172,252,132
Ex 6
Mvaa 32,168,248
Ta 188,241,35616
Ad 142,4
Mvaa 32,164,244
Ex 7
Mvaa 32,160,240
Ta 190,241,36640
Ad 143,5
Cvav 36,160,248,136
Ex 8
Mvav 36,164,252
Pk 92,4,5
Ad 142,6
Mvav 36,168,150
Ex 9
Mvav 36,172,154
Wl 4
Ad 143,7
Pk 93,6,7
Ad 144,8
Maav 80,176,40
Ex 10
Maav 80,180,44
Ra 160,240,0
Ad 145,9
Maav 84,176,48
Ex 11
Maav 84,180,52
Ra 164,240,256
Pk 100,8,9
Maav 88,176,56
Ex 12
Maav 88,180,60
Ra 168,240,512
Ad 144,10
Maav 92,176,64
Ex 13
Maav 92,180,68
Ra 172,240,768
Ad 145,11
Maav 96,176,72
Ex 14
Maav 96,180,76
Wl 4
Pk 101,10,11
Ad 146,12
Maav 100,184,40
Ex 15
Maav 100,188,44
Ad 147,13
Pk 108,12,13
Maav 104,184,48
Ex 16
Maav 104,188,52
Ad 146,14
Ad 147,15
Maav 108,184,56
)A"
R"A(Ex 17
Maav 108,188,60
Pk 109,14,15
Ad 148,16
Maav 112,184,64
Ex 18
Maav 112,188,68
Ad 149,17
Pk 116,16,17
Maav 116,184,72
Ex 19
Maav 116,188,76
Wl 0
Ad 148,18
Ad 149,19
Pk 117,18,19
Cvaa 0,160,192,120
s_mov_b32 s57, 16384
Ex 20
Mvaa 0,164,196
Ta 176,241,33792
buffer_store_dwordx4 v[188:191], v242, s[96:99], s57 offen offset:3072 sc1
Mvaa 0,168,200
Ex 21
Mvaa 0,172,204
Ta 178,241,34816
Ad 140,20
Cvaa 4,172,220,124
Ex 22
Mvaa 4,168,216
Ta 180,241,35840
Ad 141,21
Mvaa 4,164,212
Ex 23
Mvaa 4,160,208
Ta 182,241,36864
Pk 86,20,21
Cvaa 8,160,224,128
Ex 24
Mvaa 8,164,228
Ta 184,241,33824
Ad 140,22
Mvaa 8,168,232
Ex 25
Mvaa 8,172,236
Ta 186,241,34848
Ad 141,23
Cvaa 12,172,252,132
Ex 26
Mvaa 12,168,248
Ta 188,241,35872
Pk 87,22,23
Ad 142,24
Mvaa 12,164,244
Ex 27
Mvaa 12,160,240
Ta 190,241,36896
Ad 143,25
Cvav 16,160,248,136
Ex 28
Mvav 16,164,252
Pk 94,24,25
Ad 142,26
Mvav 16,168,150
Ex 29
Mvav 16,172,154
Wl 4
Ad 143,27
Pk 95,26,27
Maav 120,176,40
Ex 30
Maav 120,180,44
Ra 160,240,1024
Ad 144,28
Maav 124,176,48
Ex 31
Maav 124,180,52
Ra 164,240,1280
Ad 145,29
Maav 128,176,56
Ex 32
Maav 128,180,60
Ra 168,240,1536
Pk 102,28,29
Maav 132,176,64
Ex 33
Maav 132,180,68
Ra 172,240,1792
Ad 144,30
Ad 145,31
Maav 136,176,72
Ex 34
Maav 136,180,76
Wl 4
Pk 103,30,31
Ad 146,32
Maav 140,184,40
Ex 35
Maav 140,188,44
Ad 147,33
Pk 110,32,33
Maav 144,184,48
Ex 36
Maav 144,188,52
Ad 146,34
Ad 147,35
Maav 148,184,56
Ex 37
Maav 148,188,60
Pk 111,34,35
Ad 148,36
Maav 152,184,64
Ex 38
Maav 152,188,68
Ad 149,37
Pk 118,36,37
Maav 156,184,72
Ex 39
Maav 156,188,76
Wl 0
Ad 148,38
Ad 149,39
Pk 119,38,39
Cvaa 20,160,192,120
s_barrier
s_mov_b32 s57, 32768
Ex 0
Mvaa 20,164,196
Ta 176,241,49536
buffer_store_dwordx4 v[192:195], v242, s[96:99], s57 offen offset:0 sc1
Mvaa 20,168,200
Ex 1
Mvaa 20,172,204
Ta 178,241,50560
Ad 140,0
Cvaa 24,172,220,124
Ex 2
Mvaa 24,168,216
Ta 180,241,51584
Ad 141,1
Mvaa 24,164,212
Ex 3
Mvaa 24,160,208
Ta 182,241,52608
Pk 40,0,1
Cvaa 28,160,224,128
Ex 4
Mvaa 28,164,228
Ta 184,241,49568
Ad 140,2
Mvaa 28,168,232
Ex 5
Mvaa 28,172,236
Ta 186,241,50592
.p2align 12
.org PG0_%=+53248
s_cbranch_execz VRET_%=
s_nop 0
Ad 141,3
Cvaa 32,172,252,132
Ex 6
Mvaa 32,168,248
Ta 188,241,51616
Pk 41,2,3
Ad 142,4
Mvaa 32,164,244
Ex 7
Mvaa 32,160,240
Ta 190,241,52640
Ad 143,5
Cvav 36,160,248,136
Ex 8
Mvav 36,164,252
Pk 48,4,5
Ad 142,6
Mvav 36,168,150
Ex 9
Mvav 36,172,154
Wl 4
Ad 143,7
Pk 49,6,7
Maav 0,176,80
Ex 10
Maav 0,180,84
Ra 160,240,2048
Ad 144,8
Maav 4,176,88
Ex 11
Maav 4,180,92
Ra 164,240,2304
Ad 145,9
Maav 8,176,96
Ex 12
Maav 8,180,100
Ra 168,240,2560
Pk 56,8,9
Maav 12,176,104
Ex 13
Maav 12,180,108
Ra 172,240,2816
Ad 144,10
Ad 145,11
Maav 16,176,112
Ex 14
Maav 16,180,116
Wl 4
Pk 57,10,11
Ad 146,12
Maav 20,184,80
Ex 15
Maav 20,188,84
Ad 147,13
Pk 64,12,13
Maav 24,184,88
Ex 16
Maav 24,188,92
Ad 146,14
Ad 147,15
Maav 28,184,96
Ex 17
Maav 28,188,100
Pk 65,14,15
Ad 148,16
Maav 32,184,104
Ex 18
Maav 32,188,108
Ad 149,17
Pk 72,16,17
Maav 36,184,112
Ex 19
Maav 36,188,116
Wl 0
Ad 148,18
Ad 149,19
Pk 73,18,19
Cvaa 0,160,192,120
Ex 20
Mvaa 0,164,196
Ta 176,241,49792
Ad 140,20
Mvaa 0,168,200
Ex 21
Mvaa 0,172,204
Ta 178,241,50816
Ad 141,21
Cvaa 4,172,220,124
Ex 22
Mvaa 4,168,216
Ta 180,241,51840
Pk 42,20,21
Mvaa 4,164,212
Ex 23
Mvaa 4,160,208
Ta 182,241,52864
Ad 140,22
Cvaa 8,160,224,128
Ex 24
Mvaa 8,164,228
Ta 184,241,49824
Ad 141,23
Mvaa 8,168,232
Ex 25
Mvaa 8,172,236
Ta 186,241,50848
Pk 43,22,23
Cvaa 12,172,252,132
Ex 26
Mvaa 12,168,248
Ta 188,241,51872
Ad 142,24
Mvaa 12,164,244
Ex 27
Mvaa 12,160,240
Ta 190,241,52896
Ad 143,25
Cvav 16,160,248,136
Ex 28
Mvav 16,164,252
Pk 50,24,25
Ad 142,26
Mvav 16,168,150
Ex 29
Mvav 16,172,154
Wl 4
Ad 143,27
Pk 51,26,27
Ad 144,28
Maav 40,176,80
Ex 30
Maav 40,180,84
Ra 160,240,3072
Ad 145,29
Maav 44,176,88
Ex 31
Maav 44,180,92
Ra 164,240,3328
Pk 58,28,29
Maav 48,176,96
Ex 32
Maav 48,180,100
Ra 168,240,3584
Ad 144,30
Maav 52,176,104
Ex 33
Maav 52,180,108
Ra 172,240,3840
Ad 145,31
Maav 56,176,112
Ex 34
Maav 56,180,116
Wl 4
Pk 59,30,31
Ad 146,32
Maav 60,184,80
Ex 35
Maav 60,188,84
Ad 147,33
Pk 66,32,33
Maav 64,184,88
Ex 36
Maav 64,188,92
Ad 146,34
Ad 147,35
Maav 68,184,96
Ex 37
Maav 68,188,100
Pk 67,34,35
Ad 148,36
Maav 72,184,104
Ex 38
Maav 72,188,108
Ad 149,37
Pk 74,36,37
Maav 76,184,112
Ex 39
Maav 76,188,116
Wl 0
Ad 148,38
Ad 149,39
Pk 75,38,39
Cvaa 20,160,192,120
s_mov_b32 s57, 32768
Ex 0
Mvaa 20,164,196
Ta 176,241,50048
buffer_store_dwordx4 v[196:199], v242, s[96:99], s57 offen offset:1024 sc1
Mvaa 20,168,200
Ex 1
Mvaa 20,172,204
Ta 178,241,51072
Ad 140,0
Cvaa 24,172,220,124
Ex 2
Mvaa 24,168,216
Ta 180,241,52096
Ad 141,1
Mvaa 24,164,212
Ex 3
Mvaa 24,160,208
Ta 182,241,53120
Pk 44,0,1
Cvaa 28,160,224,128
Ex 4
Mvaa 28,164,228
Ta 184,241,50080
Ad 140,2
Mvaa 28,168,232
Ex 5
Mvaa 28,172,236
Ta 186,241,51104
Ad 141,3
Cvaa 32,172,252,132
Ex 6
Mvaa 32,168,248
Ta 188,241,52128
Pk 45,2,3
Ad 142,4
Mvaa 32,164,244
Ex 7
Mvaa 32,160,240
Ta 190,241,53152
Ad 143,5
Cvav 36,160,248,136
Ex 8
Mvav 36,164,252
Pk 52,4,5
Ad 142,6
Mvav 36,168,150
Ex 9
Mvav 36,172,154
Wl 4
Ad 143,7
Pk 53,6,7
Maav 80,176,80
Ex 10
Maav 80,180,84
Ra 160,240,16512
Ad 144,8
Maav 84,176,88
Ex 11
)A"
R"A(Maav 84,180,92
Ra 164,240,16768
Ad 145,9
Maav 88,176,96
Ex 12
Maav 88,180,100
Ra 168,240,17024
Pk 60,8,9
Maav 92,176,104
Ex 13
Maav 92,180,108
Ra 172,240,17280
Ad 144,10
Ad 145,11
Maav 96,176,112
Ex 14
Maav 96,180,116
Wl 4
Pk 61,10,11
Ad 146,12
Maav 100,184,80
Ex 15
Maav 100,188,84
Ad 147,13
Pk 68,12,13
Maav 104,184,88
Ex 16
Maav 104,188,92
Ad 146,14
Ad 147,15
Maav 108,184,96
Ex 17
Maav 108,188,100
Pk 69,14,15
Ad 148,16
Maav 112,184,104
Ex 18
Maav 112,188,108
Ad 149,17
Pk 76,16,17
Maav 116,184,112
Ex 19
Maav 116,188,116
Wl 0
Ad 148,18
Ad 149,19
Pk 77,18,19
Cvaa 0,160,192,120
Ex 20
Mvaa 0,164,196
Ta 176,241,50304
Ad 140,20
Mvaa 0,168,200
Ex 21
Mvaa 0,172,204
Ta 178,241,51328
Ad 141,21
Cvaa 4,172,220,124
Ex 22
Mvaa 4,168,216
Ta 180,241,52352
Pk 46,20,21
Mvaa 4,164,212
Ex 23
Mvaa 4,160,208
Ta 182,241,53376
Ad 140,22
Cvaa 8,160,224,128
Ex 24
Mvaa 8,164,228
Ta 184,241,50336
Ad 141,23
Mvaa 8,168,232
Ex 25
Mvaa 8,172,236
Ta 186,241,51360
Pk 47,22,23
Cvaa 12,172,252,132
Ex 26
Mvaa 12,168,248
Ta 188,241,52384
Ad 142,24
Mvaa 12,164,244
Ex 27
Mvaa 12,160,240
Ta 190,241,53408
Ad 143,25
Cvav 16,160,248,136
Ex 28
Mvav 16,164,252
Pk 54,24,25
Ad 142,26
Mvav 16,168,150
Ex 29
Mvav 16,172,154
Wl 4
Ad 143,27
Pk 55,26,27
Ad 144,28
Maav 120,176,80
Ex 30
Maav 120,180,84
Ra 160,240,17536
Ad 145,29
Maav 124,176,88
Ex 31
Maav 124,180,92
Ra 164,240,17792
Pk 62,28,29
Maav 128,176,96
Ex 32
Maav 128,180,100
Ra 168,240,18048
Ad 144,30
Maav 132,176,104
Ex 33
Maav 132,180,108
Ra 172,240,18304
Ad 145,31
Maav 136,176,112
Ex 34
Maav 136,180,116
Wl 4
Pk 63,30,31
Ad 146,32
Maav 140,184,80
Ex 35
Maav 140,188,84
Ad 147,33
Pk 70,32,33
Maav 144,184,88
Ex 36
Maav 144,188,92
Ad 146,34
Ad 147,35
Maav 148,184,96
Ex 37
Maav 148,188,100
Pk 71,34,35
Ad 148,36
Maav 152,184,104
Ex 38
Maav 152,188,108
Ad 149,37
Pk 78,36,37
Maav 156,184,112
Ex 39
Maav 156,188,116
Wl 0
Ad 148,38
Ad 149,39
Pk 79,38,39
Cvaa 20,160,192,120
s_barrier
s_mov_b32 s57, 32768
Ex 0
Mvaa 20,164,196
Ta 176,241,0
buffer_store_dwordx4 v[200:203], v242, s[96:99], s57 offen offset:2048 sc1
Mvaa 20,168,200
Ex 1
Mvaa 20,172,204
Ta 178,241,1024
Ad 140,0
Cvaa 24,172,220,124
Ex 2
Mvaa 24,168,216
Ta 180,241,2048
Ad 141,1
Mvaa 24,164,212
Ex 3
Mvaa 24,160,208
Ta 182,241,3072
Pk 80,0,1
Cvaa 28,160,224,128
Ex 4
Mvaa 28,164,228
Ta 184,241,32
Ad 140,2
Mvaa 28,168,232
Ex 5
Mvaa 28,172,236
Ta 186,241,1056
Ad 141,3
Cvaa 32,172,252,132
Ex 6
Mvaa 32,168,248
Ta 188,241,2080
Pk 81,2,3
Ad 142,4
Mvaa 32,164,244
Ex 7
Mvaa 32,160,240
Ta 190,241,3104
Ad 143,5
Cvav 36,160,248,136
Ex 8
Mvav 36,164,252
Pk 88,4,5
Ad 142,6
Mvav 36,168,150
Ex 9
Mvav 36,172,154
Wl 4
Ad 143,7
Pk 89,6,7
Maav 0,176,40
Ex 10
Maav 0,180,44
Ra 160,240,18560
Ad 144,8
Maav 4,176,48
Ex 11
Maav 4,180,52
Ra 164,240,18816
Ad 145,9
Maav 8,176,56
Ex 12
Maav 8,180,60
Ra 168,240,19072
Pk 96,8,9
Maav 12,176,64
Ex 13
Maav 12,180,68
Ra 172,240,19328
Ad 144,10
Ad 145,11
Maav 16,176,72
Ex 14
Maav 16,180,76
Wl 4
Pk 97,10,11
Ad 146,12
Maav 20,184,40
Ex 15
Maav 20,188,44
Ad 147,13
Pk 104,12,13
Maav 24,184,48
Ex 16
Maav 24,188,52
Ad 146,14
Ad 147,15
Maav 28,184,56
Ex 17
Maav 28,188,60
Pk 105,14,15
Ad 148,16
Maav 32,184,64
Ex 18
Maav 32,188,68
Ad 149,17
Pk 112,16,17
Maav 36,184,72
Ex 19
Maav 36,188,76
Wl 0
Ad 148,18
Ad 149,19
Pk 113,18,19
Cvaa 0,160,192,120
s_mov_b32 s57, 32768
Ex 20
Mvaa 0,164,196
Ta 176,241,256
buffer_store_dwordx4 v[204:207], v242, s[96:99], s57 offen offset:3072 sc1
Mvaa 0,168,200
Ex 21
Mvaa 0,172,204
Ta 178,241,1280
Ad 140,20
Cvaa 4,172,220,124
Ex 22
Mvaa 4,168,216
Ta 180,241,2304
Ad 141,21
Mvaa 4,164,212
Ex 23
Mvaa 4,160,208
Ta 182,241,3328
Pk 82,20,21
Cvaa 8,160,224,128
Ex 24
Mvaa 8,164,228
Ta 184,241,288
Ad 140,22
Mvaa 8,168,232
Ex 25
Mvaa 8,172,236
Ta 186,241,1312
Ad 141,23
Cvaa 12,172,252,132
Ex 26
Mvaa 12,168,248
Ta 188,241,2336
Pk 83,22,23
Ad 142,24
Mvaa 12,164,244
Ex 27
Mvaa 12,160,240
Ta 190,241,3360
Ad 143,25
Cvav 16,160,248,136
Ex 28
Mvav 16,164,252
Pk 90,24,25
Ad 142,26
Mvav 16,168,150
Ex 29
Mvav 16,172,154
Wl 4
Ad 143,27
Pk 91,26,27
Maav 40,176,40
Ex 30
Maav 40,180,44
Ra 160,240,19584
Ad 144,28
Maav 44,176,48
Ex 31
Maav 44,180,52
Ra 164,240,19840
Ad 145,29
Maav 48,176,56
Ex 32
Maav 48,180,60
Ra 168,240,20096
Pk 98,28,29
Maav 52,176,64
Ex 33
Maav 52,180,68
Ra 172,240,20352
Ad 144,30
Ad 145,31
Maav 56,176,72
Ex 34
Maav 56,180,76
Wl 4
Pk 99,30,31
Ad 146,32
Maav 60,184,40
Ex 35
Maav 60,188,44
Ad 147,33
Pk 106,32,33
Maav 64,184,48
Ex 36
Maav 64,188,52
Ad 146,34
Ad 147,35
Maav 68,184,56
Ex 37
Maav 68,188,60
Pk 107,34,35
Ad 148,36
Maav 72,184,64
Ex 38
Maav 72,188,68
Ad 149,37
Pk 114,36,37
Maav 76,184,72
Ex 39
Maav 76,188,76
Wl 0
Ad 148,38
Ad 149,39
Pk 115,38,39
Cvaa 20,160,192,120
Ex 0
Mvaa 20,164,196
Ta 176,241,512
Ad 140,0
Mvaa 20,168,200
Ex 1
Mvaa 20,172,204
Ta 178,241,1536
Ad 141,1
Cvaa 24,172,220,124
Ex 2
Mvaa 24,168,216
Ta 180,241,2560
Pk 84,0,1
Mvaa 24,164,212
Ex 3
Mvaa 24,160,208
Ta 182,241,3584
Ad 140,2
Cvaa 28,160,224,128
Ex 4
Mvaa 28,164,228
Ta 184,241,544
Ad 141,3
Mvaa 28,168,232
Ex 5
Mvaa 28,172,236
Ta 186,241,1568
Pk 85,2,3
Cvaa 32,172,252,132
Ex 6
.p2align 12
)A"
R"A(.org PG0_%=+57344
s_cbranch_execz VRET_%=
Mvaa 32,168,248
Ta 188,241,2592
Ad 142,4
Mvaa 32,164,244
Ex 7
Mvaa 32,160,240
Ta 190,241,3616
Ad 143,5
Cvav 36,160,248,136
Ex 8
Mvav 36,164,252
Pk 92,4,5
Ad 142,6
Mvav 36,168,150
Ex 9
Mvav 36,172,154
Wl 4
Ad 143,7
Pk 93,6,7
Ad 144,8
Maav 80,176,40
Ex 10
Maav 80,180,44
Ra 160,240,33024
Ad 145,9
Maav 84,176,48
Ex 11
Maav 84,180,52
Ra 164,240,33280
Pk 100,8,9
Maav 88,176,56
Ex 12
Maav 88,180,60
Ra 168,240,33536
Ad 144,10
Maav 92,176,64
Ex 13
Maav 92,180,68
Ra 172,240,33792
Ad 145,11
Maav 96,176,72
Ex 14
Maav 96,180,76
Wl 4
Pk 101,10,11
Ad 146,12
Maav 100,184,40
Ex 15
Maav 100,188,44
Ad 147,13
Pk 108,12,13
Maav 104,184,48
Ex 16
Maav 104,188,52
Ad 146,14
Ad 147,15
Maav 108,184,56
Ex 17
Maav 108,188,60
Pk 109,14,15
Ad 148,16
Maav 112,184,64
Ex 18
Maav 112,188,68
Ad 149,17
Pk 116,16,17
Maav 116,184,72
Ex 19
Maav 116,188,76
Wl 0
Ad 148,18
Ad 149,19
Pk 117,18,19
Cvaa 0,160,192,120
s_mov_b32 s57, 49152
Ex 20
Mvaa 0,164,196
Ta 176,241,768
buffer_store_dwordx4 v[208:211], v242, s[96:99], s57 offen offset:0 sc1
Mvaa 0,168,200
Ex 21
Mvaa 0,172,204
Ta 178,241,1792
Ad 140,20
Cvaa 4,172,220,124
Ex 22
Mvaa 4,168,216
Ta 180,241,2816
Ad 141,21
Mvaa 4,164,212
Ex 23
Mvaa 4,160,208
Ta 182,241,3840
Pk 86,20,21
Cvaa 8,160,224,128
Ex 24
Mvaa 8,164,228
Ta 184,241,800
Ad 140,22
Mvaa 8,168,232
Ex 25
Mvaa 8,172,236
Ta 186,241,1824
Ad 141,23
Cvaa 12,172,252,132
Ex 26
Mvaa 12,168,248
Ta 188,241,2848
Pk 87,22,23
Ad 142,24
Mvaa 12,164,244
Ex 27
Mvaa 12,160,240
Ta 190,241,3872
Ad 143,25
Cvav 16,160,248,136
Ex 28
Mvav 16,164,252
Pk 94,24,25
Ad 142,26
Mvav 16,168,150
Ex 29
Mvav 16,172,154
Wl 4
Ad 143,27
Pk 95,26,27
Maav 120,176,40
Ex 30
Maav 120,180,44
Ra 160,240,34048
Ad 144,28
Maav 124,176,48
Ex 31
Maav 124,180,52
Ra 164,240,34304
Ad 145,29
Maav 128,176,56
Ex 32
Maav 128,180,60
Ra 168,240,34560
Pk 102,28,29
Maav 132,176,64
Ex 33
Maav 132,180,68
Ra 172,240,34816
Ad 144,30
Ad 145,31
Maav 136,176,72
Ex 34
Maav 136,180,76
Wl 4
Pk 103,30,31
Ad 146,32
Maav 140,184,40
Ex 35
Maav 140,188,44
Ad 147,33
Pk 110,32,33
Maav 144,184,48
Ex 36
Maav 144,188,52
Ad 146,34
Ad 147,35
Maav 148,184,56
Ex 37
Maav 148,188,60
Pk 111,34,35
Ad 148,36
Maav 152,184,64
Ex 38
Maav 152,188,68
Ad 149,37
Pk 118,36,37
Maav 156,184,72
Ex 39
Maav 156,188,76
Wl 0
Ad 148,38
Ad 149,39
Pk 119,38,39
Cvaa 20,160,192,120
s_barrier
s_mov_b32 s57, 49152
Ex 0
Mvaa 20,164,196
Ta 176,241,16512
buffer_store_dwordx4 v[212:215], v242, s[96:99], s57 offen offset:1024 sc1
Mvaa 20,168,200
Ex 1
Mvaa 20,172,204
Ta 178,241,17536
Ad 140,0
Cvaa 24,172,220,124
Ex 2
Mvaa 24,168,216
Ta 180,241,18560
Ad 141,1
Mvaa 24,164,212
Ex 3
Mvaa 24,160,208
Ta 182,241,19584
Pk 40,0,1
Cvaa 28,160,224,128
Ex 4
Mvaa 28,164,228
Ta 184,241,16544
Ad 140,2
Mvaa 28,168,232
Ex 5
Mvaa 28,172,236
Ta 186,241,17568
Ad 141,3
Cvaa 32,172,252,132
Ex 6
Mvaa 32,168,248
Ta 188,241,18592
Pk 41,2,3
Ad 142,4
Mvaa 32,164,244
Ex 7
Mvaa 32,160,240
Ta 190,241,19616
Ad 143,5
Cvav 36,160,248,136
Ex 8
Mvav 36,164,252
Pk 48,4,5
Ad 142,6
Mvav 36,168,150
Ex 9
Mvav 36,172,154
Wl 4
Ad 143,7
Pk 49,6,7
Maav 0,176,80
Ex 10
Maav 0,180,84
Ra 160,240,35072
Ad 144,8
Maav 4,176,88
Ex 11
Maav 4,180,92
Ra 164,240,35328
Ad 145,9
Maav 8,176,96
Ex 12
Maav 8,180,100
Ra 168,240,35584
Pk 56,8,9
Maav 12,176,104
Ex 13
Maav 12,180,108
Ra 172,240,35840
Ad 144,10
Ad 145,11
Maav 16,176,112
Ex 14
Maav 16,180,116
Wl 4
Pk 57,10,11
Ad 146,12
Maav 20,184,80
Ex 15
Maav 20,188,84
Ad 147,13
Pk 64,12,13
Maav 24,184,88
Ex 16
Maav 24,188,92
Ad 146,14
Ad 147,15
Maav 28,184,96
Ex 17
Maav 28,188,100
Pk 65,14,15
Ad 148,16
Maav 32,184,104
Ex 18
Maav 32,188,108
Ad 149,17
Pk 72,16,17
Maav 36,184,112
Ex 19
Maav 36,188,116
Wl 0
Ad 148,18
Ad 149,19
Pk 73,18,19
Cvaa 0,160,192,120
Ex 20
Mvaa 0,164,196
Ta 176,241,16768
Ad 140,20
Mvaa 0,168,200
Ex 21
Mvaa 0,172,204
Ta 178,241,17792
Ad 141,21
Cvaa 4,172,220,124
Ex 22
Mvaa 4,168,216
Ta 180,241,18816
Pk 42,20,21
Mvaa 4,164,212
Ex 23
Mvaa 4,160,208
Ta 182,241,19840
Ad 140,22
Cvaa 8,160,224,128
Ex 24
Mvaa 8,164,228
Ta 184,241,16800
Ad 141,23
Mvaa 8,168,232
Ex 25
Mvaa 8,172,236
Ta 186,241,17824
Pk 43,22,23
Cvaa 12,172,252,132
Ex 26
Mvaa 12,168,248
Ta 188,241,18848
Ad 142,24
Mvaa 12,164,244
Ex 27
Mvaa 12,160,240
Ta 190,241,19872
Ad 143,25
Cvav 16,160,248,136
Ex 28
Mvav 16,164,252
Pk 50,24,25
Ad 142,26
Mvav 16,168,150
Ex 29
Mvav 16,172,154
Wl 4
Ad 143,27
Pk 51,26,27
Ad 144,28
Maav 40,176,80
Ex 30
Maav 40,180,84
Ra 160,240,36096
Ad 145,29
Maav 44,176,88
Ex 31
Maav 44,180,92
Ra 164,240,36352
Pk 58,28,29
Maav 48,176,96
Ex 32
Maav 48,180,100
Ra 168,240,36608
Ad 144,30
Maav 52,176,104
Ex 33
Maav 52,180,108
Ra 172,240,36864
Ad 145,31
Maav 56,176,112
Ex 34
Maav 56,180,116
Wl 4
Pk 59,30,31
Ad 146,32
Maav 60,184,80
Ex 35
Maav 60,188,84
Ad 147,33
Pk 66,32,33
Maav 64,184,88
Ex 36
Maav 64,188,92
Ad 146,34
Ad 147,35
Maav 68,184,96
Ex 37
Maav 68,188,100
Pk 67,34,35
Ad 148,36
Maav 72,184,104
Ex 38
Maav 72,188,108
Ad 149,37
Pk 74,36,37
Maav 76,184,112
Ex 39
Maav 76,188,116
Wl 0
Ad 148,38
Ad 149,39
Pk 75,38,39
Cvaa 20,160,192,120
s_mov_b32 s57, 49152
Ex 0
Mvaa 20,164,196
Ta 176,241,17024
)A"
R"A(buffer_store_dwordx4 v[216:219], v242, s[96:99], s57 offen offset:2048 sc1
Mvaa 20,168,200
Ex 1
Mvaa 20,172,204
Ta 178,241,18048
Ad 140,0
Cvaa 24,172,220,124
Ex 2
Mvaa 24,168,216
Ta 180,241,19072
Ad 141,1
Mvaa 24,164,212
Ex 3
Mvaa 24,160,208
Ta 182,241,20096
Pk 44,0,1
Cvaa 28,160,224,128
Ex 4
Mvaa 28,164,228
Ta 184,241,17056
Ad 140,2
Mvaa 28,168,232
Ex 5
Mvaa 28,172,236
Ta 186,241,18080
Ad 141,3
Cvaa 32,172,252,132
Ex 6
Mvaa 32,168,248
Ta 188,241,19104
Pk 45,2,3
Ad 142,4
Mvaa 32,164,244
Ex 7
Mvaa 32,160,240
Ta 190,241,20128
Ad 143,5
Cvav 36,160,248,136
Ex 8
Mvav 36,164,252
Pk 52,4,5
Ad 142,6
Mvav 36,168,150
Ex 9
Mvav 36,172,154
Wl 4
Ad 143,7
Pk 53,6,7
Maav 80,176,80
Ex 10
Maav 80,180,84
Ra 160,240,49536
Ad 144,8
Maav 84,176,88
Ex 11
Maav 84,180,92
Ra 164,240,49792
Ad 145,9
Maav 88,176,96
Ex 12
Maav 88,180,100
Ra 168,240,50048
Pk 60,8,9
Maav 92,176,104
Ex 13
Maav 92,180,108
Ra 172,240,50304
Ad 144,10
Ad 145,11
Maav 96,176,112
Ex 14
Maav 96,180,116
Wl 4
Pk 61,10,11
Ad 146,12
Maav 100,184,80
Ex 15
Maav 100,188,84
Ad 147,13
Pk 68,12,13
Maav 104,184,88
Ex 16
Maav 104,188,92
Ad 146,14
Ad 147,15
Maav 108,184,96
Ex 17
Maav 108,188,100
Pk 69,14,15
Ad 148,16
Maav 112,184,104
Ex 18
Maav 112,188,108
Ad 149,17
Pk 76,16,17
Maav 116,184,112
Ex 19
Maav 116,188,116
Wl 0
Ad 148,18
Ad 149,19
Pk 77,18,19
Cvaa 0,160,192,120
Ex 20
Mvaa 0,164,196
Ta 176,241,17280
Ad 140,20
Mvaa 0,168,200
Ex 21
Mvaa 0,172,204
Ta 178,241,18304
Ad 141,21
Cvaa 4,172,220,124
Ex 22
Mvaa 4,168,216
Ta 180,241,19328
Pk 46,20,21
Mvaa 4,164,212
Ex 23
Mvaa 4,160,208
Ta 182,241,20352
Ad 140,22
Cvaa 8,160,224,128
Ex 24
Mvaa 8,164,228
Ta 184,241,17312
Ad 141,23
Mvaa 8,168,232
Ex 25
Mvaa 8,172,236
Ta 186,241,18336
Pk 47,22,23
Cvaa 12,172,252,132
Ex 26
Mvaa 12,168,248
Ta 188,241,19360
Ad 142,24
Mvaa 12,164,244
Ex 27
Mvaa 12,160,240
Ta 190,241,20384
Ad 143,25
Cvav 16,160,248,136
Ex 28
Mvav 16,164,252
Pk 54,24,25
Ad 142,26
Mvav 16,168,150
Ex 29
Mvav 16,172,154
Wl 4
Ad 143,27
Pk 55,26,27
Ad 144,28
Maav 120,176,80
Ex 30
Maav 120,180,84
Ra 160,240,50560
Ad 145,29
Maav 124,176,88
Ex 31
Maav 124,180,92
Ra 164,240,50816
Pk 62,28,29
Maav 128,176,96
Ex 32
Maav 128,180,100
Ra 168,240,51072
Ad 144,30
Maav 132,176,104
Ex 33
Maav 132,180,108
Ra 172,240,51328
Ad 145,31
Maav 136,176,112
Ex 34
Maav 136,180,116
Wl 4
Pk 63,30,31
Ad 146,32
Maav 140,184,80
Ex 35
Maav 140,188,84
Ad 147,33
Pk 70,32,33
Maav 144,184,88
Ex 36
Maav 144,188,92
Ad 146,34
Ad 147,35
Maav 148,184,96
Ex 37
Maav 148,188,100
Pk 71,34,35
Ad 148,36
Maav 152,184,104
Ex 38
Maav 152,188,108
Ad 149,37
Pk 78,36,37
Maav 156,184,112
Ex 39
Maav 156,188,116
Wl 0
Ad 148,38
Ad 149,39
Pk 79,38,39
Cvaa 20,160,192,120
s_barrier
s_mov_b32 s57, 49152
Ex 0
Mvaa 20,164,196
Ta 176,241,33024
buffer_store_dwordx4 v[220:223], v242, s[96:99], s57 offen offset:3072 sc1
Mvaa 20,168,200
Ex 1
Mvaa 20,172,204
Ta 178,241,34048
Ad 140,0
Cvaa 24,172,220,124
Ex 2
Mvaa 24,168,216
Ta 180,241,35072
Ad 141,1
Mvaa 24,164,212
Ex 3
Mvaa 24,160,208
Ta 182,241,36096
Pk 80,0,1
Cvaa 28,160,224,128
Ex 4
Mvaa 28,164,228
Ta 184,241,33056
Ad 140,2
Mvaa 28,168,232
Ex 5
Mvaa 28,172,236
Ta 186,241,34080
Ad 141,3
Cvaa 32,172,252,132
Ex 6
Mvaa 32,168,248
Ta 188,241,35104
.p2align 12
.org PG0_%=+61440
s_cbranch_execz VRET_%=
Pk 81,2,3
Ad 142,4
Mvaa 32,164,244
Ex 7
Mvaa 32,160,240
Ta 190,241,36128
Ad 143,5
Cvav 36,160,248,136
Ex 8
Mvav 36,164,252
Pk 88,4,5
Ad 142,6
Mvav 36,168,150
Ex 9
Mvav 36,172,154
Wl 4
Ad 143,7
Pk 89,6,7
Maav 0,176,40
Ex 10
Maav 0,180,44
Ra 160,240,51584
Ad 144,8
Maav 4,176,48
Ex 11
Maav 4,180,52
Ra 164,240,51840
Ad 145,9
Maav 8,176,56
Ex 12
Maav 8,180,60
Ra 168,240,52096
Pk 96,8,9
Maav 12,176,64
Ex 13
Maav 12,180,68
Ra 172,240,52352
Ad 144,10
Ad 145,11
Maav 16,176,72
Ex 14
Maav 16,180,76
Wl 4
Pk 97,10,11
Ad 146,12
Maav 20,184,40
Ex 15
Maav 20,188,44
Ad 147,13
Pk 104,12,13
Maav 24,184,48
Ex 16
Maav 24,188,52
Ad 146,14
Ad 147,15
Maav 28,184,56
Ex 17
Maav 28,188,60
Pk 105,14,15
Ad 148,16
Maav 32,184,64
Ex 18
Maav 32,188,68
Ad 149,17
Pk 112,16,17
Maav 36,184,72
Ex 19
Maav 36,188,76
Wl 0
Ad 148,18
Ad 149,19
Pk 113,18,19
Cvaa 0,160,192,120
s_mov_b32 s57, 65536
Ex 20
Mvaa 0,164,196
Ta 176,241,33280
buffer_store_dwordx4 v[224:227], v242, s[96:99], s57 offen offset:0 sc1
Mvaa 0,168,200
Ex 21
Mvaa 0,172,204
Ta 178,241,34304
Ad 140,20
Cvaa 4,172,220,124
Ex 22
Mvaa 4,168,216
Ta 180,241,35328
Ad 141,21
Mvaa 4,164,212
Ex 23
Mvaa 4,160,208
Ta 182,241,36352
Pk 82,20,21
Cvaa 8,160,224,128
Ex 24
Mvaa 8,164,228
Ta 184,241,33312
Ad 140,22
Mvaa 8,168,232
Ex 25
Mvaa 8,172,236
Ta 186,241,34336
Ad 141,23
Cvaa 12,172,252,132
Ex 26
Mvaa 12,168,248
Ta 188,241,35360
Pk 83,22,23
Ad 142,24
Mvaa 12,164,244
Ex 27
Mvaa 12,160,240
Ta 190,241,36384
Ad 143,25
Cvav 16,160,248,136
Ex 28
Mvav 16,164,252
Pk 90,24,25
Ad 142,26
Mvav 16,168,150
Ex 29
Mvav 16,172,154
Wl 4
Ad 143,27
Pk 91,26,27
Maav 40,176,40
Ex 30
Maav 40,180,44
Ra 160,240,52608
Ad 144,28
Maav 44,176,48
Ex 31
Maav 44,180,52
Ra 164,240,52864
Ad 145,29
Maav 48,176,56
Ex 32
Maav 48,180,60
Ra 168,240,53120
Pk 98,28,29
Maav 52,176,64
Ex 33
Maav 52,180,68
Ra 172,240,53376
Ad 144,30
Ad 145,31
Maav 56,176,72
Ex 34
Maav 56,180,76
Wl 4
Pk 99,30,31
Ad 146,32
Maav 60,184,40
Ex 35
)A"
R"A(Maav 60,188,44
Ad 147,33
Pk 106,32,33
Maav 64,184,48
Ex 36
Maav 64,188,52
Ad 146,34
Ad 147,35
Maav 68,184,56
Ex 37
Maav 68,188,60
Pk 107,34,35
Ad 148,36
Maav 72,184,64
Ex 38
Maav 72,188,68
Ad 149,37
Pk 114,36,37
Maav 76,184,72
Ex 39
Maav 76,188,76
Wl 0
Ad 148,38
Ad 149,39
Pk 115,38,39
Cvaa 20,160,192,120
Ex 0
Mvaa 20,164,196
Ta 176,241,33536
Ad 140,0
Mvaa 20,168,200
Ex 1
Mvaa 20,172,204
Ta 178,241,34560
Ad 141,1
Cvaa 24,172,220,124
Ex 2
Mvaa 24,168,216
Ta 180,241,35584
Pk 84,0,1
Mvaa 24,164,212
Ex 3
Mvaa 24,160,208
Ta 182,241,36608
Ad 140,2
Cvaa 28,160,224,128
Ex 4
Mvaa 28,164,228
Ta 184,241,33568
Ad 141,3
Mvaa 28,168,232
Ex 5
Mvaa 28,172,236
Ta 186,241,34592
Pk 85,2,3
Cvaa 32,172,252,132
Ex 6
Mvaa 32,168,248
Ta 188,241,35616
Ad 142,4
Mvaa 32,164,244
Ex 7
Mvaa 32,160,240
Ta 190,241,36640
Ad 143,5
Cvav 36,160,248,136
Ex 8
Mvav 36,164,252
Pk 92,4,5
Ad 142,6
Mvav 36,168,150
Ex 9
Mvav 36,172,154
Wl 4
Ad 143,7
Pk 93,6,7
Ad 144,8
Maav 80,176,40
Ex 10
Maav 80,180,44
Ra 160,240,0
Ad 145,9
Maav 84,176,48
Ex 11
Maav 84,180,52
Ra 164,240,256
Pk 100,8,9
Maav 88,176,56
Ex 12
Maav 88,180,60
Ra 168,240,512
Ad 144,10
Maav 92,176,64
Ex 13
Maav 92,180,68
Ra 172,240,768
Ad 145,11
Maav 96,176,72
Ex 14
Maav 96,180,76
Wl 4
Pk 101,10,11
Ad 146,12
Maav 100,184,40
Ex 15
Maav 100,188,44
Ad 147,13
Pk 108,12,13
Maav 104,184,48
Ex 16
Maav 104,188,52
Ad 146,14
Ad 147,15
Maav 108,184,56
Ex 17
Maav 108,188,60
Pk 109,14,15
Ad 148,16
Maav 112,184,64
Ex 18
Maav 112,188,68
Ad 149,17
Pk 116,16,17
Maav 116,184,72
Ex 19
Maav 116,188,76
Wl 0
Ad 148,18
Ad 149,19
Pk 117,18,19
Cvaa 0,160,192,120
s_mov_b32 s57, 65536
Ex 20
Mvaa 0,164,196
Ta 176,241,33792
buffer_store_dwordx4 v[228:231], v242, s[96:99], s57 offen offset:1024 sc1
Mvaa 0,168,200
Ex 21
Mvaa 0,172,204
Ta 178,241,34816
Ad 140,20
Cvaa 4,172,220,124
Ex 22
Mvaa 4,168,216
Ta 180,241,35840
Ad 141,21
Mvaa 4,164,212
Ex 23
Mvaa 4,160,208
Ta 182,241,36864
Pk 86,20,21
Cvaa 8,160,224,128
Ex 24
Mvaa 8,164,228
Ta 184,241,33824
Ad 140,22
Mvaa 8,168,232
Ex 25
Mvaa 8,172,236
Ta 186,241,34848
Ad 141,23
Cvaa 12,172,252,132
Ex 26
Mvaa 12,168,248
Ta 188,241,35872
Pk 87,22,23
Ad 142,24
Mvaa 12,164,244
Ex 27
Mvaa 12,160,240
Ta 190,241,36896
Ad 143,25
Cvav 16,160,248,136
Ex 28
Mvav 16,164,252
Pk 94,24,25
Ad 142,26
Mvav 16,168,150
Ex 29
Mvav 16,172,154
Wl 4
Ad 143,27
Pk 95,26,27
Maav 120,176,40
Ex 30
Maav 120,180,44
Ra 160,240,1024
Ad 144,28
Maav 124,176,48
Ex 31
Maav 124,180,52
Ra 164,240,1280
Ad 145,29
Maav 128,176,56
Ex 32
Maav 128,180,60
Ra 168,240,1536
Pk 102,28,29
Maav 132,176,64
Ex 33
Maav 132,180,68
Ra 172,240,1792
Ad 144,30
Ad 145,31
Maav 136,176,72
Ex 34
Maav 136,180,76
Wl 4
Pk 103,30,31
Ad 146,32
Maav 140,184,40
Ex 35
Maav 140,188,44
Ad 147,33
Pk 110,32,33
Maav 144,184,48
Ex 36
Maav 144,188,52
Ad 146,34
Ad 147,35
Maav 148,184,56
Ex 37
Maav 148,188,60
Pk 111,34,35
Ad 148,36
Maav 152,184,64
Ex 38
Maav 152,188,68
Ad 149,37
Pk 118,36,37
Maav 156,184,72
Ex 39
Maav 156,188,76
Wl 0
Ad 148,38
Ad 149,39
Pk 119,38,39
Cvaa 20,160,192,120
s_barrier
s_mov_b32 s57, 65536
Ex 0
Mvaa 20,164,196
Ta 176,241,49536
buffer_store_dwordx4 v[232:235], v242, s[96:99], s57 offen offset:2048 sc1
Mvaa 20,168,200
Ex 1
Mvaa 20,172,204
Ta 178,241,50560
Ad 140,0
Cvaa 24,172,220,124
Ex 2
Mvaa 24,168,216
Ta 180,241,51584
Ad 141,1
Mvaa 24,164,212
Ex 3
Mvaa 24,160,208
Ta 182,241,52608
Pk 40,0,1
Cvaa 28,160,224,128
Ex 4
Mvaa 28,164,228
Ta 184,241,49568
Ad 140,2
Mvaa 28,168,232
Ex 5
Mvaa 28,172,236
Ta 186,241,50592
Ad 141,3
Cvaa 32,172,252,132
Ex 6
Mvaa 32,168,248
Ta 188,241,51616
Pk 41,2,3
Ad 142,4
Mvaa 32,164,244
Ex 7
Mvaa 32,160,240
Ta 190,241,52640
Ad 143,5
Cvav 36,160,248,136
Ex 8
Mvav 36,164,252
Pk 48,4,5
Ad 142,6
Mvav 36,168,150
Ex 9
Mvav 36,172,154
Wl 4
Ad 143,7
Pk 49,6,7
Maav 0,176,80
Ex 10
Maav 0,180,84
Ra 160,240,2048
Ad 144,8
Maav 4,176,88
Ex 11
Maav 4,180,92
Ra 164,240,2304
Ad 145,9
Maav 8,176,96
Ex 12
Maav 8,180,100
Ra 168,240,2560
Pk 56,8,9
Maav 12,176,104
Ex 13
Maav 12,180,108
Ra 172,240,2816
Ad 144,10
Ad 145,11
Maav 16,176,112
Ex 14
Maav 16,180,116
Wl 4
Pk 57,10,11
Ad 146,12
Maav 20,184,80
Ex 15
Maav 20,188,84
Ad 147,13
Pk 64,12,13
Maav 24,184,88
Ex 16
Maav 24,188,92
Ad 146,14
Ad 147,15
Maav 28,184,96
Ex 17
Maav 28,188,100
Pk 65,14,15
Ad 148,16
Maav 32,184,104
Ex 18
Maav 32,188,108
Ad 149,17
Pk 72,16,17
Maav 36,184,112
Ex 19
Maav 36,188,116
Wl 0
Ad 148,18
Ad 149,19
Pk 73,18,19
Cvaa 0,160,192,120
Ex 20
Mvaa 0,164,196
Ta 176,241,49792
Ad 140,20
Mvaa 0,168,200
Ex 21
Mvaa 0,172,204
Ta 178,241,50816
Ad 141,21
Cvaa 4,172,220,124
Ex 22
Mvaa 4,168,216
Ta 180,241,51840
Pk 42,20,21
Mvaa 4,164,212
Ex 23
Mvaa 4,160,208
Ta 182,241,52864
Ad 140,22
Cvaa 8,160,224,128
Ex 24
Mvaa 8,164,228
Ta 184,241,49824
Ad 141,23
Mvaa 8,168,232
Ex 25
Mvaa 8,172,236
Ta 186,241,50848
Pk 43,22,23
Cvaa 12,172,252,132
Ex 26
Mvaa 12,168,248
Ta 188,241,51872
Ad 142,24
Mvaa 12,164,244
Ex 27
Mvaa 12,160,240
Ta 190,241,52896
Ad 143,25
Cvav 16,160,248,136
Ex 28
Mvav 16,164,252
Pk 50,24,25
Ad 142,26
Mvav 16,168,150
Ex 29
Mvav 16,172,154
Wl 4
Ad 143,27
Pk 51,26,27
Ad 144,28
Maav 40,176,80
Ex 30
Maav 40,180,84
)A"
R"A(Ra 160,240,3072
Ad 145,29
Maav 44,176,88
Ex 31
Maav 44,180,92
Ra 164,240,3328
Pk 58,28,29
Maav 48,176,96
Ex 32
Maav 48,180,100
Ra 168,240,3584
Ad 144,30
Maav 52,176,104
Ex 33
Maav 52,180,108
Ra 172,240,3840
Ad 145,31
Maav 56,176,112
Ex 34
Maav 56,180,116
Wl 4
Pk 59,30,31
Ad 146,32
Maav 60,184,80
Ex 35
Maav 60,188,84
Ad 147,33
Pk 66,32,33
Maav 64,184,88
Ex 36
Maav 64,188,92
Ad 146,34
Ad 147,35
Maav 68,184,96
Ex 37
Maav 68,188,100
Pk 67,34,35
Ad 148,36
Maav 72,184,104
Ex 38
Maav 72,188,108
Ad 149,37
Pk 74,36,37
Maav 76,184,112
Ex 39
Maav 76,188,116
Wl 0
Ad 148,38
Ad 149,39
Pk 75,38,39
Cvaa 20,160,192,120
s_mov_b32 s57, 65536
Ex 0
Mvaa 20,164,196
Ta 176,241,50048
buffer_store_dwordx4 v[236:239], v242, s[96:99], s57 offen offset:3072 sc1
Mvaa 20,168,200
Ex 1
Mvaa 20,172,204
Ta 178,241,51072
Ad 140,0
Cvaa 24,172,220,124
Ex 2
Mvaa 24,168,216
Ta 180,241,52096
Ad 141,1
Mvaa 24,164,212
Ex 3
Mvaa 24,160,208
Ta 182,241,53120
Pk 44,0,1
Cvaa 28,160,224,128
Ex 4
Mvaa 28,164,228
Ta 184,241,50080
Ad 140,2
Mvaa 28,168,232
Ex 5
Mvaa 28,172,236
Ta 186,241,51104
Ad 141,3
Cvaa 32,172,252,132
Ex 6
Mvaa 32,168,248
Ta 188,241,52128
Pk 45,2,3
Ad 142,4
.p2align 12
.org PG0_%=+65536
s_cbranch_execz VRET_%=
Mvaa 32,164,244
Ex 7
Mvaa 32,160,240
Ta 190,241,53152
Ad 143,5
Cvav 36,160,248,136
Ex 8
Mvav 36,164,252
Pk 52,4,5
Ad 142,6
Mvav 36,168,150
Ex 9
Mvav 36,172,154
Wl 4
Ad 143,7
Pk 53,6,7
Maav 80,176,80
Ex 10
Maav 80,180,84
Ra 160,240,16512
Ad 144,8
Maav 84,176,88
Ex 11
Maav 84,180,92
Ra 164,240,16768
Ad 145,9
Maav 88,176,96
Ex 12
Maav 88,180,100
Ra 168,240,17024
Pk 60,8,9
Maav 92,176,104
Ex 13
Maav 92,180,108
Ra 172,240,17280
Ad 144,10
Ad 145,11
Maav 96,176,112
Ex 14
Maav 96,180,116
Wl 4
Pk 61,10,11
Ad 146,12
Maav 100,184,80
Ex 15
Maav 100,188,84
Ad 147,13
Pk 68,12,13
Maav 104,184,88
Ex 16
Maav 104,188,92
Ad 146,14
Ad 147,15
Maav 108,184,96
Ex 17
Maav 108,188,100
Pk 69,14,15
Ad 148,16
Maav 112,184,104
Ex 18
Maav 112,188,108
Ad 149,17
Pk 76,16,17
Maav 116,184,112
Ex 19
Maav 116,188,116
Wl 0
Ad 148,18
Ad 149,19
Pk 77,18,19
Cvaa 0,160,192,120
Ex 20
Mvaa 0,164,196
Ta 176,241,50304
Ad 140,20
Mvaa 0,168,200
Ex 21
Mvaa 0,172,204
Ta 178,241,51328
Ad 141,21
Cvaa 4,172,220,124
Ex 22
Mvaa 4,168,216
Ta 180,241,52352
Pk 46,20,21
Mvaa 4,164,212
Ex 23
Mvaa 4,160,208
Ta 182,241,53376
Ad 140,22
Cvaa 8,160,224,128
Ex 24
Mvaa 8,164,228
Ta 184,241,50336
Ad 141,23
Mvaa 8,168,232
Ex 25
Mvaa 8,172,236
Ta 186,241,51360
Pk 47,22,23
Cvaa 12,172,252,132
Ex 26
Mvaa 12,168,248
Ta 188,241,52384
Ad 142,24
Mvaa 12,164,244
Ex 27
Mvaa 12,160,240
Ta 190,241,53408
Ad 143,25
Cvav 16,160,248,136
Ex 28
Mvav 16,164,252
Pk 54,24,25
Ad 142,26
Mvav 16,168,150
Ex 29
Mvav 16,172,154
Wl 4
Ad 143,27
Pk 55,26,27
Ad 144,28
Maav 120,176,80
Ex 30
Maav 120,180,84
Ra 160,240,17536
Ad 145,29
Maav 124,176,88
Ex 31
Maav 124,180,92
Ra 164,240,17792
Pk 62,28,29
Maav 128,176,96
Ex 32
Maav 128,180,100
Ra 168,240,18048
Ad 144,30
Maav 132,176,104
Ex 33
Maav 132,180,108
Ra 172,240,18304
Ad 145,31
Maav 136,176,112
Ex 34
Maav 136,180,116
Wl 4
Pk 63,30,31
Ad 146,32
Maav 140,184,80
Ex 35
Maav 140,188,84
Ad 147,33
Pk 70,32,33
Maav 144,184,88
Ex 36
Maav 144,188,92
Ad 146,34
Ad 147,35
Maav 148,184,96
Ex 37
Maav 148,188,100
Pk 71,34,35
Ad 148,36
Maav 152,184,104
Ex 38
Maav 152,188,108
Ad 149,37
Pk 78,36,37
Maav 156,184,112
Ex 39
Maav 156,188,116
Wl 0
Ad 148,38
Ad 149,39
Pk 79,38,39
Cvaa 20,160,192,120
s_barrier
Ex 0
Mvaa 20,164,196
Ta 176,241,0
Ad 140,0
Mvaa 20,168,200
Ex 1
Mvaa 20,172,204
Ta 178,241,1024
Ad 141,1
Cvaa 24,172,220,124
Ex 2
Mvaa 24,168,216
Ta 180,241,2048
Pk 80,0,1
Mvaa 24,164,212
Ex 3
Mvaa 24,160,208
Ta 182,241,3072
Ad 140,2
Cvaa 28,160,224,128
Ex 4
Mvaa 28,164,228
Ta 184,241,32
Ad 141,3
Mvaa 28,168,232
Ex 5
Mvaa 28,172,236
Ta 186,241,1056
Pk 81,2,3
Cvaa 32,172,252,132
Ex 6
Mvaa 32,168,248
Ta 188,241,2080
Ad 142,4
Mvaa 32,164,244
Ex 7
Mvaa 32,160,240
Ta 190,241,3104
Ad 143,5
Cvav 36,160,248,136
Ex 8
Mvav 36,164,252
Pk 88,4,5
Ad 142,6
Mvav 36,168,150
Ex 9
Mvav 36,172,154
Wl 4
Ad 143,7
Pk 89,6,7
Ad 144,8
Maav 0,176,40
Ex 10
Maav 0,180,44
Ra 160,240,18560
Ad 145,9
Maav 4,176,48
Ex 11
Maav 4,180,52
Ra 164,240,18816
Pk 96,8,9
Maav 8,176,56
Ex 12
Maav 8,180,60
Ra 168,240,19072
Ad 144,10
Maav 12,176,64
Ex 13
Maav 12,180,68
Ra 172,240,19328
Ad 145,11
Maav 16,176,72
Ex 14
Maav 16,180,76
Wl 4
Pk 97,10,11
Ad 146,12
Maav 20,184,40
Ex 15
Maav 20,188,44
Ad 147,13
Pk 104,12,13
Maav 24,184,48
Ex 16
Maav 24,188,52
Ad 146,14
Ad 147,15
Maav 28,184,56
Ex 17
Maav 28,188,60
Pk 105,14,15
Ad 148,16
Maav 32,184,64
Ex 18
Maav 32,188,68
Ad 149,17
Pk 112,16,17
Maav 36,184,72
Ex 19
Maav 36,188,76
Wl 0
Ad 148,18
Ad 149,19
Pk 113,18,19
Cvaa 0,160,192,120
Ex 20
Mvaa 0,164,196
Ta 176,241,256
Ad 140,20
Mvaa 0,168,200
Ex 21
Mvaa 0,172,204
Ta 178,241,1280
Ad 141,21
Cvaa 4,172,220,124
Ex 22
Mvaa 4,168,216
Ta 180,241,2304
Pk 82,20,21
Mvaa 4,164,212
Ex 23
Mvaa 4,160,208
Ta 182,241,3328
Ad 140,22
Cvaa 8,160,224,128
Ex 24
Mvaa 8,164,228
Ta 184,241,288
Ad 141,23
Mvaa 8,168,232
Ex 25
Mvaa 8,172,236
Ta 186,241,1312
)A"
R"A(Pk 83,22,23
Cvaa 12,172,252,132
Ex 26
Mvaa 12,168,248
Ta 188,241,2336
Ad 142,24
Mvaa 12,164,244
Ex 27
Mvaa 12,160,240
Ta 190,241,3360
Ad 143,25
Cvav 16,160,248,136
Ex 28
Mvav 16,164,252
Pk 90,24,25
Ad 142,26
Mvav 16,168,150
Ex 29
Mvav 16,172,154
Wl 4
Ad 143,27
Pk 91,26,27
Ad 144,28
Maav 40,176,40
Ex 30
Maav 40,180,44
Ra 160,240,19584
Ad 145,29
Maav 44,176,48
Ex 31
Maav 44,180,52
Ra 164,240,19840
Pk 98,28,29
Maav 48,176,56
Ex 32
Maav 48,180,60
Ra 168,240,20096
Ad 144,30
Maav 52,176,64
Ex 33
Maav 52,180,68
Ra 172,240,20352
Ad 145,31
Maav 56,176,72
Ex 34
Maav 56,180,76
Wl 4
Pk 99,30,31
Ad 146,32
Maav 60,184,40
Ex 35
Maav 60,188,44
Ad 147,33
Pk 106,32,33
Maav 64,184,48
Ex 36
Maav 64,188,52
Ad 146,34
Ad 147,35
Maav 68,184,56
Ex 37
Maav 68,188,60
Pk 107,34,35
Ad 148,36
Maav 72,184,64
Ex 38
Maav 72,188,68
Ad 149,37
Pk 114,36,37
Maav 76,184,72
Ex 39
Maav 76,188,76
Wl 0
Ad 148,38
Ad 149,39
Pk 115,38,39
Cvaa 20,160,192,120
Ex 0
Mvaa 20,164,196
Ta 176,241,512
Ad 140,0
Mvaa 20,168,200
Ex 1
Mvaa 20,172,204
Ta 178,241,1536
Ad 141,1
Cvaa 24,172,220,124
Ex 2
Mvaa 24,168,216
Ta 180,241,2560
Pk 84,0,1
Mvaa 24,164,212
Ex 3
Mvaa 24,160,208
Ta 182,241,3584
Ad 140,2
Cvaa 28,160,224,128
Ex 4
Mvaa 28,164,228
Ta 184,241,544
Ad 141,3
Mvaa 28,168,232
Ex 5
Mvaa 28,172,236
Ta 186,241,1568
Pk 85,2,3
Cvaa 32,172,252,132
Ex 6
Mvaa 32,168,248
Ta 188,241,2592
Ad 142,4
Mvaa 32,164,244
Ex 7
Mvaa 32,160,240
Ta 190,241,3616
Ad 143,5
Cvav 36,160,248,136
Ex 8
Mvav 36,164,252
Pk 92,4,5
Ad 142,6
Mvav 36,168,150
Ex 9
Mvav 36,172,154
Wl 4
Ad 143,7
Pk 93,6,7
Ad 144,8
Maav 80,176,40
Ex 10
Maav 80,180,44
Ra 160,240,33024
Ad 145,9
Maav 84,176,48
Ex 11
Maav 84,180,52
Ra 164,240,33280
Pk 100,8,9
Maav 88,176,56
Ex 12
Maav 88,180,60
Ra 168,240,33536
Ad 144,10
Maav 92,176,64
Ex 13
Maav 92,180,68
Ra 172,240,33792
Ad 145,11
Maav 96,176,72
Ex 14
Maav 96,180,76
Wl 4
Pk 101,10,11
Ad 146,12
Maav 100,184,40
Ex 15
Maav 100,188,44
Ad 147,13
Pk 108,12,13
Maav 104,184,48
Ex 16
Maav 104,188,52
Ad 146,14
Ad 147,15
Maav 108,184,56
Ex 17
Maav 108,188,60
Pk 109,14,15
Ad 148,16
Maav 112,184,64
Ex 18
Maav 112,188,68
Ad 149,17
Pk 116,16,17
Maav 116,184,72
Ex 19
Maav 116,188,76
Wl 0
Ad 148,18
Ad 149,19
Pk 117,18,19
Cvaa 0,160,192,120
Ex 20
Mvaa 0,164,196
Ta 176,241,768
Ad 140,20
Mvaa 0,168,200
Ex 21
Mvaa 0,172,204
Ta 178,241,1792
Ad 141,21
Cvaa 4,172,220,124
Ex 22
Mvaa 4,168,216
Ta 180,241,2816
Pk 86,20,21
Mvaa 4,164,212
Ex 23
Mvaa 4,160,208
Ta 182,241,3840
Ad 140,22
Cvaa 8,160,224,128
Ex 24
Mvaa 8,164,228
Ta 184,241,800
Ad 141,23
Mvaa 8,168,232
Ex 25
Mvaa 8,172,236
Ta 186,241,1824
Pk 87,22,23
Cvaa 12,172,252,132
Ex 26
Mvaa 12,168,248
Ta 188,241,2848
Ad 142,24
Mvaa 12,164,244
Ex 27
Mvaa 12,160,240
Ta 190,241,3872
Ad 143,25
Cvav 16,160,248,136
Ex 28
Mvav 16,164,252
Pk 94,24,25
Ad 142,26
Mvav 16,168,150
Ex 29
Mvav 16,172,154
Wl 4
Ad 143,27
Pk 95,26,27
Ad 144,28
Maav 120,176,40
Ex 30
Maav 120,180,44
Ra 160,240,34048
Ad 145,29
Maav 124,176,48
Ex 31
Maav 124,180,52
Ra 164,240,34304
Pk 102,28,29
Maav 128,176,56
Ex 32
Maav 128,180,60
Ra 168,240,34560
Ad 144,30
Maav 132,176,64
Ex 33
Maav 132,180,68
Ra 172,240,34816
Ad 145,31
Maav 136,176,72
Ex 34
Maav 136,180,76
Wl 4
Pk 103,30,31
Ad 146,32
Maav 140,184,40
Ex 35
Maav 140,188,44
Ad 147,33
Pk 110,32,33
Maav 144,184,48
Ex 36
Maav 144,188,52
Ad 146,34
Ad 147,35
Maav 148,184,56
Ex 37
Maav 148,188,60
Pk 111,34,35
Ad 148,36
Maav 152,184,64
Ex 38
Maav 152,188,68
Ad 149,37
Pk 118,36,37
Maav 156,184,72
Ex 39
Maav 156,188,76
Wl 0
Ad 148,38
Ad 149,39
Pk 119,38,39
Cvaa 20,160,192,120
s_barrier
Ex 0
Mvaa 20,164,196
Ta 176,241,16512
Ad 140,0
Mvaa 20,168,200
Ex 1
Mvaa 20,172,204
Ta 178,241,17536
Ad 141,1
Cvaa 24,172,220,124
Ex 2
Mvaa 24,168,216
Ta 180,241,18560
Pk 40,0,1
Mvaa 24,164,212
Ex 3
Mvaa 24,160,208
Ta 182,241,19584
Ad 140,2
Cvaa 28,160,224,128
Ex 4
Mvaa 28,164,228
Ta 184,241,16544
Ad 141,3
Mvaa 28,168,232
Ex 5
Mvaa 28,172,236
Ta 186,241,17568
Pk 41,2,3
Cvaa 32,172,252,132
Ex 6
Mvaa 32,168,248
Ta 188,241,18592
Ad 142,4
Mvaa 32,164,244
Ex 7
Mvaa 32,160,240
Ta 190,241,19616
Ad 143,5
Cvav 36,160,248,136
Ex 8
Mvav 36,164,252
Pk 48,4,5
Ad 142,6
Mvav 36,168,150
Ex 9
.p2align 12
.org PG0_%=+69632
s_cbranch_execz VRET_%=
s_nop 0
Mvav 36,172,154
Wl 4
Ad 143,7
Pk 49,6,7
Ad 144,8
Maav 0,176,80
Ex 10
Maav 0,180,84
Ra 160,240,35072
Ad 145,9
Maav 4,176,88
Ex 11
Maav 4,180,92
Ra 164,240,35328
Pk 56,8,9
Maav 8,176,96
Ex 12
Maav 8,180,100
Ra 168,240,35584
Ad 144,10
Maav 12,176,104
Ex 13
Maav 12,180,108
Ra 172,240,35840
Ad 145,11
Maav 16,176,112
Ex 14
Maav 16,180,116
Wl 4
Pk 57,10,11
Ad 146,12
Maav 20,184,80
Ex 15
Maav 20,188,84
Ad 147,13
Pk 64,12,13
Maav 24,184,88
Ex 16
Maav 24,188,92
Ad 146,14
Ad 147,15
Maav 28,184,96
Ex 17
Maav 28,188,100
Pk 65,14,15
Ad 148,16
Maav 32,184,104
Ex 18
Maav 32,188,108
Ad 149,17
Pk 72,16,17
Maav 36,184,112
Ex 19
Maav 36,188,116
Wl 0
Ad 148,18
Ad 149,19
Pk 73,18,19
Cvaa 0,160,192,120
s_waitcnt vmcnt(0)
v_xor_b32 v158, s66, v158
Ex 20
)A"
R"A(Mvaa 0,164,196
Ta 176,241,16768
global_store_dword v159, v158, s[94:95] sc1
Mvaa 0,168,200
Ex 21
Mvaa 0,172,204
Ta 178,241,17792
Ad 140,20
Cvaa 4,172,220,124
Ex 22
Mvaa 4,168,216
Ta 180,241,18816
Ad 141,21
Mvaa 4,164,212
Ex 23
Mvaa 4,160,208
Ta 182,241,19840
Pk 42,20,21
Cvaa 8,160,224,128
Ex 24
Mvaa 8,164,228
Ta 184,241,16800
Ad 140,22
Mvaa 8,168,232
Ex 25
Mvaa 8,172,236
Ta 186,241,17824
Ad 141,23
Cvaa 12,172,252,132
Ex 26
Mvaa 12,168,248
Ta 188,241,18848
Pk 43,22,23
Ad 142,24
Mvaa 12,164,244
Ex 27
Mvaa 12,160,240
Ta 190,241,19872
Ad 143,25
Cvav 16,160,248,136
Ex 28
Mvav 16,164,252
Pk 50,24,25
Ad 142,26
Mvav 16,168,150
Ex 29
Mvav 16,172,154
Wl 4
Ad 143,27
Pk 51,26,27
Maav 40,176,80
Ex 30
Maav 40,180,84
Ra 160,240,36096
Ad 144,28
Maav 44,176,88
Ex 31
Maav 44,180,92
Ra 164,240,36352
Ad 145,29
Maav 48,176,96
Ex 32
Maav 48,180,100
Ra 168,240,36608
Pk 58,28,29
Maav 52,176,104
Ex 33
Maav 52,180,108
Ra 172,240,36864
Ad 144,30
Ad 145,31
Maav 56,176,112
Ex 34
Maav 56,180,116
Wl 4
Pk 59,30,31
Ad 146,32
Maav 60,184,80
Ex 35
Maav 60,188,84
Ad 147,33
Pk 66,32,33
Maav 64,184,88
Ex 36
Maav 64,188,92
Ad 146,34
Ad 147,35
Maav 68,184,96
Ex 37
Maav 68,188,100
Pk 67,34,35
Ad 148,36
Maav 72,184,104
Ex 38
Maav 72,188,108
Ad 149,37
Pk 74,36,37
Maav 76,184,112
Ex 39
Maav 76,188,116
Wl 0
Ad 148,38
Ad 149,39
Pk 75,38,39
Cvaa 20,160,192,120
Ex 0
Mvaa 20,164,196
Ta 176,241,17024
Ad 140,0
Mvaa 20,168,200
Ex 1
Mvaa 20,172,204
Ta 178,241,18048
Ad 141,1
Cvaa 24,172,220,124
Ex 2
Mvaa 24,168,216
Ta 180,241,19072
Pk 44,0,1
Mvaa 24,164,212
Ex 3
Mvaa 24,160,208
Ta 182,241,20096
Ad 140,2
Cvaa 28,160,224,128
Ex 4
Mvaa 28,164,228
Ta 184,241,17056
Ad 141,3
Mvaa 28,168,232
Ex 5
Mvaa 28,172,236
Ta 186,241,18080
Pk 45,2,3
Cvaa 32,172,252,132
Ex 6
Mvaa 32,168,248
Ta 188,241,19104
Ad 142,4
Mvaa 32,164,244
Ex 7
Mvaa 32,160,240
Ta 190,241,20128
Ad 143,5
Cvav 36,160,248,136
Ex 8
Mvav 36,164,252
Pk 52,4,5
Ad 142,6
Mvav 36,168,150
Ex 9
Mvav 36,172,154
Wl 4
Ad 143,7
Pk 53,6,7
Ad 144,8
Maav 80,176,80
Ex 10
Maav 80,180,84
Ra 160,240,49536
Ad 145,9
Maav 84,176,88
Ex 11
Maav 84,180,92
Ra 164,240,49792
Pk 60,8,9
Maav 88,176,96
Ex 12
Maav 88,180,100
Ra 168,240,50048
Ad 144,10
Maav 92,176,104
Ex 13
Maav 92,180,108
Ra 172,240,50304
Ad 145,11
Maav 96,176,112
Ex 14
Maav 96,180,116
Wl 4
Pk 61,10,11
Ad 146,12
Maav 100,184,80
Ex 15
Maav 100,188,84
Ad 147,13
Pk 68,12,13
Maav 104,184,88
Ex 16
Maav 104,188,92
Ad 146,14
Ad 147,15
Maav 108,184,96
Ex 17
Maav 108,188,100
Pk 69,14,15
Ad 148,16
Maav 112,184,104
Ex 18
Maav 112,188,108
Ad 149,17
Pk 76,16,17
Maav 116,184,112
Ex 19
Maav 116,188,116
Wl 0
Ad 148,18
Ad 149,19
Pk 77,18,19
Cvaa 0,160,192,120
Ex 20
Mvaa 0,164,196
Ta 176,241,17280
Ad 140,20
Mvaa 0,168,200
Ex 21
Mvaa 0,172,204
Ta 178,241,18304
Ad 141,21
Cvaa 4,172,220,124
Ex 22
Mvaa 4,168,216
Ta 180,241,19328
Pk 46,20,21
Mvaa 4,164,212
Ex 23
Mvaa 4,160,208
Ta 182,241,20352
Ad 140,22
Cvaa 8,160,224,128
Ex 24
Mvaa 8,164,228
Ta 184,241,17312
Ad 141,23
Mvaa 8,168,232
Ex 25
Mvaa 8,172,236
Ta 186,241,18336
Pk 47,22,23
Cvaa 12,172,252,132
Ex 26
Mvaa 12,168,248
Ta 188,241,19360
Ad 142,24
Mvaa 12,164,244
Ex 27
Mvaa 12,160,240
Ta 190,241,20384
Ad 143,25
Cvav 16,160,248,136
Ex 28
Mvav 16,164,252
Pk 54,24,25
Ad 142,26
Mvav 16,168,150
Ex 29
Mvav 16,172,154
Wl 4
Ad 143,27
Pk 55,26,27
Ad 144,28
Maav 120,176,80
Ex 30
Maav 120,180,84
Ra 160,240,50560
Ad 145,29
Maav 124,176,88
Ex 31
Maav 124,180,92
Ra 164,240,50816
Pk 62,28,29
Maav 128,176,96
Ex 32
Maav 128,180,100
Ra 168,240,51072
Ad 144,30
Maav 132,176,104
Ex 33
Maav 132,180,108
Ra 172,240,51328
Ad 145,31
Maav 136,176,112
Ex 34
Maav 136,180,116
Wl 4
Pk 63,30,31
Ad 146,32
Maav 140,184,80
Ex 35
Maav 140,188,84
Ad 147,33
Pk 70,32,33
Maav 144,184,88
Ex 36
Maav 144,188,92
Ad 146,34
Ad 147,35
Maav 148,184,96
Ex 37
Maav 148,188,100
Pk 71,34,35
Ad 148,36
Maav 152,184,104
Ex 38
Maav 152,188,108
Ad 149,37
Pk 78,36,37
Maav 156,184,112
Ex 39
Maav 156,188,116
Wl 0
Ad 148,38
Ad 149,39
Pk 79,38,39
Cvaa 20,160,192,120
s_barrier
Ex 0
Mvaa 20,164,196
Ta 176,241,33024
Ad 140,0
Mvaa 20,168,200
Ex 1
Mvaa 20,172,204
Ta 178,241,34048
Ad 141,1
Cvaa 24,172,220,124
Ex 2
Mvaa 24,168,216
Ta 180,241,35072
Pk 80,0,1
Mvaa 24,164,212
Ex 3
Mvaa 24,160,208
Ta 182,241,36096
Ad 140,2
Cvaa 28,160,224,128
Ex 4
Mvaa 28,164,228
Ta 184,241,33056
Ad 141,3
Mvaa 28,168,232
Ex 5
Mvaa 28,172,236
Ta 186,241,34080
Pk 81,2,3
Cvaa 32,172,252,132
Ex 6
Mvaa 32,168,248
Ta 188,241,35104
Ad 142,4
Mvaa 32,164,244
Ex 7
Mvaa 32,160,240
Ta 190,241,36128
Ad 143,5
Cvav 36,160,248,136
Ex 8
Mvav 36,164,252
Pk 88,4,5
Ad 142,6
Mvav 36,168,150
Ex 9
Mvav 36,172,154
Wl 4
Ad 143,7
Pk 89,6,7
Ad 144,8
Maav 0,176,40
Ex 10
Maav 0,180,44
Ra 160,240,51584
Ad 145,9
Maav 4,176,48
Ex 11
Maav 4,180,52
Ra 164,240,51840
Pk 96,8,9
Maav 8,176,56
Ex 12
Maav 8,180,60
Ra 168,240,52096
Ad 144,10
Maav 12,176,64
Ex 13
Maav 12,180,68
Ra 172,240,52352
Ad 145,11
Maav 16,176,72
Ex 14
Maav 16,180,76
Wl 4
Pk 97,10,11
Ad 146,12
Maav 20,184,40
Ex 15
Maav 20,188,44
Ad 147,13
Pk 104,12,13
Maav 24,184,48
Ex 16
)A"
R"A(Maav 24,188,52
Ad 146,14
Ad 147,15
Maav 28,184,56
Ex 17
Maav 28,188,60
Pk 105,14,15
Ad 148,16
Maav 32,184,64
Ex 18
Maav 32,188,68
Ad 149,17
Pk 112,16,17
Maav 36,184,72
Ex 19
Maav 36,188,76
Wl 0
Ad 148,18
Ad 149,19
Pk 113,18,19
Cvaa 0,160,192,120
Ex 20
Mvaa 0,164,196
Ta 176,241,33280
Ad 140,20
Mvaa 0,168,200
Ex 21
Mvaa 0,172,204
Ta 178,241,34304
Ad 141,21
Cvaa 4,172,220,124
Ex 22
Mvaa 4,168,216
Ta 180,241,35328
Pk 82,20,21
Mvaa 4,164,212
Ex 23
Mvaa 4,160,208
Ta 182,241,36352
Ad 140,22
Cvaa 8,160,224,128
Ex 24
Mvaa 8,164,228
Ta 184,241,33312
Ad 141,23
Mvaa 8,168,232
Ex 25
Mvaa 8,172,236
Ta 186,241,34336
Pk 83,22,23
Cvaa 12,172,252,132
Ex 26
Mvaa 12,168,248
Ta 188,241,35360
Ad 142,24
Mvaa 12,164,244
Ex 27
Mvaa 12,160,240
Ta 190,241,36384
Ad 143,25
Cvav 16,160,248,136
Ex 28
Mvav 16,164,252
Pk 90,24,25
Ad 142,26
Mvav 16,168,150
Ex 29
Mvav 16,172,154
Wl 4
Ad 143,27
Pk 91,26,27
Ad 144,28
Maav 40,176,40
Ex 30
Maav 40,180,44
Ra 160,240,52608
Ad 145,29
Maav 44,176,48
Ex 31
Maav 44,180,52
Ra 164,240,52864
Pk 98,28,29
Maav 48,176,56
Ex 32
Maav 48,180,60
Ra 168,240,53120
Ad 144,30
Maav 52,176,64
Ex 33
Maav 52,180,68
Ra 172,240,53376
Ad 145,31
Maav 56,176,72
Ex 34
Maav 56,180,76
Wl 4
Pk 99,30,31
Ad 146,32
Maav 60,184,40
Ex 35
Maav 60,188,44
Ad 147,33
Pk 106,32,33
Maav 64,184,48
Ex 36
Maav 64,188,52
Ad 146,34
Ad 147,35
Maav 68,184,56
Ex 37
Maav 68,188,60
Pk 107,34,35
Ad 148,36
Maav 72,184,64
Ex 38
Maav 72,188,68
Ad 149,37
Pk 114,36,37
Maav 76,184,72
Ex 39
Maav 76,188,76
Wl 0
Ad 148,38
Ad 149,39
Pk 115,38,39
Cvaa 20,160,192,120
Ex 0
Mvaa 20,164,196
Ta 176,241,33536
Ad 140,0
Mvaa 20,168,200
Ex 1
Mvaa 20,172,204
Ta 178,241,34560
Ad 141,1
Cvaa 24,172,220,124
Ex 2
Mvaa 24,168,216
Ta 180,241,35584
Pk 84,0,1
Mvaa 24,164,212
Ex 3
Mvaa 24,160,208
Ta 182,241,36608
Ad 140,2
Cvaa 28,160,224,128
Ex 4
Mvaa 28,164,228
Ta 184,241,33568
Ad 141,3
Mvaa 28,168,232
Ex 5
Mvaa 28,172,236
Ta 186,241,34592
Pk 85,2,3
Cvaa 32,172,252,132
Ex 6
Mvaa 32,168,248
Ta 188,241,35616
Ad 142,4
Mvaa 32,164,244
Ex 7
Mvaa 32,160,240
Ta 190,241,36640
Ad 143,5
Cvav 36,160,248,136
Ex 8
Mvav 36,164,252
Pk 92,4,5
Ad 142,6
Mvav 36,168,150
Ex 9
Mvav 36,172,154
Wl 4
Ad 143,7
Pk 93,6,7
Ad 144,8
Maav 80,176,40
Ex 10
Maav 80,180,44
Ra 160,240,0
Ad 145,9
.p2align 12
.org PG0_%=+73728
s_cbranch_execz VRET_%=
s_nop 0
Maav 84,176,48
Ex 11
Maav 84,180,52
Ra 164,240,256
Pk 100,8,9
Maav 88,176,56
Ex 12
Maav 88,180,60
Ra 168,240,512
Ad 144,10
Maav 92,176,64
Ex 13
Maav 92,180,68
Ra 172,240,768
Ad 145,11
Maav 96,176,72
Ex 14
Maav 96,180,76
Wl 4
Pk 101,10,11
Ad 146,12
Maav 100,184,40
Ex 15
Maav 100,188,44
Ad 147,13
Pk 108,12,13
Maav 104,184,48
Ex 16
Maav 104,188,52
Ad 146,14
Ad 147,15
Maav 108,184,56
Ex 17
Maav 108,188,60
Pk 109,14,15
Ad 148,16
Maav 112,184,64
Ex 18
Maav 112,188,68
Ad 149,17
Pk 116,16,17
Maav 116,184,72
Ex 19
Maav 116,188,76
Wl 0
Ad 148,18
Ad 149,19
Pk 117,18,19
Cvaa 0,160,192,120
Ex 20
Mvaa 0,164,196
Ta 176,241,33792
Ad 140,20
Mvaa 0,168,200
Ex 21
Mvaa 0,172,204
Ta 178,241,34816
Ad 141,21
Cvaa 4,172,220,124
Ex 22
Mvaa 4,168,216
Ta 180,241,35840
Pk 86,20,21
Mvaa 4,164,212
Ex 23
Mvaa 4,160,208
Ta 182,241,36864
Ad 140,22
Cvaa 8,160,224,128
Ex 24
Mvaa 8,164,228
Ta 184,241,33824
Ad 141,23
Mvaa 8,168,232
Ex 25
Mvaa 8,172,236
Ta 186,241,34848
Pk 87,22,23
Cvaa 12,172,252,132
Ex 26
Mvaa 12,168,248
Ta 188,241,35872
Ad 142,24
Mvaa 12,164,244
Ex 27
Mvaa 12,160,240
Ta 190,241,36896
Ad 143,25
Cvav 16,160,248,136
Ex 28
Mvav 16,164,252
Pk 94,24,25
Ad 142,26
Mvav 16,168,150
Ex 29
Mvav 16,172,154
Wl 4
Ad 143,27
Pk 95,26,27
Ad 144,28
Maav 120,176,40
Ex 30
Maav 120,180,44
Ra 160,240,1024
Ad 145,29
Maav 124,176,48
Ex 31
Maav 124,180,52
Ra 164,240,1280
Pk 102,28,29
Maav 128,176,56
Ex 32
Maav 128,180,60
Ra 168,240,1536
Ad 144,30
Maav 132,176,64
Ex 33
Maav 132,180,68
Ra 172,240,1792
Ad 145,31
Maav 136,176,72
Ex 34
Maav 136,180,76
Wl 4
Pk 103,30,31
Ad 146,32
Maav 140,184,40
Ex 35
Maav 140,188,44
Ad 147,33
Pk 110,32,33
Maav 144,184,48
Ex 36
Maav 144,188,52
Ad 146,34
Ad 147,35
Maav 148,184,56
Ex 37
Maav 148,188,60
Pk 111,34,35
Ad 148,36
Maav 152,184,64
Ex 38
Maav 152,188,68
Ad 149,37
Pk 118,36,37
Maav 156,184,72
Ex 39
Maav 156,188,76
Wl 0
Ad 148,38
Ad 149,39
Pk 119,38,39
Cvaa 20,160,192,120
s_barrier
Ex 0
Mvaa 20,164,196
Ta 176,241,49536
Ad 140,0
Mvaa 20,168,200
Ex 1
Mvaa 20,172,204
Ta 178,241,50560
Ad 141,1
Cvaa 24,172,220,124
Ex 2
Mvaa 24,168,216
Ta 180,241,51584
Pk 40,0,1
Mvaa 24,164,212
Ex 3
Mvaa 24,160,208
Ta 182,241,52608
Ad 140,2
Cvaa 28,160,224,128
Ex 4
Mvaa 28,164,228
Ta 184,241,49568
Ad 141,3
Mvaa 28,168,232
Ex 5
Mvaa 28,172,236
Ta 186,241,50592
Pk 41,2,3
Cvaa 32,172,252,132
Ex 6
Mvaa 32,168,248
Ta 188,241,51616
Ad 142,4
Mvaa 32,164,244
Ex 7
Mvaa 32,160,240
Ta 190,241,52640
Ad 143,5
Cvav 36,160,248,136
Ex 8
Mvav 36,164,252
Pk 48,4,5
Ad 142,6
Mvav 36,168,150
Ex 9
Mvav 36,172,154
Wl 4
Ad 143,7
Pk 49,6,7
Ad 144,8
Maav 0,176,80
Ex 10
Maav 0,180,84
Ra 160,240,2048
Ad 145,9
Maav 4,176,88
Ex 11
Maav 4,180,92
)A"
R"A(Ra 164,240,2304
Pk 56,8,9
Maav 8,176,96
Ex 12
Maav 8,180,100
Ra 168,240,2560
Ad 144,10
Maav 12,176,104
Ex 13
Maav 12,180,108
Ra 172,240,2816
Ad 145,11
Maav 16,176,112
Ex 14
Maav 16,180,116
Wl 4
Pk 57,10,11
Ad 146,12
Maav 20,184,80
Ex 15
Maav 20,188,84
Ad 147,13
Pk 64,12,13
Maav 24,184,88
Ex 16
Maav 24,188,92
Ad 146,14
Ad 147,15
Maav 28,184,96
Ex 17
Maav 28,188,100
Pk 65,14,15
Ad 148,16
Maav 32,184,104
Ex 18
Maav 32,188,108
Ad 149,17
Pk 72,16,17
Maav 36,184,112
Ex 19
Maav 36,188,116
Wl 0
Ad 148,18
Ad 149,19
Pk 73,18,19
Cvaa 0,160,192,120
Ex 20
Mvaa 0,164,196
Ta 176,241,49792
Ad 140,20
Mvaa 0,168,200
Ex 21
Mvaa 0,172,204
Ta 178,241,50816
Ad 141,21
Cvaa 4,172,220,124
Ex 22
Mvaa 4,168,216
Ta 180,241,51840
Pk 42,20,21
Mvaa 4,164,212
Ex 23
Mvaa 4,160,208
Ta 182,241,52864
Ad 140,22
Cvaa 8,160,224,128
Ex 24
Mvaa 8,164,228
Ta 184,241,49824
Ad 141,23
Mvaa 8,168,232
Ex 25
Mvaa 8,172,236
Ta 186,241,50848
Pk 43,22,23
Cvaa 12,172,252,132
Ex 26
Mvaa 12,168,248
Ta 188,241,51872
Ad 142,24
Mvaa 12,164,244
Ex 27
Mvaa 12,160,240
Ta 190,241,52896
Ad 143,25
Cvav 16,160,248,136
Ex 28
Mvav 16,164,252
Pk 50,24,25
Ad 142,26
Mvav 16,168,150
Ex 29
Mvav 16,172,154
Wl 4
Ad 143,27
Pk 51,26,27
Ad 144,28
Maav 40,176,80
Ex 30
Maav 40,180,84
Ra 160,240,3072
Ad 145,29
Maav 44,176,88
Ex 31
Maav 44,180,92
Ra 164,240,3328
Pk 58,28,29
Maav 48,176,96
Ex 32
Maav 48,180,100
Ra 168,240,3584
Ad 144,30
Maav 52,176,104
Ex 33
Maav 52,180,108
Ra 172,240,3840
Ad 145,31
Maav 56,176,112
Ex 34
Maav 56,180,116
Wl 4
Pk 59,30,31
Ad 146,32
Maav 60,184,80
Ex 35
Maav 60,188,84
Ad 147,33
Pk 66,32,33
Maav 64,184,88
Ex 36
Maav 64,188,92
Ad 146,34
Ad 147,35
Maav 68,184,96
Ex 37
Maav 68,188,100
Pk 67,34,35
Ad 148,36
Maav 72,184,104
Ex 38
Maav 72,188,108
Ad 149,37
Pk 74,36,37
Maav 76,184,112
Ex 39
Maav 76,188,116
Wl 0
Ad 148,38
Ad 149,39
Pk 75,38,39
Cvaa 20,160,192,120
Ex 0
Mvaa 20,164,196
Ta 176,241,50048
Ad 140,0
Mvaa 20,168,200
Ex 1
Mvaa 20,172,204
Ta 178,241,51072
Ad 141,1
Cvaa 24,172,220,124
Ex 2
Mvaa 24,168,216
Ta 180,241,52096
Pk 44,0,1
Mvaa 24,164,212
Ex 3
Mvaa 24,160,208
Ta 182,241,53120
Ad 140,2
Cvaa 28,160,224,128
Ex 4
Mvaa 28,164,228
Ta 184,241,50080
Ad 141,3
Mvaa 28,168,232
Ex 5
Mvaa 28,172,236
Ta 186,241,51104
Pk 45,2,3
Cvaa 32,172,252,132
Ex 6
Mvaa 32,168,248
Ta 188,241,52128
Ad 142,4
Mvaa 32,164,244
Ex 7
Mvaa 32,160,240
Ta 190,241,53152
Ad 143,5
Cvav 36,160,248,136
Ex 8
Mvav 36,164,252
Pk 52,4,5
Ad 142,6
Mvav 36,168,150
Ex 9
Mvav 36,172,154
Wl 4
Ad 143,7
Pk 53,6,7
Ad 144,8
Maav 80,176,80
Ex 10
Maav 80,180,84
Ra 160,240,16512
Ad 145,9
Maav 84,176,88
Ex 11
Maav 84,180,92
Ra 164,240,16768
Pk 60,8,9
Maav 88,176,96
Ex 12
Maav 88,180,100
Ra 168,240,17024
Ad 144,10
Maav 92,176,104
Ex 13
Maav 92,180,108
Ra 172,240,17280
Ad 145,11
Maav 96,176,112
Ex 14
Maav 96,180,116
Wl 4
Pk 61,10,11
Ad 146,12
Maav 100,184,80
Ex 15
Maav 100,188,84
Ad 147,13
Pk 68,12,13
Maav 104,184,88
Ex 16
Maav 104,188,92
Ad 146,14
Ad 147,15
Maav 108,184,96
Ex 17
Maav 108,188,100
Pk 69,14,15
Ad 148,16
Maav 112,184,104
Ex 18
Maav 112,188,108
Ad 149,17
Pk 76,16,17
Maav 116,184,112
Ex 19
Maav 116,188,116
Wl 0
Ad 148,18
Ad 149,19
Pk 77,18,19
Cvaa 0,160,192,120
Ex 20
Mvaa 0,164,196
Ta 176,241,50304
Ad 140,20
Mvaa 0,168,200
Ex 21
Mvaa 0,172,204
Ta 178,241,51328
Ad 141,21
Cvaa 4,172,220,124
Ex 22
Mvaa 4,168,216
Ta 180,241,52352
Pk 46,20,21
Mvaa 4,164,212
Ex 23
Mvaa 4,160,208
Ta 182,241,53376
Ad 140,22
Cvaa 8,160,224,128
Ex 24
Mvaa 8,164,228
Ta 184,241,50336
Ad 141,23
Mvaa 8,168,232
Ex 25
Mvaa 8,172,236
Ta 186,241,51360
Pk 47,22,23
Cvaa 12,172,252,132
Ex 26
Mvaa 12,168,248
Ta 188,241,52384
Ad 142,24
Mvaa 12,164,244
Ex 27
Mvaa 12,160,240
Ta 190,241,53408
Ad 143,25
Cvav 16,160,248,136
Ex 28
Mvav 16,164,252
Pk 54,24,25
Ad 142,26
Mvav 16,168,150
Ex 29
Mvav 16,172,154
Wl 4
Ad 143,27
Pk 55,26,27
Ad 144,28
Maav 120,176,80
Ex 30
Maav 120,180,84
Ra 160,240,17536
Ad 145,29
Maav 124,176,88
Ex 31
Maav 124,180,92
Ra 164,240,17792
Pk 62,28,29
Maav 128,176,96
Ex 32
Maav 128,180,100
Ra 168,240,18048
Ad 144,30
Maav 132,176,104
Ex 33
Maav 132,180,108
Ra 172,240,18304
Ad 145,31
Maav 136,176,112
Ex 34
Maav 136,180,116
Wl 4
Pk 63,30,31
Ad 146,32
Maav 140,184,80
Ex 35
Maav 140,188,84
Ad 147,33
Pk 70,32,33
Maav 144,184,88
Ex 36
Maav 144,188,92
Ad 146,34
Ad 147,35
Maav 148,184,96
Ex 37
Maav 148,188,100
Pk 71,34,35
Ad 148,36
Maav 152,184,104
Ex 38
Maav 152,188,108
Ad 149,37
Pk 78,36,37
Maav 156,184,112
Ex 39
Maav 156,188,116
Wl 0
Ad 148,38
Ad 149,39
Pk 79,38,39
Cvaa 20,160,192,120
s_barrier
Ex 0
Mvaa 20,164,196
Ta 176,241,0
Ad 140,0
Mvaa 20,168,200
Ex 1
Mvaa 20,172,204
Ta 178,241,1024
Ad 141,1
Cvaa 24,172,220,124
Ex 2
Mvaa 24,168,216
Ta 180,241,2048
Pk 80,0,1
Mvaa 24,164,212
Ex 3
Mvaa 24,160,208
Ta 182,241,3072
Ad 140,2
Cvaa 28,160,224,128
Ex 4
Mvaa 28,164,228
Ta 184,241,32
Ad 141,3
Mvaa 28,168,232
Ex 5
Mvaa 28,172,236
Ta 186,241,1056
Pk 81,2,3
Cvaa 32,172,252,132
Ex 6
Mvaa 32,168,248
Ta 188,241,2080
Ad 142,4
Mvaa 32,164,244
Ex 7
Mvaa 32,160,240
Ta 190,241,3104
)A"
R"A(Ad 143,5
Cvav 36,160,248,136
Ex 8
Mvav 36,164,252
Pk 88,4,5
Ad 142,6
Mvav 36,168,150
Ex 9
Mvav 36,172,154
Wl 4
Ad 143,7
Pk 89,6,7
Ad 144,8
Maav 0,176,40
Ex 10
Maav 0,180,44
Ra 160,240,18560
Ad 145,9
Maav 4,176,48
Ex 11
Maav 4,180,52
Ra 164,240,18816
Pk 96,8,9
Maav 8,176,56
Ex 12
Maav 8,180,60
Ra 168,240,19072
Ad 144,10
.p2align 12
.org PG0_%=+77824
s_cbranch_execz VRET_%=
Maav 12,176,64
Ex 13
Maav 12,180,68
Ra 172,240,19328
Ad 145,11
Maav 16,176,72
Ex 14
Maav 16,180,76
Wl 4
Pk 97,10,11
Ad 146,12
Maav 20,184,40
Ex 15
Maav 20,188,44
Ad 147,13
Pk 104,12,13
Maav 24,184,48
Ex 16
Maav 24,188,52
Ad 146,14
Ad 147,15
Maav 28,184,56
Ex 17
Maav 28,188,60
Pk 105,14,15
Ad 148,16
Maav 32,184,64
Ex 18
Maav 32,188,68
Ad 149,17
Pk 112,16,17
Maav 36,184,72
Ex 19
Maav 36,188,76
Wl 0
Ad 148,18
Ad 149,19
Pk 113,18,19
Cvaa 0,160,192,120
Ex 20
Mvaa 0,164,196
Ta 176,241,256
global_load_dword v244, v243, s[50:51] sc1
Mvaa 0,168,200
Ex 21
Mvaa 0,172,204
Ta 178,241,1280
Ad 140,20
Cvaa 4,172,220,124
Ex 22
Mvaa 4,168,216
Ta 180,241,2304
Ad 141,21
Mvaa 4,164,212
Ex 23
Mvaa 4,160,208
Ta 182,241,3328
Pk 82,20,21
Cvaa 8,160,224,128
Ex 24
Mvaa 8,164,228
Ta 184,241,288
Ad 140,22
Mvaa 8,168,232
Ex 25
Mvaa 8,172,236
Ta 186,241,1312
Ad 141,23
Cvaa 12,172,252,132
Ex 26
Mvaa 12,168,248
Ta 188,241,2336
Pk 83,22,23
Ad 142,24
Mvaa 12,164,244
Ex 27
Mvaa 12,160,240
Ta 190,241,3360
Ad 143,25
Cvav 16,160,248,136
Ex 28
Mvav 16,164,252
Pk 90,24,25
Ad 142,26
Mvav 16,168,150
Ex 29
Mvav 16,172,154
Wl 4
Ad 143,27
Pk 91,26,27
Maav 40,176,40
Ex 30
Maav 40,180,44
Ra 160,240,19584
Ad 144,28
Maav 44,176,48
Ex 31
Maav 44,180,52
Ra 164,240,19840
Ad 145,29
Maav 48,176,56
Ex 32
Maav 48,180,60
Ra 168,240,20096
Pk 98,28,29
Maav 52,176,64
Ex 33
Maav 52,180,68
Ra 172,240,20352
Ad 144,30
Ad 145,31
Maav 56,176,72
Ex 34
Maav 56,180,76
Wl 4
Pk 99,30,31
Ad 146,32
Maav 60,184,40
Ex 35
Maav 60,188,44
Ad 147,33
Pk 106,32,33
Maav 64,184,48
Ex 36
Maav 64,188,52
Ad 146,34
Ad 147,35
Maav 68,184,56
Ex 37
Maav 68,188,60
Pk 107,34,35
Ad 148,36
Maav 72,184,64
Ex 38
Maav 72,188,68
Ad 149,37
Pk 114,36,37
Maav 76,184,72
Ex 39
Maav 76,188,76
Wl 0
Ad 148,38
Ad 149,39
Pk 115,38,39
Cvaa 20,160,192,120
Ex 0
Mvaa 20,164,196
Ta 176,241,512
Ad 140,0
Mvaa 20,168,200
Ex 1
Mvaa 20,172,204
Ta 178,241,1536
Ad 141,1
Cvaa 24,172,220,124
Ex 2
Mvaa 24,168,216
Ta 180,241,2560
Pk 84,0,1
Mvaa 24,164,212
Ex 3
Mvaa 24,160,208
Ta 182,241,3584
Ad 140,2
Cvaa 28,160,224,128
Ex 4
Mvaa 28,164,228
Ta 184,241,544
Ad 141,3
Mvaa 28,168,232
Ex 5
Mvaa 28,172,236
Ta 186,241,1568
Pk 85,2,3
Cvaa 32,172,252,132
Ex 6
Mvaa 32,168,248
Ta 188,241,2592
Ad 142,4
Mvaa 32,164,244
Ex 7
Mvaa 32,160,240
Ta 190,241,3616
Ad 143,5
Cvav 36,160,248,136
Ex 8
Mvav 36,164,252
Pk 92,4,5
Ad 142,6
Mvav 36,168,150
Ex 9
Mvav 36,172,154
Wl 4
Ad 143,7
Pk 93,6,7
Ad 144,8
Maav 80,176,40
Ex 10
Maav 80,180,44
Ra 160,240,33024
Ad 145,9
Maav 84,176,48
Ex 11
Maav 84,180,52
Ra 164,240,33280
Pk 100,8,9
Maav 88,176,56
Ex 12
Maav 88,180,60
Ra 168,240,33536
Ad 144,10
Maav 92,176,64
Ex 13
Maav 92,180,68
Ra 172,240,33792
Ad 145,11
Maav 96,176,72
Ex 14
Maav 96,180,76
Wl 4
Pk 101,10,11
Ad 146,12
Maav 100,184,40
Ex 15
Maav 100,188,44
Ad 147,13
Pk 108,12,13
Maav 104,184,48
Ex 16
Maav 104,188,52
Ad 146,14
Ad 147,15
Maav 108,184,56
Ex 17
Maav 108,188,60
Pk 109,14,15
Ad 148,16
Maav 112,184,64
Ex 18
Maav 112,188,68
Ad 149,17
Pk 116,16,17
Maav 116,184,72
Ex 19
Maav 116,188,76
Wl 0
Ad 148,18
Ad 149,19
Pk 117,18,19
Cvaa 0,160,192,120
Ex 20
Mvaa 0,164,196
Ta 176,241,768
Ad 140,20
Mvaa 0,168,200
Ex 21
Mvaa 0,172,204
Ta 178,241,1792
Ad 141,21
Cvaa 4,172,220,124
Ex 22
Mvaa 4,168,216
Ta 180,241,2816
Pk 86,20,21
Mvaa 4,164,212
Ex 23
Mvaa 4,160,208
Ta 182,241,3840
Ad 140,22
Cvaa 8,160,224,128
Ex 24
Mvaa 8,164,228
Ta 184,241,800
Ad 141,23
Mvaa 8,168,232
Ex 25
Mvaa 8,172,236
Ta 186,241,1824
Pk 87,22,23
Cvaa 12,172,252,132
Ex 26
Mvaa 12,168,248
Ta 188,241,2848
Ad 142,24
Mvaa 12,164,244
Ex 27
Mvaa 12,160,240
Ta 190,241,3872
Ad 143,25
Cvav 16,160,248,136
Ex 28
Mvav 16,164,252
Pk 94,24,25
Ad 142,26
Mvav 16,168,150
Ex 29
Mvav 16,172,154
Wl 4
Ad 143,27
Pk 95,26,27
Ad 144,28
Maav 120,176,40
Ex 30
Maav 120,180,44
Ra 160,240,34048
Ad 145,29
Maav 124,176,48
Ex 31
Maav 124,180,52
Ra 164,240,34304
Pk 102,28,29
Maav 128,176,56
Ex 32
Maav 128,180,60
Ra 168,240,34560
Ad 144,30
Maav 132,176,64
Ex 33
Maav 132,180,68
Ra 172,240,34816
Ad 145,31
Maav 136,176,72
Ex 34
Maav 136,180,76
Wl 4
Pk 103,30,31
Ad 146,32
Maav 140,184,40
Ex 35
Maav 140,188,44
Ad 147,33
Pk 110,32,33
Maav 144,184,48
Ex 36
Maav 144,188,52
Ad 146,34
Ad 147,35
Maav 148,184,56
Ex 37
Maav 148,188,60
Pk 111,34,35
Ad 148,36
Maav 152,184,64
Ex 38
Maav 152,188,68
Ad 149,37
Pk 118,36,37
Maav 156,184,72
Ex 39
Maav 156,188,76
Wl 0
Ad 148,38
Ad 149,39
Pk 119,38,39
Cvaa 20,160,192,120
s_barrier
Ex 0
Mvaa 20,164,196
Ta 176,241,16512
Ad 140,0
Mvaa 20,168,200
Ex 1
Mvaa 20,172,204
Ta 178,241,17536
Ad 141,1
Cvaa 24,172,220,124
Ex 2
Mvaa 24,168,216
Ta 180,241,18560
)A"
R"A(Pk 40,0,1
Mvaa 24,164,212
Ex 3
Mvaa 24,160,208
Ta 182,241,19584
Ad 140,2
Cvaa 28,160,224,128
Ex 4
Mvaa 28,164,228
Ta 184,241,16544
Ad 141,3
Mvaa 28,168,232
Ex 5
Mvaa 28,172,236
Ta 186,241,17568
Pk 41,2,3
Cvaa 32,172,252,132
Ex 6
Mvaa 32,168,248
Ta 188,241,18592
Ad 142,4
Mvaa 32,164,244
Ex 7
Mvaa 32,160,240
Ta 190,241,19616
Ad 143,5
Cvav 36,160,248,136
Ex 8
Mvav 36,164,252
Pk 48,4,5
Ad 142,6
Mvav 36,168,150
Ex 9
Mvav 36,172,154
Wl 4
Ad 143,7
Pk 49,6,7
Ad 144,8
Maav 0,176,80
Ex 10
Maav 0,180,84
Ra 160,240,35072
Ad 145,9
Maav 4,176,88
Ex 11
Maav 4,180,92
Ra 164,240,35328
Pk 56,8,9
Maav 8,176,96
Ex 12
Maav 8,180,100
Ra 168,240,35584
Ad 144,10
Maav 12,176,104
Ex 13
Maav 12,180,108
Ra 172,240,35840
Ad 145,11
Maav 16,176,112
Ex 14
Maav 16,180,116
Wl 4
Pk 57,10,11
Ad 146,12
Maav 20,184,80
Ex 15
Maav 20,188,84
Ad 147,13
Pk 64,12,13
Maav 24,184,88
Ex 16
Maav 24,188,92
Ad 146,14
Ad 147,15
Maav 28,184,96
Ex 17
Maav 28,188,100
Pk 65,14,15
Ad 148,16
Maav 32,184,104
Ex 18
Maav 32,188,108
Ad 149,17
Pk 72,16,17
Maav 36,184,112
Ex 19
Maav 36,188,116
Wl 0
Ad 148,18
Ad 149,19
Pk 73,18,19
s_waitcnt vmcnt(0)
s_mov_b32 s58, 0
v_cmp_gt_i32 s[64:65], 0, v244
v_and_b32 v244, 0x7fffffff, v244
v_cmp_ne_u32 vcc, s81, v244
s_nop 0
s_and_b64 vcc, vcc, s[34:35]
s_cbranch_scc1 SPINP0A_%=
OKP0A_%=:
Cvaa 0,160,192,120
s_and_b32 s56, s24, 3
Ex 20
Mvaa 0,164,196
s_cmp_le_u32 s56, 0
s_cselect_b32 s56, 1, 0
Ta 176,241,16768
Ad 140,20
Mvaa 0,168,200
s_andn2_b32 s16, s49, 0xc0000
Ex 21
Mvaa 0,172,204
s_lshl_b32 s17, s56, 18
Ta 178,241,17792
Ad 141,21
Cvaa 4,172,220,124
s_add_u32 s16, s16, s17
Ex 22
Mvaa 4,168,216
s_lshr_b32 s17, s64, s56
Ta 180,241,18816
Pk 42,20,21
Mvaa 4,164,212
s_and_b32 s17, s17, 16
Ex 23
Mvaa 4,160,208
s_lshl_b32 s17, s17, 9
Ta 182,241,19840
Ad 140,22
Cvaa 8,160,224,128
s_lshl_b32 s56, s56, 19
Ex 24
Mvaa 8,164,228
s_add_u32 s17, s17, s56
Ta 184,241,16800
Ad 141,23
Mvaa 8,168,232
s_and_b32 s56, s24, 3
Ex 25
Mvaa 8,172,236
s_cmp_le_u32 s56, 1
s_cselect_b32 s56, 2, 1
Ta 186,241,17824
Pk 43,22,23
Cvaa 12,172,252,132
s_andn2_b32 s82, s49, 0xc0000
Ex 26
Mvaa 12,168,248
s_lshl_b32 s18, s56, 18
Ta 188,241,18848
Ad 142,24
Mvaa 12,164,244
s_add_u32 s82, s82, s18
Ex 27
Mvaa 12,160,240
s_lshr_b32 s18, s64, s56
Ta 190,241,19872
Ad 143,25
Cvav 16,160,248,136
s_and_b32 s18, s18, 16
Ex 28
Mvav 16,164,252
s_lshl_b32 s18, s18, 9
Pk 50,24,25
Ad 142,26
Mvav 16,168,150
s_lshl_b32 s56, s56, 19
Ex 29
Mvav 16,172,154
Wl 4
Ad 143,27
Pk 51,26,27
Ad 144,28
Maav 40,176,80
s_add_u32 s18, s18, s56
Ex 30
Maav 40,180,84
s_and_b32 s56, s24, 3
Ra 160,240,36096
Ad 145,29
Maav 44,176,88
s_cmp_le_u32 s56, 2
s_cselect_b32 s56, 3, 2
Ex 31
Maav 44,180,92
s_andn2_b32 s83, s49, 0xc0000
Ra 164,240,36352
Pk 58,28,29
Maav 48,176,96
s_lshl_b32 s22, s56, 18
Ex 32
Maav 48,180,100
s_add_u32 s83, s83, s22
Ra 168,240,36608
Ad 144,30
Maav 52,176,104
s_lshr_b32 s22, s64, s56
Ex 33
Maav 52,180,108
s_and_b32 s22, s22, 16
Ra 172,240,36864
Ad 145,31
Maav 56,176,112
s_lshl_b32 s22, s22, 9
Ex 34
Maav 56,180,116
Wl 4
Pk 59,30,31
Ad 146,32
Maav 60,184,80
s_lshl_b32 s56, s56, 19
Ex 35
Maav 60,188,84
s_add_u32 s22, s22, s56
Ad 147,33
Pk 66,32,33
Maav 64,184,88
s_and_b64 s[64:65], s[64:65], s[34:35]
Ex 36
Maav 64,188,92
s_and_b32 s27, s64, 0xffff0000
s_cselect_b32 s27, 8192, 0
Ad 146,34
Ad 147,35
Maav 68,184,96
s_xor_b32 s56, s24, 4
Ex 37
Maav 68,188,100
s_lshl_b32 s56, s56, 19
Pk 67,34,35
Ad 148,36
Maav 72,184,104
s_add_u32 s27, s27, s56
Ex 38
Maav 72,188,108
s_and_b32 s52, s65, 0xffff
s_cselect_b32 s52, 8192, 0
Ad 149,37
Pk 74,36,37
Maav 76,184,112
s_xor_b32 s56, s24, 8
Ex 39
Maav 76,188,116
Wl 0
Ad 148,38
Ad 149,39
Pk 75,38,39
Cvaa 20,160,192,120
s_lshl_b32 s56, s56, 19
Ex 0
Mvaa 20,164,196
s_add_u32 s52, s52, s56
Ta 176,241,17024
Ad 140,0
Mvaa 20,168,200
s_and_b32 s55, s65, 0xffff0000
s_cselect_b32 s55, 8192, 0
Ex 1
Mvaa 20,172,204
s_xor_b32 s56, s24, 12
Ta 178,241,18048
Ad 141,1
Cvaa 24,172,220,124
s_lshl_b32 s56, s56, 19
Ex 2
Mvaa 24,168,216
s_add_u32 s55, s55, s56
Ta 180,241,19072
Pk 44,0,1
Mvaa 24,164,212
Ex 3
Mvaa 24,160,208
Ta 182,241,20096
Ad 140,2
Cvaa 28,160,224,128
Ex 4
Mvaa 28,164,228
Ta 184,241,17056
Ad 141,3
Mvaa 28,168,232
Ex 5
Mvaa 28,172,236
Ta 186,241,18080
Pk 45,2,3
Cvaa 32,172,252,132
Ex 6
Mvaa 32,168,248
Ta 188,241,19104
Ad 142,4
.p2align 12
.org PG0_%=+81920
s_cbranch_execz VRET_%=
Mvaa 32,164,244
Ex 7
Mvaa 32,160,240
Ta 190,241,20128
Ad 143,5
Cvav 36,160,248,136
Ex 8
Mvav 36,164,252
Pk 52,4,5
Ad 142,6
Mvav 36,168,150
Ex 9
Mvav 36,172,154
Wl 4
Ad 143,7
Pk 53,6,7
Ad 144,8
Maav 80,176,80
Ex 10
Maav 80,180,84
Ra 160,240,49536
Ad 145,9
Maav 84,176,88
Ex 11
Maav 84,180,92
Ra 164,240,49792
Pk 60,8,9
Maav 88,176,96
Ex 12
Maav 88,180,100
Ra 168,240,50048
Ad 144,10
Maav 92,176,104
Ex 13
Maav 92,180,108
Ra 172,240,50304
Ad 145,11
Maav 96,176,112
Ex 14
Maav 96,180,116
Wl 4
Pk 61,10,11
Ad 146,12
Maav 100,184,80
Ex 15
Maav 100,188,84
Ad 147,13
Pk 68,12,13
Maav 104,184,88
Ex 16
Maav 104,188,92
Ad 146,14
Ad 147,15
Maav 108,184,96
Ex 17
Maav 108,188,100
Pk 69,14,15
Ad 148,16
Maav 112,184,104
Ex 18
Maav 112,188,108
Ad 149,17
Pk 76,16,17
Maav 116,184,112
Ex 19
Maav 116,188,116
Wl 0
Ad 148,18
Ad 149,19
Pk 77,18,19
Cvaa 0,160,192,120
Ex 20
Mvaa 0,164,196
Ta 176,241,17280
Ad 140,20
Mvaa 0,168,200
Ex 21
Mvaa 0,172,204
Ta 178,241,18304
Ad 141,21
Cvaa 4,172,220,124
Ex 22
Mvaa 4,168,216
Ta 180,241,19328
Pk 46,20,21
Mvaa 4,164,212
Ex 23
Mvaa 4,160,208
Ta 182,241,20352
Ad 140,22
Cvaa 8,160,224,128
Ex 24
Mvaa 8,164,228
Ta 184,241,17312
Ad 141,23
Mvaa 8,168,232
Ex 25
Mvaa 8,172,236
Ta 186,241,18336
Pk 47,22,23
Cvaa 12,172,252,132
Ex 26
Mvaa 12,168,248
Ta 188,241,19360
Ad 142,24
Mvaa 12,164,244
Ex 27
Mvaa 12,160,240
)A"
R"A(Ta 190,241,20384
Ad 143,25
Cvav 16,160,248,136
Ex 28
Mvav 16,164,252
Pk 54,24,25
Ad 142,26
Mvav 16,168,150
Ex 29
Mvav 16,172,154
Wl 4
Ad 143,27
Pk 55,26,27
Ad 144,28
Maav 120,176,80
Ex 30
Maav 120,180,84
Ra 160,240,50560
Ad 145,29
Maav 124,176,88
Ex 31
Maav 124,180,92
Ra 164,240,50816
Pk 62,28,29
Maav 128,176,96
Ex 32
Maav 128,180,100
Ra 168,240,51072
Ad 144,30
Maav 132,176,104
Ex 33
Maav 132,180,108
Ra 172,240,51328
Ad 145,31
Maav 136,176,112
Ex 34
Maav 136,180,116
Wl 4
Pk 63,30,31
Ad 146,32
Maav 140,184,80
Ex 35
Maav 140,188,84
Ad 147,33
Pk 70,32,33
Maav 144,184,88
Ex 36
Maav 144,188,92
Ad 146,34
Ad 147,35
Maav 148,184,96
Ex 37
Maav 148,188,100
Pk 71,34,35
Ad 148,36
Maav 152,184,104
Ex 38
Maav 152,188,108
Ad 149,37
Pk 78,36,37
Maav 156,184,112
Ex 39
Maav 156,188,116
Wl 0
Ad 148,38
Ad 149,39
Pk 79,38,39
Cvaa 20,160,192,120
s_barrier
Ex 0
Mvaa 20,164,196
Ta 176,241,33024
Ad 140,0
Mvaa 20,168,200
Ex 1
Mvaa 20,172,204
Ta 178,241,34048
Ad 141,1
Cvaa 24,172,220,124
Ex 2
Mvaa 24,168,216
Ta 180,241,35072
Pk 80,0,1
Mvaa 24,164,212
Ex 3
Mvaa 24,160,208
Ta 182,241,36096
Ad 140,2
Cvaa 28,160,224,128
Ex 4
Mvaa 28,164,228
Ta 184,241,33056
Ad 141,3
Mvaa 28,168,232
Ex 5
Mvaa 28,172,236
Ta 186,241,34080
Pk 81,2,3
Cvaa 32,172,252,132
Ex 6
Mvaa 32,168,248
Ta 188,241,35104
Ad 142,4
Mvaa 32,164,244
Ex 7
Mvaa 32,160,240
Ta 190,241,36128
Ad 143,5
Cvav 36,160,248,136
Ex 8
Mvav 36,164,252
Pk 88,4,5
Ad 142,6
Mvav 36,168,150
Ex 9
Mvav 36,172,154
Wl 4
Ad 143,7
Pk 89,6,7
Ad 144,8
Maav 0,176,40
Ex 10
Maav 0,180,44
Ra 160,240,51584
Ad 145,9
Maav 4,176,48
Ex 11
Maav 4,180,52
Ra 164,240,51840
Pk 96,8,9
Maav 8,176,56
Ex 12
Maav 8,180,60
Ra 168,240,52096
Ad 144,10
Maav 12,176,64
Ex 13
Maav 12,180,68
Ra 172,240,52352
Ad 145,11
Maav 16,176,72
Ex 14
Maav 16,180,76
Wl 4
Pk 97,10,11
Ad 146,12
Maav 20,184,40
Ex 15
Maav 20,188,44
Ad 147,13
Pk 104,12,13
Maav 24,184,48
Ex 16
Maav 24,188,52
Ad 146,14
Ad 147,15
Maav 28,184,56
Ex 17
Maav 28,188,60
Pk 105,14,15
Ad 148,16
Maav 32,184,64
Ex 18
Maav 32,188,68
Ad 149,17
Pk 112,16,17
Maav 36,184,72
Ex 19
Maav 36,188,76
Wl 0
Ad 148,18
Ad 149,19
Pk 113,18,19
Cvaa 0,160,192,120
Ex 20
Mvaa 0,164,196
Ta 176,241,33280
Ad 140,20
Mvaa 0,168,200
Ex 21
Mvaa 0,172,204
Ta 178,241,34304
Ad 141,21
Cvaa 4,172,220,124
Ex 22
Mvaa 4,168,216
Ta 180,241,35328
Pk 82,20,21
Mvaa 4,164,212
Ex 23
Mvaa 4,160,208
Ta 182,241,36352
Ad 140,22
Cvaa 8,160,224,128
Ex 24
Mvaa 8,164,228
Ta 184,241,33312
Ad 141,23
Mvaa 8,168,232
Ex 25
Mvaa 8,172,236
Ta 186,241,34336
Pk 83,22,23
Cvaa 12,172,252,132
Ex 26
Mvaa 12,168,248
Ta 188,241,35360
Ad 142,24
Mvaa 12,164,244
Ex 27
Mvaa 12,160,240
Ta 190,241,36384
Ad 143,25
Cvav 16,160,248,136
Ex 28
Mvav 16,164,252
Pk 90,24,25
Ad 142,26
Mvav 16,168,150
Ex 29
Mvav 16,172,154
Wl 4
Ad 143,27
Pk 91,26,27
Ad 144,28
Maav 40,176,40
Ex 30
Maav 40,180,44
Ra 160,240,52608
Ad 145,29
Maav 44,176,48
Ex 31
Maav 44,180,52
Ra 164,240,52864
Pk 98,28,29
Maav 48,176,56
Ex 32
Maav 48,180,60
Ra 168,240,53120
Ad 144,30
Maav 52,176,64
Ex 33
Maav 52,180,68
Ra 172,240,53376
Ad 145,31
Maav 56,176,72
Ex 34
Maav 56,180,76
Wl 4
Pk 99,30,31
Ad 146,32
Maav 60,184,40
Ex 35
Maav 60,188,44
Ad 147,33
Pk 106,32,33
Maav 64,184,48
Ex 36
Maav 64,188,52
Ad 146,34
Ad 147,35
Maav 68,184,56
Ex 37
Maav 68,188,60
Pk 107,34,35
Ad 148,36
Maav 72,184,64
Ex 38
Maav 72,188,68
Ad 149,37
Pk 114,36,37
Maav 76,184,72
Ex 39
Maav 76,188,76
Wl 0
Ad 148,38
Ad 149,39
Pk 115,38,39
Cvaa 20,160,192,120
Ex 0
Mvaa 20,164,196
Ta 176,241,33536
Ad 140,0
Mvaa 20,168,200
Ex 1
Mvaa 20,172,204
Ta 178,241,34560
Ad 141,1
Cvaa 24,172,220,124
Ex 2
Mvaa 24,168,216
Ta 180,241,35584
Pk 84,0,1
Mvaa 24,164,212
Ex 3
Mvaa 24,160,208
Ta 182,241,36608
Ad 140,2
Cvaa 28,160,224,128
Ex 4
Mvaa 28,164,228
Ta 184,241,33568
Ad 141,3
Mvaa 28,168,232
Ex 5
Mvaa 28,172,236
Ta 186,241,34592
Pk 85,2,3
Cvaa 32,172,252,132
Ex 6
Mvaa 32,168,248
Ta 188,241,35616
Ad 142,4
Mvaa 32,164,244
Ex 7
Mvaa 32,160,240
Ta 190,241,36640
Ad 143,5
Cvav 36,160,248,136
Ex 8
Mvav 36,164,252
Pk 92,4,5
Ad 142,6
Mvav 36,168,150
Ex 9
Mvav 36,172,154
Wl 4
Ad 143,7
Pk 93,6,7
Ad 144,8
Maav 80,176,40
Ex 10
Maav 80,180,44
Ra 160,240,0
Ad 145,9
Maav 84,176,48
Ex 11
Maav 84,180,52
Ra 164,240,256
Pk 100,8,9
Maav 88,176,56
Ex 12
Maav 88,180,60
Ra 168,240,512
Ad 144,10
Maav 92,176,64
Ex 13
Maav 92,180,68
Ra 172,240,768
Ad 145,11
Maav 96,176,72
Ex 14
Maav 96,180,76
Wl 4
Pk 101,10,11
Ad 146,12
Maav 100,184,40
Ex 15
Maav 100,188,44
Ad 147,13
Pk 108,12,13
Maav 104,184,48
Ex 16
Maav 104,188,52
Ad 146,14
Ad 147,15
Maav 108,184,56
Ex 17
Maav 108,188,60
Pk 109,14,15
Ad 148,16
Maav 112,184,64
Ex 18
Maav 112,188,68
Ad 149,17
Pk 116,16,17
Maav 116,184,72
Ex 19
Maav 116,188,76
Wl 0
Ad 148,18
Ad 149,19
Pk 117,18,19
FINA_%=:
Ta 176,241,33792
Ta 178,241,34816
Ta 180,241,35840
Ta 182,241,36864
Ta 184,241,33824
Ta 186,241,34848
Ta 188,241,35872
Ta 190,241,36896
Ex 20
Ex 21
Ad 140,20
Ex 22
Ad 141,21
Pk 86,20,21
Ex 23
Ad 140,22
)A"
R"A(Ex 24
Ad 141,23
Pk 87,22,23
Ex 25
Ad 142,24
Ex 26
Ad 143,25
Wl 4
Maav 120,176,40
Pk 94,24,25
Maav 120,180,44
Ex 27
Ad 142,26
Maav 124,176,48
Ex 28
Ad 143,27
Maav 124,180,52
Pk 95,26,27
Ex 29
Maav 128,176,56
Ad 144,28
Ex 30
Maav 128,180,60
Ad 145,29
Maav 132,176,64
Pk 102,28,29
Ex 31
Maav 132,180,68
Ad 144,30
Ex 32
Maav 136,176,72
Ad 145,31
Pk 103,30,31
Maav 136,180,76
Wl 0
Ex 33
Ad 146,32
Maav 140,184,40
s_barrier
Ta 160,241,49536
Ex 34
Maav 140,188,44
Ta 162,241,50560
Ad 147,33
Pk 110,32,33
Maav 144,184,48
Ta 164,241,51584
Ex 35
Ad 146,34
Maav 144,188,52
Ta 166,241,52608
Ex 36
Ad 147,35
Maav 148,184,56
Ta 168,241,49568
Pk 111,34,35
Ex 37
Maav 148,188,60
Ta 170,241,50592
Ad 148,36
Maav 152,184,64
Ta 172,241,51616
Ex 38
Ad 149,37
Maav 152,188,68
Ta 174,241,52640
Pk 118,36,37
Ex 39
Maav 156,184,72
Ad 148,38
s_nop 0
Maav 156,188,76
Wl 4
Ad 149,39
Pk 119,38,39
Maav 0,160,80
v_lshlrev_b32 v124, 4, v245
Ad 140,141
Maav 0,164,84
Ta 176,241,49792
Ad 142,143
Maav 4,160,88
Ta 178,241,50816
Ad 144,145
Maav 4,164,92
Ta 180,241,51840
Ad 146,147
Maav 8,160,96
Ta 182,241,52864
Ad 148,149
Maav 8,164,100
Ta 184,241,49824
v_mov_b32 v141, v140
Maav 12,160,104
Ta 186,241,50848
v_mov_b32 v143, v142
Maav 12,164,108
Ta 188,241,51872
v_mov_b32 v145, v144
Maav 16,160,112
Ta 190,241,52896
v_mov_b32 v147, v146
Maav 16,164,116
Wl 8
v_mov_b32 v149, v148
s_nop 0
Maav 20,168,80
v_permlane32_swap_b32 v140, v141
v_permlane32_swap_b32 v142, v143
Maav 20,172,84
v_permlane32_swap_b32 v144, v145
v_permlane32_swap_b32 v146, v147
Maav 24,168,88
v_permlane32_swap_b32 v148, v149
Ad 140,141
Maav 24,172,92
Ad 142,143
Ad 144,145
Maav 28,168,96
Ad 146,147
Ad 148,149
Maav 28,172,100
v_mov_b32 v141, v140
v_mov_b32 v143, v142
Maav 32,168,104
v_mov_b32 v145, v144
v_mov_b32 v147, v146
Maav 32,172,108
v_mov_b32 v149, v148
s_nop 0
Maav 36,168,112
v_permlane16_swap_b32 v140, v141
v_permlane16_swap_b32 v142, v143
Maav 36,172,116
v_permlane16_swap_b32 v144, v145
v_permlane16_swap_b32 v146, v147
v_permlane16_swap_b32 v148, v149
Ad 140,141
Ad 142,143
Ad 144,145
Ad 146,147
Ad 148,149
v_add_f32 v141, v140, v142
Ad 141,144
Ad 141,146
Ad 141,148
v_cmp_nge_f32 vcc, 0x5d800000, v141
v_rcp_f32 v158, v140
v_rcp_f32 v159, v142
v_rcp_f32 v242, v144
v_rcp_f32 v243, v146
v_rcp_f32 v244, v148
s_or_b32 s54, vcc_lo, vcc_hi
s_nop 0
.p2align 12
.org PG0_%=+86016
s_cbranch_execz VRET_%=
s_nop 0
s_cmp_lg_u32 s54, 0
s_cbranch_scc1 PREA_%=
Wl 4
Maav 40,176,80
v_bfe_u32 v120, v245, 2, 2
v_and_b32 v121, 3, v245
v_lshrrev_b32 v122, 4, v245
v_mul_u32_u24 v240, 1032, v122
v_lshl_add_u32 v240, v120, 6, v240
Maav 40,180,84
Ta 160,241,50048
v_lshl_add_u32 v240, v121, 4, v240
v_add_u32 v255, 66048, v240
Ar 136,0
Ar 137,1
Ar 138,2
Maav 44,176,88
Ta 162,241,51072
Ar 139,3
Ar 140,20
Ar 141,21
Ar 142,22
Ar 143,23
v_mul_f32 v136, v158, v136
Maav 44,180,92
Ta 164,241,52096
v_mul_f32 v137, v158, v137
v_mul_f32 v138, v158, v138
v_mul_f32 v139, v158, v139
v_mul_f32 v140, v158, v140
v_mul_f32 v141, v158, v141
Maav 48,176,96
Ta 166,241,53120
v_mul_f32 v142, v158, v142
v_mul_f32 v143, v158, v143
Pk 0,136,137
Pk 1,138,139
Pk 2,140,141
Pk 3,142,143
Maav 48,180,100
Ta 168,241,50080
Ar 144,4
Ar 145,5
Ar 146,6
Ar 147,7
Ar 148,24
Maav 52,176,104
Ta 170,241,51104
Ar 149,25
Ar 150,26
Ar 151,27
v_mul_f32 v144, v159, v144
v_mul_f32 v145, v159, v145
v_mul_f32 v146, v159, v146
Maav 52,180,108
Ta 172,241,52128
v_mul_f32 v147, v159, v147
v_mul_f32 v148, v159, v148
v_mul_f32 v149, v159, v149
v_mul_f32 v150, v159, v150
v_mul_f32 v151, v159, v151
Maav 56,176,112
Ta 174,241,53152
Pk 16,144,145
Pk 17,146,147
Pk 18,148,149
Pk 19,150,151
Ar 136,8
Ar 137,9
Maav 56,180,116
Wl 8
Ar 138,10
Ar 139,11
Ar 140,28
Ar 141,29
Ar 142,30
Maav 60,184,80
Ar 143,31
v_mul_f32 v136, v242, v136
v_mul_f32 v137, v242, v137
v_mul_f32 v138, v242, v138
v_mul_f32 v139, v242, v139
Maav 60,188,84
v_mul_f32 v140, v242, v140
v_mul_f32 v141, v242, v141
v_mul_f32 v142, v242, v142
v_mul_f32 v143, v242, v143
Pk 32,136,137
Pk 33,138,139
Maav 64,184,88
Pk 34,140,141
Pk 35,142,143
Ar 144,12
Ar 145,13
Maav 64,188,92
s_mov_b32 s57, 4096
buffer_store_dwordx4 v[0:3], v124, s[96:99], s57 offen offset:0 sc1
Ar 146,14
Ar 147,15
Ar 148,32
Ar 149,33
Ar 150,34
Ar 151,35
Maav 68,184,96
v_mul_f32 v144, v243, v144
v_mul_f32 v145, v243, v145
v_mul_f32 v146, v243, v146
v_mul_f32 v147, v243, v147
v_mul_f32 v148, v243, v148
Maav 68,188,100
v_mul_f32 v149, v243, v149
v_mul_f32 v150, v243, v150
v_mul_f32 v151, v243, v151
Pk 48,144,145
Pk 49,146,147
Pk 50,148,149
Maav 72,184,104
Pk 51,150,151
Ar 136,16
Ar 137,17
Ar 138,18
Ar 139,19
Maav 72,188,108
Ar 140,36
Ar 141,37
Ar 142,38
Ar 143,39
v_mul_f32 v136, v244, v136
v_mul_f32 v137, v244, v137
Maav 76,184,112
v_mul_f32 v138, v244, v138
v_mul_f32 v139, v244, v139
v_mul_f32 v140, v244, v140
v_mul_f32 v141, v244, v141
v_mul_f32 v142, v244, v142
Maav 76,188,116
Wl 4
v_mul_f32 v143, v244, v143
Pk 64,136,137
Pk 65,138,139
Pk 66,140,141
Pk 67,142,143
s_mov_b32 s57, 20480
buffer_store_dwordx4 v[16:19], v124, s[96:99], s57 offen offset:0 sc1
Maav 80,160,80
Maav 80,164,84
Ta 176,241,50304
Ar 136,40
Ar 137,41
Ar 138,42
Ar 139,43
Ar 140,60
Maav 84,160,88
Ta 178,241,51328
Ar 141,61
Ar 142,62
Ar 143,63
v_mul_f32 v136, v158, v136
v_mul_f32 v137, v158, v137
Maav 84,164,92
Ta 180,241,52352
v_mul_f32 v138, v158, v138
v_mul_f32 v139, v158, v139
v_mul_f32 v140, v158, v140
v_mul_f32 v141, v158, v141
v_mul_f32 v142, v158, v142
v_mul_f32 v143, v158, v143
Maav 88,160,96
Ta 182,241,53376
Pk 4,136,137
Pk 5,138,139
Pk 6,140,141
Pk 7,142,143
Ar 144,44
Maav 88,164,100
Ta 184,241,50336
Ar 145,45
Ar 146,46
Ar 147,47
Ar 148,64
Ar 149,65
Maav 92,160,104
Ta 186,241,51360
Ar 150,66
Ar 151,67
v_mul_f32 v144, v159, v144
v_mul_f32 v145, v159, v145
v_mul_f32 v146, v159, v146
v_mul_f32 v147, v159, v147
Maav 92,164,108
Ta 188,241,52384
v_mul_f32 v148, v159, v148
v_mul_f32 v149, v159, v149
v_mul_f32 v150, v159, v150
v_mul_f32 v151, v159, v151
Pk 20,144,145
Maav 96,160,112
Ta 190,241,53408
Pk 21,146,147
Pk 22,148,149
Pk 23,150,151
Ar 136,48
Ar 137,49
Maav 96,164,116
Wl 8
Ar 138,50
Ar 139,51
Ar 140,68
Ar 141,69
Ar 142,70
Ar 143,71
Maav 100,168,80
v_mul_f32 v136, v242, v136
v_mul_f32 v137, v242, v137
v_mul_f32 v138, v242, v138
v_mul_f32 v139, v242, v139
v_mul_f32 v140, v242, v140
Maav 100,172,84
v_mul_f32 v141, v242, v141
v_mul_f32 v142, v242, v142
v_mul_f32 v143, v242, v143
Pk 36,136,137
Pk 37,138,139
Pk 38,140,141
Maav 104,168,88
Pk 39,142,143
Ar 144,52
Ar 145,53
Ar 146,54
Maav 104,172,92
s_mov_b32 s57, 36864
buffer_store_dwordx4 v[32:35], v124, s[96:99], s57 offen offset:0 sc1
Ar 147,55
Ar 148,72
Ar 149,73
Ar 150,74
Ar 151,75
Maav 108,168,96
v_mul_f32 v144, v243, v144
v_mul_f32 v145, v243, v145
v_mul_f32 v146, v243, v146
v_mul_f32 v147, v243, v147
)A"
R"A(v_mul_f32 v148, v243, v148
v_mul_f32 v149, v243, v149
Maav 108,172,100
v_mul_f32 v150, v243, v150
v_mul_f32 v151, v243, v151
Pk 52,144,145
Pk 53,146,147
Pk 54,148,149
Maav 112,168,104
Pk 55,150,151
Ar 136,56
Ar 137,57
Ar 138,58
Ar 139,59
Maav 112,172,108
Ar 140,76
Ar 141,77
Ar 142,78
Ar 143,79
v_mul_f32 v136, v244, v136
v_mul_f32 v137, v244, v137
Maav 116,168,112
v_mul_f32 v138, v244, v138
v_mul_f32 v139, v244, v139
v_mul_f32 v140, v244, v140
v_mul_f32 v141, v244, v141
v_mul_f32 v142, v244, v142
Maav 116,172,116
Wl 4
v_mul_f32 v143, v244, v143
Pk 68,136,137
Pk 69,138,139
Pk 70,140,141
Pk 71,142,143
s_mov_b32 s57, 53248
buffer_store_dwordx4 v[48:51], v124, s[96:99], s57 offen offset:0 sc1
Maav 120,176,80
Ta 192,255,256
Maav 120,180,84
Ta 194,255,4384
Ar 136,80
Ar 137,81
Ar 138,82
Ar 139,83
Ar 140,100
Maav 124,176,88
Ta 196,255,8512
Ar 141,101
Ar 142,102
Ar 143,103
v_mul_f32 v136, v158, v136
v_mul_f32 v137, v158, v137
Maav 124,180,92
Ta 198,255,12640
v_mul_f32 v138, v158, v138
v_mul_f32 v139, v158, v139
v_mul_f32 v140, v158, v140
v_mul_f32 v141, v158, v141
v_mul_f32 v142, v158, v142
v_mul_f32 v143, v158, v143
Maav 128,176,96
Ta 200,240,256
Pk 8,136,137
Pk 9,138,139
Pk 10,140,141
Pk 11,142,143
Ar 144,84
Maav 128,180,100
Ta 202,240,4384
Ar 145,85
Ar 146,86
Ar 147,87
Ar 148,104
Ar 149,105
Ar 150,106
Maav 132,176,104
Ta 204,240,8512
Ar 151,107
v_mul_f32 v144, v159, v144
v_mul_f32 v145, v159, v145
v_mul_f32 v146, v159, v146
v_mul_f32 v147, v159, v147
Maav 132,180,108
Ta 206,240,12640
v_mul_f32 v148, v159, v148
v_mul_f32 v149, v159, v149
v_mul_f32 v150, v159, v150
v_mul_f32 v151, v159, v151
Pk 24,144,145
Maav 136,176,112
Ta 208,255,264
Pk 25,146,147
Pk 26,148,149
Pk 27,150,151
Ar 136,88
Ar 137,89
Maav 136,180,116
Wl 9
s_mov_b32 s57, 69632
Ta 210,255,4392
buffer_store_dwordx4 v[64:67], v124, s[96:99], s57 offen offset:0 sc1
Ar 138,90
Ar 139,91
Ar 140,108
Ar 141,109
Ar 142,110
Maav 140,184,80
Ta 212,255,8520
Ar 143,111
v_mul_f32 v136, v242, v136
v_mul_f32 v137, v242, v137
v_mul_f32 v138, v242, v138
v_mul_f32 v139, v242, v139
v_mul_f32 v140, v242, v140
Maav 140,188,84
Ta 214,255,12648
v_mul_f32 v141, v242, v141
v_mul_f32 v142, v242, v142
v_mul_f32 v143, v242, v143
Pk 40,136,137
Pk 41,138,139
Maav 144,184,88
Ta 216,240,264
Pk 42,140,141
Pk 43,142,143
Ar 144,92
Ar 145,93
Ar 146,94
Ar 147,95
Maav 144,188,92
Ta 218,240,4392
Ar 148,112
Ar 149,113
Ar 150,114
Ar 151,115
v_mul_f32 v144, v243, v144
Maav 148,184,96
Ta 220,240,8520
v_mul_f32 v145, v243, v145
v_mul_f32 v146, v243, v146
v_mul_f32 v147, v243, v147
v_mul_f32 v148, v243, v148
v_mul_f32 v149, v243, v149
Maav 148,188,100
Ta 222,240,12648
v_mul_f32 v150, v243, v150
v_mul_f32 v151, v243, v151
Pk 56,144,145
Pk 57,146,147
Pk 58,148,149
Pk 59,150,151
Maav 152,184,104
Ar 136,96
Ar 137,97
Ar 138,98
Ar 139,99
Maav 152,188,108
s_mov_b32 s57, 4096
buffer_store_dwordx4 v[4:7], v124, s[96:99], s57 offen offset:1024 sc1
Ar 140,116
Ar 141,117
Ar 142,118
Ar 143,119
v_mul_f32 v136, v244, v136
v_mul_f32 v137, v244, v137
Maav 156,184,112
v_mul_f32 v138, v244, v138
v_mul_f32 v139, v244, v139
v_mul_f32 v140, v244, v140
v_mul_f32 v141, v244, v141
v_mul_f32 v142, v244, v142
Maav 156,188,116
v_mul_f32 v143, v244, v143
Pk 72,136,137
Pk 73,138,139
Pk 74,140,141
Pk 75,142,143
s_mov_b32 s57, 20480
buffer_store_dwordx4 v[20:23], v124, s[96:99], s57 offen offset:1024 sc1
v_mov_b32 v241, v124
.p2align 3
Ar 136,120
Ar 137,121
Ar 138,122
Ar 139,123
Ar 140,140
Ar 141,141
Ar 142,142
Ar 143,143
v_pk_mul_f32 v[136:137], v[136:137], v[158:159] op_sel_hi:[1,0]
v_pk_mul_f32 v[138:139], v[138:139], v[158:159] op_sel_hi:[1,0]
v_pk_mul_f32 v[140:141], v[140:141], v[158:159] op_sel_hi:[1,0]
v_pk_mul_f32 v[142:143], v[142:143], v[158:159] op_sel_hi:[1,0]
Pk 12,136,137
Pk 13,138,139
Pk 14,140,141
Pk 15,142,143
Ar 144,124
Ar 145,125
Ar 146,126
Ar 147,127
Ar 148,144
Ar 149,145
Ar 150,146
Ar 151,147
v_pk_mul_f32 v[144:145], v[144:145], v[158:159] op_sel:[0,1] op_sel_hi:[1,1]
v_pk_mul_f32 v[146:147], v[146:147], v[158:159] op_sel:[0,1] op_sel_hi:[1,1]
v_pk_mul_f32 v[148:149], v[148:149], v[158:159] op_sel:[0,1] op_sel_hi:[1,1]
v_pk_mul_f32 v[150:151], v[150:151], v[158:159] op_sel:[0,1] op_sel_hi:[1,1]
Pk 28,144,145
Pk 29,146,147
Pk 30,148,149
Pk 31,150,151
Ar 136,128
Ar 137,129
Ar 138,130
Ar 139,131
Ar 140,148
Ar 141,149
Ar 142,150
Ar 143,151
v_pk_mul_f32 v[136:137], v[136:137], v[242:243] op_sel_hi:[1,0]
v_pk_mul_f32 v[138:139], v[138:139], v[242:243] op_sel_hi:[1,0]
v_pk_mul_f32 v[140:141], v[140:141], v[242:243] op_sel_hi:[1,0]
v_pk_mul_f32 v[142:143], v[142:143], v[242:243] op_sel_hi:[1,0]
Pk 44,136,137
Pk 45,138,139
Pk 46,140,141
Pk 47,142,143
Ar 144,132
Ar 145,133
Ar 146,134
Ar 147,135
Ar 148,152
Ar 149,153
Ar 150,154
Ar 151,155
v_pk_mul_f32 v[144:145], v[144:145], v[242:243] op_sel:[0,1] op_sel_hi:[1,1]
v_pk_mul_f32 v[146:147], v[146:147], v[242:243] op_sel:[0,1] op_sel_hi:[1,1]
v_pk_mul_f32 v[148:149], v[148:149], v[242:243] op_sel:[0,1] op_sel_hi:[1,1]
v_pk_mul_f32 v[150:151], v[150:151], v[242:243] op_sel:[0,1] op_sel_hi:[1,1]
Pk 60,144,145
Pk 61,146,147
Pk 62,148,149
Pk 63,150,151
Ar 136,136
Ar 137,137
Ar 138,138
Ar 139,139
Ar 140,156
Ar 141,157
Ar 142,158
Ar 143,159
s_mov_b32 s57, 36864
buffer_store_dwordx4 v[36:39], v124, s[96:99], s57 offen offset:1024 sc1
v_pk_mul_f32 v[136:137], v[136:137], v[244:245] op_sel_hi:[1,0]
v_pk_mul_f32 v[138:139], v[138:139], v[244:245] op_sel_hi:[1,0]
v_pk_mul_f32 v[140:141], v[140:141], v[244:245] op_sel_hi:[1,0]
v_pk_mul_f32 v[142:143], v[142:143], v[244:245] op_sel_hi:[1,0]
Pk 76,136,137
Pk 77,138,139
Pk 78,140,141
Pk 79,142,143
Wl 0
PPH_%=:
s_bitcmp0_b32 s24, 3
s_cbranch_scc1 PPE_%=
v_and_b32 v243, 3, v245
v_lshl_add_u32 v243, v243, 10, s59
v_writelane_b32 v243, s77, 4
v_writelane_b32 v243, s78, 5
v_writelane_b32 v243, s80, 6
global_load_dword v244, v243, s[92:93] sc1
Zaav 0,192,160
Maav 0,196,164
s_mov_b32 s57, 53248
Ta 224,255,512
Maav 0,200,168
Ta 226,255,4640
buffer_store_dwordx4 v[52:55], v241, s[96:99], s57 offen offset:1024 sc1
Maav 0,204,172
Ta 228,255,8768
Zaav 4,204,188
Ta 230,255,12896
Maav 4,200,184
Ta 232,240,512
Maav 4,196,180
Ta 234,240,4640
Maav 4,192,176
Ta 236,240,8768
Zaav 8,192,192
Ta 238,240,12896
Maav 8,196,196
Ta 240,255,520
Maav 8,200,200
Ta 242,255,4648
Maav 8,204,204
Ta 244,255,8776
Zaav 12,204,220
Ta 246,255,12904
Maav 12,200,216
Ta 248,240,520
Maav 12,196,212
s_mov_b32 s57, 69632
Ta 250,240,4648
Maav 12,192,208
Ta 252,240,8776
buffer_store_dwordx4 v[68:71], v241, s[96:99], s57 offen offset:1024 sc1
Zaav 16,192,224
Ta 254,240,12904
Maav 16,196,228
Maav 16,200,232
Maav 16,204,236
Zaav 20,220,172
Maav 20,216,168
Maav 20,212,164
Maav 20,208,160
Zaav 24,208,176
Maav 24,212,180
Maav 24,216,184
Maav 24,220,188
Zaav 28,220,204
s_add_u32 s53, s17, 0
Maav 28,216,200
buffer_load_dwordx4 a[80:83], v241, s[84:87], s53 offen offset:0 sc1
Maav 28,212,196
Maav 28,208,192
Zaav 32,208,208
Maav 32,212,212
Maav 32,216,216
buffer_load_dwordx4 a[84:87], v241, s[84:87], s53 offen offset:1024 sc1
Maav 32,220,220
Zaav 36,220,236
Maav 36,216,232
Maav 36,212,228
buffer_load_dwordx4 a[88:91], v241, s[84:87], s53 offen offset:2048 sc1
Maav 36,208,224
Wl 0
Zaav 40,224,160
.p2align 12
.org PG0_%=+90112
s_cbranch_execz VRET_%=
s_nop 0
Maav 40,228,164
Ta 192,255,768
Maav 40,232,168
Ta 194,255,4896
Maav 40,236,172
Ta 196,255,9024
buffer_load_dwordx4 a[92:95], v241, s[84:87], s53 offen offset:3072 sc1
Zaav 44,236,188
Ta 198,255,13152
Maav 44,232,184
Ta 200,240,768
Maav 44,228,180
Ta 202,240,4896
Maav 44,224,176
s_mov_b32 s57, 4096
Ta 204,240,9024
Zaav 48,224,192
Ta 206,240,13152
buffer_store_dwordx4 v[8:11], v241, s[96:99], s57 offen offset:2048 sc1
Maav 48,228,196
Ta 208,255,776
Maav 48,232,200
Ta 210,255,4904
Maav 48,236,204
Ta 212,255,9032
Zaav 52,236,220
Ta 214,255,13160
Maav 52,232,216
Ta 216,240,776
Maav 52,228,212
Ta 218,240,4904
Maav 52,224,208
Ta 220,240,9032
Zaav 56,224,224
Ta 222,240,13160
Maav 56,228,228
Maav 56,232,232
Maav 56,236,236
Zaav 60,252,172
s_mov_b32 s57, 20480
Maav 60,248,168
buffer_store_dwordx4 v[24:27], v241, s[96:99], s57 offen offset:2048 sc1
Maav 60,244,164
Maav 60,240,160
Zaav 64,240,176
Maav 64,244,180
Maav 64,248,184
Maav 64,252,188
Zaav 68,252,204
Maav 68,248,200
Maav 68,244,196
Maav 68,240,192
Zaav 72,240,208
Maav 72,244,212
Maav 72,248,216
s_add_u32 s53, s17, 16384
Maav 72,252,220
)A"
R"A(buffer_load_dwordx4 a[96:99], v241, s[84:87], s53 offen offset:0 sc1
Zaav 76,252,236
Maav 76,248,232
Maav 76,244,228
Maav 76,240,224
Wl 0
Zvav 120,192,160
buffer_load_dwordx4 a[100:103], v241, s[84:87], s53 offen offset:1024 sc1
Mvav 120,196,164
Ta 224,255,0
Mvav 120,200,168
Ta 226,255,4128
Mvav 120,204,172
Ta 228,255,8256
Zvav 124,204,188
Ta 230,255,12384
Mvav 124,200,184
Ta 232,240,0
buffer_load_dwordx4 a[104:107], v241, s[84:87], s53 offen offset:2048 sc1
Mvav 124,196,180
Ta 234,240,4128
Mvav 124,192,176
Ta 236,240,8256
Zvav 128,192,192
Ta 238,240,12384
Mvav 128,196,196
Ta 240,255,8
buffer_load_dwordx4 a[108:111], v241, s[84:87], s53 offen offset:3072 sc1
Mvav 128,200,200
Ta 242,255,4136
Mvav 128,204,204
Ta 244,255,8264
Zvav 132,204,220
Ta 246,255,12392
Mvav 132,200,216
s_mov_b32 s57, 36864
Ta 248,240,8
Mvav 132,196,212
Ta 250,240,4136
buffer_store_dwordx4 v[40:43], v241, s[96:99], s57 offen offset:2048 sc1
Mvav 132,192,208
Ta 252,240,8264
Zvav 136,192,224
Ta 254,240,12392
Mvav 136,196,228
Mvav 136,200,232
Mvav 136,204,236
Zvav 140,220,172
Mvav 140,216,168
Mvav 140,212,164
Mvav 140,208,160
Zvav 144,208,176
Mvav 144,212,180
Mvav 144,216,184
Mvav 144,220,188
s_mov_b32 s57, 53248
Zvav 148,220,204
buffer_store_dwordx4 v[56:59], v241, s[96:99], s57 offen offset:2048 sc1
Mvav 148,216,200
Mvav 148,212,196
Mvav 148,208,192
Zvav 152,208,208
Mvav 152,212,212
Mvav 152,216,216
Mvav 152,220,220
Zvav 156,220,236
Mvav 156,216,232
Mvav 156,212,228
Mvav 156,208,224
Wl 0
Zvav 80,224,160
s_barrier
s_mov_b32 s57, 69632
Mvav 80,228,164
Ta 192,240,16768
buffer_store_dwordx4 v[72:75], v241, s[96:99], s57 offen offset:2048 sc1
Mvav 80,232,168
Ta 194,240,20896
Mvav 80,236,172
Ta 196,240,25024
Zvav 84,236,188
Ta 198,240,29152
Mvav 84,232,184
Ta 200,255,16768
Mvav 84,228,180
Ta 202,255,20896
Mvav 84,224,176
Ta 204,255,25024
Zvav 88,224,192
Ta 206,255,29152
Mvav 88,228,196
Ta 208,240,16776
Mvav 88,232,200
Ta 210,240,20904
Mvav 88,236,204
Ta 212,240,25032
Zvav 92,236,220
Ta 214,240,29160
Mvav 92,232,216
Ta 216,255,16776
Mvav 92,228,212
s_add_u32 s53, s17, 32768
Ta 218,255,20904
Mvav 92,224,208
Ta 220,255,25032
buffer_load_dwordx4 a[112:115], v241, s[84:87], s53 offen offset:0 sc1
Zvav 96,224,224
Ta 222,255,29160
Mvav 96,228,228
Mvav 96,232,232
Mvav 96,236,236
Zvav 100,252,172
buffer_load_dwordx4 a[116:119], v241, s[84:87], s53 offen offset:1024 sc1
Mvav 100,248,168
Mvav 100,244,164
Mvav 100,240,160
Zvav 104,240,176
buffer_load_dwordx4 a[120:123], v241, s[84:87], s53 offen offset:2048 sc1
Mvav 104,244,180
Mvav 104,248,184
Mvav 104,252,188
Zvav 108,252,204
Mvav 108,248,200
buffer_load_dwordx4 a[124:127], v241, s[84:87], s53 offen offset:3072 sc1
Mvav 108,244,196
Mvav 108,240,192
Zvav 112,240,208
Mvav 112,244,212
s_mov_b32 s57, 4096
Mvav 112,248,216
buffer_store_dwordx4 v[12:15], v241, s[96:99], s57 offen offset:3072 sc1
Mvav 112,252,220
Zvav 116,252,236
Mvav 116,248,232
Mvav 116,244,228
Mvav 116,240,224
Wl 0
Maav 0,192,0
Maav 0,196,4
Ta 224,240,17024
Maav 0,200,8
Ta 226,240,21152
Maav 0,204,12
Ta 228,240,25280
Maav 4,204,28
Ta 230,240,29408
Maav 4,200,24
Ta 232,255,17024
Maav 4,196,20
Ta 234,255,21152
Maav 4,192,16
s_mov_b32 s57, 20480
Ta 236,255,25280
Maav 8,192,32
Ta 238,255,29408
buffer_store_dwordx4 v[28:31], v241, s[96:99], s57 offen offset:3072 sc1
Maav 8,196,36
Ta 240,240,17032
Maav 8,200,40
Ta 242,240,21160
Maav 8,204,44
Ta 244,240,25288
Maav 12,204,60
Ta 246,240,29416
Maav 12,200,56
Ta 248,255,17032
Maav 12,196,52
Ta 250,255,21160
Maav 12,192,48
Ta 252,255,25288
Maav 16,192,64
Ta 254,255,29416
Maav 16,196,68
Maav 16,200,72
Maav 16,204,76
Maav 20,220,12
s_add_u32 s53, s17, 49152
Maav 20,216,8
buffer_load_dwordx4 a[128:131], v241, s[84:87], s53 offen offset:0 sc1
Maav 20,212,4
Maav 20,208,0
Maav 24,208,16
Maav 24,212,20
Maav 24,216,24
buffer_load_dwordx4 a[132:135], v241, s[84:87], s53 offen offset:1024 sc1
Maav 24,220,28
Maav 28,220,44
Maav 28,216,40
Maav 28,212,36
Maav 28,208,32
buffer_load_dwordx4 a[136:139], v241, s[84:87], s53 offen offset:2048 sc1
Maav 32,208,48
Maav 32,212,52
Maav 32,216,56
Maav 32,220,60
buffer_load_dwordx4 a[140:143], v241, s[84:87], s53 offen offset:3072 sc1
Maav 36,220,76
Maav 36,216,72
Maav 36,212,68
Maav 36,208,64
s_mov_b32 s57, 36864
Wl 0
Maav 40,224,0
buffer_store_dwordx4 v[44:47], v241, s[96:99], s57 offen offset:3072 sc1
Maav 40,228,4
Ta 192,240,17280
Maav 40,232,8
Ta 194,240,21408
Maav 40,236,12
Ta 196,240,25536
Maav 44,236,28
Ta 198,240,29664
Maav 44,232,24
Ta 200,255,17280
Maav 44,228,20
Ta 202,255,21408
Maav 44,224,16
Ta 204,255,25536
Maav 48,224,32
Ta 206,255,29664
Maav 48,228,36
Ta 208,240,17288
Maav 48,232,40
Ta 210,240,21416
Maav 48,236,44
Ta 212,240,25544
Maav 52,236,60
Ta 214,240,29672
Maav 52,232,56
Ta 216,255,17288
Maav 52,228,52
Ta 218,255,21416
Maav 52,224,48
Ta 220,255,25544
Maav 56,224,64
Ta 222,255,29672
Maav 56,228,68
s_mov_b32 s57, 53248
Maav 56,232,72
buffer_store_dwordx4 v[60:63], v241, s[96:99], s57 offen offset:3072 sc1
Maav 56,236,76
Maav 60,252,12
Maav 60,248,8
Maav 60,244,4
Maav 60,240,0
Maav 64,240,16
Maav 64,244,20
Maav 64,248,24
Maav 64,252,28
Maav 68,252,44
Maav 68,248,40
Maav 68,244,36
Maav 68,240,32
Maav 72,240,48
Maav 72,244,52
Maav 72,248,56
s_mov_b32 s57, 69632
Maav 72,252,60
buffer_store_dwordx4 v[76:79], v241, s[96:99], s57 offen offset:3072 sc1
Maav 76,252,76
Maav 76,248,72
Maav 76,244,68
Maav 76,240,64
Wl 0
Mvav 120,192,0
Mvav 120,196,4
Ta 224,240,16512
Mvav 120,200,8
Ta 226,240,20640
Mvav 120,204,12
Ta 228,240,24768
Mvav 124,204,28
Ta 230,240,28896
Mvav 124,200,24
Ta 232,255,16512
Mvav 124,196,20
Ta 234,255,20640
Mvav 124,192,16
Ta 236,255,24768
Mvav 128,192,32
Ta 238,255,28896
Mvav 128,196,36
Ta 240,240,16520
Mvav 128,200,40
Ta 242,240,20648
Mvav 128,204,44
s_add_u32 s53, s17, 65536
Ta 244,240,24776
Mvav 132,204,60
Ta 246,240,28904
buffer_load_dwordx4 a[144:147], v241, s[84:87], s53 offen offset:0 sc1
Mvav 132,200,56
Ta 248,255,16520
Mvav 132,196,52
Ta 250,255,20648
Mvav 132,192,48
Ta 252,255,24776
Mvav 136,192,64
Ta 254,255,28904
Mvav 136,196,68
Mvav 136,200,72
buffer_load_dwordx4 a[148:151], v241, s[84:87], s53 offen offset:1024 sc1
Mvav 136,204,76
Mvav 140,220,12
Mvav 140,216,8
Mvav 140,212,4
Mvav 140,208,0
Mvav 144,208,16
buffer_load_dwordx4 a[152:155], v241, s[84:87], s53 offen offset:2048 sc1
Mvav 144,212,20
Mvav 144,216,24
Mvav 144,220,28
Mvav 148,220,44
Mvav 148,216,40
Mvav 148,212,36
buffer_load_dwordx4 a[156:159], v241, s[84:87], s53 offen offset:3072 sc1
Mvav 148,208,32
Mvav 152,208,48
Mvav 152,212,52
Mvav 152,216,56
Mvav 152,220,60
s_add_u32 s53, s18, 0
Mvav 156,220,76
buffer_load_dwordx4 v[160:163], v241, s[84:87], s53 offen offset:0 sc1
Mvav 156,216,72
Mvav 156,212,68
Mvav 156,208,64
Wl 0
Mvav 80,224,0
s_barrier
Mvav 80,228,4
Ta 192,240,33280
Mvav 80,232,8
Ta 194,240,37408
buffer_load_dwordx4 v[164:167], v241, s[84:87], s53 offen offset:1024 sc1
Mvav 80,236,12
Ta 196,240,41536
Mvav 84,236,28
Ta 198,240,45664
Mvav 84,232,24
Ta 200,255,33280
Mvav 84,228,20
Ta 202,255,37408
Mvav 84,224,16
Ta 204,255,41536
Mvav 88,224,32
Ta 206,255,45664
buffer_load_dwordx4 v[168:171], v241, s[84:87], s53 offen offset:2048 sc1
Mvav 88,228,36
Ta 208,240,33288
Mvav 88,232,40
s_waitcnt vmcnt(7)
Ta 210,240,37416
v_xor_b32 v244, s66, v244
Mvav 88,236,44
Ta 212,240,41544
global_store_dword v243, v244, s[94:95] offset:0 sc1
v_readlane_b32 s56, v244, 5
Mvav 92,236,60
s_bitset0_b32 s56, 31
s_lshl_b32 s58, s22, 18
s_or_b32 s56, s56, s58
Ta 214,240,45672
Mvav 92,232,56
Ta 216,255,33288
v_writelane_b32 v244, s56, 5
Mvav 92,228,52
Ta 218,255,37416
buffer_load_dwordx4 v[172:175], v241, s[84:87], s53 offen offset:3072 sc1
global_store_dword v243, v244, s[94:95] offset:16 sc1
Mvav 92,224,48
Ta 220,255,41544
Mvav 96,224,64
Ta 222,255,45672
Mvav 96,228,68
Mvav 96,232,72
Mvav 96,236,76
s_add_u32 s53, s18, 16384
Mvav 100,252,12
buffer_load_dwordx4 v[176:179], v241, s[84:87], s53 offen offset:0 sc1
Mvav 100,248,8
Mvav 100,244,4
Mvav 100,240,0
Mvav 104,240,16
Mvav 104,244,20
Mvav 104,248,24
buffer_load_dwordx4 v[180:183], v241, s[84:87], s53 offen offset:1024 sc1
Mvav 104,252,28
Mvav 108,252,44
Mvav 108,248,40
Mvav 108,244,36
Mvav 108,240,32
Mvav 112,240,48
buffer_load_dwordx4 v[184:187], v241, s[84:87], s53 offen offset:2048 sc1
Mvav 112,244,52
Mvav 112,248,56
Mvav 112,252,60
Mvav 116,252,76
Mvav 116,248,72
Mvav 116,244,68
)A"
R"A(buffer_load_dwordx4 v[188:191], v241, s[84:87], s53 offen offset:3072 sc1
Mvav 116,240,64
Wl 0
s_waitcnt vmcnt(10)
Maaa 0,192,80
Maaa 0,196,84
Ta 224,240,33536
Maaa 0,200,88
Ta 226,240,37664
Maaa 0,204,92
s_add_u32 s53, s18, 32768
Ta 228,240,41792
Maaa 4,204,108
Ta 230,240,45920
buffer_load_dwordx4 v[192:195], v241, s[84:87], s53 offen offset:0 sc1
Maaa 4,200,104
Ta 232,255,33536
Maaa 4,196,100
Ta 234,255,37664
Maaa 4,192,96
Ta 236,255,41792
Maaa 8,192,112
Ta 238,255,45920
Maaa 8,196,116
Ta 240,240,33544
Maaa 8,200,120
Ta 242,240,37672
buffer_load_dwordx4 v[196:199], v241, s[84:87], s53 offen offset:1024 sc1
Maaa 8,204,124
Ta 244,240,41800
Maaa 12,204,140
Ta 246,240,45928
Maaa 12,200,136
Ta 248,255,33544
Maaa 12,196,132
Ta 250,255,37672
Maaa 12,192,128
Ta 252,255,41800
Maaa 16,192,144
Ta 254,255,45928
buffer_load_dwordx4 v[200:203], v241, s[84:87], s53 offen offset:2048 sc1
Maaa 16,196,148
Maaa 16,200,152
Maaa 16,204,156
Maaa 20,220,92
Maaa 20,216,88
Maaa 20,212,84
buffer_load_dwordx4 v[204:207], v241, s[84:87], s53 offen offset:3072 sc1
Maaa 20,208,80
Maaa 24,208,96
Maaa 24,212,100
Maaa 24,216,104
Maaa 24,220,108
s_add_u32 s53, s18, 49152
Maaa 28,220,124
buffer_load_dwordx4 v[208:211], v241, s[84:87], s53 offen offset:0 sc1
Maaa 28,216,120
Maaa 28,212,116
Maaa 28,208,112
Maaa 32,208,128
Maaa 32,212,132
Maaa 32,216,136
.p2align 12
.org PG0_%=+94208
s_cbranch_execz VRET_%=
s_nop 0
buffer_load_dwordx4 v[212:215], v241, s[84:87], s53 offen offset:1024 sc1
Maaa 32,220,140
Maaa 36,220,156
Maaa 36,216,152
Maaa 36,212,148
Maaa 36,208,144
Wl 0
Maaa 40,224,80
buffer_load_dwordx4 v[216:219], v241, s[84:87], s53 offen offset:2048 sc1
Maaa 40,228,84
Ta 192,240,33792
Maaa 40,232,88
Ta 194,240,37920
Maaa 40,236,92
Ta 196,240,42048
Maaa 44,236,108
Ta 198,240,46176
Maaa 44,232,104
Ta 200,255,33792
Maaa 44,228,100
Ta 202,255,37920
buffer_load_dwordx4 v[220:223], v241, s[84:87], s53 offen offset:3072 sc1
Maaa 44,224,96
Ta 204,255,42048
Maaa 48,224,112
Ta 206,255,46176
Maaa 48,228,116
Ta 208,240,33800
Maaa 48,232,120
Ta 210,240,37928
Maaa 48,236,124
s_add_u32 s53, s18, 65536
Ta 212,240,42056
Maaa 52,236,140
Ta 214,240,46184
buffer_load_dwordx4 v[224:227], v241, s[84:87], s53 offen offset:0 sc1
Maaa 52,232,136
Ta 216,255,33800
Maaa 52,228,132
Ta 218,255,37928
Maaa 52,224,128
Ta 220,255,42056
Maaa 56,224,144
Ta 222,255,46184
Maaa 56,228,148
Maaa 56,232,152
buffer_load_dwordx4 v[228:231], v241, s[84:87], s53 offen offset:1024 sc1
Maaa 56,236,156
Maaa 60,252,92
Maaa 60,248,88
Maaa 60,244,84
Maaa 60,240,80
Maaa 64,240,96
buffer_load_dwordx4 v[232:235], v241, s[84:87], s53 offen offset:2048 sc1
Maaa 64,244,100
Maaa 64,248,104
Maaa 64,252,108
Maaa 68,252,124
Maaa 68,248,120
Maaa 68,244,116
buffer_load_dwordx4 v[236:239], v241, s[84:87], s53 offen offset:3072 sc1
Maaa 68,240,112
Maaa 72,240,128
Maaa 72,244,132
Maaa 72,248,136
Maaa 72,252,140
Maaa 76,252,156
Maaa 76,248,152
Maaa 76,244,148
Maaa 76,240,144
Wl 0
Mvaa 120,192,80
Mvaa 120,196,84
Ta 224,240,33024
Mvaa 120,200,88
Ta 226,240,37152
Mvaa 120,204,92
Ta 228,240,41280
Mvaa 124,204,108
Ta 230,240,45408
Mvaa 124,200,104
Ta 232,255,33024
Mvaa 124,196,100
Ta 234,255,37152
Mvaa 124,192,96
Ta 236,255,41280
Mvaa 128,192,112
Ta 238,255,45408
Mvaa 128,196,116
Ta 240,240,33032
Mvaa 128,200,120
Ta 242,240,37160
Mvaa 128,204,124
Ta 244,240,41288
Mvaa 132,204,140
Ta 246,240,45416
Mvaa 132,200,136
Ta 248,255,33032
Mvaa 132,196,132
Ta 250,255,37160
Mvaa 132,192,128
Ta 252,255,41288
Mvaa 136,192,144
Ta 254,255,45416
Mvaa 136,196,148
Mvaa 136,200,152
Mvaa 136,204,156
Mvaa 140,220,92
Mvaa 140,216,88
Mvaa 140,212,84
Mvaa 140,208,80
Mvaa 144,208,96
Mvaa 144,212,100
Mvaa 144,216,104
Mvaa 144,220,108
Mvaa 148,220,124
Mvaa 148,216,120
Mvaa 148,212,116
Mvaa 148,208,112
Mvaa 152,208,128
Mvaa 152,212,132
Mvaa 152,216,136
Mvaa 152,220,140
Mvaa 156,220,156
Mvaa 156,216,152
v_lshlrev_b32 v242, 2, v245
global_load_dword v242, v242, s[50:51] sc1
Mvaa 156,212,148
Mvaa 156,208,144
Wl 0
Mvaa 80,224,80
s_barrier
Mvaa 80,228,84
Ta 192,240,49792
Mvaa 80,232,88
Ta 194,240,53920
Mvaa 80,236,92
Ta 196,240,58048
Mvaa 84,236,108
Ta 198,240,62176
Mvaa 84,232,104
Ta 200,255,49792
Mvaa 84,228,100
Ta 202,255,53920
Mvaa 84,224,96
Ta 204,255,58048
Mvaa 88,224,112
Ta 206,255,62176
Mvaa 88,228,116
Ta 208,240,49800
Mvaa 88,232,120
Ta 210,240,53928
Mvaa 88,236,124
Ta 212,240,58056
Mvaa 92,236,140
Ta 214,240,62184
Mvaa 92,232,136
Ta 216,255,49800
Mvaa 92,228,132
Ta 218,255,53928
Mvaa 92,224,128
Ta 220,255,58056
Mvaa 96,224,144
Ta 222,255,62184
Mvaa 96,228,148
Mvaa 96,232,152
Mvaa 96,236,156
Mvaa 100,252,92
Mvaa 100,248,88
Mvaa 100,244,84
Mvaa 100,240,80
Mvaa 104,240,96
Mvaa 104,244,100
Mvaa 104,248,104
Mvaa 104,252,108
Mvaa 108,252,124
Mvaa 108,248,120
Mvaa 108,244,116
Mvaa 108,240,112
Mvaa 112,240,128
Mvaa 112,244,132
Mvaa 112,248,136
Mvaa 112,252,140
Mvaa 116,252,156
Mvaa 116,248,152
Mvaa 116,244,148
Mvaa 116,240,144
Wl 0
s_waitcnt vmcnt(1)
Maav 0,192,160
Maav 0,196,164
Ta 224,240,50048
Maav 0,200,168
s_waitcnt vmcnt(0)
s_mov_b32 s58, 0
v_cmp_gt_i32 s[64:65], 0, v242
v_and_b32 v242, 0x7fffffff, v242
v_cmp_ne_u32 vcc, s81, v242
s_nop 0
s_and_b64 vcc, vcc, 0xf00
s_cbranch_scc1 SPINP1o5_%=
OKP1o5_%=:
Ta 226,240,54176
Maav 0,204,172
s_add_u32 s53, s17, 4096
Ta 228,240,58304
Maav 4,204,188
Ta 230,240,62432
buffer_load_dwordx4 a[80:83], v241, s[84:87], s53 offen offset:0 sc1
Maav 4,200,184
Ta 232,255,50048
Maav 4,196,180
Ta 234,255,54176
Maav 4,192,176
s_lshl_b32 s56, s25, 6
Ta 236,255,58304
Maav 8,192,192
s_add_u32 s56, s56, s76
Ta 238,255,62432
buffer_load_dwordx4 a[84:87], v241, s[84:87], s53 offen offset:1024 sc1
Maav 8,196,196
s_lshl_b32 s56, s56, 17
Ta 240,240,50056
Maav 8,200,200
s_add_u32 s40, s20, s56
s_addc_u32 s41, s21, 0
Ta 242,240,54184
Maav 8,204,204
s_mov_b32 s42, 7995392
Ta 244,240,58312
Maav 12,204,220
s_mov_b32 s98, 119808
Ta 246,240,62440
Maav 12,200,216
s_mov_b32 s70, 0
Ta 248,255,50056
buffer_load_dwordx4 a[88:91], v241, s[84:87], s53 offen offset:2048 sc1
Maav 12,196,212
s_and_b32 s71, s35, 0xffff
Ta 250,255,54184
Maav 12,192,208
s_lshl_b32 s71, s71, 4
Ta 252,255,58312
Maav 16,192,224
s_andn2_b32 s34, s34, 0xf0
Ta 254,255,62440
Maav 16,196,228
s_lshl_b64 s[34:35], s[34:35], 8
buffer_load_dwordx4 a[92:95], v241, s[84:87], s53 offen offset:3072 sc1
Maav 16,200,232
s_and_b32 s48, s35, 0xffff0000
Maav 16,204,236
s_and_b32 s35, s35, 0xffff
Maav 20,220,172
Maav 20,216,168
s_add_u32 s53, s18, 4096
Maav 20,212,164
buffer_load_dwordx4 v[0:3], v241, s[84:87], s53 offen offset:0 sc1
Maav 20,208,160
Maav 24,208,176
Maav 24,212,180
Maav 24,216,184
Maav 24,220,188
buffer_load_dwordx4 v[4:7], v241, s[84:87], s53 offen offset:1024 sc1
Maav 28,220,204
Maav 28,216,200
Maav 28,212,196
Maav 28,208,192
buffer_load_dwordx4 v[8:11], v241, s[84:87], s53 offen offset:2048 sc1
Maav 32,208,208
Maav 32,212,212
Maav 32,216,216
Maav 32,220,220
Maav 36,220,236
buffer_load_dwordx4 v[12:15], v241, s[84:87], s53 offen offset:3072 sc1
Maav 36,216,232
Maav 36,212,228
Maav 36,208,224
s_add_u32 s53, s17, 20480
Wl 0
Maav 40,224,160
buffer_load_dwordx4 a[96:99], v241, s[84:87], s53 offen offset:0 sc1
Maav 40,228,164
Ta 192,240,50304
Maav 40,232,168
Ta 194,240,54432
Maav 40,236,172
Ta 196,240,58560
buffer_load_dwordx4 a[100:103], v241, s[84:87], s53 offen offset:1024 sc1
Maav 44,236,188
Ta 198,240,62688
Maav 44,232,184
Ta 200,255,50304
buffer_load_dwordx4 a[104:107], v241, s[84:87], s53 offen offset:2048 sc1
Maav 44,228,180
Ta 202,255,54432
Maav 44,224,176
Ta 204,255,58560
Maav 48,224,192
Ta 206,255,62688
buffer_load_dwordx4 a[108:111], v241, s[84:87], s53 offen offset:3072 sc1
Maav 48,228,196
Ta 208,240,50312
Maav 48,232,200
s_add_u32 s53, s17, 36864
Ta 210,240,54440
Maav 48,236,204
Ta 212,240,58568
buffer_load_dwordx4 a[112:115], v241, s[84:87], s53 offen offset:0 sc1
Maav 52,236,220
Ta 214,240,62696
Maav 52,232,216
Ta 216,255,50312
Maav 52,228,212
Ta 218,255,54440
Maav 52,224,208
Ta 220,255,58568
buffer_load_dwordx4 a[116:119], v241, s[84:87], s53 offen offset:1024 sc1
Maav 56,224,224
Ta 222,255,62696
Maav 56,228,228
Maav 56,232,232
Maav 56,236,236
Maav 60,252,172
buffer_load_dwordx4 a[120:123], v241, s[84:87], s53 offen offset:2048 sc1
Maav 60,248,168
Maav 60,244,164
Maav 60,240,160
Maav 64,240,176
Maav 64,244,180
buffer_load_dwordx4 a[124:127], v241, s[84:87], s53 offen offset:3072 sc1
Maav 64,248,184
Maav 64,252,188
Maav 68,252,204
)A"
R"A(Maav 68,248,200
s_add_u32 s53, s18, 20480
Maav 68,244,196
buffer_load_dwordx4 v[16:19], v241, s[84:87], s53 offen offset:0 sc1
Maav 68,240,192
Maav 72,240,208
Maav 72,244,212
Maav 72,248,216
buffer_load_dwordx4 v[20:23], v241, s[84:87], s53 offen offset:1024 sc1
Maav 72,252,220
Maav 76,252,236
Maav 76,248,232
Maav 76,244,228
Maav 76,240,224
Wl 0
buffer_load_dwordx4 v[24:27], v241, s[84:87], s53 offen offset:2048 sc1
Mvav 120,192,160
Mvav 120,196,164
Ta 224,240,49536
Mvav 120,200,168
Ta 226,240,53664
Mvav 120,204,172
Ta 228,240,57792
Mvav 124,204,188
Ta 230,240,61920
buffer_load_dwordx4 v[28:31], v241, s[84:87], s53 offen offset:3072 sc1
Mvav 124,200,184
Ta 232,255,49536
Mvav 124,196,180
Ta 234,255,53664
Mvav 124,192,176
Ta 236,255,57792
Mvav 128,192,192
s_add_u32 s53, s17, 53248
Ta 238,255,61920
Mvav 128,196,196
Ta 240,240,49544
buffer_load_dwordx4 a[128:131], v241, s[84:87], s53 offen offset:0 sc1
Mvav 128,200,200
Ta 242,240,53672
Mvav 128,204,204
Ta 244,240,57800
buffer_load_dwordx4 a[132:135], v241, s[84:87], s53 offen offset:1024 sc1
Mvav 132,204,220
Ta 246,240,61928
Mvav 132,200,216
Ta 248,255,49544
buffer_load_dwordx4 a[136:139], v241, s[84:87], s53 offen offset:2048 sc1
Mvav 132,196,212
Ta 250,255,53672
Mvav 132,192,208
Ta 252,255,57800
buffer_load_dwordx4 a[140:143], v241, s[84:87], s53 offen offset:3072 sc1
Mvav 136,192,224
s_add_u32 s53, s17, 69632
Ta 254,255,61928
Mvav 136,196,228
buffer_load_dwordx4 a[144:147], v241, s[84:87], s53 offen offset:0 sc1
Mvav 136,200,232
Mvav 136,204,236
Mvav 140,220,172
Mvav 140,216,168
Mvav 140,212,164
buffer_load_dwordx4 a[148:151], v241, s[84:87], s53 offen offset:1024 sc1
Mvav 140,208,160
Mvav 144,208,176
Mvav 144,212,180
Mvav 144,216,184
Mvav 144,220,188
buffer_load_dwordx4 a[152:155], v241, s[84:87], s53 offen offset:2048 sc1
Mvav 148,220,204
Mvav 148,216,200
Mvav 148,212,196
Mvav 148,208,192
Mvav 152,208,208
buffer_load_dwordx4 a[156:159], v241, s[84:87], s53 offen offset:3072 sc1
Mvav 152,212,212
Mvav 152,216,216
Mvav 152,220,220
Mvav 156,220,236
s_add_u32 s53, s18, 36864
Mvav 156,216,232
buffer_load_dwordx4 v[32:35], v241, s[84:87], s53 offen offset:0 sc1
Mvav 156,212,228
Mvav 156,208,224
Wl 0
Mvav 80,224,160
s_barrier
Mvav 80,228,164
Ta 192,255,256
Mvav 80,232,168
Ta 194,255,4384
buffer_load_dwordx4 v[36:39], v241, s[84:87], s53 offen offset:1024 sc1
Mvav 80,236,172
Ta 196,255,8512
Mvav 84,236,188
Ta 198,255,12640
Mvav 84,232,184
Ta 200,240,256
Mvav 84,228,180
Ta 202,240,4384
Mvav 84,224,176
Ta 204,240,8512
buffer_load_dwordx4 v[40:43], v241, s[84:87], s53 offen offset:2048 sc1
Mvav 88,224,192
Ta 206,240,12640
Mvav 88,228,196
Ta 208,255,264
Mvav 88,232,200
Ta 210,255,4392
Mvav 88,236,204
Ta 212,255,8520
Mvav 92,236,220
Ta 214,255,12648
buffer_load_dwordx4 v[44:47], v241, s[84:87], s53 offen offset:3072 sc1
Mvav 92,232,216
Ta 216,240,264
Mvav 92,228,212
Ta 218,240,4392
Mvav 92,224,208
Ta 220,240,8520
Mvav 96,224,224
s_add_u32 s53, s18, 53248
Ta 222,240,12648
Mvav 96,228,228
buffer_load_dwordx4 a[160:163], v241, s[84:87], s53 offen offset:0 sc1
Mvav 96,232,232
Mvav 96,236,236
Mvav 100,252,172
Mvav 100,248,168
Mvav 100,244,164
buffer_load_dwordx4 a[164:167], v241, s[84:87], s53 offen offset:1024 sc1
Mvav 100,240,160
Mvav 104,240,176
Mvav 104,244,180
Mvav 104,248,184
Mvav 104,252,188
buffer_load_dwordx4 a[168:171], v241, s[84:87], s53 offen offset:2048 sc1
Mvav 108,252,204
Mvav 108,248,200
Mvav 108,244,196
Mvav 108,240,192
Mvav 112,240,208
buffer_load_dwordx4 a[172:175], v241, s[84:87], s53 offen offset:3072 sc1
Mvav 112,244,212
Mvav 112,248,216
Mvav 112,252,220
Mvav 116,252,236
Mvav 116,248,232
s_add_u32 s53, s18, 69632
Mvav 116,244,228
buffer_load_dwordx4 a[176:179], v241, s[84:87], s53 offen offset:0 sc1
Mvav 116,240,224
Wl 0
s_waitcnt vmcnt(9)
Maaa 0,192,80
Maaa 0,196,84
Ta 224,255,512
Maaa 0,200,88
Ta 226,255,4640
Maaa 0,204,92
Ta 228,255,8768
buffer_load_dwordx4 a[180:183], v241, s[84:87], s53 offen offset:1024 sc1
Maaa 4,204,108
Ta 230,255,12896
Maaa 4,200,104
Ta 232,240,512
Maaa 4,196,100
Ta 234,240,4640
Maaa 4,192,96
Ta 236,240,8768
Maaa 8,192,112
Ta 238,240,12896
buffer_load_dwordx4 a[184:187], v241, s[84:87], s53 offen offset:2048 sc1
Maaa 8,196,116
Ta 240,255,520
Maaa 8,200,120
Ta 242,255,4648
Maaa 8,204,124
Ta 244,255,8776
Maaa 12,204,140
Ta 246,255,12904
Maaa 12,200,136
Ta 248,240,520
buffer_load_dwordx4 a[188:191], v241, s[84:87], s53 offen offset:3072 sc1
Maaa 12,196,132
Ta 250,240,4648
Maaa 12,192,128
Ta 252,240,8776
Maaa 16,192,144
Ta 254,240,12904
.p2align 12
.org PG0_%=+98304
s_cbranch_execz VRET_%=
s_nop 0
Maaa 16,196,148
s_add_u32 s53, s22, 4096
Maaa 16,200,152
buffer_load_dwordx4 v[160:163], v241, s[84:87], s53 offen offset:0 sc1
Maaa 16,204,156
Maaa 20,220,92
Maaa 20,216,88
Maaa 20,212,84
Maaa 20,208,80
buffer_load_dwordx4 v[164:167], v241, s[84:87], s53 offen offset:1024 sc1
Maaa 24,208,96
Maaa 24,212,100
Maaa 24,216,104
Maaa 24,220,108
Maaa 28,220,124
buffer_load_dwordx4 v[168:171], v241, s[84:87], s53 offen offset:2048 sc1
Maaa 28,216,120
Maaa 28,212,116
Maaa 28,208,112
Maaa 32,208,128
Maaa 32,212,132
buffer_load_dwordx4 v[172:175], v241, s[84:87], s53 offen offset:3072 sc1
Maaa 32,216,136
Maaa 32,220,140
Maaa 36,220,156
Maaa 36,216,152
s_add_u32 s53, s22, 20480
Maaa 36,212,148
buffer_load_dwordx4 v[176:179], v241, s[84:87], s53 offen offset:0 sc1
Maaa 36,208,144
Wl 0
Maaa 40,224,80
s_barrier
Maaa 40,228,84
Ta 192,240,16768
Maaa 40,232,88
Ta 194,240,20896
Maaa 40,236,92
Ta 196,240,25024
buffer_load_dwordx4 v[180:183], v241, s[84:87], s53 offen offset:1024 sc1
Maaa 44,236,108
Ta 198,240,29152
Maaa 44,232,104
Ta 200,255,16768
Maaa 44,228,100
Ta 202,255,20896
Maaa 44,224,96
Ta 204,255,25024
Maaa 48,224,112
Ta 206,255,29152
buffer_load_dwordx4 v[184:187], v241, s[84:87], s53 offen offset:2048 sc1
Maaa 48,228,116
Ta 208,240,16776
Maaa 48,232,120
Ta 210,240,20904
Maaa 48,236,124
Ta 212,240,25032
Maaa 52,236,140
Ta 214,240,29160
Maaa 52,232,136
Ta 216,255,16776
buffer_load_dwordx4 v[188:191], v241, s[84:87], s53 offen offset:3072 sc1
Maaa 52,228,132
Ta 218,255,20904
Maaa 52,224,128
Ta 220,255,25032
Maaa 56,224,144
Ta 222,255,29160
Maaa 56,228,148
Maaa 56,232,152
s_add_u32 s53, s22, 36864
Maaa 56,236,156
buffer_load_dwordx4 v[192:195], v241, s[84:87], s53 offen offset:0 sc1
Maaa 60,252,92
Maaa 60,248,88
Maaa 60,244,84
Maaa 60,240,80
Maaa 64,240,96
buffer_load_dwordx4 v[196:199], v241, s[84:87], s53 offen offset:1024 sc1
Maaa 64,244,100
Maaa 64,248,104
Maaa 64,252,108
Maaa 68,252,124
Maaa 68,248,120
buffer_load_dwordx4 v[200:203], v241, s[84:87], s53 offen offset:2048 sc1
Maaa 68,244,116
Maaa 68,240,112
Maaa 72,240,128
Maaa 72,244,132
Maaa 72,248,136
buffer_load_dwordx4 v[204:207], v241, s[84:87], s53 offen offset:3072 sc1
Maaa 72,252,140
Maaa 76,252,156
Maaa 76,248,152
Maaa 76,244,148
s_add_u32 s53, s22, 53248
Maaa 76,240,144
Wl 0
s_waitcnt vmcnt(12)
buffer_load_dwordx4 v[208:211], v241, s[84:87], s53 offen offset:0 sc1
Maav 0,192,0
Maav 0,196,4
Ta 224,240,17024
Maav 0,200,8
Ta 226,240,21152
Maav 0,204,12
Ta 228,240,25280
Maav 4,204,28
Ta 230,240,29408
buffer_load_dwordx4 v[212:215], v241, s[84:87], s53 offen offset:1024 sc1
Maav 4,200,24
Ta 232,255,17024
Maav 4,196,20
Ta 234,255,21152
Maav 4,192,16
Ta 236,255,25280
Maav 8,192,32
Ta 238,255,29408
Maav 8,196,36
Ta 240,240,17032
buffer_load_dwordx4 v[216:219], v241, s[84:87], s53 offen offset:2048 sc1
Maav 8,200,40
Ta 242,240,21160
Maav 8,204,44
Ta 244,240,25288
Maaa 12,204,172
Ta 246,240,29416
Maaa 12,200,168
Ta 248,255,17032
Maaa 12,196,164
Ta 250,255,21160
buffer_load_dwordx4 v[220:223], v241, s[84:87], s53 offen offset:3072 sc1
Maaa 12,192,160
Ta 252,255,25288
Maaa 16,192,176
Ta 254,255,29416
Maaa 16,196,180
Maaa 16,200,184
s_add_u32 s53, s22, 69632
Maaa 16,204,188
buffer_load_dwordx4 v[224:227], v241, s[84:87], s53 offen offset:0 sc1
Maav 20,220,12
Maav 20,216,8
Maav 20,212,4
Maav 20,208,0
Maav 24,208,16
buffer_load_dwordx4 v[228:231], v241, s[84:87], s53 offen offset:1024 sc1
Maav 24,212,20
Maav 24,216,24
Maav 24,220,28
Maav 28,220,44
Maav 28,216,40
buffer_load_dwordx4 v[232:235], v241, s[84:87], s53 offen offset:2048 sc1
Maav 28,212,36
Maav 28,208,32
Maaa 32,208,160
Maaa 32,212,164
Maaa 32,216,168
buffer_load_dwordx4 v[236:239], v241, s[84:87], s53 offen offset:3072 sc1
Maaa 32,220,172
Maaa 36,220,188
Maaa 36,216,184
Maaa 36,212,180
Maaa 36,208,176
Wl 0
Maav 40,224,0
s_barrier
Maav 40,228,4
Ta 192,240,33280
Maav 40,232,8
Ta 194,240,37408
Maav 40,236,12
Ta 196,240,41536
Maav 44,236,28
Ta 198,240,45664
Maav 44,232,24
Ta 200,255,33280
Maav 44,228,20
Ta 202,255,37408
Maav 44,224,16
Ta 204,255,41536
Maav 48,224,32
Ta 206,255,45664
Maav 48,228,36
Ta 208,240,33288
Maav 48,232,40
Ta 210,240,37416
Maav 48,236,44
Ta 212,240,41544
Maaa 52,236,172
Ta 214,240,45672
Maaa 52,232,168
Ta 216,255,33288
Maaa 52,228,164
Ta 218,255,37416
Maaa 52,224,160
Ta 220,255,41544
Maaa 56,224,176
Ta 222,255,45672
Maaa 56,228,180
Maaa 56,232,184
Maaa 56,236,188
Maav 60,252,12
Maav 60,248,8
Maav 60,244,4
)A"
R"A(Maav 60,240,0
Maav 64,240,16
Maav 64,244,20
Maav 64,248,24
Maav 64,252,28
Maav 68,252,44
Maav 68,248,40
Maav 68,244,36
Maav 68,240,32
Maaa 72,240,160
Maaa 72,244,164
Maaa 72,248,168
Maaa 72,252,172
Maaa 76,252,188
Maaa 76,248,184
Maaa 76,244,180
Maaa 76,240,176
Wl 0
s_waitcnt vmcnt(0)
Maav 0,192,160
Maav 0,196,164
Ta 224,240,33536
Maav 0,200,168
Ta 226,240,37664
Maav 0,204,172
Ta 228,240,41792
Maav 4,204,188
Ta 230,240,45920
Maav 4,200,184
Ta 232,255,33536
Maav 4,196,180
Ta 234,255,37664
Maav 4,192,176
Ta 236,255,41792
Maav 8,192,192
Ta 238,255,45920
Maav 8,196,196
Ta 240,240,33544
Maav 8,200,200
Ta 242,240,37672
Maav 8,204,204
Ta 244,240,41800
Maav 12,204,220
Ta 246,240,45928
Maav 12,200,216
Ta 248,255,33544
Maav 12,196,212
Ta 250,255,37672
Maav 12,192,208
Ta 252,255,41800
Maav 16,192,224
Ta 254,255,45928
Maav 16,196,228
Maav 16,200,232
Maav 16,204,236
Maav 20,220,172
Maav 20,216,168
Maav 20,212,164
Maav 20,208,160
Maav 24,208,176
Maav 24,212,180
Maav 24,216,184
Maav 24,220,188
Maav 28,220,204
Maav 28,216,200
Maav 28,212,196
Maav 28,208,192
Maav 32,208,208
Ar 48,0
Ar 49,1
Maav 32,212,212
Ar 50,2
Ar 51,3
Maav 32,216,216
Pk 56,48,49
Pk 57,50,51
Maav 32,220,220
Ar 52,20
Ar 53,21
Maav 36,220,236
Ar 54,22
Ar 55,23
Maav 36,216,232
Pk 58,52,53
Pk 59,54,55
Maav 36,212,228
s_mov_b32 s57, 81920
buffer_store_dwordx4 v[56:59], v241, s[96:99], s57 offen offset:0 sc1
Maav 36,208,224
Wl 0
Ar 48,4
Ar 49,5
Maav 40,224,160
Ar 50,6
Ar 51,7
Maav 40,228,164
Pk 60,48,49
Pk 61,50,51
Maav 40,232,168
Ar 52,24
Ar 53,25
Maav 40,236,172
Ar 54,26
Ar 55,27
Maav 44,236,188
Pk 62,52,53
Pk 63,54,55
Maav 44,232,184
buffer_store_dwordx4 v[60:63], v241, s[96:99], s57 offen offset:1024 sc1
Maav 44,228,180
Ar 48,8
Ar 49,9
Maav 44,224,176
Ar 50,10
Ar 51,11
Maav 48,224,192
Pk 64,48,49
Pk 65,50,51
Maav 48,228,196
Ar 52,28
Ar 53,29
Maav 48,232,200
Ar 54,30
Ar 55,31
Maav 48,236,204
Pk 66,52,53
Pk 67,54,55
Maav 52,236,220
buffer_store_dwordx4 v[64:67], v241, s[96:99], s57 offen offset:2048 sc1
Maav 52,232,216
Ar 48,12
Ar 49,13
Maav 52,228,212
Ar 50,14
Ar 51,15
Maav 52,224,208
Pk 68,48,49
Pk 69,50,51
Maav 56,224,224
Ar 52,32
Ar 53,33
Maav 56,228,228
Ar 54,34
Ar 55,35
Maav 56,232,232
Pk 70,52,53
Pk 71,54,55
Maav 56,236,236
buffer_store_dwordx4 v[68:71], v241, s[96:99], s57 offen offset:3072 sc1
Maav 60,252,172
Ta 192,255,768
Ar 48,16
Maav 60,248,168
Ta 194,255,4896
Ar 49,17
Maav 60,244,164
Ta 196,255,9024
Ar 50,18
Maav 60,240,160
Ta 198,255,13152
Ar 51,19
Maav 64,240,176
Ta 200,240,768
Pk 72,48,49
Maav 64,244,180
Ta 202,240,4896
Pk 73,50,51
Maav 64,248,184
Ta 204,240,9024
Ar 52,36
Maav 64,252,188
Ta 206,240,13152
Ar 53,37
Maav 68,252,204
Ta 208,255,776
Ar 54,38
Maav 68,248,200
Ta 210,255,4904
Ar 55,39
Maav 68,244,196
Ta 212,255,9032
Pk 74,52,53
Maav 68,240,192
Ta 214,255,13160
Pk 75,54,55
Maav 72,240,208
s_mov_b32 s57, 86016
Ta 216,240,776
buffer_store_dwordx4 v[72:75], v241, s[96:99], s57 offen offset:0 sc1
Maav 72,244,212
Ta 218,240,4904
Ar 48,40
Maav 72,248,216
Ta 220,240,9032
Ar 49,41
Maav 72,252,220
Ta 222,240,13160
Ar 50,42
Maav 76,252,236
Ar 51,43
Pk 56,48,49
Maav 76,248,232
Pk 57,50,51
Ar 52,60
Maav 76,244,228
Ar 53,61
Ar 54,62
Maav 76,240,224
Wl 0
Ar 55,63
Pk 58,52,53
Mvaa 120,192,80
Pk 59,54,55
Mvaa 120,196,84
s_mov_b32 s57, 98304
buffer_store_dwordx4 v[56:59], v241, s[96:99], s57 offen offset:0 sc1
Mvaa 120,200,88
Ar 48,44
Ar 49,45
Mvaa 120,204,92
Ar 50,46
Ar 51,47
Mvaa 124,204,108
Pk 60,48,49
Pk 61,50,51
Mvaa 124,200,104
Ar 52,64
Ar 53,65
Mvaa 124,196,100
Ar 54,66
Ar 55,67
Mvaa 124,192,96
Pk 62,52,53
Pk 63,54,55
Mvaa 128,192,112
buffer_store_dwordx4 v[60:63], v241, s[96:99], s57 offen offset:1024 sc1
Mvaa 128,196,116
Ar 48,48
Ar 49,49
Mvaa 128,200,120
Ar 50,50
Ar 51,51
Mvaa 128,204,124
Pk 64,48,49
Pk 65,50,51
Mvaa 132,204,140
Ar 52,68
Ar 53,69
Mvaa 132,200,136
Ar 54,70
Ar 55,71
Mvaa 132,196,132
Pk 66,52,53
Pk 67,54,55
Mvaa 132,192,128
buffer_store_dwordx4 v[64:67], v241, s[96:99], s57 offen offset:2048 sc1
Mvaa 136,192,144
Ar 48,52
Ar 49,53
Mvaa 136,196,148
Ar 50,54
Ar 51,55
Mvaa 136,200,152
Pk 68,48,49
Pk 69,50,51
Mvaa 136,204,156
Ar 52,72
Ar 53,73
Mvaa 140,220,92
Ta 224,240,17280
Ar 54,74
Mvaa 140,216,88
Ta 226,240,21408
Ar 55,75
Mvaa 140,212,84
Ta 228,240,25536
Pk 70,52,53
Mvaa 140,208,80
Ta 230,240,29664
Pk 71,54,55
Mvaa 144,208,96
Ta 232,255,17280
buffer_store_dwordx4 v[68:71], v241, s[96:99], s57 offen offset:3072 sc1
Mvaa 144,212,100
Ta 234,255,21408
Ar 48,56
Mvaa 144,216,104
Ta 236,255,25536
Ar 49,57
Mvaa 144,220,108
Ta 238,255,29664
Ar 50,58
Mvaa 148,220,124
Ta 240,240,17288
Ar 51,59
Mvaa 148,216,120
Ta 242,240,21416
Pk 72,48,49
Mvaa 148,212,116
Ta 244,240,25544
Pk 73,50,51
Mvaa 148,208,112
Ta 246,240,29672
Ar 52,76
Mvaa 152,208,128
Ta 248,255,17288
Ar 53,77
Mvaa 152,212,132
Ta 250,255,21416
Ar 54,78
Mvaa 152,216,136
.p2align 12
.org PG0_%=+102400
s_cbranch_execz VRET_%=
s_nop 0
Ta 252,255,25544
Ar 55,79
Mvaa 152,220,140
Ta 254,255,29672
Pk 74,52,53
Mvaa 156,220,156
Pk 75,54,55
Mvaa 156,216,152
s_mov_b32 s57, 102400
buffer_store_dwordx4 v[72:75], v241, s[96:99], s57 offen offset:0 sc1
Mvaa 156,212,148
Mvaa 156,208,144
Wl 0
v_bfe_u32 v76, v245, 2, 2
v_lshrrev_b32 v77, 4, v245
Mvav 120,224,0
v_cmp_eq_u32 vcc, v76, v77
v_and_b32 v76, 1, v245
Mvav 120,228,4
Ta 192,240,33792
v_lshlrev_b32 v76, 4, v76
Mvav 120,232,8
Ta 194,240,37920
v_mov_b32 v77, 0x3f80
Mvav 120,236,12
Ta 196,240,42048
v_lshlrev_b32 v77, v76, v77
Mvav 124,236,28
Ta 198,240,46176
v_cndmask_b32 v77, 0, v77, vcc
Mvav 124,232,24
Ta 200,255,33792
v_and_b32 v76, 2, v245
Mvav 124,228,20
Ta 202,255,37920
v_cmp_eq_u32 vcc, 0, v76
Mvav 124,224,16
Ta 204,255,42048
v_mov_b32 v50, 0
Mvav 128,224,32
Ta 206,255,46176
v_mov_b32 v51, 0
Mvav 128,228,36
Ta 208,240,33800
v_cndmask_b32 v48, 0, v77, vcc
Mvav 128,232,40
Ta 210,240,37928
v_sub_u32 v49, v77, v48
Mvav 128,236,44
Ta 212,240,42056
v_mov_b32 v52, v48
Mvaa 132,236,172
Ta 214,240,46184
v_mov_b32 v53, v49
Mvaa 132,232,168
Ta 216,255,33800
v_and_b32 v250, 15, v245
Mvaa 132,228,164
Ta 218,255,37928
v_lshlrev_b32 v250, 11, v250
Mvaa 132,224,160
Ta 220,255,42056
v_lshrrev_b32 v251, 4, v245
Mvaa 136,224,176
Ta 222,255,46184
v_lshl_add_u32 v250, v251, 4, v250
Mvaa 136,228,180
v_lshlrev_b32 v251, 2, v245
Mvaa 136,232,184
Mvaa 136,236,188
Mvav 140,252,12
Mvav 140,248,8
Mvav 140,244,4
Mvav 140,240,0
Mvav 144,240,16
Mvav 144,244,20
Mvav 144,248,24
Mvav 144,252,28
Mvav 148,252,44
Mvav 148,248,40
Mvav 148,244,36
Mvav 148,240,32
Mvaa 152,240,160
Mvaa 152,244,164
Mvaa 152,248,168
)A"
R"A(Mvaa 152,252,172
Mvaa 156,252,188
Mvaa 156,248,184
Mvaa 156,244,180
Mvaa 156,240,176
Wl 0
Mvav 120,192,160
Mvav 120,196,164
Ta 224,255,0
Mvav 120,200,168
Ta 226,255,4128
Mvav 120,204,172
Ta 228,255,8256
Mvav 124,204,188
Ta 230,255,12384
Mvav 124,200,184
Ta 232,240,0
Mvav 124,196,180
Ta 234,240,4128
Mvav 124,192,176
s_waitcnt vmcnt(0)
s_mov_b64 exec, 0x30
global_store_dword v243, v244, s[94:95] offset:32 sc1
s_mov_b64 exec, -1
Ta 236,240,8256
Mvav 128,192,192
Ta 238,240,12384
Mvav 128,196,196
Ta 240,255,8
Mvav 128,200,200
Ta 242,255,4136
Mvav 128,204,204
Ta 244,255,8264
Mvav 132,204,220
Ta 246,255,12392
Mvav 132,200,216
Ta 248,240,8
Mvav 132,196,212
Ta 250,240,4136
Mvav 132,192,208
Ta 252,240,8264
Mvav 136,192,224
Ta 254,240,12392
Mvav 136,196,228
Mvav 136,200,232
Mvav 136,204,236
Mvav 140,220,172
Mvav 140,216,168
Mvav 140,212,164
Mvav 140,208,160
Mvav 144,208,176
Mvav 144,212,180
Mvav 144,216,184
Mvav 144,220,188
Mvav 148,220,204
Mvav 148,216,200
Mvav 148,212,196
Mvav 148,208,192
Mvav 152,208,208
Pk 56,120,121
Pk 57,122,123
Mvav 152,212,212
Pk 58,140,141
Pk 59,142,143
Mvav 152,216,216
s_mov_b32 s57, 114688
buffer_store_dwordx4 v[56:59], v241, s[96:99], s57 offen offset:0 sc1
Mvav 152,220,220
Mvav 156,220,236
Pk 60,124,125
Pk 61,126,127
Mvav 156,216,232
Pk 62,144,145
Pk 63,146,147
Mvav 156,212,228
buffer_store_dwordx4 v[60:63], v241, s[96:99], s57 offen offset:1024 sc1
Mvav 156,208,224
Wl 0
Mvaa 80,224,80
Pk 64,128,129
Pk 65,130,131
Mvaa 80,228,84
Pk 66,148,149
Pk 67,150,151
Mvaa 80,232,88
buffer_store_dwordx4 v[64:67], v241, s[96:99], s57 offen offset:2048 sc1
Mvaa 80,236,92
Mvaa 84,236,108
Pk 68,132,133
Pk 69,134,135
Mvaa 84,232,104
Pk 70,152,153
Pk 71,154,155
Mvaa 84,228,100
buffer_store_dwordx4 v[68:71], v241, s[96:99], s57 offen offset:3072 sc1
Mvaa 84,224,96
Mvaa 88,224,112
Pk 72,136,137
Pk 73,138,139
Mvaa 88,228,116
Pk 74,156,157
Pk 75,158,159
Mvaa 88,232,120
s_mov_b32 s57, 118784
buffer_store_dwordx4 v[72:75], v241, s[96:99], s57 offen offset:0 sc1
Mvaa 88,236,124
Mvaa 92,236,140
Mvaa 92,232,136
Mvaa 92,228,132
Mvaa 92,224,128
Mvaa 96,224,144
Mvaa 96,228,148
Mvaa 96,232,152
Mvaa 96,236,156
Mvaa 100,252,92
Ta 192,240,16512
Mvaa 100,248,88
Ta 194,240,20640
Mvaa 100,244,84
Ta 196,240,24768
v_lshlrev_b32 v242, 2, v245
global_load_dword v242, v242, s[50:51] sc1
Mvaa 100,240,80
Ta 198,240,28896
Mvaa 104,240,96
Ta 200,255,16512
Mvaa 104,244,100
Ta 202,255,20640
Mvaa 104,248,104
Ta 204,255,24768
Mvaa 104,252,108
Ta 206,255,28896
Mvaa 108,252,124
Ta 208,240,16520
Mvaa 108,248,120
Ta 210,240,20648
Mvaa 108,244,116
Ta 212,240,24776
Mvaa 108,240,112
Ta 214,240,28904
Mvaa 112,240,128
Ta 216,255,16520
Mvaa 112,244,132
Ta 218,255,20648
Mvaa 112,248,136
Ta 220,255,24776
Mvaa 112,252,140
Ta 222,255,28904
Mvaa 116,252,156
Mvaa 116,248,152
Mvaa 116,244,148
Mvaa 116,240,144
Wl 0
Mvav 80,192,0
Mvav 80,196,4
Ta 224,240,33024
Mvav 80,200,8
Ta 226,240,37152
Mvav 80,204,12
Ta 228,240,41280
Mvav 84,204,28
Ta 230,240,45408
Mvav 84,200,24
Ta 232,255,33024
Mvav 84,196,20
Ta 234,255,37152
Mvav 84,192,16
Ta 236,255,41280
Mvav 88,192,32
Ta 238,255,45408
Mvav 88,196,36
Ta 240,240,33032
Mvav 88,200,40
s_waitcnt vmcnt(1)
s_mov_b64 exec, 0x40
global_store_dword v243, v244, s[94:95] offset:32 sc1
s_mov_b64 exec, -1
Ta 242,240,37160
Mvav 88,204,44
Ta 244,240,41288
Mvaa 92,204,172
Ta 246,240,45416
Mvaa 92,200,168
Ta 248,255,33032
Mvaa 92,196,164
Ta 250,255,37160
Mvaa 92,192,160
Ta 252,255,41288
Mvaa 96,192,176
Ta 254,255,45416
Mvaa 96,196,180
Mvaa 96,200,184
Mvaa 96,204,188
Mvav 100,220,12
Mvav 100,216,8
Mvav 100,212,4
Mvav 100,208,0
Mvav 104,208,16
Mvav 104,212,20
Mvav 104,216,24
s_waitcnt vmcnt(1)
s_mov_b32 s58, 0
v_cmp_gt_i32 s[64:65], 0, v242
v_and_b32 v242, 0x7fffffff, v242
v_cmp_ne_u32 vcc, s81, v242
s_nop 0
s_and_b64 vcc, vcc, s[34:35]
s_cbranch_scc1 SPINP2o5_%=
OKP2o5_%=:
Mvav 104,220,28
s_add_u32 s53, s27, 81920
buffer_load_dwordx4 a[0:3], v241, s[40:43], s53 offen offset:0 sc1
Mvav 108,220,44
Mvav 108,216,40
Mvav 108,212,36
buffer_load_dwordx4 a[4:7], v241, s[40:43], s53 offen offset:1024 sc1
Mvav 108,208,32
Mvaa 112,208,160
Mvaa 112,212,164
buffer_load_dwordx4 a[8:11], v241, s[40:43], s53 offen offset:2048 sc1
Mvaa 112,216,168
Mvaa 112,220,172
Mvaa 116,220,188
buffer_load_dwordx4 a[12:15], v241, s[40:43], s53 offen offset:3072 sc1
Mvaa 116,216,184
Mvaa 116,212,180
Mvaa 116,208,176
Wl 0
s_add_u32 s53, s27, 86016
buffer_load_dwordx4 a[16:19], v241, s[40:43], s53 offen offset:0 sc1
Mvav 80,224,160
Mvav 80,228,164
Mvav 80,232,168
s_add_u32 s53, s52, 98304
buffer_load_dwordx4 a[20:23], v241, s[40:43], s53 offen offset:0 sc1
Mvav 80,236,172
Mvav 84,236,188
Mvav 84,232,184
buffer_load_dwordx4 a[24:27], v241, s[40:43], s53 offen offset:1024 sc1
Mvav 84,228,180
Mvav 84,224,176
Mvav 88,224,192
buffer_load_dwordx4 a[28:31], v241, s[40:43], s53 offen offset:2048 sc1
Mvav 88,228,196
Mvav 88,232,200
Mvav 88,236,204
buffer_load_dwordx4 a[32:35], v241, s[40:43], s53 offen offset:3072 sc1
Mvav 92,236,220
Mvav 92,232,216
Mvav 92,228,212
s_add_u32 s53, s52, 102400
buffer_load_dwordx4 a[36:39], v241, s[40:43], s53 offen offset:0 sc1
Mvav 92,224,208
Mvav 96,224,224
Mvav 96,228,228
Mvav 96,232,232
Mvav 96,236,236
Mvav 100,252,172
Mvav 100,248,168
Mvav 100,244,164
Mvav 100,240,160
Mvav 104,240,176
Mvav 104,244,180
Mvav 104,248,184
Mvav 104,252,188
Mvav 108,252,204
global_load_dword v242, v251, s[50:51] sc1
Mvav 108,248,200
Mvav 108,244,196
Mvav 108,240,192
Mvav 112,240,208
Mvav 112,244,212
Mvav 112,248,216
Mvav 112,252,220
Mvav 116,252,236
Mvav 116,248,232
Mvav 116,244,228
Mvav 116,240,224
s_waitcnt vmcnt(6)
Mvva 80,48,0
Mvva 100,50,0
Mvva 84,48,4
Mvva 104,50,4
Mvva 88,48,8
Mvva 108,50,8
Mvva 92,48,12
Mvva 112,50,12
Mvva 96,48,16
Mvva 116,50,16
s_mov_b32 s58, 0
POLLo5_%=:
s_waitcnt vmcnt(0)
v_and_b32 v242, 0x7fffffff, v242
v_cmp_ne_u32 vcc, s81, v242
s_add_u32 s58, s58, 1
s_and_b32 s57, vcc_hi, s48
s_cbranch_scc0 GOTo5_%=
global_load_dword v242, v251, s[50:51] sc1
s_cmp_lt_u32 s58, 0x100000
s_cbranch_scc1 POLLo5_%=
GOTo5_%=:
s_add_u32 s53, s55, 114688
buffer_load_dwordx4 a[40:43], v241, s[40:43], s53 offen offset:0 sc1
buffer_load_dwordx4 a[44:47], v241, s[40:43], s53 offen offset:1024 sc1
buffer_load_dwordx4 a[48:51], v241, s[40:43], s53 offen offset:2048 sc1
buffer_load_dwordx4 a[52:55], v241, s[40:43], s53 offen offset:3072 sc1
s_add_u32 s53, s55, 118784
buffer_load_dwordx4 a[56:59], v241, s[40:43], s53 offen offset:0 sc1
Mvva 80,48,20
Mvva 100,50,20
Mvva 84,48,24
Mvva 104,50,24
Mvva 88,48,28
Mvva 108,50,28
Mvva 92,48,32
Mvva 112,50,32
Mvva 96,48,36
Mvva 116,50,36
s_waitcnt vmcnt(0)
Mvva 80,48,40
Mvva 100,50,40
Mvva 84,48,44
Mvva 104,50,44
Mvva 88,48,48
Mvva 108,50,48
Mvva 92,48,52
Mvva 112,50,52
Mvva 96,48,56
Mvva 116,50,56
Pk 160,80,81
Pk 161,82,83
Pk 162,100,101
Pk 163,102,103
buffer_store_dwordx4 v[160:163], v250, s[88:91], 0 offen sc1
v_add_u32 v250, 32768, v250
Pk 164,84,85
Pk 165,86,87
Pk 166,104,105
Pk 167,106,107
buffer_store_dwordx4 v[164:167], v250, s[88:91], 0 offen sc1
v_add_u32 v250, 32768, v250
Pk 168,88,89
Pk 169,90,91
Pk 170,108,109
Pk 171,110,111
buffer_store_dwordx4 v[168:171], v250, s[88:91], 0 offen sc1
v_add_u32 v250, 32768, v250
Pk 172,92,93
Pk 173,94,95
Pk 174,112,113
Pk 175,114,115
buffer_store_dwordx4 v[172:175], v250, s[88:91], 0 offen sc1
v_add_u32 v250, 32768, v250
Pk 176,96,97
Pk 177,98,99
Pk 178,116,117
Pk 179,118,119
buffer_store_dwordx4 v[176:179], v250, s[88:91], 0 offen sc1
v_add_u32 v250, 32768, v250
s_endpgm
VRET_%=:
s_mov_b64 exec, -1
s_setpc_b64 s[12:13]
SPINP1o5_%=:
s_sleep 4
v_lshlrev_b32 v242, 2, v245
global_load_dword v242, v242, s[50:51] sc1
s_waitcnt vmcnt(0)
v_cmp_gt_i32 s[64:65], 0, v242
v_and_b32 v242, 0x7fffffff, v242
v_cmp_ne_u32 vcc, s81, v242
s_nop 0
s_and_b64 vcc, vcc, 0xf00
s_cbranch_scc0 OKP1o5_%=
s_add_u32 s58, s58, 1
s_cmp_lt_u32 s58, 0x80000
s_cbranch_scc1 SPINP1o5_%=
s_branch OKP1o5_%=
SPINP2o5_%=:
s_sleep 1
v_lshlrev_b32 v242, 2, v245
global_load_dword v242, v242, s[50:51] sc1
s_waitcnt vmcnt(0)
v_cmp_gt_i32 s[64:65], 0, v242
v_and_b32 v242, 0x7fffffff, v242
v_cmp_ne_u32 vcc, s81, v242
s_nop 0
s_and_b64 vcc, vcc, s[34:35]
s_cbranch_scc0 OKP2o5_%=
s_add_u32 s58, s58, 1
s_cmp_lt_u32 s58, 0x80000
s_cbranch_scc1 SPINP2o5_%=
s_branch OKP2o5_%=
.p2align 12
.org PG0_%=+106496
s_cbranch_execz VRET_%=
s_nop 0
PPE_%=:
v_and_b32 v243, 3, v245
v_lshl_add_u32 v243, v243, 10, s59
)A"
R"A(v_writelane_b32 v243, s77, 4
v_writelane_b32 v243, s78, 5
v_writelane_b32 v243, s80, 6
global_load_dword v244, v243, s[92:93] sc1
Zaav 0,192,160
Maav 0,196,164
s_mov_b32 s57, 53248
Ta 224,255,512
Maav 0,200,168
Ta 226,255,4640
buffer_store_dwordx4 v[52:55], v241, s[96:99], s57 offen offset:1024 sc1
Maav 0,204,172
Ta 228,255,8768
Zaav 4,204,188
Ta 230,255,12896
Maav 4,200,184
Ta 232,240,512
Maav 4,196,180
Ta 234,240,4640
Maav 4,192,176
Ta 236,240,8768
Zaav 8,192,192
Ta 238,240,12896
Maav 8,196,196
Ta 240,255,520
Maav 8,200,200
Ta 242,255,4648
Maav 8,204,204
Ta 244,255,8776
Zaav 12,204,220
Ta 246,255,12904
Maav 12,200,216
Ta 248,240,520
Maav 12,196,212
s_mov_b32 s57, 69632
Ta 250,240,4648
Maav 12,192,208
Ta 252,240,8776
buffer_store_dwordx4 v[68:71], v241, s[96:99], s57 offen offset:1024 sc1
Zaav 16,192,224
Ta 254,240,12904
Maav 16,196,228
Maav 16,200,232
Maav 16,204,236
Zaav 20,220,172
Maav 20,216,168
Maav 20,212,164
Maav 20,208,160
Zaav 24,208,176
Maav 24,212,180
Maav 24,216,184
Maav 24,220,188
Zaav 28,220,204
s_add_u32 s53, s17, 0
Maav 28,216,200
buffer_load_dwordx4 a[80:83], v241, s[84:87], s53 offen offset:0 sc1
Maav 28,212,196
Maav 28,208,192
Zaav 32,208,208
Maav 32,212,212
Maav 32,216,216
buffer_load_dwordx4 a[84:87], v241, s[84:87], s53 offen offset:1024 sc1
Maav 32,220,220
Zaav 36,220,236
Maav 36,216,232
Maav 36,212,228
buffer_load_dwordx4 a[88:91], v241, s[84:87], s53 offen offset:2048 sc1
Maav 36,208,224
Wl 0
Zaav 40,224,160
Maav 40,228,164
Ta 192,255,768
Maav 40,232,168
Ta 194,255,4896
Maav 40,236,172
Ta 196,255,9024
buffer_load_dwordx4 a[92:95], v241, s[84:87], s53 offen offset:3072 sc1
Zaav 44,236,188
Ta 198,255,13152
Maav 44,232,184
Ta 200,240,768
Maav 44,228,180
Ta 202,240,4896
Maav 44,224,176
s_mov_b32 s57, 4096
Ta 204,240,9024
Zaav 48,224,192
Ta 206,240,13152
buffer_store_dwordx4 v[8:11], v241, s[96:99], s57 offen offset:2048 sc1
Maav 48,228,196
Ta 208,255,776
Maav 48,232,200
Ta 210,255,4904
Maav 48,236,204
Ta 212,255,9032
Zaav 52,236,220
Ta 214,255,13160
Maav 52,232,216
Ta 216,240,776
Maav 52,228,212
Ta 218,240,4904
Maav 52,224,208
Ta 220,240,9032
Zaav 56,224,224
Ta 222,240,13160
Maav 56,228,228
Maav 56,232,232
Maav 56,236,236
Zaav 60,252,172
s_mov_b32 s57, 20480
Maav 60,248,168
buffer_store_dwordx4 v[24:27], v241, s[96:99], s57 offen offset:2048 sc1
Maav 60,244,164
Maav 60,240,160
Zaav 64,240,176
Maav 64,244,180
Maav 64,248,184
Maav 64,252,188
Zaav 68,252,204
Maav 68,248,200
Maav 68,244,196
Maav 68,240,192
Zaav 72,240,208
Maav 72,244,212
Maav 72,248,216
s_add_u32 s53, s17, 16384
Maav 72,252,220
buffer_load_dwordx4 a[96:99], v241, s[84:87], s53 offen offset:0 sc1
Zaav 76,252,236
Maav 76,248,232
Maav 76,244,228
Maav 76,240,224
Wl 0
Zvav 120,192,160
buffer_load_dwordx4 a[100:103], v241, s[84:87], s53 offen offset:1024 sc1
Mvav 120,196,164
Ta 224,255,0
Mvav 120,200,168
Ta 226,255,4128
Mvav 120,204,172
Ta 228,255,8256
Zvav 124,204,188
Ta 230,255,12384
Mvav 124,200,184
Ta 232,240,0
buffer_load_dwordx4 a[104:107], v241, s[84:87], s53 offen offset:2048 sc1
Mvav 124,196,180
Ta 234,240,4128
Mvav 124,192,176
Ta 236,240,8256
Zvav 128,192,192
Ta 238,240,12384
Mvav 128,196,196
Ta 240,255,8
buffer_load_dwordx4 a[108:111], v241, s[84:87], s53 offen offset:3072 sc1
Mvav 128,200,200
Ta 242,255,4136
Mvav 128,204,204
Ta 244,255,8264
Zvav 132,204,220
Ta 246,255,12392
Mvav 132,200,216
s_mov_b32 s57, 36864
Ta 248,240,8
Mvav 132,196,212
Ta 250,240,4136
buffer_store_dwordx4 v[40:43], v241, s[96:99], s57 offen offset:2048 sc1
Mvav 132,192,208
Ta 252,240,8264
Zvav 136,192,224
Ta 254,240,12392
Mvav 136,196,228
Mvav 136,200,232
Mvav 136,204,236
Zvav 140,220,172
Mvav 140,216,168
Mvav 140,212,164
Mvav 140,208,160
Zvav 144,208,176
Mvav 144,212,180
Mvav 144,216,184
Mvav 144,220,188
s_mov_b32 s57, 53248
Zvav 148,220,204
buffer_store_dwordx4 v[56:59], v241, s[96:99], s57 offen offset:2048 sc1
Mvav 148,216,200
Mvav 148,212,196
Mvav 148,208,192
Zvav 152,208,208
Mvav 152,212,212
Mvav 152,216,216
Mvav 152,220,220
Zvav 156,220,236
Mvav 156,216,232
Mvav 156,212,228
Mvav 156,208,224
Wl 0
Zvav 80,224,160
s_barrier
s_mov_b32 s57, 69632
Mvav 80,228,164
Ta 192,240,16768
buffer_store_dwordx4 v[72:75], v241, s[96:99], s57 offen offset:2048 sc1
Mvav 80,232,168
Ta 194,240,20896
Mvav 80,236,172
Ta 196,240,25024
Zvav 84,236,188
Ta 198,240,29152
Mvav 84,232,184
Ta 200,255,16768
Mvav 84,228,180
Ta 202,255,20896
Mvav 84,224,176
Ta 204,255,25024
Zvav 88,224,192
Ta 206,255,29152
Mvav 88,228,196
Ta 208,240,16776
Mvav 88,232,200
Ta 210,240,20904
Mvav 88,236,204
Ta 212,240,25032
Zvav 92,236,220
Ta 214,240,29160
Mvav 92,232,216
Ta 216,255,16776
Mvav 92,228,212
s_add_u32 s53, s17, 32768
Ta 218,255,20904
Mvav 92,224,208
Ta 220,255,25032
buffer_load_dwordx4 a[112:115], v241, s[84:87], s53 offen offset:0 sc1
Zvav 96,224,224
Ta 222,255,29160
Mvav 96,228,228
Mvav 96,232,232
Mvav 96,236,236
Zvav 100,252,172
buffer_load_dwordx4 a[116:119], v241, s[84:87], s53 offen offset:1024 sc1
Mvav 100,248,168
Mvav 100,244,164
Mvav 100,240,160
Zvav 104,240,176
buffer_load_dwordx4 a[120:123], v241, s[84:87], s53 offen offset:2048 sc1
Mvav 104,244,180
Mvav 104,248,184
Mvav 104,252,188
Zvav 108,252,204
Mvav 108,248,200
buffer_load_dwordx4 a[124:127], v241, s[84:87], s53 offen offset:3072 sc1
Mvav 108,244,196
Mvav 108,240,192
Zvav 112,240,208
Mvav 112,244,212
s_mov_b32 s57, 4096
Mvav 112,248,216
buffer_store_dwordx4 v[12:15], v241, s[96:99], s57 offen offset:3072 sc1
Mvav 112,252,220
Zvav 116,252,236
Mvav 116,248,232
Mvav 116,244,228
Mvav 116,240,224
Wl 0
Maav 0,192,0
Maav 0,196,4
Ta 224,240,17024
Maav 0,200,8
Ta 226,240,21152
Maav 0,204,12
Ta 228,240,25280
Maav 4,204,28
Ta 230,240,29408
Maav 4,200,24
Ta 232,255,17024
Maav 4,196,20
Ta 234,255,21152
Maav 4,192,16
s_mov_b32 s57, 20480
Ta 236,255,25280
Maav 8,192,32
Ta 238,255,29408
buffer_store_dwordx4 v[28:31], v241, s[96:99], s57 offen offset:3072 sc1
Maav 8,196,36
Ta 240,240,17032
Maav 8,200,40
Ta 242,240,21160
Maav 8,204,44
Ta 244,240,25288
Maav 12,204,60
Ta 246,240,29416
Maav 12,200,56
Ta 248,255,17032
Maav 12,196,52
Ta 250,255,21160
Maav 12,192,48
Ta 252,255,25288
Maav 16,192,64
Ta 254,255,29416
Maav 16,196,68
Maav 16,200,72
Maav 16,204,76
Maav 20,220,12
s_add_u32 s53, s17, 49152
Maav 20,216,8
buffer_load_dwordx4 a[128:131], v241, s[84:87], s53 offen offset:0 sc1
Maav 20,212,4
Maav 20,208,0
Maav 24,208,16
Maav 24,212,20
Maav 24,216,24
buffer_load_dwordx4 a[132:135], v241, s[84:87], s53 offen offset:1024 sc1
Maav 24,220,28
Maav 28,220,44
Maav 28,216,40
Maav 28,212,36
Maav 28,208,32
buffer_load_dwordx4 a[136:139], v241, s[84:87], s53 offen offset:2048 sc1
Maav 32,208,48
Maav 32,212,52
Maav 32,216,56
Maav 32,220,60
buffer_load_dwordx4 a[140:143], v241, s[84:87], s53 offen offset:3072 sc1
Maav 36,220,76
Maav 36,216,72
Maav 36,212,68
Maav 36,208,64
s_mov_b32 s57, 36864
Wl 0
Maav 40,224,0
buffer_store_dwordx4 v[44:47], v241, s[96:99], s57 offen offset:3072 sc1
Maav 40,228,4
Ta 192,240,17280
Maav 40,232,8
Ta 194,240,21408
Maav 40,236,12
Ta 196,240,25536
Maav 44,236,28
Ta 198,240,29664
Maav 44,232,24
Ta 200,255,17280
Maav 44,228,20
Ta 202,255,21408
Maav 44,224,16
Ta 204,255,25536
Maav 48,224,32
Ta 206,255,29664
Maav 48,228,36
Ta 208,240,17288
Maav 48,232,40
Ta 210,240,21416
Maav 48,236,44
Ta 212,240,25544
Maav 52,236,60
Ta 214,240,29672
Maav 52,232,56
Ta 216,255,17288
Maav 52,228,52
Ta 218,255,21416
Maav 52,224,48
Ta 220,255,25544
Maav 56,224,64
Ta 222,255,29672
Maav 56,228,68
s_mov_b32 s57, 53248
Maav 56,232,72
buffer_store_dwordx4 v[60:63], v241, s[96:99], s57 offen offset:3072 sc1
Maav 56,236,76
Maav 60,252,12
Maav 60,248,8
Maav 60,244,4
Maav 60,240,0
Maav 64,240,16
Maav 64,244,20
Maav 64,248,24
Maav 64,252,28
Maav 68,252,44
Maav 68,248,40
Maav 68,244,36
Maav 68,240,32
Maav 72,240,48
Maav 72,244,52
Maav 72,248,56
s_mov_b32 s57, 69632
Maav 72,252,60
buffer_store_dwordx4 v[76:79], v241, s[96:99], s57 offen offset:3072 sc1
Maav 76,252,76
Maav 76,248,72
Maav 76,244,68
Maav 76,240,64
Wl 0
Mvav 120,192,0
Mvav 120,196,4
Ta 224,240,16512
Mvav 120,200,8
Ta 226,240,20640
Mvav 120,204,12
Ta 228,240,24768
Mvav 124,204,28
Ta 230,240,28896
)A"
R"A(Mvav 124,200,24
Ta 232,255,16512
Mvav 124,196,20
Ta 234,255,20640
Mvav 124,192,16
Ta 236,255,24768
Mvav 128,192,32
Ta 238,255,28896
Mvav 128,196,36
Ta 240,240,16520
Mvav 128,200,40
Ta 242,240,20648
Mvav 128,204,44
s_add_u32 s53, s17, 65536
Ta 244,240,24776
Mvav 132,204,60
Ta 246,240,28904
buffer_load_dwordx4 a[144:147], v241, s[84:87], s53 offen offset:0 sc1
Mvav 132,200,56
Ta 248,255,16520
Mvav 132,196,52
Ta 250,255,20648
Mvav 132,192,48
Ta 252,255,24776
Mvav 136,192,64
Ta 254,255,28904
Mvav 136,196,68
Mvav 136,200,72
buffer_load_dwordx4 a[148:151], v241, s[84:87], s53 offen offset:1024 sc1
Mvav 136,204,76
Mvav 140,220,12
Mvav 140,216,8
Mvav 140,212,4
Mvav 140,208,0
Mvav 144,208,16
buffer_load_dwordx4 a[152:155], v241, s[84:87], s53 offen offset:2048 sc1
Mvav 144,212,20
Mvav 144,216,24
Mvav 144,220,28
Mvav 148,220,44
Mvav 148,216,40
Mvav 148,212,36
buffer_load_dwordx4 a[156:159], v241, s[84:87], s53 offen offset:3072 sc1
Mvav 148,208,32
Mvav 152,208,48
Mvav 152,212,52
Mvav 152,216,56
Mvav 152,220,60
s_add_u32 s53, s18, 0
Mvav 156,220,76
buffer_load_dwordx4 v[160:163], v241, s[84:87], s53 offen offset:0 sc1
Mvav 156,216,72
Mvav 156,212,68
Mvav 156,208,64
Wl 0
Mvav 80,224,0
s_barrier
Mvav 80,228,4
Ta 192,240,33280
Mvav 80,232,8
Ta 194,240,37408
buffer_load_dwordx4 v[164:167], v241, s[84:87], s53 offen offset:1024 sc1
Mvav 80,236,12
Ta 196,240,41536
Mvav 84,236,28
Ta 198,240,45664
Mvav 84,232,24
Ta 200,255,33280
Mvav 84,228,20
Ta 202,255,37408
Mvav 84,224,16
Ta 204,255,41536
Mvav 88,224,32
Ta 206,255,45664
buffer_load_dwordx4 v[168:171], v241, s[84:87], s53 offen offset:2048 sc1
Mvav 88,228,36
Ta 208,240,33288
Mvav 88,232,40
s_waitcnt vmcnt(7)
Ta 210,240,37416
v_xor_b32 v244, s66, v244
Mvav 88,236,44
Ta 212,240,41544
global_store_dword v243, v244, s[94:95] offset:0 sc1
Mvav 92,236,60
Ta 214,240,45672
Mvav 92,232,56
Ta 216,255,33288
Mvav 92,228,52
Ta 218,255,37416
buffer_load_dwordx4 v[172:175], v241, s[84:87], s53 offen offset:3072 sc1
global_store_dword v243, v244, s[94:95] offset:16 sc1
Mvav 92,224,48
Ta 220,255,41544
Mvav 96,224,64
Ta 222,255,45672
Mvav 96,228,68
Mvav 96,232,72
Mvav 96,236,76
s_add_u32 s53, s18, 16384
Mvav 100,252,12
buffer_load_dwordx4 v[176:179], v241, s[84:87], s53 offen offset:0 sc1
Mvav 100,248,8
Mvav 100,244,4
Mvav 100,240,0
Mvav 104,240,16
Mvav 104,244,20
Mvav 104,248,24
buffer_load_dwordx4 v[180:183], v241, s[84:87], s53 offen offset:1024 sc1
Mvav 104,252,28
Mvav 108,252,44
Mvav 108,248,40
Mvav 108,244,36
Mvav 108,240,32
Mvav 112,240,48
buffer_load_dwordx4 v[184:187], v241, s[84:87], s53 offen offset:2048 sc1
.p2align 12
.org PG0_%=+110592
s_cbranch_execz VRET_%=
s_nop 0
Mvav 112,244,52
Mvav 112,248,56
Mvav 112,252,60
Mvav 116,252,76
Mvav 116,248,72
Mvav 116,244,68
buffer_load_dwordx4 v[188:191], v241, s[84:87], s53 offen offset:3072 sc1
Mvav 116,240,64
Wl 0
s_waitcnt vmcnt(10)
Maaa 0,192,80
Maaa 0,196,84
Ta 224,240,33536
Maaa 0,200,88
Ta 226,240,37664
Maaa 0,204,92
s_add_u32 s53, s18, 32768
Ta 228,240,41792
Maaa 4,204,108
Ta 230,240,45920
buffer_load_dwordx4 v[192:195], v241, s[84:87], s53 offen offset:0 sc1
Maaa 4,200,104
Ta 232,255,33536
Maaa 4,196,100
Ta 234,255,37664
Maaa 4,192,96
Ta 236,255,41792
Maaa 8,192,112
Ta 238,255,45920
Maaa 8,196,116
Ta 240,240,33544
Maaa 8,200,120
Ta 242,240,37672
buffer_load_dwordx4 v[196:199], v241, s[84:87], s53 offen offset:1024 sc1
Maaa 8,204,124
Ta 244,240,41800
Maaa 12,204,140
Ta 246,240,45928
Maaa 12,200,136
Ta 248,255,33544
Maaa 12,196,132
Ta 250,255,37672
Maaa 12,192,128
Ta 252,255,41800
Maaa 16,192,144
Ta 254,255,45928
buffer_load_dwordx4 v[200:203], v241, s[84:87], s53 offen offset:2048 sc1
Maaa 16,196,148
Maaa 16,200,152
Maaa 16,204,156
Maaa 20,220,92
Maaa 20,216,88
Maaa 20,212,84
buffer_load_dwordx4 v[204:207], v241, s[84:87], s53 offen offset:3072 sc1
Maaa 20,208,80
Maaa 24,208,96
Maaa 24,212,100
Maaa 24,216,104
Maaa 24,220,108
s_add_u32 s53, s18, 49152
Maaa 28,220,124
buffer_load_dwordx4 v[208:211], v241, s[84:87], s53 offen offset:0 sc1
Maaa 28,216,120
Maaa 28,212,116
Maaa 28,208,112
Maaa 32,208,128
Maaa 32,212,132
Maaa 32,216,136
buffer_load_dwordx4 v[212:215], v241, s[84:87], s53 offen offset:1024 sc1
Maaa 32,220,140
Maaa 36,220,156
Maaa 36,216,152
Maaa 36,212,148
Maaa 36,208,144
Wl 0
Maaa 40,224,80
buffer_load_dwordx4 v[216:219], v241, s[84:87], s53 offen offset:2048 sc1
Maaa 40,228,84
Ta 192,240,33792
Maaa 40,232,88
Ta 194,240,37920
Maaa 40,236,92
Ta 196,240,42048
Maaa 44,236,108
Ta 198,240,46176
Maaa 44,232,104
Ta 200,255,33792
Maaa 44,228,100
Ta 202,255,37920
buffer_load_dwordx4 v[220:223], v241, s[84:87], s53 offen offset:3072 sc1
Maaa 44,224,96
Ta 204,255,42048
Maaa 48,224,112
Ta 206,255,46176
Maaa 48,228,116
Ta 208,240,33800
Maaa 48,232,120
Ta 210,240,37928
Maaa 48,236,124
s_add_u32 s53, s18, 65536
Ta 212,240,42056
Maaa 52,236,140
Ta 214,240,46184
buffer_load_dwordx4 v[224:227], v241, s[84:87], s53 offen offset:0 sc1
Maaa 52,232,136
Ta 216,255,33800
Maaa 52,228,132
Ta 218,255,37928
Maaa 52,224,128
Ta 220,255,42056
Maaa 56,224,144
Ta 222,255,46184
Maaa 56,228,148
Maaa 56,232,152
buffer_load_dwordx4 v[228:231], v241, s[84:87], s53 offen offset:1024 sc1
Maaa 56,236,156
Maaa 60,252,92
Maaa 60,248,88
Maaa 60,244,84
Maaa 60,240,80
Maaa 64,240,96
buffer_load_dwordx4 v[232:235], v241, s[84:87], s53 offen offset:2048 sc1
Maaa 64,244,100
Maaa 64,248,104
Maaa 64,252,108
Maaa 68,252,124
Maaa 68,248,120
Maaa 68,244,116
buffer_load_dwordx4 v[236:239], v241, s[84:87], s53 offen offset:3072 sc1
Maaa 68,240,112
Maaa 72,240,128
Maaa 72,244,132
Maaa 72,248,136
Maaa 72,252,140
Maaa 76,252,156
Maaa 76,248,152
Maaa 76,244,148
Maaa 76,240,144
Wl 0
Mvaa 120,192,80
Mvaa 120,196,84
Ta 224,240,33024
Mvaa 120,200,88
Ta 226,240,37152
Mvaa 120,204,92
Ta 228,240,41280
Mvaa 124,204,108
Ta 230,240,45408
Mvaa 124,200,104
Ta 232,255,33024
Mvaa 124,196,100
Ta 234,255,37152
Mvaa 124,192,96
Ta 236,255,41280
Mvaa 128,192,112
Ta 238,255,45408
Mvaa 128,196,116
Ta 240,240,33032
Mvaa 128,200,120
Ta 242,240,37160
Mvaa 128,204,124
Ta 244,240,41288
Mvaa 132,204,140
Ta 246,240,45416
Mvaa 132,200,136
Ta 248,255,33032
Mvaa 132,196,132
Ta 250,255,37160
Mvaa 132,192,128
Ta 252,255,41288
Mvaa 136,192,144
Ta 254,255,45416
Mvaa 136,196,148
Mvaa 136,200,152
Mvaa 136,204,156
Mvaa 140,220,92
Mvaa 140,216,88
Mvaa 140,212,84
Mvaa 140,208,80
Mvaa 144,208,96
Mvaa 144,212,100
Mvaa 144,216,104
Mvaa 144,220,108
Mvaa 148,220,124
Mvaa 148,216,120
Mvaa 148,212,116
Mvaa 148,208,112
Mvaa 152,208,128
Mvaa 152,212,132
Mvaa 152,216,136
Mvaa 152,220,140
Mvaa 156,220,156
Mvaa 156,216,152
Mvaa 156,212,148
Mvaa 156,208,144
Wl 0
Mvaa 80,224,80
s_barrier
Mvaa 80,228,84
Ta 192,240,49792
Mvaa 80,232,88
Ta 194,240,53920
Mvaa 80,236,92
Ta 196,240,58048
Mvaa 84,236,108
Ta 198,240,62176
Mvaa 84,232,104
Ta 200,255,49792
Mvaa 84,228,100
Ta 202,255,53920
Mvaa 84,224,96
Ta 204,255,58048
Mvaa 88,224,112
Ta 206,255,62176
Mvaa 88,228,116
Ta 208,240,49800
Mvaa 88,232,120
Ta 210,240,53928
Mvaa 88,236,124
Ta 212,240,58056
Mvaa 92,236,140
Ta 214,240,62184
Mvaa 92,232,136
Ta 216,255,49800
Mvaa 92,228,132
Ta 218,255,53928
Mvaa 92,224,128
Ta 220,255,58056
Mvaa 96,224,144
Ta 222,255,62184
Mvaa 96,228,148
Mvaa 96,232,152
Mvaa 96,236,156
Mvaa 100,252,92
Mvaa 100,248,88
Mvaa 100,244,84
Mvaa 100,240,80
Mvaa 104,240,96
Mvaa 104,244,100
Mvaa 104,248,104
Mvaa 104,252,108
Mvaa 108,252,124
Mvaa 108,248,120
Mvaa 108,244,116
Mvaa 108,240,112
Mvaa 112,240,128
Mvaa 112,244,132
Mvaa 112,248,136
Mvaa 112,252,140
Mvaa 116,252,156
Mvaa 116,248,152
Mvaa 116,244,148
Mvaa 116,240,144
Wl 0
s_waitcnt vmcnt(0)
Maav 0,192,160
s_add_u32 s53, s22, 0
Maav 0,196,164
Ta 224,240,50048
buffer_load_dwordx4 a[80:83], v241, s[84:87], s53 offen offset:0 sc1
Maav 0,200,168
Ta 226,240,54176
Maav 0,204,172
Ta 228,240,58304
Maav 4,204,188
Ta 230,240,62432
Maav 4,200,184
Ta 232,255,50048
Maav 4,196,180
Ta 234,255,54176
buffer_load_dwordx4 a[84:87], v241, s[84:87], s53 offen offset:1024 sc1
Maav 4,192,176
Ta 236,255,58304
Maav 8,192,192
Ta 238,255,62432
Maav 8,196,196
Ta 240,240,50056
Maav 8,200,200
Ta 242,240,54184
Maav 8,204,204
)A"
R"A(Ta 244,240,58312
Maav 12,204,220
Ta 246,240,62440
buffer_load_dwordx4 a[88:91], v241, s[84:87], s53 offen offset:2048 sc1
Maav 12,200,216
Ta 248,255,50056
Maav 12,196,212
Ta 250,255,54184
Maav 12,192,208
Ta 252,255,58312
Maav 16,192,224
Ta 254,255,62440
Maav 16,196,228
Maav 16,200,232
buffer_load_dwordx4 a[92:95], v241, s[84:87], s53 offen offset:3072 sc1
Maav 16,204,236
Maav 20,220,172
Maav 20,216,168
Maav 20,212,164
Maav 20,208,160
s_add_u32 s53, s22, 16384
Maav 24,208,176
buffer_load_dwordx4 a[96:99], v241, s[84:87], s53 offen offset:0 sc1
Maav 24,212,180
Maav 24,216,184
Maav 24,220,188
Maav 28,220,204
Maav 28,216,200
Maav 28,212,196
buffer_load_dwordx4 a[100:103], v241, s[84:87], s53 offen offset:1024 sc1
Maav 28,208,192
Maav 32,208,208
Maav 32,212,212
Maav 32,216,216
Maav 32,220,220
Maav 36,220,236
buffer_load_dwordx4 a[104:107], v241, s[84:87], s53 offen offset:2048 sc1
Maav 36,216,232
Maav 36,212,228
Maav 36,208,224
Wl 0
Maav 40,224,160
Maav 40,228,164
Ta 192,240,50304
Maav 40,232,168
Ta 194,240,54432
buffer_load_dwordx4 a[108:111], v241, s[84:87], s53 offen offset:3072 sc1
Maav 40,236,172
Ta 196,240,58560
Maav 44,236,188
Ta 198,240,62688
Maav 44,232,184
Ta 200,255,50304
Maav 44,228,180
Ta 202,255,54432
Maav 44,224,176
s_add_u32 s53, s22, 32768
Ta 204,255,58560
Maav 48,224,192
Ta 206,255,62688
buffer_load_dwordx4 a[112:115], v241, s[84:87], s53 offen offset:0 sc1
Maav 48,228,196
Ta 208,240,50312
Maav 48,232,200
Ta 210,240,54440
Maav 48,236,204
Ta 212,240,58568
Maav 52,236,220
Ta 214,240,62696
Maav 52,232,216
Ta 216,255,50312
Maav 52,228,212
Ta 218,255,54440
buffer_load_dwordx4 a[116:119], v241, s[84:87], s53 offen offset:1024 sc1
Maav 52,224,208
Ta 220,255,58568
Maav 56,224,224
Ta 222,255,62696
Maav 56,228,228
Maav 56,232,232
Maav 56,236,236
Maav 60,252,172
buffer_load_dwordx4 a[120:123], v241, s[84:87], s53 offen offset:2048 sc1
Maav 60,248,168
Maav 60,244,164
Maav 60,240,160
Maav 64,240,176
Maav 64,244,180
Maav 64,248,184
buffer_load_dwordx4 a[124:127], v241, s[84:87], s53 offen offset:3072 sc1
Maav 64,252,188
Maav 68,252,204
Maav 68,248,200
Maav 68,244,196
Maav 68,240,192
s_add_u32 s53, s22, 49152
Maav 72,240,208
buffer_load_dwordx4 a[128:131], v241, s[84:87], s53 offen offset:0 sc1
Maav 72,244,212
Maav 72,248,216
Maav 72,252,220
Maav 76,252,236
Maav 76,248,232
Maav 76,244,228
buffer_load_dwordx4 a[132:135], v241, s[84:87], s53 offen offset:1024 sc1
Maav 76,240,224
Wl 0
Mvav 120,192,160
Mvav 120,196,164
Ta 224,240,49536
Mvav 120,200,168
Ta 226,240,53664
Mvav 120,204,172
Ta 228,240,57792
Mvav 124,204,188
Ta 230,240,61920
buffer_load_dwordx4 a[136:139], v241, s[84:87], s53 offen offset:2048 sc1
Mvav 124,200,184
Ta 232,255,49536
Mvav 124,196,180
Ta 234,255,53664
Mvav 124,192,176
Ta 236,255,57792
Mvav 128,192,192
Ta 238,255,61920
Mvav 128,196,196
Ta 240,240,49544
Mvav 128,200,200
Ta 242,240,53672
buffer_load_dwordx4 a[140:143], v241, s[84:87], s53 offen offset:3072 sc1
Mvav 128,204,204
Ta 244,240,57800
Mvav 132,204,220
Ta 246,240,61928
Mvav 132,200,216
Ta 248,255,49544
Mvav 132,196,212
Ta 250,255,53672
Mvav 132,192,208
s_add_u32 s53, s22, 65536
Ta 252,255,57800
Mvav 136,192,224
Ta 254,255,61928
buffer_load_dwordx4 a[144:147], v241, s[84:87], s53 offen offset:0 sc1
Mvav 136,196,228
Mvav 136,200,232
Mvav 136,204,236
Mvav 140,220,172
Mvav 140,216,168
Mvav 140,212,164
buffer_load_dwordx4 a[148:151], v241, s[84:87], s53 offen offset:1024 sc1
Mvav 140,208,160
Mvav 144,208,176
Mvav 144,212,180
Mvav 144,216,184
Mvav 144,220,188
Mvav 148,220,204
buffer_load_dwordx4 a[152:155], v241, s[84:87], s53 offen offset:2048 sc1
Mvav 148,216,200
Mvav 148,212,196
Mvav 148,208,192
Mvav 152,208,208
Mvav 152,212,212
Mvav 152,216,216
buffer_load_dwordx4 a[156:159], v241, s[84:87], s53 offen offset:3072 sc1
Mvav 152,220,220
Mvav 156,220,236
Mvav 156,216,232
v_lshlrev_b32 v242, 2, v245
global_load_dword v242, v242, s[50:51] sc1
Mvav 156,212,228
Mvav 156,208,224
Wl 0
Mvav 80,224,160
s_barrier
Mvav 80,228,164
Ta 192,255,256
Mvav 80,232,168
Ta 194,255,4384
Mvav 80,236,172
Ta 196,255,8512
Mvav 84,236,188
Ta 198,255,12640
Mvav 84,232,184
Ta 200,240,256
Mvav 84,228,180
Ta 202,240,4384
Mvav 84,224,176
Ta 204,240,8512
Mvav 88,224,192
Ta 206,240,12640
Mvav 88,228,196
Ta 208,255,264
Mvav 88,232,200
Ta 210,255,4392
Mvav 88,236,204
Ta 212,255,8520
Mvav 92,236,220
Ta 214,255,12648
Mvav 92,232,216
Ta 216,240,264
Mvav 92,228,212
Ta 218,240,4392
Mvav 92,224,208
Ta 220,240,8520
Mvav 96,224,224
Ta 222,240,12648
Mvav 96,228,228
Mvav 96,232,232
Mvav 96,236,236
Mvav 100,252,172
Mvav 100,248,168
Mvav 100,244,164
Mvav 100,240,160
Mvav 104,240,176
Mvav 104,244,180
Mvav 104,248,184
Mvav 104,252,188
Mvav 108,252,204
Mvav 108,248,200
Mvav 108,244,196
Mvav 108,240,192
Mvav 112,240,208
Mvav 112,244,212
Mvav 112,248,216
Mvav 112,252,220
Mvav 116,252,236
Mvav 116,248,232
Mvav 116,244,228
Mvav 116,240,224
Wl 0
s_waitcnt vmcnt(1)
Maaa 0,192,80
Maaa 0,196,84
Ta 224,255,512
Maaa 0,200,88
s_waitcnt vmcnt(0)
s_mov_b32 s58, 0
v_cmp_gt_i32 s[64:65], 0, v242
v_and_b32 v242, 0x7fffffff, v242
.p2align 12
.org PG0_%=+114688
s_cbranch_execz VRET_%=
s_nop 0
v_cmp_ne_u32 vcc, s81, v242
s_nop 0
s_and_b64 vcc, vcc, 0xf00
s_cbranch_scc1 SPINP1e5_%=
OKP1e5_%=:
Ta 226,255,4640
Maaa 0,204,92
s_add_u32 s53, s17, 4096
Ta 228,255,8768
Maaa 4,204,108
Ta 230,255,12896
buffer_load_dwordx4 v[160:163], v241, s[84:87], s53 offen offset:0 sc1
Maaa 4,200,104
Ta 232,240,512
Maaa 4,196,100
Ta 234,240,4640
Maaa 4,192,96
s_lshl_b32 s56, s25, 6
Ta 236,240,8768
Maaa 8,192,112
s_add_u32 s56, s56, s76
Ta 238,240,12896
Maaa 8,196,116
s_lshl_b32 s56, s56, 17
Ta 240,255,520
buffer_load_dwordx4 v[164:167], v241, s[84:87], s53 offen offset:1024 sc1
Maaa 8,200,120
s_add_u32 s40, s20, s56
s_addc_u32 s41, s21, 0
Ta 242,255,4648
Maaa 8,204,124
s_mov_b32 s42, 7995392
Ta 244,255,8776
Maaa 12,204,140
s_mov_b32 s98, 119808
Ta 246,255,12904
Maaa 12,200,136
s_mov_b32 s70, 0
Ta 248,240,520
Maaa 12,196,132
s_and_b32 s71, s35, 0xffff
Ta 250,240,4648
buffer_load_dwordx4 v[168:171], v241, s[84:87], s53 offen offset:2048 sc1
Maaa 12,192,128
s_lshl_b32 s71, s71, 4
Ta 252,240,8776
Maaa 16,192,144
s_andn2_b32 s34, s34, 0xf0
Ta 254,240,12904
Maaa 16,196,148
s_lshl_b64 s[34:35], s[34:35], 8
Maaa 16,200,152
s_and_b32 s48, s35, 0xffff0000
Maaa 16,204,156
s_and_b32 s35, s35, 0xffff
Maaa 20,220,92
buffer_load_dwordx4 v[172:175], v241, s[84:87], s53 offen offset:3072 sc1
Maaa 20,216,88
Maaa 20,212,84
Maaa 20,208,80
Maaa 24,208,96
s_add_u32 s53, s18, 4096
Maaa 24,212,100
buffer_load_dwordx4 v[0:3], v241, s[84:87], s53 offen offset:0 sc1
Maaa 24,216,104
Maaa 24,220,108
Maaa 28,220,124
Maaa 28,216,120
Maaa 28,212,116
Maaa 28,208,112
buffer_load_dwordx4 v[4:7], v241, s[84:87], s53 offen offset:1024 sc1
Maaa 32,208,128
Maaa 32,212,132
Maaa 32,216,136
Maaa 32,220,140
Maaa 36,220,156
buffer_load_dwordx4 v[8:11], v241, s[84:87], s53 offen offset:2048 sc1
Maaa 36,216,152
Maaa 36,212,148
Maaa 36,208,144
Wl 0
Maaa 40,224,80
Maaa 40,228,84
Ta 192,255,768
Maaa 40,232,88
Ta 194,255,4896
buffer_load_dwordx4 v[12:15], v241, s[84:87], s53 offen offset:3072 sc1
Maaa 40,236,92
Ta 196,255,9024
Maaa 44,236,108
Ta 198,255,13152
Maaa 44,232,104
Ta 200,240,768
Maaa 44,228,100
s_add_u32 s53, s17, 20480
Ta 202,240,4896
Maaa 44,224,96
Ta 204,240,9024
buffer_load_dwordx4 v[176:179], v241, s[84:87], s53 offen offset:0 sc1
Maaa 48,224,112
Ta 206,240,13152
buffer_load_dwordx4 v[180:183], v241, s[84:87], s53 offen offset:1024 sc1
Maaa 48,228,116
Ta 208,255,776
buffer_load_dwordx4 v[184:187], v241, s[84:87], s53 offen offset:2048 sc1
Maaa 48,232,120
Ta 210,255,4904
buffer_load_dwordx4 v[188:191], v241, s[84:87], s53 offen offset:3072 sc1
Maaa 48,236,124
s_add_u32 s53, s17, 36864
Ta 212,255,9032
Maaa 52,236,140
Ta 214,255,13160
buffer_load_dwordx4 v[192:195], v241, s[84:87], s53 offen offset:0 sc1
Maaa 52,232,136
Ta 216,240,776
Maaa 52,228,132
Ta 218,240,4904
Maaa 52,224,128
Ta 220,240,9032
Maaa 56,224,144
Ta 222,240,13160
Maaa 56,228,148
buffer_load_dwordx4 v[196:199], v241, s[84:87], s53 offen offset:1024 sc1
Maaa 56,232,152
Maaa 56,236,156
Maaa 60,252,92
Maaa 60,248,88
Maaa 60,244,84
Maaa 60,240,80
buffer_load_dwordx4 v[200:203], v241, s[84:87], s53 offen offset:2048 sc1
Maaa 64,240,96
Maaa 64,244,100
Maaa 64,248,104
Maaa 64,252,108
Maaa 68,252,124
buffer_load_dwordx4 v[204:207], v241, s[84:87], s53 offen offset:3072 sc1
Maaa 68,248,120
Maaa 68,244,116
Maaa 68,240,112
Maaa 72,240,128
Maaa 72,244,132
s_add_u32 s53, s17, 53248
Maaa 72,248,136
buffer_load_dwordx4 v[208:211], v241, s[84:87], s53 offen offset:0 sc1
Maaa 72,252,140
Maaa 76,252,156
Maaa 76,248,152
Maaa 76,244,148
Maaa 76,240,144
Wl 0
buffer_load_dwordx4 v[212:215], v241, s[84:87], s53 offen offset:1024 sc1
Mvaa 120,192,80
Mvaa 120,196,84
Ta 224,255,0
Mvaa 120,200,88
)A"
R"A(Ta 226,255,4128
Mvaa 120,204,92
Ta 228,255,8256
Mvaa 124,204,108
Ta 230,255,12384
Mvaa 124,200,104
Ta 232,240,0
buffer_load_dwordx4 v[216:219], v241, s[84:87], s53 offen offset:2048 sc1
Mvaa 124,196,100
Ta 234,240,4128
Mvaa 124,192,96
Ta 236,240,8256
Mvaa 128,192,112
Ta 238,240,12384
Mvaa 128,196,116
Ta 240,255,8
Mvaa 128,200,120
Ta 242,255,4136
Mvaa 128,204,124
Ta 244,255,8264
buffer_load_dwordx4 v[220:223], v241, s[84:87], s53 offen offset:3072 sc1
Mvaa 132,204,140
Ta 246,255,12392
Mvaa 132,200,136
Ta 248,240,8
Mvaa 132,196,132
Ta 250,240,4136
Mvaa 132,192,128
s_add_u32 s53, s17, 69632
Ta 252,240,8264
Mvaa 136,192,144
Ta 254,240,12392
buffer_load_dwordx4 v[224:227], v241, s[84:87], s53 offen offset:0 sc1
Mvaa 136,196,148
Mvaa 136,200,152
Mvaa 136,204,156
Mvaa 140,220,92
Mvaa 140,216,88
Mvaa 140,212,84
buffer_load_dwordx4 v[228:231], v241, s[84:87], s53 offen offset:1024 sc1
Mvaa 140,208,80
Mvaa 144,208,96
Mvaa 144,212,100
Mvaa 144,216,104
Mvaa 144,220,108
Mvaa 148,220,124
buffer_load_dwordx4 v[232:235], v241, s[84:87], s53 offen offset:2048 sc1
Mvaa 148,216,120
Mvaa 148,212,116
Mvaa 148,208,112
Mvaa 152,208,128
Mvaa 152,212,132
Mvaa 152,216,136
buffer_load_dwordx4 v[236:239], v241, s[84:87], s53 offen offset:3072 sc1
Mvaa 152,220,140
v_lshlrev_b32 v242, 2, v245
global_load_dword v242, v242, s[50:51] sc1
Mvaa 156,220,156
Mvaa 156,216,152
Mvaa 156,212,148
s_add_u32 s53, s18, 20480
Mvaa 156,208,144
Wl 0
buffer_load_dwordx4 v[16:19], v241, s[84:87], s53 offen offset:0 sc1
Mvaa 80,224,80
s_barrier
Mvaa 80,228,84
Ta 192,240,16768
Mvaa 80,232,88
Ta 194,240,20896
Mvaa 80,236,92
Ta 196,240,25024
Mvaa 84,236,108
Ta 198,240,29152
Mvaa 84,232,104
Ta 200,255,16768
buffer_load_dwordx4 v[20:23], v241, s[84:87], s53 offen offset:1024 sc1
Mvaa 84,228,100
Ta 202,255,20896
Mvaa 84,224,96
Ta 204,255,25024
Mvaa 88,224,112
Ta 206,255,29152
Mvaa 88,228,116
Ta 208,240,16776
Mvaa 88,232,120
Ta 210,240,20904
Mvaa 88,236,124
Ta 212,240,25032
buffer_load_dwordx4 v[24:27], v241, s[84:87], s53 offen offset:2048 sc1
Mvaa 92,236,140
Ta 214,240,29160
Mvaa 92,232,136
Ta 216,255,16776
Mvaa 92,228,132
Ta 218,255,20904
Mvaa 92,224,128
Ta 220,255,25032
Mvaa 96,224,144
Ta 222,255,29160
buffer_load_dwordx4 v[28:31], v241, s[84:87], s53 offen offset:3072 sc1
Mvaa 96,228,148
Mvaa 96,232,152
Mvaa 96,236,156
Mvaa 100,252,92
Mvaa 100,248,88
s_add_u32 s53, s18, 36864
Mvaa 100,244,84
buffer_load_dwordx4 v[32:35], v241, s[84:87], s53 offen offset:0 sc1
Mvaa 100,240,80
Mvaa 104,240,96
Mvaa 104,244,100
Mvaa 104,248,104
Mvaa 104,252,108
Mvaa 108,252,124
buffer_load_dwordx4 v[36:39], v241, s[84:87], s53 offen offset:1024 sc1
Mvaa 108,248,120
Mvaa 108,244,116
Mvaa 108,240,112
Mvaa 112,240,128
Mvaa 112,244,132
Mvaa 112,248,136
buffer_load_dwordx4 v[40:43], v241, s[84:87], s53 offen offset:2048 sc1
Mvaa 112,252,140
Mvaa 116,252,156
Mvaa 116,248,152
Mvaa 116,244,148
Mvaa 116,240,144
Wl 0
s_waitcnt vmcnt(8)
buffer_load_dwordx4 v[44:47], v241, s[84:87], s53 offen offset:3072 sc1
Maav 0,192,160
s_waitcnt vmcnt(8)
s_mov_b32 s58, 0
v_cmp_gt_i32 s[64:65], 0, v242
v_and_b32 v242, 0x7fffffff, v242
v_cmp_ne_u32 vcc, s81, v242
s_nop 0
s_and_b64 vcc, vcc, s[70:71]
s_cbranch_scc1 SPINP3e5_%=
OKP3e5_%=:
s_and_b32 s23, s65, s71
s_cselect_b32 s23, 8192, 0
s_andn2_b32 s56, s22, 8192
s_add_u32 s23, s23, s56
s_add_u32 s23, s23, 4194304
Maav 0,196,164
Ta 224,240,17024
Maav 0,200,168
Ta 226,240,21152
Maav 0,204,172
Ta 228,240,25280
Maav 4,204,188
s_add_u32 s53, s23, 0
Ta 230,240,29408
Maav 4,200,184
Ta 232,255,17024
buffer_load_dwordx4 a[80:83], v241, s[84:87], s53 offen offset:0 sc1
Maav 4,196,180
Ta 234,255,21152
Maav 4,192,176
Ta 236,255,25280
Maav 8,192,192
Ta 238,255,29408
Maav 8,196,196
Ta 240,240,17032
Maav 8,200,200
Ta 242,240,21160
Maav 8,204,204
Ta 244,240,25288
buffer_load_dwordx4 a[84:87], v241, s[84:87], s53 offen offset:1024 sc1
Maav 12,204,220
Ta 246,240,29416
Maav 12,200,216
Ta 248,255,17032
Maav 12,196,212
Ta 250,255,21160
Maav 12,192,208
Ta 252,255,25288
Maav 16,192,224
Ta 254,255,29416
buffer_load_dwordx4 a[88:91], v241, s[84:87], s53 offen offset:2048 sc1
Maav 16,196,228
Maav 16,200,232
Maav 16,204,236
Maav 20,220,172
Maav 20,216,168
Maav 20,212,164
buffer_load_dwordx4 a[92:95], v241, s[84:87], s53 offen offset:3072 sc1
Maav 20,208,160
Maav 24,208,176
Maav 24,212,180
Maav 24,216,184
Maav 24,220,188
s_add_u32 s53, s23, 16384
Maav 28,220,204
buffer_load_dwordx4 a[96:99], v241, s[84:87], s53 offen offset:0 sc1
Maav 28,216,200
Maav 28,212,196
Maav 28,208,192
Maav 32,208,208
Maav 32,212,212
Maav 32,216,216
buffer_load_dwordx4 a[100:103], v241, s[84:87], s53 offen offset:1024 sc1
Maav 32,220,220
Maav 36,220,236
Maav 36,216,232
Maav 36,212,228
Maav 36,208,224
Wl 0
buffer_load_dwordx4 a[104:107], v241, s[84:87], s53 offen offset:2048 sc1
Maav 40,224,160
Maav 40,228,164
Ta 192,240,17280
Maav 40,232,168
Ta 194,240,21408
Maav 40,236,172
Ta 196,240,25536
Maav 44,236,188
Ta 198,240,29664
Maav 44,232,184
Ta 200,255,17280
buffer_load_dwordx4 a[108:111], v241, s[84:87], s53 offen offset:3072 sc1
Maav 44,228,180
Ta 202,255,21408
Maav 44,224,176
Ta 204,255,25536
Maav 48,224,192
Ta 206,255,29664
Maav 48,228,196
Ta 208,240,17288
Maav 48,232,200
s_add_u32 s53, s23, 32768
Ta 210,240,21416
Maav 48,236,204
Ta 212,240,25544
buffer_load_dwordx4 a[112:115], v241, s[84:87], s53 offen offset:0 sc1
Maav 52,236,220
Ta 214,240,29672
Maav 52,232,216
Ta 216,255,17288
Maav 52,228,212
Ta 218,255,21416
Maav 52,224,208
Ta 220,255,25544
Maav 56,224,224
Ta 222,255,29672
buffer_load_dwordx4 a[116:119], v241, s[84:87], s53 offen offset:1024 sc1
Maav 56,228,228
Maav 56,232,232
Maav 56,236,236
Maav 60,252,172
Maav 60,248,168
Maav 60,244,164
buffer_load_dwordx4 a[120:123], v241, s[84:87], s53 offen offset:2048 sc1
Maav 60,240,160
Maav 64,240,176
Maav 64,244,180
Maav 64,248,184
Maav 64,252,188
Maav 68,252,204
buffer_load_dwordx4 a[124:127], v241, s[84:87], s53 offen offset:3072 sc1
Maav 68,248,200
Maav 68,244,196
Maav 68,240,192
Maav 72,240,208
Maav 72,244,212
s_add_u32 s53, s23, 49152
Maav 72,248,216
buffer_load_dwordx4 a[128:131], v241, s[84:87], s53 offen offset:0 sc1
Maav 72,252,220
Maav 76,252,236
Maav 76,248,232
Maav 76,244,228
Maav 76,240,224
Wl 0
buffer_load_dwordx4 a[132:135], v241, s[84:87], s53 offen offset:1024 sc1
Mvav 120,192,160
Mvav 120,196,164
Ta 224,240,16512
Mvav 120,200,168
Ta 226,240,20640
Mvav 120,204,172
Ta 228,240,24768
Mvav 124,204,188
Ta 230,240,28896
Mvav 124,200,184
Ta 232,255,16512
buffer_load_dwordx4 a[136:139], v241, s[84:87], s53 offen offset:2048 sc1
Mvav 124,196,180
Ta 234,255,20640
Mvav 124,192,176
Ta 236,255,24768
Mvav 128,192,192
Ta 238,255,28896
Mvav 128,196,196
Ta 240,240,16520
Mvav 128,200,200
Ta 242,240,20648
Mvav 128,204,204
Ta 244,240,24776
buffer_load_dwordx4 a[140:143], v241, s[84:87], s53 offen offset:3072 sc1
Mvav 132,204,220
Ta 246,240,28904
Mvav 132,200,216
Ta 248,255,16520
Mvav 132,196,212
Ta 250,255,20648
Mvav 132,192,208
Ta 252,255,24776
Mvav 136,192,224
s_add_u32 s53, s23, 65536
Ta 254,255,28904
Mvav 136,196,228
buffer_load_dwordx4 a[144:147], v241, s[84:87], s53 offen offset:0 sc1
Mvav 136,200,232
Mvav 136,204,236
Mvav 140,220,172
Mvav 140,216,168
Mvav 140,212,164
buffer_load_dwordx4 a[148:151], v241, s[84:87], s53 offen offset:1024 sc1
Mvav 140,208,160
Mvav 144,208,176
Mvav 144,212,180
Mvav 144,216,184
Mvav 144,220,188
Mvav 148,220,204
buffer_load_dwordx4 a[152:155], v241, s[84:87], s53 offen offset:2048 sc1
Mvav 148,216,200
Mvav 148,212,196
Mvav 148,208,192
Mvav 152,208,208
Mvav 152,212,212
Mvav 152,216,216
buffer_load_dwordx4 a[156:159], v241, s[84:87], s53 offen offset:3072 sc1
Mvav 152,220,220
Mvav 156,220,236
Mvav 156,216,232
Mvav 156,212,228
s_add_u32 s53, s18, 53248
Mvav 156,208,224
Wl 0
buffer_load_dwordx4 a[160:163], v241, s[84:87], s53 offen offset:0 sc1
Mvav 80,224,160
s_barrier
Mvav 80,228,164
Ta 192,240,33280
Mvav 80,232,168
Ta 194,240,37408
Mvav 80,236,172
Ta 196,240,41536
Mvav 84,236,188
Ta 198,240,45664
Mvav 84,232,184
Ta 200,255,33280
buffer_load_dwordx4 a[164:167], v241, s[84:87], s53 offen offset:1024 sc1
Mvav 84,228,180
Ta 202,255,37408
Mvav 84,224,176
Ta 204,255,41536
Mvav 88,224,192
Ta 206,255,45664
Mvav 88,228,196
Ta 208,240,33288
Mvav 88,232,200
Ta 210,240,37416
Mvav 88,236,204
Ta 212,240,41544
buffer_load_dwordx4 a[168:171], v241, s[84:87], s53 offen offset:2048 sc1
Mvav 92,236,220
Ta 214,240,45672
Mvav 92,232,216
.p2align 12
.org PG0_%=+118784
s_cbranch_execz VRET_%=
Ta 216,255,33288
Mvav 92,228,212
Ta 218,255,37416
Mvav 92,224,208
Ta 220,255,41544
Mvav 96,224,224
Ta 222,255,45672
Mvav 96,228,228
buffer_load_dwordx4 a[172:175], v241, s[84:87], s53 offen offset:3072 sc1
Mvav 96,232,232
Mvav 96,236,236
Mvav 100,252,172
Mvav 100,248,168
s_add_u32 s53, s18, 69632
Mvav 100,244,164
buffer_load_dwordx4 a[176:179], v241, s[84:87], s53 offen offset:0 sc1
Mvav 100,240,160
Mvav 104,240,176
Mvav 104,244,180
Mvav 104,248,184
Mvav 104,252,188
Mvav 108,252,204
buffer_load_dwordx4 a[180:183], v241, s[84:87], s53 offen offset:1024 sc1
Mvav 108,248,200
Mvav 108,244,196
)A"
R"A(Mvav 108,240,192
Mvav 112,240,208
Mvav 112,244,212
Mvav 112,248,216
buffer_load_dwordx4 a[184:187], v241, s[84:87], s53 offen offset:2048 sc1
Mvav 112,252,220
Mvav 116,252,236
Mvav 116,248,232
Mvav 116,244,228
Mvav 116,240,224
Wl 0
s_waitcnt vmcnt(7)
buffer_load_dwordx4 a[188:191], v241, s[84:87], s53 offen offset:3072 sc1
Maaa 0,192,80
Maaa 0,196,84
Ta 224,240,33536
Maaa 0,200,88
Ta 226,240,37664
Maaa 0,204,92
Ta 228,240,41792
Maaa 4,204,108
s_add_u32 s53, s22, 4096
Ta 230,240,45920
Maaa 4,200,104
Ta 232,255,33536
buffer_load_dwordx4 v[160:163], v241, s[84:87], s53 offen offset:0 sc1
Maaa 4,196,100
Ta 234,255,37664
Maaa 4,192,96
Ta 236,255,41792
Maaa 8,192,112
Ta 238,255,45920
Maaa 8,196,116
Ta 240,240,33544
Maaa 8,200,120
Ta 242,240,37672
Maaa 8,204,124
Ta 244,240,41800
buffer_load_dwordx4 v[164:167], v241, s[84:87], s53 offen offset:1024 sc1
Maaa 12,204,140
Ta 246,240,45928
Maaa 12,200,136
Ta 248,255,33544
Maaa 12,196,132
Ta 250,255,37672
Maaa 12,192,128
Ta 252,255,41800
Maaa 16,192,144
Ta 254,255,45928
Maaa 16,196,148
buffer_load_dwordx4 v[168:171], v241, s[84:87], s53 offen offset:2048 sc1
Maaa 16,200,152
Maaa 16,204,156
Maaa 20,220,92
Maaa 20,216,88
Maaa 20,212,84
buffer_load_dwordx4 v[172:175], v241, s[84:87], s53 offen offset:3072 sc1
Maaa 20,208,80
Maaa 24,208,96
Maaa 24,212,100
Maaa 24,216,104
Maaa 24,220,108
s_add_u32 s53, s22, 20480
Maaa 28,220,124
buffer_load_dwordx4 v[176:179], v241, s[84:87], s53 offen offset:0 sc1
Maaa 28,216,120
Maaa 28,212,116
Maaa 28,208,112
Maaa 32,208,128
Maaa 32,212,132
Maaa 32,216,136
buffer_load_dwordx4 v[180:183], v241, s[84:87], s53 offen offset:1024 sc1
Maaa 32,220,140
Maaa 36,220,156
Maaa 36,216,152
Maaa 36,212,148
Maaa 36,208,144
Wl 0
buffer_load_dwordx4 v[184:187], v241, s[84:87], s53 offen offset:2048 sc1
Maaa 40,224,80
s_barrier
Maaa 40,228,84
Ta 192,240,49792
Maaa 40,232,88
Ta 194,240,53920
Maaa 40,236,92
Ta 196,240,58048
Maaa 44,236,108
Ta 198,240,62176
Maaa 44,232,104
Ta 200,255,49792
buffer_load_dwordx4 v[188:191], v241, s[84:87], s53 offen offset:3072 sc1
Maaa 44,228,100
Ta 202,255,53920
Maaa 44,224,96
Ta 204,255,58048
Maaa 48,224,112
Ta 206,255,62176
Maaa 48,228,116
Ta 208,240,49800
Maaa 48,232,120
s_add_u32 s53, s22, 36864
Ta 210,240,53928
Maaa 48,236,124
Ta 212,240,58056
buffer_load_dwordx4 v[192:195], v241, s[84:87], s53 offen offset:0 sc1
Maaa 52,236,140
Ta 214,240,62184
Maaa 52,232,136
Ta 216,255,49800
Maaa 52,228,132
Ta 218,255,53928
Maaa 52,224,128
Ta 220,255,58056
Maaa 56,224,144
Ta 222,255,62184
Maaa 56,228,148
buffer_load_dwordx4 v[196:199], v241, s[84:87], s53 offen offset:1024 sc1
Maaa 56,232,152
Maaa 56,236,156
Maaa 60,252,92
Maaa 60,248,88
Maaa 60,244,84
buffer_load_dwordx4 v[200:203], v241, s[84:87], s53 offen offset:2048 sc1
Maaa 60,240,80
Maaa 64,240,96
Maaa 64,244,100
Maaa 64,248,104
Maaa 64,252,108
Maaa 68,252,124
buffer_load_dwordx4 v[204:207], v241, s[84:87], s53 offen offset:3072 sc1
Maaa 68,248,120
Maaa 68,244,116
Maaa 68,240,112
Maaa 72,240,128
Maaa 72,244,132
s_add_u32 s53, s22, 53248
Maaa 72,248,136
buffer_load_dwordx4 v[208:211], v241, s[84:87], s53 offen offset:0 sc1
Maaa 72,252,140
Maaa 76,252,156
Maaa 76,248,152
Maaa 76,244,148
Maaa 76,240,144
Wl 0
s_waitcnt vmcnt(13)
buffer_load_dwordx4 v[212:215], v241, s[84:87], s53 offen offset:1024 sc1
Maav 0,192,0
Maav 0,196,4
Ta 224,240,50048
Maav 0,200,8
Ta 226,240,54176
Maav 0,204,12
Ta 228,240,58304
Maav 4,204,28
Ta 230,240,62432
Maav 4,200,24
Ta 232,255,50048
buffer_load_dwordx4 v[216:219], v241, s[84:87], s53 offen offset:2048 sc1
Maav 4,196,20
Ta 234,255,54176
Maav 4,192,16
Ta 236,255,58304
Maav 8,192,32
Ta 238,255,62432
Maav 8,196,36
Ta 240,240,50056
Maav 8,200,40
Ta 242,240,54184
Maav 8,204,44
Ta 244,240,58312
buffer_load_dwordx4 v[220:223], v241, s[84:87], s53 offen offset:3072 sc1
Maaa 12,204,172
Ta 246,240,62440
Maaa 12,200,168
Ta 248,255,50056
Maaa 12,196,164
Ta 250,255,54184
Maaa 12,192,160
Ta 252,255,58312
Maaa 16,192,176
s_add_u32 s53, s22, 69632
Ta 254,255,62440
Maaa 16,196,180
buffer_load_dwordx4 v[224:227], v241, s[84:87], s53 offen offset:0 sc1
Maaa 16,200,184
Maaa 16,204,188
Maav 20,220,12
Maav 20,216,8
Maav 20,212,4
buffer_load_dwordx4 v[228:231], v241, s[84:87], s53 offen offset:1024 sc1
Maav 20,208,0
Maav 24,208,16
Maav 24,212,20
Maav 24,216,24
Maav 24,220,28
Maav 28,220,44
buffer_load_dwordx4 v[232:235], v241, s[84:87], s53 offen offset:2048 sc1
Maav 28,216,40
Maav 28,212,36
Maav 28,208,32
Maaa 32,208,160
Maaa 32,212,164
Maaa 32,216,168
buffer_load_dwordx4 v[236:239], v241, s[84:87], s53 offen offset:3072 sc1
Maaa 32,220,172
Maaa 36,220,188
Maaa 36,216,184
Maaa 36,212,180
Maaa 36,208,176
Wl 0
Maav 40,224,0
s_barrier
Maav 40,228,4
Ta 192,255,256
Maav 40,232,8
Ta 194,255,4384
Maav 40,236,12
Ta 196,255,8512
Maav 44,236,28
Ta 198,255,12640
Maav 44,232,24
Ta 200,240,256
Maav 44,228,20
Ta 202,240,4384
Maav 44,224,16
Ta 204,240,8512
Maav 48,224,32
Ta 206,240,12640
Maav 48,228,36
Ta 208,255,264
Maav 48,232,40
Ta 210,255,4392
Maav 48,236,44
Ta 212,255,8520
Maaa 52,236,172
Ta 214,255,12648
Maaa 52,232,168
Ta 216,240,264
Maaa 52,228,164
Ta 218,240,4392
Maaa 52,224,160
Ta 220,240,8520
Maaa 56,224,176
Ta 222,240,12648
Maaa 56,228,180
Maaa 56,232,184
Maaa 56,236,188
Maav 60,252,12
Maav 60,248,8
Maav 60,244,4
Maav 60,240,0
Maav 64,240,16
Maav 64,244,20
Maav 64,248,24
Maav 64,252,28
Maav 68,252,44
Maav 68,248,40
Maav 68,244,36
Maav 68,240,32
Maaa 72,240,160
Maaa 72,244,164
Maaa 72,248,168
Maaa 72,252,172
Maaa 76,252,188
Maaa 76,248,184
Maaa 76,244,180
Maaa 76,240,176
Wl 0
s_waitcnt vmcnt(0)
Maav 0,192,160
Maav 0,196,164
Ta 224,255,512
Maav 0,200,168
Ta 226,255,4640
Maav 0,204,172
Ta 228,255,8768
Maav 4,204,188
Ta 230,255,12896
Maav 4,200,184
Ta 232,240,512
Maav 4,196,180
Ta 234,240,4640
Maav 4,192,176
Ta 236,240,8768
Maav 8,192,192
Ta 238,240,12896
Maav 8,196,196
Ta 240,255,520
Maav 8,200,200
Ta 242,255,4648
Maav 8,204,204
Ta 244,255,8776
Maav 12,204,220
Ta 246,255,12904
Maav 12,200,216
Ta 248,240,520
Maav 12,196,212
Ta 250,240,4648
Maav 12,192,208
Ta 252,240,8776
Maav 16,192,224
Ta 254,240,12904
Maav 16,196,228
Maav 16,200,232
Maav 16,204,236
Maav 20,220,172
Maav 20,216,168
Maav 20,212,164
Maav 20,208,160
Maav 24,208,176
Maav 24,212,180
Maav 24,216,184
Maav 24,220,188
Maav 28,220,204
Maav 28,216,200
Maav 28,212,196
Maav 28,208,192
Maav 32,208,208
Ar 48,0
Ar 49,1
Maav 32,212,212
Ar 50,2
Ar 51,3
Maav 32,216,216
Pk 56,48,49
Pk 57,50,51
Maav 32,220,220
Ar 52,20
Ar 53,21
Maav 36,220,236
Ar 54,22
Ar 55,23
Maav 36,216,232
Pk 58,52,53
Pk 59,54,55
Maav 36,212,228
s_mov_b32 s57, 81920
buffer_store_dwordx4 v[56:59], v241, s[96:99], s57 offen offset:0 sc1
Maav 36,208,224
Wl 0
Ar 48,4
Ar 49,5
Maav 40,224,160
Ar 50,6
Ar 51,7
Maav 40,228,164
Pk 60,48,49
Pk 61,50,51
Maav 40,232,168
Ar 52,24
Ar 53,25
Maav 40,236,172
Ar 54,26
Ar 55,27
Maav 44,236,188
Pk 62,52,53
Pk 63,54,55
Maav 44,232,184
buffer_store_dwordx4 v[60:63], v241, s[96:99], s57 offen offset:1024 sc1
Maav 44,228,180
Ar 48,8
Ar 49,9
Maav 44,224,176
Ar 50,10
Ar 51,11
Maav 48,224,192
Pk 64,48,49
Pk 65,50,51
Maav 48,228,196
Ar 52,28
Ar 53,29
Maav 48,232,200
Ar 54,30
Ar 55,31
Maav 48,236,204
Pk 66,52,53
Pk 67,54,55
Maav 52,236,220
buffer_store_dwordx4 v[64:67], v241, s[96:99], s57 offen offset:2048 sc1
Maav 52,232,216
Ar 48,12
Ar 49,13
Maav 52,228,212
Ar 50,14
Ar 51,15
Maav 52,224,208
Pk 68,48,49
Pk 69,50,51
Maav 56,224,224
Ar 52,32
Ar 53,33
Maav 56,228,228
Ar 54,34
Ar 55,35
Maav 56,232,232
Pk 70,52,53
Pk 71,54,55
Maav 56,236,236
buffer_store_dwordx4 v[68:71], v241, s[96:99], s57 offen offset:3072 sc1
)A"
R"A(Maav 60,252,172
Ta 192,240,33792
Ar 48,16
Maav 60,248,168
Ta 194,240,37920
Ar 49,17
Maav 60,244,164
Ta 196,240,42048
Ar 50,18
Maav 60,240,160
Ta 198,240,46176
Ar 51,19
Maav 64,240,176
Ta 200,255,33792
Pk 72,48,49
Maav 64,244,180
Ta 202,255,37920
Pk 73,50,51
Maav 64,248,184
Ta 204,255,42048
Ar 52,36
Maav 64,252,188
Ta 206,255,46176
Ar 53,37
Maav 68,252,204
Ta 208,240,33800
Ar 54,38
Maav 68,248,200
Ta 210,240,37928
Ar 55,39
Maav 68,244,196
Ta 212,240,42056
Pk 74,52,53
Maav 68,240,192
Ta 214,240,46184
Pk 75,54,55
Maav 72,240,208
s_mov_b32 s57, 86016
Ta 216,255,33800
buffer_store_dwordx4 v[72:75], v241, s[96:99], s57 offen offset:0 sc1
Maav 72,244,212
Ta 218,255,37928
Ar 48,40
Maav 72,248,216
Ta 220,255,42056
Ar 49,41
Maav 72,252,220
Ta 222,255,46184
Ar 50,42
Maav 76,252,236
Ar 51,43
Pk 56,48,49
Maav 76,248,232
Pk 57,50,51
Ar 52,60
Maav 76,244,228
Ar 53,61
Ar 54,62
Maav 76,240,224
Wl 0
Ar 55,63
Pk 58,52,53
Mvaa 120,192,80
Pk 59,54,55
Mvaa 120,196,84
s_mov_b32 s57, 98304
buffer_store_dwordx4 v[56:59], v241, s[96:99], s57 offen offset:0 sc1
Mvaa 120,200,88
Ar 48,44
Ar 49,45
Mvaa 120,204,92
Ar 50,46
Ar 51,47
Mvaa 124,204,108
Pk 60,48,49
Pk 61,50,51
Mvaa 124,200,104
Ar 52,64
Ar 53,65
Mvaa 124,196,100
Ar 54,66
Ar 55,67
Mvaa 124,192,96
Pk 62,52,53
Pk 63,54,55
Mvaa 128,192,112
buffer_store_dwordx4 v[60:63], v241, s[96:99], s57 offen offset:1024 sc1
Mvaa 128,196,116
Ar 48,48
Ar 49,49
Mvaa 128,200,120
Ar 50,50
.p2align 12
.org PG0_%=+122880
s_cbranch_execz VRET_%=
Ar 51,51
Mvaa 128,204,124
Pk 64,48,49
Pk 65,50,51
Mvaa 132,204,140
Ar 52,68
Ar 53,69
Mvaa 132,200,136
Ar 54,70
Ar 55,71
Mvaa 132,196,132
Pk 66,52,53
Pk 67,54,55
Mvaa 132,192,128
buffer_store_dwordx4 v[64:67], v241, s[96:99], s57 offen offset:2048 sc1
Mvaa 136,192,144
Ar 48,52
Ar 49,53
Mvaa 136,196,148
Ar 50,54
Ar 51,55
Mvaa 136,200,152
Pk 68,48,49
Pk 69,50,51
Mvaa 136,204,156
Ar 52,72
Ar 53,73
Mvaa 140,220,92
Ta 224,240,50304
Ar 54,74
Mvaa 140,216,88
Ta 226,240,54432
Ar 55,75
Mvaa 140,212,84
Ta 228,240,58560
Pk 70,52,53
Mvaa 140,208,80
Ta 230,240,62688
Pk 71,54,55
Mvaa 144,208,96
Ta 232,255,50304
buffer_store_dwordx4 v[68:71], v241, s[96:99], s57 offen offset:3072 sc1
Mvaa 144,212,100
Ta 234,255,54432
Ar 48,56
Mvaa 144,216,104
Ta 236,255,58560
Ar 49,57
Mvaa 144,220,108
Ta 238,255,62688
Ar 50,58
Mvaa 148,220,124
Ta 240,240,50312
Ar 51,59
Mvaa 148,216,120
Ta 242,240,54440
Pk 72,48,49
Mvaa 148,212,116
Ta 244,240,58568
Pk 73,50,51
Mvaa 148,208,112
Ta 246,240,62696
Ar 52,76
Mvaa 152,208,128
Ta 248,255,50312
Ar 53,77
Mvaa 152,212,132
Ta 250,255,54440
Ar 54,78
Mvaa 152,216,136
Ta 252,255,58568
Ar 55,79
Mvaa 152,220,140
Ta 254,255,62696
Pk 74,52,53
Mvaa 156,220,156
Pk 75,54,55
Mvaa 156,216,152
s_mov_b32 s57, 102400
buffer_store_dwordx4 v[72:75], v241, s[96:99], s57 offen offset:0 sc1
Mvaa 156,212,148
Mvaa 156,208,144
Wl 0
v_bfe_u32 v76, v245, 2, 2
v_lshrrev_b32 v77, 4, v245
Mvav 120,224,0
v_cmp_eq_u32 vcc, v76, v77
v_and_b32 v76, 1, v245
Mvav 120,228,4
Ta 192,255,768
v_lshlrev_b32 v76, 4, v76
Mvav 120,232,8
Ta 194,255,4896
v_mov_b32 v77, 0x3f80
Mvav 120,236,12
Ta 196,255,9024
v_lshlrev_b32 v77, v76, v77
Mvav 124,236,28
Ta 198,255,13152
v_cndmask_b32 v77, 0, v77, vcc
Mvav 124,232,24
Ta 200,240,768
v_and_b32 v76, 2, v245
Mvav 124,228,20
Ta 202,240,4896
v_cmp_eq_u32 vcc, 0, v76
Mvav 124,224,16
Ta 204,240,9024
v_mov_b32 v50, 0
Mvav 128,224,32
Ta 206,240,13152
v_mov_b32 v51, 0
Mvav 128,228,36
Ta 208,255,776
v_cndmask_b32 v48, 0, v77, vcc
Mvav 128,232,40
Ta 210,255,4904
v_sub_u32 v49, v77, v48
Mvav 128,236,44
Ta 212,255,9032
v_mov_b32 v52, v48
Mvaa 132,236,172
Ta 214,255,13160
v_mov_b32 v53, v49
Mvaa 132,232,168
Ta 216,240,776
v_and_b32 v250, 15, v245
Mvaa 132,228,164
Ta 218,240,4904
v_lshlrev_b32 v250, 11, v250
Mvaa 132,224,160
Ta 220,240,9032
v_lshrrev_b32 v251, 4, v245
Mvaa 136,224,176
Ta 222,240,13160
v_lshl_add_u32 v250, v251, 4, v250
Mvaa 136,228,180
v_lshlrev_b32 v251, 2, v245
Mvaa 136,232,184
Mvaa 136,236,188
Mvav 140,252,12
Mvav 140,248,8
Mvav 140,244,4
Mvav 140,240,0
Mvav 144,240,16
Mvav 144,244,20
Mvav 144,248,24
Mvav 144,252,28
Mvav 148,252,44
Mvav 148,248,40
Mvav 148,244,36
Mvav 148,240,32
Mvaa 152,240,160
Mvaa 152,244,164
Mvaa 152,248,168
Mvaa 152,252,172
Mvaa 156,252,188
Mvaa 156,248,184
Mvaa 156,244,180
Mvaa 156,240,176
Wl 0
Mvav 120,192,160
Mvav 120,196,164
Ta 224,240,33024
Mvav 120,200,168
Ta 226,240,37152
Mvav 120,204,172
Ta 228,240,41280
Mvav 124,204,188
Ta 230,240,45408
Mvav 124,200,184
Ta 232,255,33024
Mvav 124,196,180
Ta 234,255,37152
Mvav 124,192,176
s_waitcnt vmcnt(0)
s_mov_b64 exec, 0x30
global_store_dword v243, v244, s[94:95] offset:32 sc1
s_mov_b64 exec, -1
Ta 236,255,41280
Mvav 128,192,192
Ta 238,255,45408
Mvav 128,196,196
Ta 240,240,33032
Mvav 128,200,200
Ta 242,240,37160
Mvav 128,204,204
Ta 244,240,41288
Mvav 132,204,220
Ta 246,240,45416
Mvav 132,200,216
Ta 248,255,33032
Mvav 132,196,212
Ta 250,255,37160
Mvav 132,192,208
Ta 252,255,41288
Mvav 136,192,224
Ta 254,255,45416
Mvav 136,196,228
Mvav 136,200,232
Mvav 136,204,236
Mvav 140,220,172
Mvav 140,216,168
Mvav 140,212,164
Mvav 140,208,160
Mvav 144,208,176
Mvav 144,212,180
Mvav 144,216,184
Mvav 144,220,188
Mvav 148,220,204
Mvav 148,216,200
Mvav 148,212,196
Mvav 148,208,192
Mvav 152,208,208
Pk 56,120,121
Pk 57,122,123
Mvav 152,212,212
Pk 58,140,141
Pk 59,142,143
Mvav 152,216,216
s_mov_b32 s57, 114688
buffer_store_dwordx4 v[56:59], v241, s[96:99], s57 offen offset:0 sc1
Mvav 152,220,220
Mvav 156,220,236
Pk 60,124,125
Pk 61,126,127
Mvav 156,216,232
Pk 62,144,145
Pk 63,146,147
Mvav 156,212,228
buffer_store_dwordx4 v[60:63], v241, s[96:99], s57 offen offset:1024 sc1
Mvav 156,208,224
Wl 0
Mvaa 80,224,80
Pk 64,128,129
Pk 65,130,131
Mvaa 80,228,84
Pk 66,148,149
Pk 67,150,151
Mvaa 80,232,88
buffer_store_dwordx4 v[64:67], v241, s[96:99], s57 offen offset:2048 sc1
Mvaa 80,236,92
Mvaa 84,236,108
Pk 68,132,133
Pk 69,134,135
Mvaa 84,232,104
Pk 70,152,153
Pk 71,154,155
Mvaa 84,228,100
buffer_store_dwordx4 v[68:71], v241, s[96:99], s57 offen offset:3072 sc1
Mvaa 84,224,96
Mvaa 88,224,112
Pk 72,136,137
Pk 73,138,139
Mvaa 88,228,116
Pk 74,156,157
Pk 75,158,159
Mvaa 88,232,120
s_mov_b32 s57, 118784
buffer_store_dwordx4 v[72:75], v241, s[96:99], s57 offen offset:0 sc1
Mvaa 88,236,124
Mvaa 92,236,140
Mvaa 92,232,136
Mvaa 92,228,132
Mvaa 92,224,128
Mvaa 96,224,144
Mvaa 96,228,148
Mvaa 96,232,152
Mvaa 96,236,156
Mvaa 100,252,92
Ta 192,240,49536
Mvaa 100,248,88
Ta 194,240,53664
Mvaa 100,244,84
Ta 196,240,57792
v_lshlrev_b32 v242, 2, v245
global_load_dword v242, v242, s[50:51] sc1
Mvaa 100,240,80
Ta 198,240,61920
Mvaa 104,240,96
Ta 200,255,49536
Mvaa 104,244,100
Ta 202,255,53664
Mvaa 104,248,104
Ta 204,255,57792
Mvaa 104,252,108
Ta 206,255,61920
Mvaa 108,252,124
Ta 208,240,49544
Mvaa 108,248,120
Ta 210,240,53672
Mvaa 108,244,116
Ta 212,240,57800
Mvaa 108,240,112
Ta 214,240,61928
Mvaa 112,240,128
Ta 216,255,49544
Mvaa 112,244,132
Ta 218,255,53672
Mvaa 112,248,136
Ta 220,255,57800
Mvaa 112,252,140
Ta 222,255,61928
Mvaa 116,252,156
Mvaa 116,248,152
)A"
R"A(Mvaa 116,244,148
Mvaa 116,240,144
Wl 0
Mvav 80,192,0
Mvav 80,196,4
Ta 224,255,0
Mvav 80,200,8
Ta 226,255,4128
Mvav 80,204,12
Ta 228,255,8256
Mvav 84,204,28
Ta 230,255,12384
Mvav 84,200,24
Ta 232,240,0
Mvav 84,196,20
Ta 234,240,4128
Mvav 84,192,16
Ta 236,240,8256
Mvav 88,192,32
Ta 238,240,12384
Mvav 88,196,36
Ta 240,255,8
Mvav 88,200,40
s_waitcnt vmcnt(1)
s_mov_b64 exec, 0x40
global_store_dword v243, v244, s[94:95] offset:32 sc1
s_mov_b64 exec, -1
Ta 242,255,4136
Mvav 88,204,44
Ta 244,255,8264
Mvaa 92,204,172
Ta 246,255,12392
Mvaa 92,200,168
Ta 248,240,8
Mvaa 92,196,164
Ta 250,240,4136
Mvaa 92,192,160
Ta 252,240,8264
Mvaa 96,192,176
Ta 254,240,12392
Mvaa 96,196,180
Mvaa 96,200,184
Mvaa 96,204,188
Mvav 100,220,12
Mvav 100,216,8
Mvav 100,212,4
Mvav 100,208,0
Mvav 104,208,16
Mvav 104,212,20
Mvav 104,216,24
s_waitcnt vmcnt(1)
s_mov_b32 s58, 0
v_cmp_gt_i32 s[64:65], 0, v242
v_and_b32 v242, 0x7fffffff, v242
v_cmp_ne_u32 vcc, s81, v242
s_nop 0
s_and_b64 vcc, vcc, s[34:35]
s_cbranch_scc1 SPINP2e5_%=
OKP2e5_%=:
Mvav 104,220,28
s_add_u32 s53, s27, 81920
buffer_load_dwordx4 a[0:3], v241, s[40:43], s53 offen offset:0 sc1
Mvav 108,220,44
Mvav 108,216,40
Mvav 108,212,36
buffer_load_dwordx4 a[4:7], v241, s[40:43], s53 offen offset:1024 sc1
Mvav 108,208,32
Mvaa 112,208,160
Mvaa 112,212,164
buffer_load_dwordx4 a[8:11], v241, s[40:43], s53 offen offset:2048 sc1
Mvaa 112,216,168
Mvaa 112,220,172
Mvaa 116,220,188
buffer_load_dwordx4 a[12:15], v241, s[40:43], s53 offen offset:3072 sc1
Mvaa 116,216,184
Mvaa 116,212,180
Mvaa 116,208,176
Wl 0
s_add_u32 s53, s27, 86016
buffer_load_dwordx4 a[16:19], v241, s[40:43], s53 offen offset:0 sc1
Mvav 80,224,160
Mvav 80,228,164
Mvav 80,232,168
s_add_u32 s53, s52, 98304
buffer_load_dwordx4 a[20:23], v241, s[40:43], s53 offen offset:0 sc1
Mvav 80,236,172
Mvav 84,236,188
Mvav 84,232,184
buffer_load_dwordx4 a[24:27], v241, s[40:43], s53 offen offset:1024 sc1
Mvav 84,228,180
Mvav 84,224,176
Mvav 88,224,192
buffer_load_dwordx4 a[28:31], v241, s[40:43], s53 offen offset:2048 sc1
Mvav 88,228,196
Mvav 88,232,200
Mvav 88,236,204
buffer_load_dwordx4 a[32:35], v241, s[40:43], s53 offen offset:3072 sc1
Mvav 92,236,220
Mvav 92,232,216
Mvav 92,228,212
s_add_u32 s53, s52, 102400
buffer_load_dwordx4 a[36:39], v241, s[40:43], s53 offen offset:0 sc1
Mvav 92,224,208
Mvav 96,224,224
Mvav 96,228,228
Mvav 96,232,232
Mvav 96,236,236
Mvav 100,252,172
Mvav 100,248,168
Mvav 100,244,164
Mvav 100,240,160
Mvav 104,240,176
Mvav 104,244,180
Mvav 104,248,184
Mvav 104,252,188
Mvav 108,252,204
global_load_dword v242, v251, s[50:51] sc1
Mvav 108,248,200
Mvav 108,244,196
Mvav 108,240,192
Mvav 112,240,208
Mvav 112,244,212
Mvav 112,248,216
Mvav 112,252,220
Mvav 116,252,236
Mvav 116,248,232
Mvav 116,244,228
Mvav 116,240,224
s_waitcnt vmcnt(6)
Mvva 80,48,0
Mvva 100,50,0
Mvva 84,48,4
Mvva 104,50,4
Mvva 88,48,8
Mvva 108,50,8
Mvva 92,48,12
Mvva 112,50,12
Mvva 96,48,16
Mvva 116,50,16
s_mov_b32 s58, 0
POLLe5_%=:
s_waitcnt vmcnt(0)
v_and_b32 v242, 0x7fffffff, v242
v_cmp_ne_u32 vcc, s81, v242
s_add_u32 s58, s58, 1
s_and_b32 s57, vcc_hi, s48
s_cbranch_scc0 GOTe5_%=
global_load_dword v242, v251, s[50:51] sc1
s_cmp_lt_u32 s58, 0x100000
s_cbranch_scc1 POLLe5_%=
GOTe5_%=:
s_add_u32 s53, s55, 114688
buffer_load_dwordx4 a[40:43], v241, s[40:43], s53 offen offset:0 sc1
buffer_load_dwordx4 a[44:47], v241, s[40:43], s53 offen offset:1024 sc1
buffer_load_dwordx4 a[48:51], v241, s[40:43], s53 offen offset:2048 sc1
buffer_load_dwordx4 a[52:55], v241, s[40:43], s53 offen offset:3072 sc1
s_add_u32 s53, s55, 118784
buffer_load_dwordx4 a[56:59], v241, s[40:43], s53 offen offset:0 sc1
Mvva 80,48,20
Mvva 100,50,20
Mvva 84,48,24
Mvva 104,50,24
Mvva 88,48,28
Mvva 108,50,28
Mvva 92,48,32
Mvva 112,50,32
Mvva 96,48,36
Mvva 116,50,36
s_waitcnt vmcnt(0)
Mvva 80,48,40
Mvva 100,50,40
Mvva 84,48,44
Mvva 104,50,44
Mvva 88,48,48
Mvva 108,50,48
Mvva 92,48,52
Mvva 112,50,52
Mvva 96,48,56
Mvva 116,50,56
Pk 160,80,81
Pk 161,82,83
Pk 162,100,101
Pk 163,102,103
buffer_store_dwordx4 v[160:163], v250, s[88:91], 0 offen sc1
v_add_u32 v250, 32768, v250
Pk 164,84,85
Pk 165,86,87
Pk 166,104,105
Pk 167,106,107
buffer_store_dwordx4 v[164:167], v250, s[88:91], 0 offen sc1
v_add_u32 v250, 32768, v250
Pk 168,88,89
Pk 169,90,91
Pk 170,108,109
Pk 171,110,111
buffer_store_dwordx4 v[168:171], v250, s[88:91], 0 offen sc1
v_add_u32 v250, 32768, v250
Pk 172,92,93
Pk 173,94,95
Pk 174,112,113
Pk 175,114,115
buffer_store_dwordx4 v[172:175], v250, s[88:91], 0 offen sc1
v_add_u32 v250, 32768, v250
Pk 176,96,97
Pk 177,98,99
Pk 178,116,117
Pk 179,118,119
buffer_store_dwordx4 v[176:179], v250, s[88:91], 0 offen sc1
v_add_u32 v250, 32768, v250
s_endpgm
SPINP1e5_%=:
s_sleep 4
v_lshlrev_b32 v242, 2, v245
global_load_dword v242, v242, s[50:51] sc1
s_waitcnt vmcnt(0)
v_cmp_gt_i32 s[64:65], 0, v242
v_and_b32 v242, 0x7fffffff, v242
v_cmp_ne_u32 vcc, s81, v242
s_nop 0
s_and_b64 vcc, vcc, 0xf00
s_cbranch_scc0 OKP1e5_%=
s_add_u32 s58, s58, 1
s_cmp_lt_u32 s58, 0x80000
s_cbranch_scc1 SPINP1e5_%=
s_branch OKP1e5_%=
SPINP3e5_%=:
s_sleep 4
v_lshlrev_b32 v242, 2, v245
global_load_dword v242, v242, s[50:51] sc1
s_waitcnt vmcnt(0)
v_cmp_gt_i32 s[64:65], 0, v242
v_and_b32 v242, 0x7fffffff, v242
v_cmp_ne_u32 vcc, s81, v242
s_nop 0
s_and_b64 vcc, vcc, s[70:71]
s_cbranch_scc0 OKP3e5_%=
s_add_u32 s58, s58, 1
s_cmp_lt_u32 s58, 0x80000
s_cbranch_scc1 SPINP3e5_%=
s_branch OKP3e5_%=
SPINP2e5_%=:
s_sleep 1
v_lshlrev_b32 v242, 2, v245
global_load_dword v242, v242, s[50:51] sc1
s_waitcnt vmcnt(0)
v_cmp_gt_i32 s[64:65], 0, v242
v_and_b32 v242, 0x7fffffff, v242
v_cmp_ne_u32 vcc, s81, v242
s_nop 0
s_and_b64 vcc, vcc, s[34:35]
s_cbranch_scc0 OKP2e5_%=
s_add_u32 s58, s58, 1
s_cmp_lt_u32 s58, 0x80000
s_cbranch_scc1 SPINP2e5_%=
s_branch OKP2e5_%=
PBO_%=:
v_and_b32 v243, 3, v245
v_lshl_add_u32 v243, v243, 10, s59
v_writelane_b32 v243, s77, 4
v_writelane_b32 v243, s78, 5
v_writelane_b32 v243, s80, 6
global_load_dword v244, v243, s[92:93] sc1
Zaav 0,192,160
Maav 0,196,164
s_mov_b32 s57, 53248
Ta 224,255,512
Maav 0,200,168
Ta 226,255,4640
buffer_store_dwordx4 v[52:55], v241, s[96:99], s57 offen offset:1024 sc1
Maav 0,204,172
Ta 228,255,8768
Zaav 4,204,188
Ta 230,255,12896
Maav 4,200,184
Ta 232,240,512
Maav 4,196,180
Ta 234,240,4640
Maav 4,192,176
Ta 236,240,8768
Zaav 8,192,192
s_mov_b32 m0, 49536
s_add_u32 s79, s82, 4294918144
Ta 238,240,12896
Maav 8,196,196
Ta 240,255,520
buffer_load_dwordx4 v248, s[72:75], s79 offen offset:0 lds
Maav 8,200,200
Ta 242,255,4648
Maav 8,204,204
Ta 244,255,8776
buffer_load_dwordx4 v249, s[72:75], s79 offen offset:1032 lds
Zaav 12,204,220
Ta 246,255,12904
Maav 12,200,216
Ta 248,240,520
Maav 12,196,212
Ta 250,240,4648
buffer_load_dwordx4 v250, s[72:75], s79 offen offset:2064 lds
Maav 12,192,208
Ta 252,240,8776
Zaav 20,208,160
Ta 254,240,12904
buffer_load_dwordx4 v251, s[72:75], s79 offen offset:3096 lds
Maav 20,212,164
Maav 20,216,168
s_mov_b32 m0, 53664
s_add_u32 s79, s82, 4294934528
Maav 20,220,172
buffer_load_dwordx4 v248, s[72:75], s79 offen offset:0 lds
Zaav 24,220,188
Maav 24,216,184
buffer_load_dwordx4 v249, s[72:75], s79 offen offset:1032 lds
Maav 24,212,180
Maav 24,208,176
Zaav 28,208,192
buffer_load_dwordx4 v250, s[72:75], s79 offen offset:2064 lds
Maav 28,212,196
Maav 28,216,200
buffer_load_dwordx4 v251, s[72:75], s79 offen offset:3096 lds
Maav 28,220,204
Zaav 32,220,220
s_mov_b32 s57, 4096
Maav 32,216,216
buffer_store_dwordx4 v[8:11], v241, s[96:99], s57 offen offset:2048 sc1
Maav 32,212,212
Maav 32,208,208
Wl 0
Zaav 40,224,160
Maav 40,228,164
Ta 192,255,768
Maav 40,232,168
Ta 194,255,4896
Maav 40,236,172
s_add_u32 s53, s17, 0
Ta 196,255,9024
Zaav 44,236,188
Ta 198,255,13152
buffer_load_dwordx4 a[80:83], v241, s[84:87], s53 offen offset:0 sc1
Maav 44,232,184
Ta 200,240,768
Maav 44,228,180
Ta 202,240,4896
Maav 44,224,176
Ta 204,240,9024
buffer_load_dwordx4 a[84:87], v241, s[84:87], s53 offen offset:1024 sc1
Zaav 48,224,192
Ta 206,240,13152
Maav 48,228,196
Ta 208,255,776
buffer_load_dwordx4 a[88:91], v241, s[84:87], s53 offen offset:2048 sc1
Maav 48,232,200
Ta 210,255,4904
Maav 48,236,204
Ta 212,255,9032
Zaav 52,236,220
Ta 214,255,13160
buffer_load_dwordx4 a[92:95], v241, s[84:87], s53 offen offset:3072 sc1
Maav 52,232,216
s_mov_b32 s57, 20480
Ta 216,240,776
Maav 52,228,212
Ta 218,240,4904
buffer_store_dwordx4 v[24:27], v241, s[96:99], s57 offen offset:2048 sc1
Maav 52,224,208
Ta 220,240,9032
Zaav 60,240,160
Ta 222,240,13160
Maav 60,244,164
Maav 60,248,168
Maav 60,252,172
Zaav 64,252,188
s_mov_b32 m0, 57792
s_add_u32 s79, s82, 4294950912
Maav 64,248,184
buffer_load_dwordx4 v248, s[72:75], s79 offen offset:0 lds
Maav 64,244,180
Maav 64,240,176
Zaav 68,240,192
buffer_load_dwordx4 v249, s[72:75], s79 offen offset:1032 lds
Maav 68,244,196
Maav 68,248,200
buffer_load_dwordx4 v250, s[72:75], s79 offen offset:2064 lds
Maav 68,252,204
Zaav 72,252,220
Maav 72,248,216
buffer_load_dwordx4 v251, s[72:75], s79 offen offset:3096 lds
Maav 72,244,212
s_mov_b32 s57, 36864
Maav 72,240,208
Wl 0
buffer_store_dwordx4 v[40:43], v241, s[96:99], s57 offen offset:2048 sc1
Zvav 120,192,160
Mvav 120,196,164
Ta 224,255,0
Mvav 120,200,168
Ta 226,255,4128
Mvav 120,204,172
Ta 228,255,8256
Zvav 124,204,188
Ta 230,255,12384
)A"
R"A(Mvav 124,200,184
Ta 232,240,0
Mvav 124,196,180
s_add_u32 s53, s17, 16384
Ta 234,240,4128
Mvav 124,192,176
Ta 236,240,8256
buffer_load_dwordx4 a[96:99], v241, s[84:87], s53 offen offset:0 sc1
Zvav 128,192,192
Ta 238,240,12384
Mvav 128,196,196
Ta 240,255,8
buffer_load_dwordx4 a[100:103], v241, s[84:87], s53 offen offset:1024 sc1
Mvav 128,200,200
Ta 242,255,4136
Mvav 128,204,204
Ta 244,255,8264
Zvav 132,204,220
Ta 246,255,12392
buffer_load_dwordx4 a[104:107], v241, s[84:87], s53 offen offset:2048 sc1
Mvav 132,200,216
Ta 248,240,8
Mvav 132,196,212
Ta 250,240,4136
buffer_load_dwordx4 a[108:111], v241, s[84:87], s53 offen offset:3072 sc1
Mvav 132,192,208
Ta 252,240,8264
Zvav 140,208,160
s_mov_b32 s57, 53248
Ta 254,240,12392
Mvav 140,212,164
buffer_store_dwordx4 v[56:59], v241, s[96:99], s57 offen offset:2048 sc1
Mvav 140,216,168
Mvav 140,220,172
Zvav 144,220,188
Mvav 144,216,184
Mvav 144,212,180
Mvav 144,208,176
s_mov_b32 m0, 61920
s_add_u32 s79, s82, 0
Zvav 148,208,192
buffer_load_dwordx4 v248, s[72:75], s79 offen offset:0 lds
Mvav 148,212,196
Mvav 148,216,200
buffer_load_dwordx4 v249, s[72:75], s79 offen offset:1032 lds
Mvav 148,220,204
Zvav 152,220,220
Mvav 152,216,216
buffer_load_dwordx4 v250, s[72:75], s79 offen offset:2064 lds
Mvav 152,212,212
Mvav 152,208,208
Wl 0
buffer_load_dwordx4 v251, s[72:75], s79 offen offset:3096 lds
Zvav 80,224,160
s_mov_b64 exec, 0
s_swappc_b64 s[12:13], s[14:15]
s_add_u32 s14, s14, 4096
s_addc_u32 s15, s15, 0
s_barrier
Mvav 80,228,164
s_mov_b32 m0, 115584
s_add_u32 s79, s82, 16384
Ta 192,240,16768
Mvav 80,232,168
Ta 194,240,20896
buffer_load_dwordx4 v248, s[72:75], s79 offen offset:0 lds
Mvav 80,236,172
Ta 196,240,25024
Zvav 84,236,188
Ta 198,240,29152
buffer_load_dwordx4 v249, s[72:75], s79 offen offset:1032 lds
Mvav 84,232,184
Ta 200,255,16768
Mvav 84,228,180
Ta 202,255,20896
Mvav 84,224,176
Ta 204,255,25024
buffer_load_dwordx4 v250, s[72:75], s79 offen offset:2064 lds
Zvav 88,224,192
Ta 206,255,29152
Mvav 88,228,196
Ta 208,240,16776
buffer_load_dwordx4 v251, s[72:75], s79 offen offset:3096 lds
Mvav 88,232,200
Ta 210,240,20904
Mvav 88,236,204
s_mov_b32 m0, 119712
s_add_u32 s79, s82, 32768
Ta 212,240,25032
Zvav 92,236,220
Ta 214,240,29160
buffer_load_dwordx4 v248, s[72:75], s79 offen offset:0 lds
Mvav 92,232,216
Ta 216,255,16776
Mvav 92,228,212
Ta 218,255,20904
buffer_load_dwordx4 v249, s[72:75], s79 offen offset:1032 lds
Mvav 92,224,208
Ta 220,255,25032
Zvav 100,240,160
Ta 222,255,29160
Mvav 100,244,164
buffer_load_dwordx4 v250, s[72:75], s79 offen offset:2064 lds
Mvav 100,248,168
Mvav 100,252,172
buffer_load_dwordx4 v251, s[72:75], s79 offen offset:3096 lds
Zvav 104,252,188
Mvav 104,248,184
s_mov_b32 s57, 4096
Mvav 104,244,180
buffer_store_dwordx4 v[12:15], v241, s[96:99], s57 offen offset:3072 sc1
Mvav 104,240,176
Zvav 108,240,192
Mvav 108,244,196
Mvav 108,248,200
Mvav 108,252,204
Zvav 112,252,220
s_add_u32 s53, s17, 32768
Mvav 112,248,216
buffer_load_dwordx4 a[112:115], v241, s[84:87], s53 offen offset:0 sc1
Mvav 112,244,212
Mvav 112,240,208
Wl 0
Maav 0,192,0
buffer_load_dwordx4 a[116:119], v241, s[84:87], s53 offen offset:1024 sc1
Maav 0,196,4
Ta 224,240,17024
Maav 0,200,8
Ta 226,240,21152
buffer_load_dwordx4 a[120:123], v241, s[84:87], s53 offen offset:2048 sc1
Maav 0,204,12
Ta 228,240,25280
Maav 4,204,28
Ta 230,240,29408
Maav 4,200,24
Ta 232,255,17024
buffer_load_dwordx4 a[124:127], v241, s[84:87], s53 offen offset:3072 sc1
Maav 4,196,20
s_mov_b32 s57, 20480
Ta 234,255,21152
Maav 4,192,16
Ta 236,255,25280
buffer_store_dwordx4 v[28:31], v241, s[96:99], s57 offen offset:3072 sc1
Maav 8,192,32
Ta 238,255,29408
Maav 8,196,36
Ta 240,240,17032
Maav 8,200,40
Ta 242,240,21160
Maav 8,204,44
Ta 244,240,25288
Maav 12,204,60
Ta 246,240,29416
Maav 12,200,56
s_mov_b32 m0, 123840
s_add_u32 s79, s82, 49152
Ta 248,255,17032
Maav 12,196,52
Ta 250,255,21160
buffer_load_dwordx4 v248, s[72:75], s79 offen offset:0 lds
Maav 12,192,48
Ta 252,255,25288
Maav 20,208,0
Ta 254,255,29416
Maav 20,212,4
buffer_load_dwordx4 v249, s[72:75], s79 offen offset:1032 lds
Maav 20,216,8
Maav 20,220,12
buffer_load_dwordx4 v250, s[72:75], s79 offen offset:2064 lds
Maav 24,220,28
Maav 24,216,24
Maav 24,212,20
buffer_load_dwordx4 v251, s[72:75], s79 offen offset:3096 lds
Maav 24,208,16
s_mov_b32 s57, 36864
Maav 28,208,32
buffer_store_dwordx4 v[44:47], v241, s[96:99], s57 offen offset:3072 sc1
Maav 28,212,36
Maav 28,216,40
Maav 28,220,44
Maav 32,220,60
Maav 32,216,56
Maav 32,212,52
Maav 32,208,48
s_mov_b32 m0, 127968
s_add_u32 s79, s82, 65536
Wl 0
Maav 40,224,0
buffer_load_dwordx4 v248, s[72:75], s79 offen offset:0 lds
Maav 40,228,4
Ta 192,240,17280
Maav 40,232,8
Ta 194,240,21408
buffer_load_dwordx4 v249, s[72:75], s79 offen offset:1032 lds
Maav 40,236,12
Ta 196,240,25536
Maav 44,236,28
Ta 198,240,29664
Maav 44,232,24
Ta 200,255,17280
buffer_load_dwordx4 v250, s[72:75], s79 offen offset:2064 lds
Maav 44,228,20
Ta 202,255,21408
Maav 44,224,16
Ta 204,255,25536
buffer_load_dwordx4 v251, s[72:75], s79 offen offset:3096 lds
Maav 48,224,32
Ta 206,255,29664
Maav 48,228,36
s_mov_b32 s57, 53248
Ta 208,240,17288
Maav 48,232,40
Ta 210,240,21416
buffer_store_dwordx4 v[60:63], v241, s[96:99], s57 offen offset:3072 sc1
Maav 48,236,44
Ta 212,240,25544
Maav 52,236,60
Ta 214,240,29672
Maav 52,232,56
Ta 216,255,17288
Maav 52,228,52
Ta 218,255,21416
Maav 52,224,48
Ta 220,255,25544
Maav 60,240,0
s_add_u32 s53, s17, 49152
Ta 222,255,29672
Maav 60,244,4
buffer_load_dwordx4 a[128:131], v241, s[84:87], s53 offen offset:0 sc1
Maav 60,248,8
Maav 60,252,12
buffer_load_dwordx4 a[132:135], v241, s[84:87], s53 offen offset:1024 sc1
Maav 64,252,28
Maav 64,248,24
Maav 64,244,20
buffer_load_dwordx4 a[136:139], v241, s[84:87], s53 offen offset:2048 sc1
Maav 64,240,16
Maav 68,240,32
buffer_load_dwordx4 a[140:143], v241, s[84:87], s53 offen offset:3072 sc1
Maav 68,244,36
Maav 68,248,40
s_add_u32 s53, s18, 0
Maav 68,252,44
buffer_load_dwordx4 v[160:163], v241, s[84:87], s53 offen offset:0 sc1
Maav 72,252,60
Maav 72,248,56
buffer_load_dwordx4 v[164:167], v241, s[84:87], s53 offen offset:1024 sc1
Maav 72,244,52
Maav 72,240,48
Wl 0
Mvav 120,192,0
buffer_load_dwordx4 v[168:171], v241, s[84:87], s53 offen offset:2048 sc1
Mvav 120,196,4
Ta 224,240,16512
Mvav 120,200,8
Ta 226,240,20640
buffer_load_dwordx4 v[172:175], v241, s[84:87], s53 offen offset:3072 sc1
Mvav 120,204,12
Ta 228,240,24768
Mvav 124,204,28
s_add_u32 s53, s18, 16384
Ta 230,240,28896
Mvav 124,200,24
Ta 232,255,16512
buffer_load_dwordx4 v[176:179], v241, s[84:87], s53 offen offset:0 sc1
Mvav 124,196,20
Ta 234,255,20640
Mvav 124,192,16
Ta 236,255,24768
buffer_load_dwordx4 v[180:183], v241, s[84:87], s53 offen offset:1024 sc1
Mvav 128,192,32
Ta 238,255,28896
Mvav 128,196,36
Ta 240,240,16520
Mvav 128,200,40
Ta 242,240,20648
buffer_load_dwordx4 v[184:187], v241, s[84:87], s53 offen offset:2048 sc1
Mvav 128,204,44
Ta 244,240,24776
Mvav 132,204,60
Ta 246,240,28904
buffer_load_dwordx4 v[188:191], v241, s[84:87], s53 offen offset:3072 sc1
Mvav 132,200,56
Ta 248,255,16520
Mvav 132,196,52
s_add_u32 s53, s18, 32768
Ta 250,255,20648
Mvav 132,192,48
Ta 252,255,24776
buffer_load_dwordx4 v[192:195], v241, s[84:87], s53 offen offset:0 sc1
Mvav 140,208,0
Ta 254,255,28904
Mvav 140,212,4
buffer_load_dwordx4 v[196:199], v241, s[84:87], s53 offen offset:1024 sc1
Mvav 140,216,8
Mvav 140,220,12
Mvav 144,220,28
buffer_load_dwordx4 v[200:203], v241, s[84:87], s53 offen offset:2048 sc1
Mvav 144,216,24
Mvav 144,212,20
buffer_load_dwordx4 v[204:207], v241, s[84:87], s53 offen offset:3072 sc1
Mvav 144,208,16
Mvav 148,208,32
s_add_u32 s53, s18, 49152
Mvav 148,212,36
buffer_load_dwordx4 v[208:211], v241, s[84:87], s53 offen offset:0 sc1
Mvav 148,216,40
Mvav 148,220,44
buffer_load_dwordx4 v[212:215], v241, s[84:87], s53 offen offset:1024 sc1
Mvav 152,220,60
Mvav 152,216,56
Mvav 152,212,52
buffer_load_dwordx4 v[216:219], v241, s[84:87], s53 offen offset:2048 sc1
Mvav 152,208,48
Wl 0
Mvav 80,224,0
s_mov_b64 exec, 0
s_swappc_b64 s[12:13], s[14:15]
s_add_u32 s14, s14, 4096
s_addc_u32 s15, s15, 0
s_barrier
buffer_load_dwordx4 v[220:223], v241, s[84:87], s53 offen offset:3072 sc1
Mvav 80,228,4
s_waitcnt vmcnt(20)
Ta 192,240,33280
v_xor_b32 v244, s66, v244
Mvav 80,232,8
s_mov_b32 m0, 66048
s_add_u32 s79, s16, 81920
Ta 194,240,37408
global_store_dword v243, v244, s[94:95] offset:0 sc1
v_readlane_b32 s56, v244, 5
Mvav 80,236,12
s_bitset0_b32 s56, 31
s_lshl_b32 s58, s22, 18
s_or_b32 s56, s56, s58
Ta 196,240,41536
buffer_load_dwordx4 v248, s[72:75], s79 offen offset:0 lds
Mvav 84,236,28
Ta 198,240,45664
v_writelane_b32 v244, s56, 5
Mvav 84,232,24
Ta 200,255,33280
buffer_load_dwordx4 v249, s[72:75], s79 offen offset:1032 lds
global_store_dword v243, v244, s[94:95] offset:16 sc1
Mvav 84,228,20
Ta 202,255,37408
Mvav 84,224,16
Ta 204,255,41536
Mvav 88,224,32
Ta 206,255,45664
buffer_load_dwordx4 v250, s[72:75], s79 offen offset:2064 lds
Mvav 88,228,36
Ta 208,240,33288
Mvav 88,232,40
Ta 210,240,37416
buffer_load_dwordx4 v251, s[72:75], s79 offen offset:3096 lds
Mvav 88,236,44
Ta 212,240,41544
Mvav 92,236,60
s_mov_b32 m0, 70176
s_add_u32 s79, s16, 98304
Ta 214,240,45672
Mvav 92,232,56
Ta 216,255,33288
buffer_load_dwordx4 v248, s[72:75], s79 offen offset:0 lds
Mvav 92,228,52
Ta 218,255,37416
Mvav 92,224,48
Ta 220,255,41544
buffer_load_dwordx4 v249, s[72:75], s79 offen offset:1032 lds
Mvav 100,240,0
Ta 222,255,45672
Mvav 100,244,4
Mvav 100,248,8
buffer_load_dwordx4 v250, s[72:75], s79 offen offset:2064 lds
Mvav 100,252,12
Mvav 104,252,28
buffer_load_dwordx4 v251, s[72:75], s79 offen offset:3096 lds
Mvav 104,248,24
Mvav 104,244,20
s_mov_b32 m0, 74304
s_add_u32 s79, s16, 114688
Mvav 104,240,16
buffer_load_dwordx4 v248, s[72:75], s79 offen offset:0 lds
Mvav 108,240,32
Mvav 108,244,36
buffer_load_dwordx4 v249, s[72:75], s79 offen offset:1032 lds
Mvav 108,248,40
Mvav 108,252,44
Mvav 112,252,60
buffer_load_dwordx4 v250, s[72:75], s79 offen offset:2064 lds
Mvav 112,248,56
Mvav 112,244,52
buffer_load_dwordx4 v251, s[72:75], s79 offen offset:3096 lds
Mvav 112,240,48
Wl 0
s_waitcnt vmcnt(30)
Maaa 0,192,80
s_mov_b32 m0, 78432
)A"
R"A(s_add_u32 s79, s16, 131072
Maaa 0,196,84
Ta 224,240,33536
buffer_load_dwordx4 v248, s[72:75], s79 offen offset:0 lds
Maaa 0,200,88
Ta 226,240,37664
Maaa 0,204,92
Ta 228,240,41792
buffer_load_dwordx4 v249, s[72:75], s79 offen offset:1032 lds
Maaa 4,204,108
Ta 230,240,45920
Maaa 4,200,104
Ta 232,255,33536
Maaa 4,196,100
Ta 234,255,37664
buffer_load_dwordx4 v250, s[72:75], s79 offen offset:2064 lds
Maaa 4,192,96
Ta 236,255,41792
Maaa 8,192,112
Ta 238,255,45920
buffer_load_dwordx4 v251, s[72:75], s79 offen offset:3096 lds
Maaa 8,196,116
Ta 240,240,33544
Maaa 8,200,120
s_mov_b32 m0, 0
s_add_u32 s79, s16, 147456
Ta 242,240,37672
Maaa 8,204,124
Ta 244,240,41800
buffer_load_dwordx4 v248, s[72:75], s79 offen offset:0 lds
Maaa 12,204,140
Ta 246,240,45928
Maaa 12,200,136
Ta 248,255,33544
buffer_load_dwordx4 v249, s[72:75], s79 offen offset:1032 lds
Maaa 12,196,132
Ta 250,255,37672
Maaa 12,192,128
Ta 252,255,41800
Maaa 20,208,80
Ta 254,255,45928
buffer_load_dwordx4 v250, s[72:75], s79 offen offset:2064 lds
Maaa 20,212,84
Maaa 20,216,88
buffer_load_dwordx4 v251, s[72:75], s79 offen offset:3096 lds
Maaa 20,220,92
Maaa 24,220,108
s_mov_b32 m0, 4128
s_add_u32 s79, s16, 163840
Maaa 24,216,104
buffer_load_dwordx4 v248, s[72:75], s79 offen offset:0 lds
Maaa 24,212,100
Maaa 24,208,96
buffer_load_dwordx4 v249, s[72:75], s79 offen offset:1032 lds
Maaa 28,208,112
Maaa 28,212,116
Maaa 28,216,120
buffer_load_dwordx4 v250, s[72:75], s79 offen offset:2064 lds
Maaa 28,220,124
Maaa 32,220,140
buffer_load_dwordx4 v251, s[72:75], s79 offen offset:3096 lds
Maaa 32,216,136
Maaa 32,212,132
s_mov_b32 m0, 8256
s_add_u32 s79, s16, 180224
Maaa 32,208,128
Wl 0
buffer_load_dwordx4 v248, s[72:75], s79 offen offset:0 lds
Maaa 40,224,80
Maaa 40,228,84
Ta 192,240,33792
buffer_load_dwordx4 v249, s[72:75], s79 offen offset:1032 lds
Maaa 40,232,88
Ta 194,240,37920
Maaa 40,236,92
Ta 196,240,42048
Maaa 44,236,108
Ta 198,240,46176
buffer_load_dwordx4 v250, s[72:75], s79 offen offset:2064 lds
Maaa 44,232,104
Ta 200,255,33792
Maaa 44,228,100
Ta 202,255,37920
buffer_load_dwordx4 v251, s[72:75], s79 offen offset:3096 lds
Maaa 44,224,96
Ta 204,255,42048
Maaa 48,224,112
s_mov_b32 m0, 12384
s_add_u32 s79, s16, 196608
Ta 206,255,46176
Maaa 48,228,116
Ta 208,240,33800
buffer_load_dwordx4 v248, s[72:75], s79 offen offset:0 lds
Maaa 48,232,120
Ta 210,240,37928
Maaa 48,236,124
Ta 212,240,42056
buffer_load_dwordx4 v249, s[72:75], s79 offen offset:1032 lds
Maaa 52,236,140
Ta 214,240,46184
Maaa 52,232,136
Ta 216,255,33800
Maaa 52,228,132
Ta 218,255,37928
buffer_load_dwordx4 v250, s[72:75], s79 offen offset:2064 lds
Maaa 52,224,128
Ta 220,255,42056
Maaa 60,240,80
Ta 222,255,46184
buffer_load_dwordx4 v251, s[72:75], s79 offen offset:3096 lds
Maaa 60,244,84
Maaa 60,248,88
s_mov_b32 m0, 16512
s_add_u32 s79, s82, 81920
Maaa 60,252,92
buffer_load_dwordx4 v248, s[72:75], s79 offen offset:0 lds
Maaa 64,252,108
Maaa 64,248,104
buffer_load_dwordx4 v249, s[72:75], s79 offen offset:1032 lds
Maaa 64,244,100
Maaa 64,240,96
Maaa 68,240,112
buffer_load_dwordx4 v250, s[72:75], s79 offen offset:2064 lds
Maaa 68,244,116
Maaa 68,248,120
buffer_load_dwordx4 v251, s[72:75], s79 offen offset:3096 lds
Maaa 68,252,124
Maaa 72,252,140
s_mov_b32 m0, 20640
s_add_u32 s79, s82, 98304
Maaa 72,248,136
buffer_load_dwordx4 v248, s[72:75], s79 offen offset:0 lds
Maaa 72,244,132
Maaa 72,240,128
Wl 0
buffer_load_dwordx4 v249, s[72:75], s79 offen offset:1032 lds
Mvaa 120,192,80
Mvaa 120,196,84
Ta 224,240,33024
Mvaa 120,200,88
Ta 226,240,37152
buffer_load_dwordx4 v250, s[72:75], s79 offen offset:2064 lds
Mvaa 120,204,92
Ta 228,240,41280
Mvaa 124,204,108
Ta 230,240,45408
buffer_load_dwordx4 v251, s[72:75], s79 offen offset:3096 lds
Mvaa 124,200,104
Ta 232,255,33024
Mvaa 124,196,100
s_mov_b32 m0, 24768
s_add_u32 s79, s82, 114688
Ta 234,255,37152
Mvaa 124,192,96
Ta 236,255,41280
buffer_load_dwordx4 v248, s[72:75], s79 offen offset:0 lds
Mvaa 128,192,112
Ta 238,255,45408
Mvaa 128,196,116
Ta 240,240,33032
buffer_load_dwordx4 v249, s[72:75], s79 offen offset:1032 lds
Mvaa 128,200,120
Ta 242,240,37160
Mvaa 128,204,124
Ta 244,240,41288
Mvaa 132,204,140
Ta 246,240,45416
buffer_load_dwordx4 v250, s[72:75], s79 offen offset:2064 lds
Mvaa 132,200,136
Ta 248,255,33032
Mvaa 132,196,132
Ta 250,255,37160
buffer_load_dwordx4 v251, s[72:75], s79 offen offset:3096 lds
Mvaa 132,192,128
Ta 252,255,41288
Mvaa 140,208,80
s_mov_b32 m0, 28896
s_add_u32 s79, s82, 131072
Ta 254,255,45416
Mvaa 140,212,84
buffer_load_dwordx4 v248, s[72:75], s79 offen offset:0 lds
Mvaa 140,216,88
Mvaa 140,220,92
buffer_load_dwordx4 v249, s[72:75], s79 offen offset:1032 lds
Mvaa 144,220,108
Mvaa 144,216,104
v_lshlrev_b32 v242, 2, v245
global_load_dword v242, v242, s[50:51] sc1
Mvaa 144,212,100
buffer_load_dwordx4 v250, s[72:75], s79 offen offset:2064 lds
Mvaa 144,208,96
Mvaa 148,208,112
buffer_load_dwordx4 v251, s[72:75], s79 offen offset:3096 lds
Mvaa 148,212,116
Mvaa 148,216,120
s_mov_b32 m0, 82560
s_add_u32 s79, s82, 147456
Mvaa 148,220,124
buffer_load_dwordx4 v248, s[72:75], s79 offen offset:0 lds
Mvaa 152,220,140
Mvaa 152,216,136
buffer_load_dwordx4 v249, s[72:75], s79 offen offset:1032 lds
Mvaa 152,212,132
Mvaa 152,208,128
Wl 0
Mvaa 80,224,80
s_mov_b64 exec, 0
s_swappc_b64 s[12:13], s[14:15]
s_add_u32 s14, s14, 4096
s_addc_u32 s15, s15, 0
s_barrier
buffer_load_dwordx4 v250, s[72:75], s79 offen offset:2064 lds
Mvaa 80,228,84
Ta 192,240,49792
Mvaa 80,232,88
Ta 194,240,53920
buffer_load_dwordx4 v251, s[72:75], s79 offen offset:3096 lds
Mvaa 80,236,92
Ta 196,240,58048
Mvaa 84,236,108
s_mov_b32 m0, 86688
s_add_u32 s79, s82, 163840
Ta 198,240,62176
Mvaa 84,232,104
Ta 200,255,49792
buffer_load_dwordx4 v248, s[72:75], s79 offen offset:0 lds
Mvaa 84,228,100
Ta 202,255,53920
Mvaa 84,224,96
Ta 204,255,58048
buffer_load_dwordx4 v249, s[72:75], s79 offen offset:1032 lds
Mvaa 88,224,112
Ta 206,255,62176
Mvaa 88,228,116
Ta 208,240,49800
Mvaa 88,232,120
Ta 210,240,53928
buffer_load_dwordx4 v250, s[72:75], s79 offen offset:2064 lds
Mvaa 88,236,124
Ta 212,240,58056
Mvaa 92,236,140
Ta 214,240,62184
buffer_load_dwordx4 v251, s[72:75], s79 offen offset:3096 lds
Mvaa 92,232,136
Ta 216,255,49800
Mvaa 92,228,132
s_mov_b32 m0, 90816
s_add_u32 s79, s82, 180224
Ta 218,255,53928
Mvaa 92,224,128
Ta 220,255,58056
buffer_load_dwordx4 v248, s[72:75], s79 offen offset:0 lds
Mvaa 100,240,80
Ta 222,255,62184
Mvaa 100,244,84
buffer_load_dwordx4 v249, s[72:75], s79 offen offset:1032 lds
Mvaa 100,248,88
Mvaa 100,252,92
Mvaa 104,252,108
buffer_load_dwordx4 v250, s[72:75], s79 offen offset:2064 lds
Mvaa 104,248,104
Mvaa 104,244,100
buffer_load_dwordx4 v251, s[72:75], s79 offen offset:3096 lds
Mvaa 104,240,96
Mvaa 108,240,112
s_mov_b32 m0, 94944
s_add_u32 s79, s82, 196608
Mvaa 108,244,116
buffer_load_dwordx4 v248, s[72:75], s79 offen offset:0 lds
Mvaa 108,248,120
Mvaa 108,252,124
buffer_load_dwordx4 v249, s[72:75], s79 offen offset:1032 lds
Mvaa 112,252,140
Mvaa 112,248,136
Mvaa 112,244,132
buffer_load_dwordx4 v250, s[72:75], s79 offen offset:2064 lds
Mvaa 112,240,128
Wl 0
s_waitcnt vmcnt(62)
Maav 0,192,160
buffer_load_dwordx4 v251, s[72:75], s79 offen offset:3096 lds
Maav 0,196,164
Ta 224,240,50048
Maav 0,200,168
s_waitcnt vmcnt(18)
s_mov_b32 s58, 0
v_cmp_gt_i32 s[64:65], 0, v242
v_and_b32 v242, 0x7fffffff, v242
v_cmp_ne_u32 vcc, s81, v242
s_nop 0
s_and_b64 vcc, vcc, 0xf00
s_cbranch_scc1 SPINP1o4_%=
OKP1o4_%=:
s_mov_b32 m0, 33024
s_add_u32 s79, s83, 81920
Ta 226,240,54176
Maav 0,204,172
Ta 228,240,58304
buffer_load_dwordx4 v248, s[72:75], s79 offen offset:0 lds
Maav 4,204,188
Ta 230,240,62432
Maav 4,200,184
Ta 232,255,50048
buffer_load_dwordx4 v249, s[72:75], s79 offen offset:1032 lds
Maav 4,196,180
Ta 234,255,54176
Maav 4,192,176
s_lshl_b32 s56, s25, 6
Ta 236,255,58304
Maav 8,192,192
s_add_u32 s56, s56, s76
Ta 238,255,62432
buffer_load_dwordx4 v250, s[72:75], s79 offen offset:2064 lds
Maav 8,196,196
s_lshl_b32 s56, s56, 17
Ta 240,240,50056
Maav 8,200,200
s_add_u32 s40, s20, s56
s_addc_u32 s41, s21, 0
Ta 242,240,54184
buffer_load_dwordx4 v251, s[72:75], s79 offen offset:3096 lds
Maav 8,204,204
s_mov_b32 s42, 7995392
Ta 244,240,58312
Maav 12,204,220
s_add_u32 s53, s17, 4096
Ta 246,240,62440
Maav 12,200,216
s_mov_b32 s98, 119808
Ta 248,255,50056
buffer_load_dwordx4 a[80:83], v241, s[84:87], s53 offen offset:0 sc1
Maav 12,196,212
s_mov_b32 s70, 0
Ta 250,255,54184
Maav 12,192,208
s_and_b32 s71, s35, 0xffff
Ta 252,255,58312
buffer_load_dwordx4 a[84:87], v241, s[84:87], s53 offen offset:1024 sc1
Maav 20,208,160
s_lshl_b32 s71, s71, 4
Ta 254,255,62440
Maav 20,212,164
s_andn2_b32 s34, s34, 0xf0
Maav 20,216,168
s_lshl_b64 s[34:35], s[34:35], 8
buffer_load_dwordx4 a[88:91], v241, s[84:87], s53 offen offset:2048 sc1
Maav 20,220,172
s_and_b32 s48, s35, 0xffff0000
Maav 24,220,188
s_and_b32 s35, s35, 0xffff
buffer_load_dwordx4 a[92:95], v241, s[84:87], s53 offen offset:3072 sc1
Maav 24,216,184
Maav 24,212,180
s_add_u32 s53, s18, 4096
Maav 24,208,176
buffer_load_dwordx4 v[0:3], v241, s[84:87], s53 offen offset:0 sc1
Maav 28,208,192
Maav 28,212,196
buffer_load_dwordx4 v[4:7], v241, s[84:87], s53 offen offset:1024 sc1
Maav 28,216,200
Maav 28,220,204
Maav 32,220,220
buffer_load_dwordx4 v[8:11], v241, s[84:87], s53 offen offset:2048 sc1
Maav 32,216,216
Maav 32,212,212
buffer_load_dwordx4 v[12:15], v241, s[84:87], s53 offen offset:3072 sc1
Maav 32,208,208
Wl 0
Maav 40,224,160
s_add_u32 s53, s17, 20480
Maav 40,228,164
Ta 192,240,50304
buffer_load_dwordx4 a[96:99], v241, s[84:87], s53 offen offset:0 sc1
Maav 40,232,168
Ta 194,240,54432
Maav 40,236,172
Ta 196,240,58560
buffer_load_dwordx4 a[100:103], v241, s[84:87], s53 offen offset:1024 sc1
Maav 44,236,188
Ta 198,240,62688
Maav 44,232,184
Ta 200,255,50304
Maav 44,228,180
Ta 202,255,54432
buffer_load_dwordx4 a[104:107], v241, s[84:87], s53 offen offset:2048 sc1
Maav 44,224,176
Ta 204,255,58560
Maav 48,224,192
Ta 206,255,62688
buffer_load_dwordx4 a[108:111], v241, s[84:87], s53 offen offset:3072 sc1
Maav 48,228,196
Ta 208,240,50312
Maav 48,232,200
s_add_u32 s53, s18, 20480
Ta 210,240,54440
Maav 48,236,204
Ta 212,240,58568
buffer_load_dwordx4 v[16:19], v241, s[84:87], s53 offen offset:0 sc1
Maav 52,236,220
Ta 214,240,62696
)A"
R"A(Maav 52,232,216
Ta 216,255,50312
buffer_load_dwordx4 v[20:23], v241, s[84:87], s53 offen offset:1024 sc1
Maav 52,228,212
Ta 218,255,54440
Maav 52,224,208
Ta 220,255,58568
Maav 60,240,160
Ta 222,255,62696
buffer_load_dwordx4 v[24:27], v241, s[84:87], s53 offen offset:2048 sc1
Maav 60,244,164
Maav 60,248,168
buffer_load_dwordx4 v[28:31], v241, s[84:87], s53 offen offset:3072 sc1
Maav 60,252,172
Maav 64,252,188
s_add_u32 s53, s17, 36864
Maav 64,248,184
buffer_load_dwordx4 a[112:115], v241, s[84:87], s53 offen offset:0 sc1
Maav 64,244,180
Maav 64,240,176
buffer_load_dwordx4 a[116:119], v241, s[84:87], s53 offen offset:1024 sc1
Maav 68,240,192
Maav 68,244,196
Maav 68,248,200
buffer_load_dwordx4 a[120:123], v241, s[84:87], s53 offen offset:2048 sc1
Maav 68,252,204
Maav 72,252,220
buffer_load_dwordx4 a[124:127], v241, s[84:87], s53 offen offset:3072 sc1
Maav 72,248,216
Maav 72,244,212
s_add_u32 s53, s18, 36864
Maav 72,240,208
Wl 0
buffer_load_dwordx4 v[32:35], v241, s[84:87], s53 offen offset:0 sc1
Mvav 120,192,160
Mvav 120,196,164
Ta 224,240,49536
buffer_load_dwordx4 v[36:39], v241, s[84:87], s53 offen offset:1024 sc1
Mvav 120,200,168
Ta 226,240,53664
Mvav 120,204,172
Ta 228,240,57792
Mvav 124,204,188
Ta 230,240,61920
buffer_load_dwordx4 v[40:43], v241, s[84:87], s53 offen offset:2048 sc1
Mvav 124,200,184
Ta 232,255,49536
Mvav 124,196,180
Ta 234,255,53664
buffer_load_dwordx4 v[44:47], v241, s[84:87], s53 offen offset:3072 sc1
Mvav 124,192,176
Ta 236,255,57792
Mvav 128,192,192
s_add_u32 s53, s17, 53248
Ta 238,255,61920
Mvav 128,196,196
Ta 240,240,49544
buffer_load_dwordx4 a[128:131], v241, s[84:87], s53 offen offset:0 sc1
Mvav 128,200,200
Ta 242,240,53672
Mvav 128,204,204
Ta 244,240,57800
buffer_load_dwordx4 a[132:135], v241, s[84:87], s53 offen offset:1024 sc1
Mvav 132,204,220
Ta 246,240,61928
Mvav 132,200,216
Ta 248,255,49544
Mvav 132,196,212
Ta 250,255,53672
buffer_load_dwordx4 a[136:139], v241, s[84:87], s53 offen offset:2048 sc1
Mvav 132,192,208
Ta 252,255,57800
Mvav 140,208,160
Ta 254,255,61928
buffer_load_dwordx4 a[140:143], v241, s[84:87], s53 offen offset:3072 sc1
Mvav 140,212,164
Mvav 140,216,168
s_mov_b32 m0, 37152
s_add_u32 s79, s83, 98304
Mvav 140,220,172
buffer_load_dwordx4 v248, s[72:75], s79 offen offset:0 lds
Mvav 144,220,188
Mvav 144,216,184
buffer_load_dwordx4 v249, s[72:75], s79 offen offset:1032 lds
Mvav 144,212,180
Mvav 144,208,176
Mvav 148,208,192
buffer_load_dwordx4 v250, s[72:75], s79 offen offset:2064 lds
Mvav 148,212,196
Mvav 148,216,200
buffer_load_dwordx4 v251, s[72:75], s79 offen offset:3096 lds
Mvav 148,220,204
Mvav 152,220,220
s_add_u32 s53, s18, 53248
Mvav 152,216,216
buffer_load_dwordx4 a[160:163], v241, s[84:87], s53 offen offset:0 sc1
Mvav 152,212,212
Mvav 152,208,208
Wl 0
buffer_load_dwordx4 a[164:167], v241, s[84:87], s53 offen offset:1024 sc1
Mvav 80,224,160
s_barrier
Mvav 80,228,164
Ta 192,255,256
Mvav 80,232,168
Ta 194,255,4384
buffer_load_dwordx4 a[168:171], v241, s[84:87], s53 offen offset:2048 sc1
Mvav 80,236,172
Ta 196,255,8512
Mvav 84,236,188
Ta 198,255,12640
buffer_load_dwordx4 a[172:175], v241, s[84:87], s53 offen offset:3072 sc1
Mvav 84,232,184
Ta 200,240,256
Mvav 84,228,180
s_mov_b32 m0, 41280
s_add_u32 s79, s83, 114688
Ta 202,240,4384
Mvav 84,224,176
Ta 204,240,8512
buffer_load_dwordx4 v248, s[72:75], s79 offen offset:0 lds
Mvav 88,224,192
Ta 206,240,12640
Mvav 88,228,196
Ta 208,255,264
buffer_load_dwordx4 v249, s[72:75], s79 offen offset:1032 lds
Mvav 88,232,200
Ta 210,255,4392
Mvav 88,236,204
Ta 212,255,8520
Mvav 92,236,220
Ta 214,255,12648
buffer_load_dwordx4 v250, s[72:75], s79 offen offset:2064 lds
Mvav 92,232,216
Ta 216,240,264
Mvav 92,228,212
Ta 218,240,4392
buffer_load_dwordx4 v251, s[72:75], s79 offen offset:3096 lds
Mvav 92,224,208
Ta 220,240,8520
Mvav 100,240,160
s_mov_b32 m0, 45408
s_add_u32 s79, s83, 131072
Ta 222,240,12648
Mvav 100,244,164
buffer_load_dwordx4 v248, s[72:75], s79 offen offset:0 lds
Mvav 100,248,168
Mvav 100,252,172
buffer_load_dwordx4 v249, s[72:75], s79 offen offset:1032 lds
Mvav 104,252,188
Mvav 104,248,184
Mvav 104,244,180
buffer_load_dwordx4 v250, s[72:75], s79 offen offset:2064 lds
Mvav 104,240,176
Mvav 108,240,192
buffer_load_dwordx4 v251, s[72:75], s79 offen offset:3096 lds
Mvav 108,244,196
Mvav 108,248,200
s_mov_b32 m0, 99072
s_add_u32 s79, s83, 147456
Mvav 108,252,204
buffer_load_dwordx4 v248, s[72:75], s79 offen offset:0 lds
Mvav 112,252,220
Mvav 112,248,216
buffer_load_dwordx4 v249, s[72:75], s79 offen offset:1032 lds
Mvav 112,244,212
Mvav 112,240,208
Wl 0
s_waitcnt vmcnt(18)
Maaa 0,192,80
buffer_load_dwordx4 v250, s[72:75], s79 offen offset:2064 lds
Maaa 0,196,84
Ta 224,255,512
Maaa 0,200,88
Ta 226,255,4640
buffer_load_dwordx4 v251, s[72:75], s79 offen offset:3096 lds
Maaa 0,204,92
Ta 228,255,8768
Maaa 4,204,108
s_mov_b32 m0, 103200
s_add_u32 s79, s83, 163840
Ta 230,255,12896
Maaa 4,200,104
Ta 232,240,512
buffer_load_dwordx4 v248, s[72:75], s79 offen offset:0 lds
Maaa 4,196,100
Ta 234,240,4640
Maaa 4,192,96
Ta 236,240,8768
buffer_load_dwordx4 v249, s[72:75], s79 offen offset:1032 lds
Maaa 8,192,112
Ta 238,240,12896
Maaa 8,196,116
Ta 240,255,520
Maaa 8,200,120
Ta 242,255,4648
buffer_load_dwordx4 v250, s[72:75], s79 offen offset:2064 lds
Maaa 8,204,124
Ta 244,255,8776
Maaa 12,204,140
Ta 246,255,12904
buffer_load_dwordx4 v251, s[72:75], s79 offen offset:3096 lds
Maaa 12,200,136
Ta 248,240,520
Maaa 12,196,132
s_mov_b32 m0, 107328
s_add_u32 s79, s83, 180224
Ta 250,240,4648
Maaa 12,192,128
Ta 252,240,8776
buffer_load_dwordx4 v248, s[72:75], s79 offen offset:0 lds
Maaa 20,208,80
Ta 254,240,12904
Maaa 20,212,84
buffer_load_dwordx4 v249, s[72:75], s79 offen offset:1032 lds
Maaa 20,216,88
Maaa 20,220,92
Maaa 24,220,108
buffer_load_dwordx4 v250, s[72:75], s79 offen offset:2064 lds
Maaa 24,216,104
Maaa 24,212,100
buffer_load_dwordx4 v251, s[72:75], s79 offen offset:3096 lds
Maaa 24,208,96
Maaa 28,208,112
s_mov_b32 m0, 111456
s_add_u32 s79, s83, 196608
Maaa 28,212,116
buffer_load_dwordx4 v248, s[72:75], s79 offen offset:0 lds
Maaa 28,216,120
Maaa 28,220,124
buffer_load_dwordx4 v249, s[72:75], s79 offen offset:1032 lds
Maaa 32,220,140
Maaa 32,216,136
Maaa 32,212,132
buffer_load_dwordx4 v250, s[72:75], s79 offen offset:2064 lds
Maaa 32,208,128
Wl 0
Maaa 40,224,80
s_barrier
buffer_load_dwordx4 v251, s[72:75], s79 offen offset:3096 lds
Maaa 40,228,84
Ta 192,240,16768
Maaa 40,232,88
s_add_u32 s53, s22, 4096
Ta 194,240,20896
Maaa 40,236,92
Ta 196,240,25024
buffer_load_dwordx4 v[160:163], v241, s[84:87], s53 offen offset:0 sc1
Maaa 44,236,108
Ta 198,240,29152
Maaa 44,232,104
Ta 200,255,16768
buffer_load_dwordx4 v[164:167], v241, s[84:87], s53 offen offset:1024 sc1
Maaa 44,228,100
Ta 202,255,20896
Maaa 44,224,96
Ta 204,255,25024
Maaa 48,224,112
Ta 206,255,29152
buffer_load_dwordx4 v[168:171], v241, s[84:87], s53 offen offset:2048 sc1
Maaa 48,228,116
Ta 208,240,16776
Maaa 48,232,120
Ta 210,240,20904
buffer_load_dwordx4 v[172:175], v241, s[84:87], s53 offen offset:3072 sc1
Maaa 48,236,124
Ta 212,240,25032
Maaa 52,236,140
s_add_u32 s53, s22, 20480
Ta 214,240,29160
Maaa 52,232,136
Ta 216,255,16776
buffer_load_dwordx4 v[176:179], v241, s[84:87], s53 offen offset:0 sc1
Maaa 52,228,132
Ta 218,255,20904
Maaa 52,224,128
Ta 220,255,25032
buffer_load_dwordx4 v[180:183], v241, s[84:87], s53 offen offset:1024 sc1
Maaa 60,240,80
Ta 222,255,29160
Maaa 60,244,84
Maaa 60,248,88
buffer_load_dwordx4 v[184:187], v241, s[84:87], s53 offen offset:2048 sc1
Maaa 60,252,92
Maaa 64,252,108
buffer_load_dwordx4 v[188:191], v241, s[84:87], s53 offen offset:3072 sc1
Maaa 64,248,104
Maaa 64,244,100
s_add_u32 s53, s22, 36864
Maaa 64,240,96
buffer_load_dwordx4 v[192:195], v241, s[84:87], s53 offen offset:0 sc1
Maaa 68,240,112
Maaa 68,244,116
buffer_load_dwordx4 v[196:199], v241, s[84:87], s53 offen offset:1024 sc1
Maaa 68,248,120
Maaa 68,252,124
Maaa 72,252,140
buffer_load_dwordx4 v[200:203], v241, s[84:87], s53 offen offset:2048 sc1
Maaa 72,248,136
Maaa 72,244,132
buffer_load_dwordx4 v[204:207], v241, s[84:87], s53 offen offset:3072 sc1
Maaa 72,240,128
Wl 0
s_waitcnt vmcnt(36)
Maav 0,192,0
s_add_u32 s53, s22, 53248
Maav 0,196,4
Ta 224,240,17024
buffer_load_dwordx4 v[208:211], v241, s[84:87], s53 offen offset:0 sc1
Maav 0,200,8
Ta 226,240,21152
Maav 0,204,12
Ta 228,240,25280
buffer_load_dwordx4 v[212:215], v241, s[84:87], s53 offen offset:1024 sc1
Maav 4,204,28
Ta 230,240,29408
Maav 4,200,24
Ta 232,255,17024
Maav 4,196,20
Ta 234,255,21152
buffer_load_dwordx4 v[216:219], v241, s[84:87], s53 offen offset:2048 sc1
Maav 4,192,16
Ta 236,255,25280
Maav 8,192,32
Ta 238,255,29408
buffer_load_dwordx4 v[220:223], v241, s[84:87], s53 offen offset:3072 sc1
Maav 8,196,36
Ta 240,240,17032
Maav 8,200,40
Ta 242,240,21160
Maav 8,204,44
Ta 244,240,25288
Maaa 12,204,172
Ta 246,240,29416
Maaa 12,200,168
Ta 248,255,17032
Maaa 12,196,164
Ta 250,255,21160
Maaa 12,192,160
Ta 252,255,25288
Maav 20,208,0
Ta 254,255,29416
Maav 20,212,4
Maav 20,216,8
Maav 20,220,12
Maav 24,220,28
Maav 24,216,24
Maav 24,212,20
Maav 24,208,16
Maav 28,208,32
Maav 28,212,36
Maav 28,216,40
Maav 28,220,44
Maaa 32,220,172
Maaa 32,216,168
Maaa 32,212,164
Maaa 32,208,160
Wl 0
Maav 40,224,0
s_waitcnt vmcnt(16)
s_barrier
Maav 40,228,4
Ta 192,240,33280
Maav 40,232,8
Ta 194,240,37408
Maav 40,236,12
Ta 196,240,41536
Maav 44,236,28
Ta 198,240,45664
Maav 44,232,24
Ta 200,255,33280
Maav 44,228,20
Ta 202,255,37408
Maav 44,224,16
Ta 204,255,41536
Maav 48,224,32
Ta 206,255,45664
Maav 48,228,36
Ta 208,240,33288
Maav 48,232,40
Ta 210,240,37416
Maav 48,236,44
Ta 212,240,41544
Maaa 52,236,172
Ta 214,240,45672
Maaa 52,232,168
Ta 216,255,33288
Maaa 52,228,164
Ta 218,255,37416
Maaa 52,224,160
Ta 220,255,41544
Maav 60,240,0
Ta 222,255,45672
Maav 60,244,4
Maav 60,248,8
Maav 60,252,12
Maav 64,252,28
Maav 64,248,24
Maav 64,244,20
Maav 64,240,16
)A"
R"A(Maav 68,240,32
Maav 68,244,36
Maav 68,248,40
Maav 68,252,44
Maaa 72,252,172
Maaa 72,248,168
Maaa 72,244,164
Maaa 72,240,160
Wl 0
s_waitcnt vmcnt(0)
Maav 0,192,160
Maav 0,196,164
Ta 224,240,33536
Maav 0,200,168
Ta 226,240,37664
Maav 0,204,172
Ta 228,240,41792
Maav 4,204,188
Ta 230,240,45920
Maav 4,200,184
Ta 232,255,33536
Maav 4,196,180
Ta 234,255,37664
Maav 4,192,176
Ta 236,255,41792
Maav 8,192,192
Ta 238,255,45920
Maav 8,196,196
Ta 240,240,33544
Maav 8,200,200
Ta 242,240,37672
Maav 8,204,204
Ta 244,240,41800
Maav 12,204,220
Ta 246,240,45928
Maav 12,200,216
Ta 248,255,33544
Maav 12,196,212
Ta 250,255,37672
Maav 12,192,208
Ta 252,255,41800
Maav 20,208,160
Ta 254,255,45928
Maav 20,212,164
Maav 20,216,168
Maav 20,220,172
Maav 24,220,188
Maav 24,216,184
Maav 24,212,180
Maav 24,208,176
Maav 28,208,192
Maav 28,212,196
Maav 28,216,200
Maav 28,220,204
Maav 32,220,220
Ar 48,0
Ar 49,1
Maav 32,216,216
Ar 50,2
Ar 51,3
Maav 32,212,212
Pk 56,48,49
Pk 57,50,51
Maav 32,208,208
Wl 0
Ar 52,20
Ar 53,21
Maav 40,224,160
Ar 54,22
Ar 55,23
Maav 40,228,164
Pk 58,52,53
Pk 59,54,55
Maav 40,232,168
s_mov_b32 s57, 81920
buffer_store_dwordx4 v[56:59], v241, s[96:99], s57 offen offset:0 sc1
Maav 40,236,172
Ar 48,4
Ar 49,5
Maav 44,236,188
Ar 50,6
Ar 51,7
Maav 44,232,184
Pk 60,48,49
Pk 61,50,51
Maav 44,228,180
Ar 52,24
Ar 53,25
Maav 44,224,176
Ar 54,26
Ar 55,27
Maav 48,224,192
Pk 62,52,53
Pk 63,54,55
Maav 48,228,196
buffer_store_dwordx4 v[60:63], v241, s[96:99], s57 offen offset:1024 sc1
Maav 48,232,200
Ar 48,8
Ar 49,9
Maav 48,236,204
Ar 50,10
Ar 51,11
Maav 52,236,220
Ta 192,255,768
Pk 64,48,49
Maav 52,232,216
Ta 194,255,4896
Pk 65,50,51
Maav 52,228,212
Ta 196,255,9024
Ar 52,28
Maav 52,224,208
Ta 198,255,13152
Ar 53,29
Maav 60,240,160
Ta 200,240,768
Ar 54,30
Maav 60,244,164
Ta 202,240,4896
Ar 55,31
Maav 60,248,168
Ta 204,240,9024
Pk 66,52,53
Maav 60,252,172
Ta 206,240,13152
Pk 67,54,55
Maav 64,252,188
Ta 208,255,776
buffer_store_dwordx4 v[64:67], v241, s[96:99], s57 offen offset:2048 sc1
Maav 64,248,184
Ta 210,255,4904
Ar 48,12
Maav 64,244,180
Ta 212,255,9032
Ar 49,13
Maav 64,240,176
Ta 214,255,13160
Ar 50,14
Maav 68,240,192
Ta 216,240,776
Ar 51,15
Maav 68,244,196
Ta 218,240,4904
Pk 68,48,49
Maav 68,248,200
Ta 220,240,9032
Pk 69,50,51
Maav 68,252,204
Ta 222,240,13160
Ar 52,32
Maav 72,252,220
Ar 53,33
Ar 54,34
Maav 72,248,216
Ar 55,35
Pk 70,52,53
Maav 72,244,212
Pk 71,54,55
Maav 72,240,208
Wl 0
buffer_store_dwordx4 v[68:71], v241, s[96:99], s57 offen offset:3072 sc1
Mvaa 120,192,80
Ar 48,40
Ar 49,41
Mvaa 120,196,84
Ar 50,42
Ar 51,43
Mvaa 120,200,88
Pk 56,48,49
Pk 57,50,51
Mvaa 120,204,92
Ar 52,60
Ar 53,61
Mvaa 124,204,108
Ar 54,62
Ar 55,63
Mvaa 124,200,104
Pk 58,52,53
Pk 59,54,55
Mvaa 124,196,100
s_mov_b32 s57, 98304
buffer_store_dwordx4 v[56:59], v241, s[96:99], s57 offen offset:0 sc1
Mvaa 124,192,96
Ar 48,44
Ar 49,45
Mvaa 128,192,112
Ar 50,46
Ar 51,47
Mvaa 128,196,116
Pk 60,48,49
Pk 61,50,51
Mvaa 128,200,120
Ar 52,64
Ar 53,65
Mvaa 128,204,124
Ar 54,66
Ar 55,67
Mvaa 132,204,140
Ta 224,240,17280
Pk 62,52,53
Mvaa 132,200,136
Ta 226,240,21408
Pk 63,54,55
Mvaa 132,196,132
Ta 228,240,25536
buffer_store_dwordx4 v[60:63], v241, s[96:99], s57 offen offset:1024 sc1
Mvaa 132,192,128
Ta 230,240,29664
Ar 48,48
Mvaa 140,208,80
Ta 232,255,17280
Ar 49,49
Mvaa 140,212,84
Ta 234,255,21408
Ar 50,50
Mvaa 140,216,88
Ta 236,255,25536
Ar 51,51
Mvaa 140,220,92
Ta 238,255,29664
Pk 64,48,49
Mvaa 144,220,108
Ta 240,240,17288
Pk 65,50,51
Mvaa 144,216,104
Ta 242,240,21416
Ar 52,68
Mvaa 144,212,100
Ta 244,240,25544
Ar 53,69
Mvaa 144,208,96
Ta 246,240,29672
Ar 54,70
Mvaa 148,208,112
Ta 248,255,17288
Ar 55,71
Mvaa 148,212,116
Ta 250,255,21416
Pk 66,52,53
Mvaa 148,216,120
Ta 252,255,25544
Pk 67,54,55
Mvaa 148,220,124
Ta 254,255,29672
buffer_store_dwordx4 v[64:67], v241, s[96:99], s57 offen offset:2048 sc1
Mvaa 152,220,140
Ar 48,52
Ar 49,53
Mvaa 152,216,136
Ar 50,54
Ar 51,55
Mvaa 152,212,132
Pk 68,48,49
Pk 69,50,51
Mvaa 152,208,128
Wl 0
Ar 52,72
Ar 53,73
Mvav 120,224,0
Ar 54,74
Ar 55,75
Mvav 120,228,4
Ta 192,240,33792
Pk 70,52,53
Mvav 120,232,8
Ta 194,240,37920
Pk 71,54,55
Mvav 120,236,12
Ta 196,240,42048
buffer_store_dwordx4 v[68:71], v241, s[96:99], s57 offen offset:3072 sc1
Mvav 124,236,28
Ta 198,240,46176
Mvav 124,232,24
Ta 200,255,33792
v_bfe_u32 v76, v245, 2, 2
Mvav 124,228,20
Ta 202,255,37920
v_lshrrev_b32 v77, 4, v245
Mvav 124,224,16
Ta 204,255,42048
v_cmp_eq_u32 vcc, v76, v77
Mvav 128,224,32
Ta 206,255,46176
v_and_b32 v76, 1, v245
Mvav 128,228,36
Ta 208,240,33800
v_lshlrev_b32 v76, 4, v76
Mvav 128,232,40
Ta 210,240,37928
v_mov_b32 v77, 0x3f80
Mvav 128,236,44
Ta 212,240,42056
v_lshlrev_b32 v77, v76, v77
Mvaa 132,236,172
Ta 214,240,46184
v_cndmask_b32 v77, 0, v77, vcc
Mvaa 132,232,168
Ta 216,255,33800
v_and_b32 v76, 2, v245
Mvaa 132,228,164
Ta 218,255,37928
v_cmp_eq_u32 vcc, 0, v76
Mvaa 132,224,160
Ta 220,255,42056
v_mov_b32 v50, 0
Mvav 140,240,0
Ta 222,255,46184
v_mov_b32 v51, 0
Mvav 140,244,4
v_cndmask_b32 v48, 0, v77, vcc
v_sub_u32 v49, v77, v48
Mvav 140,248,8
v_mov_b32 v52, v48
v_mov_b32 v53, v49
Mvav 140,252,12
v_and_b32 v250, 15, v245
v_lshlrev_b32 v250, 11, v250
Mvav 144,252,28
v_lshrrev_b32 v251, 4, v245
v_lshl_add_u32 v250, v251, 4, v250
Mvav 144,248,24
v_lshlrev_b32 v251, 2, v245
Mvav 144,244,20
Mvav 144,240,16
Mvav 148,240,32
Mvav 148,244,36
Mvav 148,248,40
Mvav 148,252,44
Mvaa 152,252,172
Mvaa 152,248,168
Mvaa 152,244,164
Mvaa 152,240,160
Wl 0
Mvav 120,192,160
Mvav 120,196,164
Ta 224,255,0
Mvav 120,200,168
Ta 226,255,4128
Mvav 120,204,172
Ta 228,255,8256
Mvav 124,204,188
Ta 230,255,12384
Mvav 124,200,184
Ta 232,240,0
Mvav 124,196,180
Ta 234,240,4128
Mvav 124,192,176
Ta 236,240,8256
Mvav 128,192,192
Ta 238,240,12384
Mvav 128,196,196
Ta 240,255,8
Mvav 128,200,200
Ta 242,255,4136
Mvav 128,204,204
Ta 244,255,8264
Mvav 132,204,220
Ta 246,255,12392
Mvav 132,200,216
Ta 248,240,8
Mvav 132,196,212
Ta 250,240,4136
Mvav 132,192,208
Ta 252,240,8264
Mvav 140,208,160
Ta 254,240,12392
Mvav 140,212,164
Mvav 140,216,168
Mvav 140,220,172
Mvav 144,220,188
Mvav 144,216,184
s_waitcnt vmcnt(0)
s_mov_b64 exec, 0x30
global_store_dword v243, v244, s[94:95] offset:32 sc1
s_mov_b64 exec, -1
Mvav 144,212,180
Mvav 144,208,176
Mvav 148,208,192
Mvav 148,212,196
Mvav 148,216,200
Mvav 148,220,204
Mvav 152,220,220
Pk 56,120,121
Pk 57,122,123
Mvav 152,216,216
Pk 58,140,141
Pk 59,142,143
Mvav 152,212,212
s_mov_b32 s57, 114688
buffer_store_dwordx4 v[56:59], v241, s[96:99], s57 offen offset:0 sc1
Mvav 152,208,208
Wl 0
Mvaa 80,224,80
Pk 60,124,125
Pk 61,126,127
Mvaa 80,228,84
Pk 62,144,145
Pk 63,146,147
Mvaa 80,232,88
buffer_store_dwordx4 v[60:63], v241, s[96:99], s57 offen offset:1024 sc1
)A"
R"A(Mvaa 80,236,92
Mvaa 84,236,108
Pk 64,128,129
Pk 65,130,131
Mvaa 84,232,104
Pk 66,148,149
Pk 67,150,151
Mvaa 84,228,100
buffer_store_dwordx4 v[64:67], v241, s[96:99], s57 offen offset:2048 sc1
Mvaa 84,224,96
Mvaa 88,224,112
Pk 68,132,133
Pk 69,134,135
Mvaa 88,228,116
Pk 70,152,153
Pk 71,154,155
Mvaa 88,232,120
buffer_store_dwordx4 v[68:71], v241, s[96:99], s57 offen offset:3072 sc1
Mvaa 88,236,124
Mvaa 92,236,140
Ta 192,240,16512
Mvaa 92,232,136
Ta 194,240,20640
Mvaa 92,228,132
Ta 196,240,24768
Mvaa 92,224,128
Ta 198,240,28896
Mvaa 100,240,80
Ta 200,255,16512
Mvaa 100,244,84
Ta 202,255,20640
Mvaa 100,248,88
Ta 204,255,24768
Mvaa 100,252,92
Ta 206,255,28896
Mvaa 104,252,108
Ta 208,240,16520
Mvaa 104,248,104
Ta 210,240,20648
Mvaa 104,244,100
Ta 212,240,24776
v_lshlrev_b32 v242, 2, v245
global_load_dword v242, v242, s[50:51] sc1
Mvaa 104,240,96
Ta 214,240,28904
Mvaa 108,240,112
Ta 216,255,16520
Mvaa 108,244,116
Ta 218,255,20648
Mvaa 108,248,120
Ta 220,255,24776
Mvaa 108,252,124
Ta 222,255,28904
Mvaa 112,252,140
Mvaa 112,248,136
Mvaa 112,244,132
Mvaa 112,240,128
Wl 0
Mvav 80,192,0
Mvav 80,196,4
Ta 224,240,33024
Mvav 80,200,8
Ta 226,240,37152
Mvav 80,204,12
Ta 228,240,41280
Mvav 84,204,28
Ta 230,240,45408
Mvav 84,200,24
Ta 232,255,33024
Mvav 84,196,20
Ta 234,255,37152
Mvav 84,192,16
Ta 236,255,41280
Mvav 88,192,32
Ta 238,255,45408
Mvav 88,196,36
Ta 240,240,33032
Mvav 88,200,40
Ta 242,240,37160
Mvav 88,204,44
Ta 244,240,41288
Mvaa 92,204,172
Ta 246,240,45416
Mvaa 92,200,168
Ta 248,255,33032
Mvaa 92,196,164
Ta 250,255,37160
Mvaa 92,192,160
Ta 252,255,41288
Mvav 100,208,0
Ta 254,255,45416
Mvav 100,212,4
Mvav 100,216,8
s_waitcnt vmcnt(1)
s_mov_b64 exec, 0x40
global_store_dword v243, v244, s[94:95] offset:32 sc1
s_mov_b64 exec, -1
Mvav 100,220,12
Mvav 104,220,28
Mvav 104,216,24
Mvav 104,212,20
s_waitcnt vmcnt(1)
s_mov_b32 s58, 0
v_cmp_gt_i32 s[64:65], 0, v242
v_and_b32 v242, 0x7fffffff, v242
v_cmp_ne_u32 vcc, s81, v242
s_nop 0
s_and_b64 vcc, vcc, s[34:35]
s_cbranch_scc1 SPINP2o4_%=
OKP2o4_%=:
Mvav 104,208,16
s_add_u32 s53, s27, 81920
buffer_load_dwordx4 a[0:3], v241, s[40:43], s53 offen offset:0 sc1
Mvav 108,208,32
Mvav 108,212,36
Mvav 108,216,40
buffer_load_dwordx4 a[4:7], v241, s[40:43], s53 offen offset:1024 sc1
Mvav 108,220,44
Mvaa 112,220,172
Mvaa 112,216,168
buffer_load_dwordx4 a[8:11], v241, s[40:43], s53 offen offset:2048 sc1
Mvaa 112,212,164
Mvaa 112,208,160
Wl 0
Mvav 80,224,160
buffer_load_dwordx4 a[12:15], v241, s[40:43], s53 offen offset:3072 sc1
Mvav 80,228,164
Mvav 80,232,168
Mvav 80,236,172
s_add_u32 s53, s52, 98304
buffer_load_dwordx4 a[20:23], v241, s[40:43], s53 offen offset:0 sc1
Mvav 84,236,188
Mvav 84,232,184
Mvav 84,228,180
buffer_load_dwordx4 a[24:27], v241, s[40:43], s53 offen offset:1024 sc1
Mvav 84,224,176
Mvav 88,224,192
Mvav 88,228,196
buffer_load_dwordx4 a[28:31], v241, s[40:43], s53 offen offset:2048 sc1
Mvav 88,232,200
Mvav 88,236,204
Mvav 92,236,220
buffer_load_dwordx4 a[32:35], v241, s[40:43], s53 offen offset:3072 sc1
Mvav 92,232,216
Mvav 92,228,212
Mvav 92,224,208
Mvav 100,240,160
Mvav 100,244,164
Mvav 100,248,168
Mvav 100,252,172
Mvav 104,252,188
global_load_dword v242, v251, s[50:51] sc1
Mvav 104,248,184
Mvav 104,244,180
Mvav 104,240,176
Mvav 108,240,192
Mvav 108,244,196
Mvav 108,248,200
Mvav 108,252,204
Mvav 112,252,220
Mvav 112,248,216
Mvav 112,244,212
Mvav 112,240,208
s_waitcnt vmcnt(5)
Mvva 80,48,0
Mvva 100,50,0
Mvva 84,48,4
Mvva 104,50,4
Mvva 88,48,8
Mvva 108,50,8
Mvva 92,48,12
Mvva 112,50,12
s_mov_b32 s58, 0
POLLo4_%=:
s_waitcnt vmcnt(0)
v_and_b32 v242, 0x7fffffff, v242
v_cmp_ne_u32 vcc, s81, v242
s_add_u32 s58, s58, 1
s_and_b32 s57, vcc_hi, s48
s_cbranch_scc0 GOTo4_%=
global_load_dword v242, v251, s[50:51] sc1
s_cmp_lt_u32 s58, 0x100000
s_cbranch_scc1 POLLo4_%=
GOTo4_%=:
s_add_u32 s53, s55, 114688
buffer_load_dwordx4 a[40:43], v241, s[40:43], s53 offen offset:0 sc1
buffer_load_dwordx4 a[44:47], v241, s[40:43], s53 offen offset:1024 sc1
buffer_load_dwordx4 a[48:51], v241, s[40:43], s53 offen offset:2048 sc1
buffer_load_dwordx4 a[52:55], v241, s[40:43], s53 offen offset:3072 sc1
Mvva 80,48,20
Mvva 100,50,20
Mvva 84,48,24
Mvva 104,50,24
Mvva 88,48,28
Mvva 108,50,28
Mvva 92,48,32
Mvva 112,50,32
s_waitcnt vmcnt(0)
Mvva 80,48,40
Mvva 100,50,40
Mvva 84,48,44
Mvva 104,50,44
Mvva 88,48,48
Mvva 108,50,48
Mvva 92,48,52
Mvva 112,50,52
s_nop 1
Pk 160,80,81
Pk 161,82,83
Pk 162,100,101
Pk 163,102,103
buffer_store_dwordx4 v[160:163], v250, s[88:91], 0 offen sc1
v_add_u32 v250, 32768, v250
Pk 164,84,85
Pk 165,86,87
Pk 166,104,105
Pk 167,106,107
buffer_store_dwordx4 v[164:167], v250, s[88:91], 0 offen sc1
v_add_u32 v250, 32768, v250
Pk 168,88,89
Pk 169,90,91
Pk 170,108,109
Pk 171,110,111
buffer_store_dwordx4 v[168:171], v250, s[88:91], 0 offen sc1
v_add_u32 v250, 32768, v250
Pk 172,92,93
Pk 173,94,95
Pk 174,112,113
Pk 175,114,115
buffer_store_dwordx4 v[172:175], v250, s[88:91], 0 offen sc1
v_add_u32 v250, 32768, v250
s_endpgm
SPINP1o4_%=:
s_sleep 4
v_lshlrev_b32 v242, 2, v245
global_load_dword v242, v242, s[50:51] sc1
s_waitcnt vmcnt(0)
v_cmp_gt_i32 s[64:65], 0, v242
v_and_b32 v242, 0x7fffffff, v242
v_cmp_ne_u32 vcc, s81, v242
s_nop 0
s_and_b64 vcc, vcc, 0xf00
s_cbranch_scc0 OKP1o4_%=
s_add_u32 s58, s58, 1
s_cmp_lt_u32 s58, 0x80000
s_cbranch_scc1 SPINP1o4_%=
s_branch OKP1o4_%=
SPINP2o4_%=:
s_sleep 1
v_lshlrev_b32 v242, 2, v245
global_load_dword v242, v242, s[50:51] sc1
s_waitcnt vmcnt(0)
v_cmp_gt_i32 s[64:65], 0, v242
v_and_b32 v242, 0x7fffffff, v242
v_cmp_ne_u32 vcc, s81, v242
s_nop 0
s_and_b64 vcc, vcc, s[34:35]
s_cbranch_scc0 OKP2o4_%=
s_add_u32 s58, s58, 1
s_cmp_lt_u32 s58, 0x80000
s_cbranch_scc1 SPINP2o4_%=
s_branch OKP2o4_%=
PBE_%=:
v_and_b32 v243, 3, v245
v_lshl_add_u32 v243, v243, 10, s59
v_writelane_b32 v243, s77, 4
v_writelane_b32 v243, s78, 5
v_writelane_b32 v243, s80, 6
global_load_dword v244, v243, s[92:93] sc1
Zaav 0,192,160
Maav 0,196,164
s_mov_b32 s57, 53248
Ta 224,255,512
Maav 0,200,168
Ta 226,255,4640
buffer_store_dwordx4 v[52:55], v241, s[96:99], s57 offen offset:1024 sc1
Maav 0,204,172
Ta 228,255,8768
Zaav 4,204,188
Ta 230,255,12896
Maav 4,200,184
Ta 232,240,512
Maav 4,196,180
Ta 234,240,4640
Maav 4,192,176
Ta 236,240,8768
Zaav 8,192,192
s_mov_b32 m0, 49536
s_add_u32 s79, s82, 4294918144
Ta 238,240,12896
Maav 8,196,196
Ta 240,255,520
buffer_load_dwordx4 v248, s[72:75], s79 offen offset:0 lds
Maav 8,200,200
Ta 242,255,4648
Maav 8,204,204
Ta 244,255,8776
buffer_load_dwordx4 v249, s[72:75], s79 offen offset:1032 lds
Zaav 12,204,220
Ta 246,255,12904
Maav 12,200,216
Ta 248,240,520
Maav 12,196,212
Ta 250,240,4648
buffer_load_dwordx4 v250, s[72:75], s79 offen offset:2064 lds
Maav 12,192,208
Ta 252,240,8776
Zaav 20,208,160
Ta 254,240,12904
buffer_load_dwordx4 v251, s[72:75], s79 offen offset:3096 lds
Maav 20,212,164
Maav 20,216,168
s_mov_b32 m0, 53664
s_add_u32 s79, s82, 4294934528
Maav 20,220,172
buffer_load_dwordx4 v248, s[72:75], s79 offen offset:0 lds
Zaav 24,220,188
Maav 24,216,184
buffer_load_dwordx4 v249, s[72:75], s79 offen offset:1032 lds
Maav 24,212,180
Maav 24,208,176
Zaav 28,208,192
buffer_load_dwordx4 v250, s[72:75], s79 offen offset:2064 lds
Maav 28,212,196
Maav 28,216,200
buffer_load_dwordx4 v251, s[72:75], s79 offen offset:3096 lds
Maav 28,220,204
Zaav 32,220,220
s_mov_b32 s57, 4096
Maav 32,216,216
buffer_store_dwordx4 v[8:11], v241, s[96:99], s57 offen offset:2048 sc1
Maav 32,212,212
Maav 32,208,208
Wl 0
Zaav 40,224,160
Maav 40,228,164
Ta 192,255,768
Maav 40,232,168
Ta 194,255,4896
Maav 40,236,172
s_add_u32 s53, s17, 0
Ta 196,255,9024
Zaav 44,236,188
Ta 198,255,13152
buffer_load_dwordx4 a[80:83], v241, s[84:87], s53 offen offset:0 sc1
Maav 44,232,184
Ta 200,240,768
Maav 44,228,180
Ta 202,240,4896
Maav 44,224,176
Ta 204,240,9024
buffer_load_dwordx4 a[84:87], v241, s[84:87], s53 offen offset:1024 sc1
Zaav 48,224,192
Ta 206,240,13152
Maav 48,228,196
Ta 208,255,776
buffer_load_dwordx4 a[88:91], v241, s[84:87], s53 offen offset:2048 sc1
Maav 48,232,200
Ta 210,255,4904
Maav 48,236,204
Ta 212,255,9032
Zaav 52,236,220
Ta 214,255,13160
buffer_load_dwordx4 a[92:95], v241, s[84:87], s53 offen offset:3072 sc1
Maav 52,232,216
s_mov_b32 s57, 20480
Ta 216,240,776
Maav 52,228,212
Ta 218,240,4904
buffer_store_dwordx4 v[24:27], v241, s[96:99], s57 offen offset:2048 sc1
Maav 52,224,208
Ta 220,240,9032
Zaav 60,240,160
Ta 222,240,13160
Maav 60,244,164
Maav 60,248,168
Maav 60,252,172
Zaav 64,252,188
s_mov_b32 m0, 57792
s_add_u32 s79, s82, 4294950912
Maav 64,248,184
buffer_load_dwordx4 v248, s[72:75], s79 offen offset:0 lds
Maav 64,244,180
Maav 64,240,176
Zaav 68,240,192
buffer_load_dwordx4 v249, s[72:75], s79 offen offset:1032 lds
Maav 68,244,196
Maav 68,248,200
buffer_load_dwordx4 v250, s[72:75], s79 offen offset:2064 lds
Maav 68,252,204
Zaav 72,252,220
Maav 72,248,216
buffer_load_dwordx4 v251, s[72:75], s79 offen offset:3096 lds
Maav 72,244,212
s_mov_b32 s57, 36864
Maav 72,240,208
Wl 0
buffer_store_dwordx4 v[40:43], v241, s[96:99], s57 offen offset:2048 sc1
Zvav 120,192,160
Mvav 120,196,164
)A"
R"A(Ta 224,255,0
Mvav 120,200,168
Ta 226,255,4128
Mvav 120,204,172
Ta 228,255,8256
Zvav 124,204,188
Ta 230,255,12384
Mvav 124,200,184
Ta 232,240,0
Mvav 124,196,180
s_add_u32 s53, s17, 16384
Ta 234,240,4128
Mvav 124,192,176
Ta 236,240,8256
buffer_load_dwordx4 a[96:99], v241, s[84:87], s53 offen offset:0 sc1
Zvav 128,192,192
Ta 238,240,12384
Mvav 128,196,196
Ta 240,255,8
buffer_load_dwordx4 a[100:103], v241, s[84:87], s53 offen offset:1024 sc1
Mvav 128,200,200
Ta 242,255,4136
Mvav 128,204,204
Ta 244,255,8264
Zvav 132,204,220
Ta 246,255,12392
buffer_load_dwordx4 a[104:107], v241, s[84:87], s53 offen offset:2048 sc1
Mvav 132,200,216
Ta 248,240,8
Mvav 132,196,212
Ta 250,240,4136
buffer_load_dwordx4 a[108:111], v241, s[84:87], s53 offen offset:3072 sc1
Mvav 132,192,208
Ta 252,240,8264
Zvav 140,208,160
s_mov_b32 s57, 53248
Ta 254,240,12392
Mvav 140,212,164
buffer_store_dwordx4 v[56:59], v241, s[96:99], s57 offen offset:2048 sc1
Mvav 140,216,168
Mvav 140,220,172
Zvav 144,220,188
Mvav 144,216,184
Mvav 144,212,180
Mvav 144,208,176
s_mov_b32 m0, 61920
s_add_u32 s79, s82, 0
Zvav 148,208,192
buffer_load_dwordx4 v248, s[72:75], s79 offen offset:0 lds
Mvav 148,212,196
Mvav 148,216,200
buffer_load_dwordx4 v249, s[72:75], s79 offen offset:1032 lds
Mvav 148,220,204
Zvav 152,220,220
Mvav 152,216,216
buffer_load_dwordx4 v250, s[72:75], s79 offen offset:2064 lds
Mvav 152,212,212
Mvav 152,208,208
Wl 0
buffer_load_dwordx4 v251, s[72:75], s79 offen offset:3096 lds
Zvav 80,224,160
s_mov_b64 exec, 0
s_swappc_b64 s[12:13], s[14:15]
s_add_u32 s14, s14, 4096
s_addc_u32 s15, s15, 0
s_barrier
Mvav 80,228,164
s_mov_b32 m0, 115584
s_add_u32 s79, s82, 16384
Ta 192,240,16768
Mvav 80,232,168
Ta 194,240,20896
buffer_load_dwordx4 v248, s[72:75], s79 offen offset:0 lds
Mvav 80,236,172
Ta 196,240,25024
Zvav 84,236,188
Ta 198,240,29152
buffer_load_dwordx4 v249, s[72:75], s79 offen offset:1032 lds
Mvav 84,232,184
Ta 200,255,16768
Mvav 84,228,180
Ta 202,255,20896
Mvav 84,224,176
Ta 204,255,25024
buffer_load_dwordx4 v250, s[72:75], s79 offen offset:2064 lds
Zvav 88,224,192
Ta 206,255,29152
Mvav 88,228,196
Ta 208,240,16776
buffer_load_dwordx4 v251, s[72:75], s79 offen offset:3096 lds
Mvav 88,232,200
Ta 210,240,20904
Mvav 88,236,204
s_mov_b32 m0, 119712
s_add_u32 s79, s82, 32768
Ta 212,240,25032
Zvav 92,236,220
Ta 214,240,29160
buffer_load_dwordx4 v248, s[72:75], s79 offen offset:0 lds
Mvav 92,232,216
Ta 216,255,16776
Mvav 92,228,212
Ta 218,255,20904
buffer_load_dwordx4 v249, s[72:75], s79 offen offset:1032 lds
Mvav 92,224,208
Ta 220,255,25032
Zvav 100,240,160
Ta 222,255,29160
Mvav 100,244,164
buffer_load_dwordx4 v250, s[72:75], s79 offen offset:2064 lds
Mvav 100,248,168
Mvav 100,252,172
buffer_load_dwordx4 v251, s[72:75], s79 offen offset:3096 lds
Zvav 104,252,188
Mvav 104,248,184
s_mov_b32 s57, 4096
Mvav 104,244,180
buffer_store_dwordx4 v[12:15], v241, s[96:99], s57 offen offset:3072 sc1
Mvav 104,240,176
Zvav 108,240,192
Mvav 108,244,196
Mvav 108,248,200
Mvav 108,252,204
Zvav 112,252,220
s_add_u32 s53, s17, 32768
Mvav 112,248,216
buffer_load_dwordx4 a[112:115], v241, s[84:87], s53 offen offset:0 sc1
Mvav 112,244,212
Mvav 112,240,208
Wl 0
Maav 0,192,0
buffer_load_dwordx4 a[116:119], v241, s[84:87], s53 offen offset:1024 sc1
Maav 0,196,4
Ta 224,240,17024
Maav 0,200,8
Ta 226,240,21152
buffer_load_dwordx4 a[120:123], v241, s[84:87], s53 offen offset:2048 sc1
Maav 0,204,12
Ta 228,240,25280
Maav 4,204,28
Ta 230,240,29408
Maav 4,200,24
Ta 232,255,17024
buffer_load_dwordx4 a[124:127], v241, s[84:87], s53 offen offset:3072 sc1
Maav 4,196,20
s_mov_b32 s57, 20480
Ta 234,255,21152
Maav 4,192,16
Ta 236,255,25280
buffer_store_dwordx4 v[28:31], v241, s[96:99], s57 offen offset:3072 sc1
Maav 8,192,32
Ta 238,255,29408
Maav 8,196,36
Ta 240,240,17032
Maav 8,200,40
Ta 242,240,21160
Maav 8,204,44
Ta 244,240,25288
Maav 12,204,60
Ta 246,240,29416
Maav 12,200,56
s_mov_b32 m0, 123840
s_add_u32 s79, s82, 49152
Ta 248,255,17032
Maav 12,196,52
Ta 250,255,21160
buffer_load_dwordx4 v248, s[72:75], s79 offen offset:0 lds
Maav 12,192,48
Ta 252,255,25288
Maav 20,208,0
Ta 254,255,29416
Maav 20,212,4
buffer_load_dwordx4 v249, s[72:75], s79 offen offset:1032 lds
Maav 20,216,8
Maav 20,220,12
buffer_load_dwordx4 v250, s[72:75], s79 offen offset:2064 lds
Maav 24,220,28
Maav 24,216,24
Maav 24,212,20
buffer_load_dwordx4 v251, s[72:75], s79 offen offset:3096 lds
Maav 24,208,16
s_mov_b32 s57, 36864
Maav 28,208,32
buffer_store_dwordx4 v[44:47], v241, s[96:99], s57 offen offset:3072 sc1
Maav 28,212,36
Maav 28,216,40
Maav 28,220,44
Maav 32,220,60
Maav 32,216,56
Maav 32,212,52
Maav 32,208,48
s_mov_b32 m0, 127968
s_add_u32 s79, s82, 65536
Wl 0
Maav 40,224,0
buffer_load_dwordx4 v248, s[72:75], s79 offen offset:0 lds
Maav 40,228,4
Ta 192,240,17280
Maav 40,232,8
Ta 194,240,21408
buffer_load_dwordx4 v249, s[72:75], s79 offen offset:1032 lds
Maav 40,236,12
Ta 196,240,25536
Maav 44,236,28
Ta 198,240,29664
Maav 44,232,24
Ta 200,255,17280
buffer_load_dwordx4 v250, s[72:75], s79 offen offset:2064 lds
Maav 44,228,20
Ta 202,255,21408
Maav 44,224,16
Ta 204,255,25536
buffer_load_dwordx4 v251, s[72:75], s79 offen offset:3096 lds
Maav 48,224,32
Ta 206,255,29664
Maav 48,228,36
s_mov_b32 s57, 53248
Ta 208,240,17288
Maav 48,232,40
Ta 210,240,21416
buffer_store_dwordx4 v[60:63], v241, s[96:99], s57 offen offset:3072 sc1
Maav 48,236,44
Ta 212,240,25544
Maav 52,236,60
Ta 214,240,29672
Maav 52,232,56
Ta 216,255,17288
Maav 52,228,52
Ta 218,255,21416
Maav 52,224,48
Ta 220,255,25544
Maav 60,240,0
s_add_u32 s53, s17, 49152
Ta 222,255,29672
Maav 60,244,4
buffer_load_dwordx4 a[128:131], v241, s[84:87], s53 offen offset:0 sc1
Maav 60,248,8
Maav 60,252,12
buffer_load_dwordx4 a[132:135], v241, s[84:87], s53 offen offset:1024 sc1
Maav 64,252,28
Maav 64,248,24
Maav 64,244,20
buffer_load_dwordx4 a[136:139], v241, s[84:87], s53 offen offset:2048 sc1
Maav 64,240,16
Maav 68,240,32
buffer_load_dwordx4 a[140:143], v241, s[84:87], s53 offen offset:3072 sc1
Maav 68,244,36
Maav 68,248,40
s_add_u32 s53, s18, 0
Maav 68,252,44
buffer_load_dwordx4 v[160:163], v241, s[84:87], s53 offen offset:0 sc1
Maav 72,252,60
Maav 72,248,56
buffer_load_dwordx4 v[164:167], v241, s[84:87], s53 offen offset:1024 sc1
Maav 72,244,52
Maav 72,240,48
Wl 0
Mvav 120,192,0
buffer_load_dwordx4 v[168:171], v241, s[84:87], s53 offen offset:2048 sc1
Mvav 120,196,4
Ta 224,240,16512
Mvav 120,200,8
Ta 226,240,20640
buffer_load_dwordx4 v[172:175], v241, s[84:87], s53 offen offset:3072 sc1
Mvav 120,204,12
Ta 228,240,24768
Mvav 124,204,28
s_add_u32 s53, s18, 16384
Ta 230,240,28896
Mvav 124,200,24
Ta 232,255,16512
buffer_load_dwordx4 v[176:179], v241, s[84:87], s53 offen offset:0 sc1
Mvav 124,196,20
Ta 234,255,20640
Mvav 124,192,16
Ta 236,255,24768
buffer_load_dwordx4 v[180:183], v241, s[84:87], s53 offen offset:1024 sc1
Mvav 128,192,32
Ta 238,255,28896
Mvav 128,196,36
Ta 240,240,16520
Mvav 128,200,40
Ta 242,240,20648
buffer_load_dwordx4 v[184:187], v241, s[84:87], s53 offen offset:2048 sc1
Mvav 128,204,44
Ta 244,240,24776
Mvav 132,204,60
Ta 246,240,28904
buffer_load_dwordx4 v[188:191], v241, s[84:87], s53 offen offset:3072 sc1
Mvav 132,200,56
Ta 248,255,16520
Mvav 132,196,52
s_add_u32 s53, s18, 32768
Ta 250,255,20648
Mvav 132,192,48
Ta 252,255,24776
buffer_load_dwordx4 v[192:195], v241, s[84:87], s53 offen offset:0 sc1
Mvav 140,208,0
Ta 254,255,28904
Mvav 140,212,4
buffer_load_dwordx4 v[196:199], v241, s[84:87], s53 offen offset:1024 sc1
Mvav 140,216,8
Mvav 140,220,12
Mvav 144,220,28
buffer_load_dwordx4 v[200:203], v241, s[84:87], s53 offen offset:2048 sc1
Mvav 144,216,24
Mvav 144,212,20
buffer_load_dwordx4 v[204:207], v241, s[84:87], s53 offen offset:3072 sc1
Mvav 144,208,16
Mvav 148,208,32
s_add_u32 s53, s18, 49152
Mvav 148,212,36
buffer_load_dwordx4 v[208:211], v241, s[84:87], s53 offen offset:0 sc1
Mvav 148,216,40
Mvav 148,220,44
buffer_load_dwordx4 v[212:215], v241, s[84:87], s53 offen offset:1024 sc1
Mvav 152,220,60
Mvav 152,216,56
Mvav 152,212,52
buffer_load_dwordx4 v[216:219], v241, s[84:87], s53 offen offset:2048 sc1
Mvav 152,208,48
Wl 0
Mvav 80,224,0
s_mov_b64 exec, 0
s_swappc_b64 s[12:13], s[14:15]
s_add_u32 s14, s14, 4096
s_addc_u32 s15, s15, 0
s_barrier
buffer_load_dwordx4 v[220:223], v241, s[84:87], s53 offen offset:3072 sc1
Mvav 80,228,4
s_waitcnt vmcnt(20)
Ta 192,240,33280
v_xor_b32 v244, s66, v244
Mvav 80,232,8
s_mov_b32 m0, 66048
s_add_u32 s79, s83, 4294918144
Ta 194,240,37408
global_store_dword v243, v244, s[94:95] offset:0 sc1
Mvav 80,236,12
Ta 196,240,41536
buffer_load_dwordx4 v248, s[72:75], s79 offen offset:0 lds
Mvav 84,236,28
Ta 198,240,45664
Mvav 84,232,24
Ta 200,255,33280
buffer_load_dwordx4 v249, s[72:75], s79 offen offset:1032 lds
global_store_dword v243, v244, s[94:95] offset:16 sc1
Mvav 84,228,20
Ta 202,255,37408
Mvav 84,224,16
Ta 204,255,41536
Mvav 88,224,32
Ta 206,255,45664
buffer_load_dwordx4 v250, s[72:75], s79 offen offset:2064 lds
Mvav 88,228,36
Ta 208,240,33288
Mvav 88,232,40
Ta 210,240,37416
buffer_load_dwordx4 v251, s[72:75], s79 offen offset:3096 lds
Mvav 88,236,44
Ta 212,240,41544
Mvav 92,236,60
s_mov_b32 m0, 70176
s_add_u32 s79, s83, 4294934528
Ta 214,240,45672
Mvav 92,232,56
Ta 216,255,33288
buffer_load_dwordx4 v248, s[72:75], s79 offen offset:0 lds
Mvav 92,228,52
Ta 218,255,37416
Mvav 92,224,48
Ta 220,255,41544
buffer_load_dwordx4 v249, s[72:75], s79 offen offset:1032 lds
Mvav 100,240,0
Ta 222,255,45672
Mvav 100,244,4
Mvav 100,248,8
buffer_load_dwordx4 v250, s[72:75], s79 offen offset:2064 lds
Mvav 100,252,12
Mvav 104,252,28
buffer_load_dwordx4 v251, s[72:75], s79 offen offset:3096 lds
Mvav 104,248,24
Mvav 104,244,20
s_mov_b32 m0, 74304
s_add_u32 s79, s83, 4294950912
Mvav 104,240,16
buffer_load_dwordx4 v248, s[72:75], s79 offen offset:0 lds
Mvav 108,240,32
Mvav 108,244,36
buffer_load_dwordx4 v249, s[72:75], s79 offen offset:1032 lds
Mvav 108,248,40
Mvav 108,252,44
Mvav 112,252,60
buffer_load_dwordx4 v250, s[72:75], s79 offen offset:2064 lds
Mvav 112,248,56
Mvav 112,244,52
buffer_load_dwordx4 v251, s[72:75], s79 offen offset:3096 lds
Mvav 112,240,48
Wl 0
s_waitcnt vmcnt(30)
)A"
R"A(Maaa 0,192,80
s_mov_b32 m0, 78432
s_add_u32 s79, s83, 0
Maaa 0,196,84
Ta 224,240,33536
buffer_load_dwordx4 v248, s[72:75], s79 offen offset:0 lds
Maaa 0,200,88
Ta 226,240,37664
Maaa 0,204,92
Ta 228,240,41792
buffer_load_dwordx4 v249, s[72:75], s79 offen offset:1032 lds
Maaa 4,204,108
Ta 230,240,45920
Maaa 4,200,104
Ta 232,255,33536
Maaa 4,196,100
Ta 234,255,37664
buffer_load_dwordx4 v250, s[72:75], s79 offen offset:2064 lds
Maaa 4,192,96
Ta 236,255,41792
Maaa 8,192,112
Ta 238,255,45920
buffer_load_dwordx4 v251, s[72:75], s79 offen offset:3096 lds
Maaa 8,196,116
Ta 240,240,33544
Maaa 8,200,120
s_mov_b32 m0, 0
s_add_u32 s79, s83, 16384
Ta 242,240,37672
Maaa 8,204,124
Ta 244,240,41800
buffer_load_dwordx4 v248, s[72:75], s79 offen offset:0 lds
Maaa 12,204,140
Ta 246,240,45928
Maaa 12,200,136
Ta 248,255,33544
buffer_load_dwordx4 v249, s[72:75], s79 offen offset:1032 lds
Maaa 12,196,132
Ta 250,255,37672
Maaa 12,192,128
Ta 252,255,41800
Maaa 20,208,80
Ta 254,255,45928
buffer_load_dwordx4 v250, s[72:75], s79 offen offset:2064 lds
Maaa 20,212,84
Maaa 20,216,88
buffer_load_dwordx4 v251, s[72:75], s79 offen offset:3096 lds
Maaa 20,220,92
Maaa 24,220,108
s_mov_b32 m0, 4128
s_add_u32 s79, s83, 32768
Maaa 24,216,104
buffer_load_dwordx4 v248, s[72:75], s79 offen offset:0 lds
Maaa 24,212,100
Maaa 24,208,96
buffer_load_dwordx4 v249, s[72:75], s79 offen offset:1032 lds
Maaa 28,208,112
Maaa 28,212,116
Maaa 28,216,120
buffer_load_dwordx4 v250, s[72:75], s79 offen offset:2064 lds
Maaa 28,220,124
Maaa 32,220,140
buffer_load_dwordx4 v251, s[72:75], s79 offen offset:3096 lds
Maaa 32,216,136
Maaa 32,212,132
s_mov_b32 m0, 8256
s_add_u32 s79, s83, 49152
Maaa 32,208,128
Wl 0
buffer_load_dwordx4 v248, s[72:75], s79 offen offset:0 lds
Maaa 40,224,80
Maaa 40,228,84
Ta 192,240,33792
buffer_load_dwordx4 v249, s[72:75], s79 offen offset:1032 lds
Maaa 40,232,88
Ta 194,240,37920
Maaa 40,236,92
Ta 196,240,42048
Maaa 44,236,108
Ta 198,240,46176
buffer_load_dwordx4 v250, s[72:75], s79 offen offset:2064 lds
Maaa 44,232,104
Ta 200,255,33792
Maaa 44,228,100
Ta 202,255,37920
buffer_load_dwordx4 v251, s[72:75], s79 offen offset:3096 lds
Maaa 44,224,96
Ta 204,255,42048
Maaa 48,224,112
s_mov_b32 m0, 12384
s_add_u32 s79, s83, 65536
Ta 206,255,46176
Maaa 48,228,116
Ta 208,240,33800
buffer_load_dwordx4 v248, s[72:75], s79 offen offset:0 lds
Maaa 48,232,120
Ta 210,240,37928
Maaa 48,236,124
Ta 212,240,42056
buffer_load_dwordx4 v249, s[72:75], s79 offen offset:1032 lds
Maaa 52,236,140
Ta 214,240,46184
Maaa 52,232,136
Ta 216,255,33800
Maaa 52,228,132
Ta 218,255,37928
buffer_load_dwordx4 v250, s[72:75], s79 offen offset:2064 lds
Maaa 52,224,128
Ta 220,255,42056
Maaa 60,240,80
Ta 222,255,46184
buffer_load_dwordx4 v251, s[72:75], s79 offen offset:3096 lds
Maaa 60,244,84
Maaa 60,248,88
s_mov_b32 m0, 16512
s_add_u32 s79, s16, 81920
Maaa 60,252,92
buffer_load_dwordx4 v248, s[72:75], s79 offen offset:0 lds
Maaa 64,252,108
Maaa 64,248,104
buffer_load_dwordx4 v249, s[72:75], s79 offen offset:1032 lds
Maaa 64,244,100
Maaa 64,240,96
Maaa 68,240,112
buffer_load_dwordx4 v250, s[72:75], s79 offen offset:2064 lds
Maaa 68,244,116
Maaa 68,248,120
buffer_load_dwordx4 v251, s[72:75], s79 offen offset:3096 lds
Maaa 68,252,124
Maaa 72,252,140
s_mov_b32 m0, 20640
s_add_u32 s79, s16, 98304
Maaa 72,248,136
buffer_load_dwordx4 v248, s[72:75], s79 offen offset:0 lds
Maaa 72,244,132
Maaa 72,240,128
Wl 0
buffer_load_dwordx4 v249, s[72:75], s79 offen offset:1032 lds
Mvaa 120,192,80
Mvaa 120,196,84
Ta 224,240,33024
Mvaa 120,200,88
Ta 226,240,37152
buffer_load_dwordx4 v250, s[72:75], s79 offen offset:2064 lds
Mvaa 120,204,92
Ta 228,240,41280
Mvaa 124,204,108
Ta 230,240,45408
buffer_load_dwordx4 v251, s[72:75], s79 offen offset:3096 lds
Mvaa 124,200,104
Ta 232,255,33024
Mvaa 124,196,100
s_mov_b32 m0, 24768
s_add_u32 s79, s16, 114688
Ta 234,255,37152
Mvaa 124,192,96
Ta 236,255,41280
buffer_load_dwordx4 v248, s[72:75], s79 offen offset:0 lds
Mvaa 128,192,112
Ta 238,255,45408
Mvaa 128,196,116
Ta 240,240,33032
buffer_load_dwordx4 v249, s[72:75], s79 offen offset:1032 lds
Mvaa 128,200,120
Ta 242,240,37160
Mvaa 128,204,124
Ta 244,240,41288
Mvaa 132,204,140
Ta 246,240,45416
buffer_load_dwordx4 v250, s[72:75], s79 offen offset:2064 lds
Mvaa 132,200,136
Ta 248,255,33032
Mvaa 132,196,132
Ta 250,255,37160
buffer_load_dwordx4 v251, s[72:75], s79 offen offset:3096 lds
Mvaa 132,192,128
Ta 252,255,41288
Mvaa 140,208,80
s_mov_b32 m0, 28896
s_add_u32 s79, s16, 131072
Ta 254,255,45416
Mvaa 140,212,84
buffer_load_dwordx4 v248, s[72:75], s79 offen offset:0 lds
Mvaa 140,216,88
Mvaa 140,220,92
buffer_load_dwordx4 v249, s[72:75], s79 offen offset:1032 lds
Mvaa 144,220,108
Mvaa 144,216,104
Mvaa 144,212,100
buffer_load_dwordx4 v250, s[72:75], s79 offen offset:2064 lds
Mvaa 144,208,96
Mvaa 148,208,112
buffer_load_dwordx4 v251, s[72:75], s79 offen offset:3096 lds
Mvaa 148,212,116
Mvaa 148,216,120
s_mov_b32 m0, 82560
s_add_u32 s79, s16, 147456
Mvaa 148,220,124
buffer_load_dwordx4 v248, s[72:75], s79 offen offset:0 lds
Mvaa 152,220,140
Mvaa 152,216,136
buffer_load_dwordx4 v249, s[72:75], s79 offen offset:1032 lds
Mvaa 152,212,132
Mvaa 152,208,128
Wl 0
Mvaa 80,224,80
s_mov_b64 exec, 0
s_swappc_b64 s[12:13], s[14:15]
s_add_u32 s14, s14, 4096
s_addc_u32 s15, s15, 0
s_barrier
buffer_load_dwordx4 v250, s[72:75], s79 offen offset:2064 lds
Mvaa 80,228,84
Ta 192,240,49792
Mvaa 80,232,88
Ta 194,240,53920
buffer_load_dwordx4 v251, s[72:75], s79 offen offset:3096 lds
Mvaa 80,236,92
Ta 196,240,58048
Mvaa 84,236,108
s_mov_b32 m0, 86688
s_add_u32 s79, s16, 163840
Ta 198,240,62176
Mvaa 84,232,104
Ta 200,255,49792
buffer_load_dwordx4 v248, s[72:75], s79 offen offset:0 lds
Mvaa 84,228,100
Ta 202,255,53920
Mvaa 84,224,96
Ta 204,255,58048
buffer_load_dwordx4 v249, s[72:75], s79 offen offset:1032 lds
Mvaa 88,224,112
Ta 206,255,62176
Mvaa 88,228,116
Ta 208,240,49800
Mvaa 88,232,120
Ta 210,240,53928
buffer_load_dwordx4 v250, s[72:75], s79 offen offset:2064 lds
Mvaa 88,236,124
Ta 212,240,58056
Mvaa 92,236,140
Ta 214,240,62184
buffer_load_dwordx4 v251, s[72:75], s79 offen offset:3096 lds
Mvaa 92,232,136
Ta 216,255,49800
Mvaa 92,228,132
s_mov_b32 m0, 90816
s_add_u32 s79, s16, 180224
Ta 218,255,53928
Mvaa 92,224,128
Ta 220,255,58056
buffer_load_dwordx4 v248, s[72:75], s79 offen offset:0 lds
Mvaa 100,240,80
Ta 222,255,62184
Mvaa 100,244,84
buffer_load_dwordx4 v249, s[72:75], s79 offen offset:1032 lds
Mvaa 100,248,88
Mvaa 100,252,92
Mvaa 104,252,108
buffer_load_dwordx4 v250, s[72:75], s79 offen offset:2064 lds
Mvaa 104,248,104
Mvaa 104,244,100
buffer_load_dwordx4 v251, s[72:75], s79 offen offset:3096 lds
Mvaa 104,240,96
Mvaa 108,240,112
s_mov_b32 m0, 94944
s_add_u32 s79, s16, 196608
Mvaa 108,244,116
buffer_load_dwordx4 v248, s[72:75], s79 offen offset:0 lds
Mvaa 108,248,120
Mvaa 108,252,124
buffer_load_dwordx4 v249, s[72:75], s79 offen offset:1032 lds
Mvaa 112,252,140
Mvaa 112,248,136
Mvaa 112,244,132
buffer_load_dwordx4 v250, s[72:75], s79 offen offset:2064 lds
Mvaa 112,240,128
Wl 0
s_waitcnt vmcnt(62)
Maav 0,192,160
buffer_load_dwordx4 v251, s[72:75], s79 offen offset:3096 lds
Maav 0,196,164
Ta 224,240,50048
Maav 0,200,168
s_add_u32 s53, s22, 0
Ta 226,240,54176
Maav 0,204,172
Ta 228,240,58304
buffer_load_dwordx4 a[80:83], v241, s[84:87], s53 offen offset:0 sc1
Maav 4,204,188
Ta 230,240,62432
Maav 4,200,184
Ta 232,255,50048
buffer_load_dwordx4 a[84:87], v241, s[84:87], s53 offen offset:1024 sc1
Maav 4,196,180
Ta 234,255,54176
Maav 4,192,176
Ta 236,255,58304
Maav 8,192,192
Ta 238,255,62432
buffer_load_dwordx4 a[88:91], v241, s[84:87], s53 offen offset:2048 sc1
Maav 8,196,196
Ta 240,240,50056
Maav 8,200,200
Ta 242,240,54184
buffer_load_dwordx4 a[92:95], v241, s[84:87], s53 offen offset:3072 sc1
Maav 8,204,204
Ta 244,240,58312
Maav 12,204,220
s_add_u32 s53, s22, 16384
Ta 246,240,62440
Maav 12,200,216
Ta 248,255,50056
buffer_load_dwordx4 a[96:99], v241, s[84:87], s53 offen offset:0 sc1
Maav 12,196,212
Ta 250,255,54184
Maav 12,192,208
Ta 252,255,58312
buffer_load_dwordx4 a[100:103], v241, s[84:87], s53 offen offset:1024 sc1
Maav 20,208,160
Ta 254,255,62440
Maav 20,212,164
Maav 20,216,168
buffer_load_dwordx4 a[104:107], v241, s[84:87], s53 offen offset:2048 sc1
Maav 20,220,172
Maav 24,220,188
buffer_load_dwordx4 a[108:111], v241, s[84:87], s53 offen offset:3072 sc1
Maav 24,216,184
Maav 24,212,180
s_add_u32 s53, s22, 32768
Maav 24,208,176
buffer_load_dwordx4 a[112:115], v241, s[84:87], s53 offen offset:0 sc1
Maav 28,208,192
Maav 28,212,196
buffer_load_dwordx4 a[116:119], v241, s[84:87], s53 offen offset:1024 sc1
Maav 28,216,200
Maav 28,220,204
Maav 32,220,220
buffer_load_dwordx4 a[120:123], v241, s[84:87], s53 offen offset:2048 sc1
Maav 32,216,216
Maav 32,212,212
buffer_load_dwordx4 a[124:127], v241, s[84:87], s53 offen offset:3072 sc1
Maav 32,208,208
Wl 0
Maav 40,224,160
s_add_u32 s53, s22, 49152
Maav 40,228,164
Ta 192,240,50304
buffer_load_dwordx4 a[128:131], v241, s[84:87], s53 offen offset:0 sc1
Maav 40,232,168
Ta 194,240,54432
Maav 40,236,172
Ta 196,240,58560
buffer_load_dwordx4 a[132:135], v241, s[84:87], s53 offen offset:1024 sc1
Maav 44,236,188
Ta 198,240,62688
Maav 44,232,184
Ta 200,255,50304
Maav 44,228,180
Ta 202,255,54432
buffer_load_dwordx4 a[136:139], v241, s[84:87], s53 offen offset:2048 sc1
Maav 44,224,176
Ta 204,255,58560
Maav 48,224,192
Ta 206,255,62688
buffer_load_dwordx4 a[140:143], v241, s[84:87], s53 offen offset:3072 sc1
Maav 48,228,196
Ta 208,240,50312
Maav 48,232,200
s_mov_b32 m0, 33024
s_add_u32 s79, s83, 2048000
Ta 210,240,54440
Maav 48,236,204
Ta 212,240,58568
buffer_load_dwordx4 v248, s[72:75], s79 offen offset:0 lds
Maav 52,236,220
Ta 214,240,62696
Maav 52,232,216
Ta 216,255,50312
buffer_load_dwordx4 v249, s[72:75], s79 offen offset:1032 lds
Maav 52,228,212
Ta 218,255,54440
Maav 52,224,208
Ta 220,255,58568
Maav 60,240,160
Ta 222,255,62696
buffer_load_dwordx4 v250, s[72:75], s79 offen offset:2064 lds
Maav 60,244,164
Maav 60,248,168
buffer_load_dwordx4 v251, s[72:75], s79 offen offset:3096 lds
Maav 60,252,172
Maav 64,252,188
s_mov_b32 m0, 37152
s_add_u32 s79, s83, 2064384
Maav 64,248,184
buffer_load_dwordx4 v248, s[72:75], s79 offen offset:0 lds
Maav 64,244,180
Maav 64,240,176
buffer_load_dwordx4 v249, s[72:75], s79 offen offset:1032 lds
Maav 68,240,192
)A"
R"A(Maav 68,244,196
Maav 68,248,200
buffer_load_dwordx4 v250, s[72:75], s79 offen offset:2064 lds
Maav 68,252,204
Maav 72,252,220
buffer_load_dwordx4 v251, s[72:75], s79 offen offset:3096 lds
Maav 72,248,216
Maav 72,244,212
s_mov_b32 m0, 41280
s_add_u32 s79, s83, 2080768
Maav 72,240,208
Wl 0
buffer_load_dwordx4 v248, s[72:75], s79 offen offset:0 lds
Mvav 120,192,160
Mvav 120,196,164
Ta 224,240,49536
buffer_load_dwordx4 v249, s[72:75], s79 offen offset:1032 lds
Mvav 120,200,168
Ta 226,240,53664
Mvav 120,204,172
Ta 228,240,57792
Mvav 124,204,188
Ta 230,240,61920
buffer_load_dwordx4 v250, s[72:75], s79 offen offset:2064 lds
Mvav 124,200,184
Ta 232,255,49536
Mvav 124,196,180
Ta 234,255,53664
buffer_load_dwordx4 v251, s[72:75], s79 offen offset:3096 lds
Mvav 124,192,176
Ta 236,255,57792
Mvav 128,192,192
s_mov_b32 m0, 45408
s_add_u32 s79, s83, 2097152
Ta 238,255,61920
Mvav 128,196,196
Ta 240,240,49544
buffer_load_dwordx4 v248, s[72:75], s79 offen offset:0 lds
Mvav 128,200,200
Ta 242,240,53672
Mvav 128,204,204
Ta 244,240,57800
buffer_load_dwordx4 v249, s[72:75], s79 offen offset:1032 lds
Mvav 132,204,220
Ta 246,240,61928
Mvav 132,200,216
Ta 248,255,49544
Mvav 132,196,212
Ta 250,255,53672
buffer_load_dwordx4 v250, s[72:75], s79 offen offset:2064 lds
Mvav 132,192,208
Ta 252,255,57800
Mvav 140,208,160
Ta 254,255,61928
buffer_load_dwordx4 v251, s[72:75], s79 offen offset:3096 lds
Mvav 140,212,164
Mvav 140,216,168
s_mov_b32 m0, 99072
s_add_u32 s79, s83, 2113536
Mvav 140,220,172
buffer_load_dwordx4 v248, s[72:75], s79 offen offset:0 lds
Mvav 144,220,188
Mvav 144,216,184
buffer_load_dwordx4 v249, s[72:75], s79 offen offset:1032 lds
v_lshlrev_b32 v242, 2, v245
global_load_dword v242, v242, s[50:51] sc1
Mvav 144,212,180
Mvav 144,208,176
Mvav 148,208,192
buffer_load_dwordx4 v250, s[72:75], s79 offen offset:2064 lds
Mvav 148,212,196
Mvav 148,216,200
buffer_load_dwordx4 v251, s[72:75], s79 offen offset:3096 lds
Mvav 148,220,204
Mvav 152,220,220
s_mov_b32 m0, 103200
s_add_u32 s79, s83, 2129920
Mvav 152,216,216
buffer_load_dwordx4 v248, s[72:75], s79 offen offset:0 lds
Mvav 152,212,212
Mvav 152,208,208
Wl 0
buffer_load_dwordx4 v249, s[72:75], s79 offen offset:1032 lds
Mvav 80,224,160
s_waitcnt vmcnt(62)
s_barrier
Mvav 80,228,164
Ta 192,255,256
Mvav 80,232,168
Ta 194,255,4384
buffer_load_dwordx4 v250, s[72:75], s79 offen offset:2064 lds
Mvav 80,236,172
Ta 196,255,8512
Mvav 84,236,188
Ta 198,255,12640
buffer_load_dwordx4 v251, s[72:75], s79 offen offset:3096 lds
Mvav 84,232,184
Ta 200,240,256
Mvav 84,228,180
s_mov_b32 m0, 107328
s_add_u32 s79, s83, 2146304
Ta 202,240,4384
Mvav 84,224,176
Ta 204,240,8512
buffer_load_dwordx4 v248, s[72:75], s79 offen offset:0 lds
Mvav 88,224,192
Ta 206,240,12640
Mvav 88,228,196
Ta 208,255,264
buffer_load_dwordx4 v249, s[72:75], s79 offen offset:1032 lds
Mvav 88,232,200
Ta 210,255,4392
Mvav 88,236,204
Ta 212,255,8520
Mvav 92,236,220
Ta 214,255,12648
buffer_load_dwordx4 v250, s[72:75], s79 offen offset:2064 lds
Mvav 92,232,216
Ta 216,240,264
Mvav 92,228,212
Ta 218,240,4392
buffer_load_dwordx4 v251, s[72:75], s79 offen offset:3096 lds
Mvav 92,224,208
Ta 220,240,8520
Mvav 100,240,160
s_mov_b32 m0, 111456
s_add_u32 s79, s83, 2162688
Ta 222,240,12648
Mvav 100,244,164
buffer_load_dwordx4 v248, s[72:75], s79 offen offset:0 lds
Mvav 100,248,168
Mvav 100,252,172
buffer_load_dwordx4 v249, s[72:75], s79 offen offset:1032 lds
Mvav 104,252,188
Mvav 104,248,184
Mvav 104,244,180
buffer_load_dwordx4 v250, s[72:75], s79 offen offset:2064 lds
Mvav 104,240,176
Mvav 108,240,192
buffer_load_dwordx4 v251, s[72:75], s79 offen offset:3096 lds
Mvav 108,244,196
Mvav 108,248,200
s_mov_b32 m0, 49536
s_add_u32 s79, s82, 81920
Mvav 108,252,204
buffer_load_dwordx4 v248, s[72:75], s79 offen offset:0 lds
Mvav 112,252,220
Mvav 112,248,216
buffer_load_dwordx4 v249, s[72:75], s79 offen offset:1032 lds
Mvav 112,244,212
Mvav 112,240,208
Wl 0
s_waitcnt vmcnt(35)
Maaa 0,192,80
buffer_load_dwordx4 v250, s[72:75], s79 offen offset:2064 lds
Maaa 0,196,84
Ta 224,255,512
Maaa 0,200,88
s_waitcnt vmcnt(17)
s_mov_b32 s58, 0
v_cmp_gt_i32 s[64:65], 0, v242
v_and_b32 v242, 0x7fffffff, v242
v_cmp_ne_u32 vcc, s81, v242
s_nop 0
s_and_b64 vcc, vcc, 0xf00
s_cbranch_scc1 SPINP1e4_%=
OKP1e4_%=:
Ta 226,255,4640
buffer_load_dwordx4 v251, s[72:75], s79 offen offset:3096 lds
Maaa 0,204,92
Ta 228,255,8768
Maaa 4,204,108
s_add_u32 s53, s17, 4096
Ta 230,255,12896
Maaa 4,200,104
Ta 232,240,512
buffer_load_dwordx4 v[160:163], v241, s[84:87], s53 offen offset:0 sc1
Maaa 4,196,100
Ta 234,240,4640
Maaa 4,192,96
s_lshl_b32 s56, s25, 6
Ta 236,240,8768
buffer_load_dwordx4 v[164:167], v241, s[84:87], s53 offen offset:1024 sc1
Maaa 8,192,112
s_add_u32 s56, s56, s76
Ta 238,240,12896
Maaa 8,196,116
s_lshl_b32 s56, s56, 17
Ta 240,255,520
Maaa 8,200,120
s_add_u32 s40, s20, s56
s_addc_u32 s41, s21, 0
Ta 242,255,4648
buffer_load_dwordx4 v[168:171], v241, s[84:87], s53 offen offset:2048 sc1
Maaa 8,204,124
s_mov_b32 s42, 7995392
Ta 244,255,8776
Maaa 12,204,140
s_mov_b32 s98, 119808
Ta 246,255,12904
buffer_load_dwordx4 v[172:175], v241, s[84:87], s53 offen offset:3072 sc1
Maaa 12,200,136
s_mov_b32 s70, 0
Ta 248,240,520
Maaa 12,196,132
s_mov_b32 m0, 53664
s_add_u32 s79, s82, 98304
Ta 250,240,4648
Maaa 12,192,128
s_and_b32 s71, s35, 0xffff
Ta 252,240,8776
buffer_load_dwordx4 v248, s[72:75], s79 offen offset:0 lds
Maaa 20,208,80
s_lshl_b32 s71, s71, 4
Ta 254,240,12904
Maaa 20,212,84
s_andn2_b32 s34, s34, 0xf0
buffer_load_dwordx4 v249, s[72:75], s79 offen offset:1032 lds
Maaa 20,216,88
s_lshl_b64 s[34:35], s[34:35], 8
Maaa 20,220,92
s_and_b32 s48, s35, 0xffff0000
Maaa 24,220,108
s_and_b32 s35, s35, 0xffff
buffer_load_dwordx4 v250, s[72:75], s79 offen offset:2064 lds
Maaa 24,216,104
Maaa 24,212,100
buffer_load_dwordx4 v251, s[72:75], s79 offen offset:3096 lds
Maaa 24,208,96
Maaa 28,208,112
s_add_u32 s53, s17, 20480
Maaa 28,212,116
buffer_load_dwordx4 v[176:179], v241, s[84:87], s53 offen offset:0 sc1
Maaa 28,216,120
Maaa 28,220,124
buffer_load_dwordx4 v[180:183], v241, s[84:87], s53 offen offset:1024 sc1
Maaa 32,220,140
Maaa 32,216,136
Maaa 32,212,132
buffer_load_dwordx4 v[184:187], v241, s[84:87], s53 offen offset:2048 sc1
Maaa 32,208,128
Wl 0
Maaa 40,224,80
buffer_load_dwordx4 v[188:191], v241, s[84:87], s53 offen offset:3072 sc1
Maaa 40,228,84
Ta 192,255,768
Maaa 40,232,88
s_mov_b32 m0, 57792
s_add_u32 s79, s82, 114688
Ta 194,255,4896
Maaa 40,236,92
Ta 196,255,9024
buffer_load_dwordx4 v248, s[72:75], s79 offen offset:0 lds
Maaa 44,236,108
Ta 198,255,13152
Maaa 44,232,104
Ta 200,240,768
buffer_load_dwordx4 v249, s[72:75], s79 offen offset:1032 lds
Maaa 44,228,100
Ta 202,240,4896
Maaa 44,224,96
Ta 204,240,9024
Maaa 48,224,112
Ta 206,240,13152
buffer_load_dwordx4 v250, s[72:75], s79 offen offset:2064 lds
Maaa 48,228,116
Ta 208,255,776
Maaa 48,232,120
Ta 210,255,4904
buffer_load_dwordx4 v251, s[72:75], s79 offen offset:3096 lds
Maaa 48,236,124
Ta 212,255,9032
Maaa 52,236,140
s_mov_b32 m0, 61920
s_add_u32 s79, s82, 131072
Ta 214,255,13160
Maaa 52,232,136
Ta 216,240,776
buffer_load_dwordx4 v248, s[72:75], s79 offen offset:0 lds
Maaa 52,228,132
Ta 218,240,4904
Maaa 52,224,128
Ta 220,240,9032
buffer_load_dwordx4 v249, s[72:75], s79 offen offset:1032 lds
Maaa 60,240,80
Ta 222,240,13160
Maaa 60,244,84
Maaa 60,248,88
buffer_load_dwordx4 v250, s[72:75], s79 offen offset:2064 lds
Maaa 60,252,92
Maaa 64,252,108
buffer_load_dwordx4 v251, s[72:75], s79 offen offset:3096 lds
Maaa 64,248,104
Maaa 64,244,100
s_add_u32 s53, s17, 36864
Maaa 64,240,96
buffer_load_dwordx4 v[192:195], v241, s[84:87], s53 offen offset:0 sc1
Maaa 68,240,112
Maaa 68,244,116
buffer_load_dwordx4 v[196:199], v241, s[84:87], s53 offen offset:1024 sc1
Maaa 68,248,120
Maaa 68,252,124
Maaa 72,252,140
buffer_load_dwordx4 v[200:203], v241, s[84:87], s53 offen offset:2048 sc1
Maaa 72,248,136
Maaa 72,244,132
buffer_load_dwordx4 v[204:207], v241, s[84:87], s53 offen offset:3072 sc1
Maaa 72,240,128
Wl 0
Mvaa 120,192,80
s_mov_b32 m0, 115584
s_add_u32 s79, s82, 147456
Mvaa 120,196,84
Ta 224,255,0
buffer_load_dwordx4 v248, s[72:75], s79 offen offset:0 lds
Mvaa 120,200,88
Ta 226,255,4128
Mvaa 120,204,92
Ta 228,255,8256
buffer_load_dwordx4 v249, s[72:75], s79 offen offset:1032 lds
Mvaa 124,204,108
Ta 230,255,12384
Mvaa 124,200,104
Ta 232,240,0
Mvaa 124,196,100
Ta 234,240,4128
buffer_load_dwordx4 v250, s[72:75], s79 offen offset:2064 lds
Mvaa 124,192,96
Ta 236,240,8256
Mvaa 128,192,112
Ta 238,240,12384
buffer_load_dwordx4 v251, s[72:75], s79 offen offset:3096 lds
Mvaa 128,196,116
Ta 240,255,8
Mvaa 128,200,120
s_add_u32 s53, s17, 53248
Ta 242,255,4136
Mvaa 128,204,124
Ta 244,255,8264
buffer_load_dwordx4 v[208:211], v241, s[84:87], s53 offen offset:0 sc1
Mvaa 132,204,140
Ta 246,255,12392
Mvaa 132,200,136
Ta 248,240,8
buffer_load_dwordx4 v[212:215], v241, s[84:87], s53 offen offset:1024 sc1
Mvaa 132,196,132
Ta 250,240,4136
Mvaa 132,192,128
Ta 252,240,8264
Mvaa 140,208,80
Ta 254,240,12392
buffer_load_dwordx4 v[216:219], v241, s[84:87], s53 offen offset:2048 sc1
Mvaa 140,212,84
Mvaa 140,216,88
buffer_load_dwordx4 v[220:223], v241, s[84:87], s53 offen offset:3072 sc1
Mvaa 140,220,92
v_lshlrev_b32 v242, 2, v245
global_load_dword v242, v242, s[50:51] sc1
Mvaa 144,220,108
s_mov_b32 m0, 119712
s_add_u32 s79, s82, 163840
Mvaa 144,216,104
buffer_load_dwordx4 v248, s[72:75], s79 offen offset:0 lds
Mvaa 144,212,100
Mvaa 144,208,96
buffer_load_dwordx4 v249, s[72:75], s79 offen offset:1032 lds
Mvaa 148,208,112
Mvaa 148,212,116
Mvaa 148,216,120
buffer_load_dwordx4 v250, s[72:75], s79 offen offset:2064 lds
Mvaa 148,220,124
Mvaa 152,220,140
buffer_load_dwordx4 v251, s[72:75], s79 offen offset:3096 lds
Mvaa 152,216,136
Mvaa 152,212,132
s_mov_b32 m0, 123840
s_add_u32 s79, s82, 180224
Mvaa 152,208,128
Wl 0
buffer_load_dwordx4 v248, s[72:75], s79 offen offset:0 lds
Mvaa 80,224,80
s_barrier
Mvaa 80,228,84
Ta 192,240,16768
buffer_load_dwordx4 v249, s[72:75], s79 offen offset:1032 lds
Mvaa 80,232,88
Ta 194,240,20896
Mvaa 80,236,92
Ta 196,240,25024
Mvaa 84,236,108
Ta 198,240,29152
buffer_load_dwordx4 v250, s[72:75], s79 offen offset:2064 lds
Mvaa 84,232,104
Ta 200,255,16768
Mvaa 84,228,100
Ta 202,255,20896
buffer_load_dwordx4 v251, s[72:75], s79 offen offset:3096 lds
Mvaa 84,224,96
Ta 204,255,25024
Mvaa 88,224,112
)A"
R"A(s_mov_b32 m0, 127968
s_add_u32 s79, s82, 196608
Ta 206,255,29152
Mvaa 88,228,116
Ta 208,240,16776
buffer_load_dwordx4 v248, s[72:75], s79 offen offset:0 lds
Mvaa 88,232,120
Ta 210,240,20904
Mvaa 88,236,124
Ta 212,240,25032
buffer_load_dwordx4 v249, s[72:75], s79 offen offset:1032 lds
Mvaa 92,236,140
Ta 214,240,29160
Mvaa 92,232,136
Ta 216,255,16776
Mvaa 92,228,132
Ta 218,255,20904
buffer_load_dwordx4 v250, s[72:75], s79 offen offset:2064 lds
Mvaa 92,224,128
Ta 220,255,25032
Mvaa 100,240,80
Ta 222,255,29160
buffer_load_dwordx4 v251, s[72:75], s79 offen offset:3096 lds
Mvaa 100,244,84
Mvaa 100,248,88
s_add_u32 s53, s18, 4096
Mvaa 100,252,92
buffer_load_dwordx4 v[0:3], v241, s[84:87], s53 offen offset:0 sc1
Mvaa 104,252,108
Mvaa 104,248,104
buffer_load_dwordx4 v[4:7], v241, s[84:87], s53 offen offset:1024 sc1
Mvaa 104,244,100
Mvaa 104,240,96
Mvaa 108,240,112
buffer_load_dwordx4 v[8:11], v241, s[84:87], s53 offen offset:2048 sc1
Mvaa 108,244,116
Mvaa 108,248,120
buffer_load_dwordx4 v[12:15], v241, s[84:87], s53 offen offset:3072 sc1
Mvaa 108,252,124
Mvaa 112,252,140
s_add_u32 s53, s18, 20480
Mvaa 112,248,136
buffer_load_dwordx4 v[16:19], v241, s[84:87], s53 offen offset:0 sc1
Mvaa 112,244,132
Mvaa 112,240,128
Wl 0
s_waitcnt vmcnt(18)
buffer_load_dwordx4 v[20:23], v241, s[84:87], s53 offen offset:1024 sc1
Maav 0,192,160
s_waitcnt vmcnt(18)
s_mov_b32 s58, 0
v_cmp_gt_i32 s[64:65], 0, v242
v_and_b32 v242, 0x7fffffff, v242
v_cmp_ne_u32 vcc, s81, v242
s_nop 0
s_and_b64 vcc, vcc, s[70:71]
s_cbranch_scc1 SPINP3e4_%=
OKP3e4_%=:
s_and_b32 s23, s65, s71
s_cselect_b32 s23, 8192, 0
s_andn2_b32 s56, s22, 8192
s_add_u32 s23, s23, s56
s_add_u32 s23, s23, 4194304
Maav 0,196,164
Ta 224,240,17024
Maav 0,200,168
Ta 226,240,21152
buffer_load_dwordx4 v[24:27], v241, s[84:87], s53 offen offset:2048 sc1
Maav 0,204,172
Ta 228,240,25280
Maav 4,204,188
Ta 230,240,29408
buffer_load_dwordx4 v[28:31], v241, s[84:87], s53 offen offset:3072 sc1
Maav 4,200,184
Ta 232,255,17024
Maav 4,196,180
s_add_u32 s53, s23, 0
Ta 234,255,21152
Maav 4,192,176
Ta 236,255,25280
buffer_load_dwordx4 a[80:83], v241, s[84:87], s53 offen offset:0 sc1
Maav 8,192,192
Ta 238,255,29408
Maav 8,196,196
Ta 240,240,17032
buffer_load_dwordx4 a[84:87], v241, s[84:87], s53 offen offset:1024 sc1
Maav 8,200,200
Ta 242,240,21160
Maav 8,204,204
Ta 244,240,25288
Maav 12,204,220
Ta 246,240,29416
buffer_load_dwordx4 a[88:91], v241, s[84:87], s53 offen offset:2048 sc1
Maav 12,200,216
Ta 248,255,17032
Maav 12,196,212
Ta 250,255,21160
buffer_load_dwordx4 a[92:95], v241, s[84:87], s53 offen offset:3072 sc1
Maav 12,192,208
Ta 252,255,25288
Maav 20,208,160
s_add_u32 s53, s23, 16384
Ta 254,255,29416
Maav 20,212,164
buffer_load_dwordx4 a[96:99], v241, s[84:87], s53 offen offset:0 sc1
Maav 20,216,168
Maav 20,220,172
buffer_load_dwordx4 a[100:103], v241, s[84:87], s53 offen offset:1024 sc1
Maav 24,220,188
Maav 24,216,184
Maav 24,212,180
buffer_load_dwordx4 a[104:107], v241, s[84:87], s53 offen offset:2048 sc1
Maav 24,208,176
Maav 28,208,192
buffer_load_dwordx4 a[108:111], v241, s[84:87], s53 offen offset:3072 sc1
Maav 28,212,196
Maav 28,216,200
s_add_u32 s53, s23, 32768
Maav 28,220,204
buffer_load_dwordx4 a[112:115], v241, s[84:87], s53 offen offset:0 sc1
Maav 32,220,220
Maav 32,216,216
buffer_load_dwordx4 a[116:119], v241, s[84:87], s53 offen offset:1024 sc1
Maav 32,212,212
Maav 32,208,208
Wl 0
Maav 40,224,160
buffer_load_dwordx4 a[120:123], v241, s[84:87], s53 offen offset:2048 sc1
Maav 40,228,164
Ta 192,240,17280
Maav 40,232,168
Ta 194,240,21408
buffer_load_dwordx4 a[124:127], v241, s[84:87], s53 offen offset:3072 sc1
Maav 40,236,172
Ta 196,240,25536
Maav 44,236,188
s_add_u32 s53, s23, 49152
Ta 198,240,29664
Maav 44,232,184
Ta 200,255,17280
buffer_load_dwordx4 a[128:131], v241, s[84:87], s53 offen offset:0 sc1
Maav 44,228,180
Ta 202,255,21408
Maav 44,224,176
Ta 204,255,25536
buffer_load_dwordx4 a[132:135], v241, s[84:87], s53 offen offset:1024 sc1
Maav 48,224,192
Ta 206,255,29664
Maav 48,228,196
Ta 208,240,17288
Maav 48,232,200
Ta 210,240,21416
buffer_load_dwordx4 a[136:139], v241, s[84:87], s53 offen offset:2048 sc1
Maav 48,236,204
Ta 212,240,25544
Maav 52,236,220
Ta 214,240,29672
buffer_load_dwordx4 a[140:143], v241, s[84:87], s53 offen offset:3072 sc1
Maav 52,232,216
Ta 216,255,17288
Maav 52,228,212
s_mov_b32 m0, 66048
s_add_u32 s79, s83, 81920
Ta 218,255,21416
Maav 52,224,208
Ta 220,255,25544
buffer_load_dwordx4 v248, s[72:75], s79 offen offset:0 lds
Maav 60,240,160
Ta 222,255,29672
Maav 60,244,164
buffer_load_dwordx4 v249, s[72:75], s79 offen offset:1032 lds
Maav 60,248,168
Maav 60,252,172
Maav 64,252,188
buffer_load_dwordx4 v250, s[72:75], s79 offen offset:2064 lds
Maav 64,248,184
Maav 64,244,180
buffer_load_dwordx4 v251, s[72:75], s79 offen offset:3096 lds
Maav 64,240,176
Maav 68,240,192
s_add_u32 s53, s18, 36864
Maav 68,244,196
buffer_load_dwordx4 v[32:35], v241, s[84:87], s53 offen offset:0 sc1
Maav 68,248,200
Maav 68,252,204
buffer_load_dwordx4 v[36:39], v241, s[84:87], s53 offen offset:1024 sc1
Maav 72,252,220
Maav 72,248,216
Maav 72,244,212
buffer_load_dwordx4 v[40:43], v241, s[84:87], s53 offen offset:2048 sc1
Maav 72,240,208
Wl 0
Mvav 120,192,160
buffer_load_dwordx4 v[44:47], v241, s[84:87], s53 offen offset:3072 sc1
Mvav 120,196,164
Ta 224,240,16512
Mvav 120,200,168
s_mov_b32 m0, 70176
s_add_u32 s79, s83, 98304
Ta 226,240,20640
Mvav 120,204,172
Ta 228,240,24768
buffer_load_dwordx4 v248, s[72:75], s79 offen offset:0 lds
Mvav 124,204,188
Ta 230,240,28896
Mvav 124,200,184
Ta 232,255,16512
buffer_load_dwordx4 v249, s[72:75], s79 offen offset:1032 lds
Mvav 124,196,180
Ta 234,255,20640
Mvav 124,192,176
Ta 236,255,24768
Mvav 128,192,192
Ta 238,255,28896
buffer_load_dwordx4 v250, s[72:75], s79 offen offset:2064 lds
Mvav 128,196,196
Ta 240,240,16520
Mvav 128,200,200
Ta 242,240,20648
buffer_load_dwordx4 v251, s[72:75], s79 offen offset:3096 lds
Mvav 128,204,204
Ta 244,240,24776
Mvav 132,204,220
s_add_u32 s53, s18, 53248
Ta 246,240,28904
Mvav 132,200,216
Ta 248,255,16520
buffer_load_dwordx4 a[160:163], v241, s[84:87], s53 offen offset:0 sc1
Mvav 132,196,212
Ta 250,255,20648
Mvav 132,192,208
Ta 252,255,24776
buffer_load_dwordx4 a[164:167], v241, s[84:87], s53 offen offset:1024 sc1
Mvav 140,208,160
Ta 254,255,28904
Mvav 140,212,164
Mvav 140,216,168
buffer_load_dwordx4 a[168:171], v241, s[84:87], s53 offen offset:2048 sc1
Mvav 140,220,172
Mvav 144,220,188
buffer_load_dwordx4 a[172:175], v241, s[84:87], s53 offen offset:3072 sc1
Mvav 144,216,184
Mvav 144,212,180
s_mov_b32 m0, 74304
s_add_u32 s79, s83, 114688
Mvav 144,208,176
buffer_load_dwordx4 v248, s[72:75], s79 offen offset:0 lds
Mvav 148,208,192
Mvav 148,212,196
buffer_load_dwordx4 v249, s[72:75], s79 offen offset:1032 lds
Mvav 148,216,200
Mvav 148,220,204
Mvav 152,220,220
buffer_load_dwordx4 v250, s[72:75], s79 offen offset:2064 lds
Mvav 152,216,216
Mvav 152,212,212
buffer_load_dwordx4 v251, s[72:75], s79 offen offset:3096 lds
Mvav 152,208,208
Wl 0
Mvav 80,224,160
s_barrier
s_mov_b32 m0, 78432
s_add_u32 s79, s83, 131072
Mvav 80,228,164
Ta 192,240,33280
buffer_load_dwordx4 v248, s[72:75], s79 offen offset:0 lds
Mvav 80,232,168
Ta 194,240,37408
Mvav 80,236,172
Ta 196,240,41536
buffer_load_dwordx4 v249, s[72:75], s79 offen offset:1032 lds
Mvav 84,236,188
Ta 198,240,45664
Mvav 84,232,184
Ta 200,255,33280
Mvav 84,228,180
Ta 202,255,37408
buffer_load_dwordx4 v250, s[72:75], s79 offen offset:2064 lds
Mvav 84,224,176
Ta 204,255,41536
Mvav 88,224,192
Ta 206,255,45664
buffer_load_dwordx4 v251, s[72:75], s79 offen offset:3096 lds
Mvav 88,228,196
Ta 208,240,33288
Mvav 88,232,200
s_mov_b32 m0, 0
s_add_u32 s79, s83, 147456
Ta 210,240,37416
Mvav 88,236,204
Ta 212,240,41544
buffer_load_dwordx4 v248, s[72:75], s79 offen offset:0 lds
Mvav 92,236,220
Ta 214,240,45672
Mvav 92,232,216
Ta 216,255,33288
buffer_load_dwordx4 v249, s[72:75], s79 offen offset:1032 lds
Mvav 92,228,212
Ta 218,255,37416
Mvav 92,224,208
Ta 220,255,41544
Mvav 100,240,160
Ta 222,255,45672
buffer_load_dwordx4 v250, s[72:75], s79 offen offset:2064 lds
Mvav 100,244,164
Mvav 100,248,168
buffer_load_dwordx4 v251, s[72:75], s79 offen offset:3096 lds
Mvav 100,252,172
Mvav 104,252,188
s_mov_b32 m0, 4128
s_add_u32 s79, s83, 163840
Mvav 104,248,184
buffer_load_dwordx4 v248, s[72:75], s79 offen offset:0 lds
Mvav 104,244,180
Mvav 104,240,176
buffer_load_dwordx4 v249, s[72:75], s79 offen offset:1032 lds
Mvav 108,240,192
Mvav 108,244,196
Mvav 108,248,200
buffer_load_dwordx4 v250, s[72:75], s79 offen offset:2064 lds
Mvav 108,252,204
Mvav 112,252,220
buffer_load_dwordx4 v251, s[72:75], s79 offen offset:3096 lds
Mvav 112,248,216
Mvav 112,244,212
s_mov_b32 m0, 8256
s_add_u32 s79, s83, 180224
Mvav 112,240,208
Wl 0
s_waitcnt vmcnt(32)
buffer_load_dwordx4 v248, s[72:75], s79 offen offset:0 lds
Maaa 0,192,80
Maaa 0,196,84
Ta 224,240,33536
buffer_load_dwordx4 v249, s[72:75], s79 offen offset:1032 lds
Maaa 0,200,88
Ta 226,240,37664
Maaa 0,204,92
Ta 228,240,41792
Maaa 4,204,108
Ta 230,240,45920
buffer_load_dwordx4 v250, s[72:75], s79 offen offset:2064 lds
Maaa 4,200,104
Ta 232,255,33536
Maaa 4,196,100
Ta 234,255,37664
buffer_load_dwordx4 v251, s[72:75], s79 offen offset:3096 lds
Maaa 4,192,96
Ta 236,255,41792
Maaa 8,192,112
s_mov_b32 m0, 12384
s_add_u32 s79, s83, 196608
Ta 238,255,45920
Maaa 8,196,116
Ta 240,240,33544
buffer_load_dwordx4 v248, s[72:75], s79 offen offset:0 lds
Maaa 8,200,120
Ta 242,240,37672
Maaa 8,204,124
Ta 244,240,41800
buffer_load_dwordx4 v249, s[72:75], s79 offen offset:1032 lds
Maaa 12,204,140
Ta 246,240,45928
Maaa 12,200,136
Ta 248,255,33544
Maaa 12,196,132
Ta 250,255,37672
buffer_load_dwordx4 v250, s[72:75], s79 offen offset:2064 lds
Maaa 12,192,128
Ta 252,255,41800
Maaa 20,208,80
Ta 254,255,45928
buffer_load_dwordx4 v251, s[72:75], s79 offen offset:3096 lds
Maaa 20,212,84
Maaa 20,216,88
s_add_u32 s53, s22, 4096
Maaa 20,220,92
buffer_load_dwordx4 v[160:163], v241, s[84:87], s53 offen offset:0 sc1
Maaa 24,220,108
Maaa 24,216,104
buffer_load_dwordx4 v[164:167], v241, s[84:87], s53 offen offset:1024 sc1
Maaa 24,212,100
Maaa 24,208,96
Maaa 28,208,112
buffer_load_dwordx4 v[168:171], v241, s[84:87], s53 offen offset:2048 sc1
Maaa 28,212,116
Maaa 28,216,120
buffer_load_dwordx4 v[172:175], v241, s[84:87], s53 offen offset:3072 sc1
Maaa 28,220,124
Maaa 32,220,140
s_add_u32 s53, s22, 20480
Maaa 32,216,136
buffer_load_dwordx4 v[176:179], v241, s[84:87], s53 offen offset:0 sc1
Maaa 32,212,132
Maaa 32,208,128
Wl 0
buffer_load_dwordx4 v[180:183], v241, s[84:87], s53 offen offset:1024 sc1
Maaa 40,224,80
s_barrier
Maaa 40,228,84
)A"
R"A(Ta 192,240,49792
Maaa 40,232,88
Ta 194,240,53920
buffer_load_dwordx4 v[184:187], v241, s[84:87], s53 offen offset:2048 sc1
Maaa 40,236,92
Ta 196,240,58048
Maaa 44,236,108
Ta 198,240,62176
buffer_load_dwordx4 v[188:191], v241, s[84:87], s53 offen offset:3072 sc1
Maaa 44,232,104
Ta 200,255,49792
Maaa 44,228,100
s_add_u32 s53, s22, 36864
Ta 202,255,53920
Maaa 44,224,96
Ta 204,255,58048
buffer_load_dwordx4 v[192:195], v241, s[84:87], s53 offen offset:0 sc1
Maaa 48,224,112
Ta 206,255,62176
Maaa 48,228,116
Ta 208,240,49800
buffer_load_dwordx4 v[196:199], v241, s[84:87], s53 offen offset:1024 sc1
Maaa 48,232,120
Ta 210,240,53928
Maaa 48,236,124
Ta 212,240,58056
Maaa 52,236,140
Ta 214,240,62184
buffer_load_dwordx4 v[200:203], v241, s[84:87], s53 offen offset:2048 sc1
Maaa 52,232,136
Ta 216,255,49800
Maaa 52,228,132
Ta 218,255,53928
buffer_load_dwordx4 v[204:207], v241, s[84:87], s53 offen offset:3072 sc1
Maaa 52,224,128
Ta 220,255,58056
Maaa 60,240,80
s_add_u32 s53, s22, 53248
Ta 222,255,62184
Maaa 60,244,84
buffer_load_dwordx4 v[208:211], v241, s[84:87], s53 offen offset:0 sc1
Maaa 60,248,88
Maaa 60,252,92
buffer_load_dwordx4 v[212:215], v241, s[84:87], s53 offen offset:1024 sc1
Maaa 64,252,108
Maaa 64,248,104
Maaa 64,244,100
buffer_load_dwordx4 v[216:219], v241, s[84:87], s53 offen offset:2048 sc1
Maaa 64,240,96
Maaa 68,240,112
buffer_load_dwordx4 v[220:223], v241, s[84:87], s53 offen offset:3072 sc1
Maaa 68,244,116
Maaa 68,248,120
Maaa 68,252,124
Maaa 72,252,140
Maaa 72,248,136
Maaa 72,244,132
Maaa 72,240,128
Wl 0
s_waitcnt vmcnt(40)
Maav 0,192,0
Maav 0,196,4
Ta 224,240,50048
Maav 0,200,8
Ta 226,240,54176
Maav 0,204,12
Ta 228,240,58304
Maav 4,204,28
Ta 230,240,62432
Maav 4,200,24
Ta 232,255,50048
Maav 4,196,20
Ta 234,255,54176
Maav 4,192,16
Ta 236,255,58304
Maav 8,192,32
Ta 238,255,62432
Maav 8,196,36
Ta 240,240,50056
Maav 8,200,40
Ta 242,240,54184
Maav 8,204,44
Ta 244,240,58312
Maaa 12,204,172
Ta 246,240,62440
Maaa 12,200,168
Ta 248,255,50056
Maaa 12,196,164
Ta 250,255,54184
Maaa 12,192,160
Ta 252,255,58312
Maav 20,208,0
Ta 254,255,62440
Maav 20,212,4
Maav 20,216,8
Maav 20,220,12
Maav 24,220,28
Maav 24,216,24
Maav 24,212,20
Maav 24,208,16
Maav 28,208,32
Maav 28,212,36
Maav 28,216,40
Maav 28,220,44
Maaa 32,220,172
Maaa 32,216,168
Maaa 32,212,164
Maaa 32,208,160
Wl 0
Maav 40,224,0
s_waitcnt vmcnt(16)
s_barrier
Maav 40,228,4
Ta 192,255,256
Maav 40,232,8
Ta 194,255,4384
Maav 40,236,12
Ta 196,255,8512
Maav 44,236,28
Ta 198,255,12640
Maav 44,232,24
Ta 200,240,256
Maav 44,228,20
Ta 202,240,4384
Maav 44,224,16
Ta 204,240,8512
Maav 48,224,32
Ta 206,240,12640
Maav 48,228,36
Ta 208,255,264
Maav 48,232,40
Ta 210,255,4392
Maav 48,236,44
Ta 212,255,8520
Maaa 52,236,172
Ta 214,255,12648
Maaa 52,232,168
Ta 216,240,264
Maaa 52,228,164
Ta 218,240,4392
Maaa 52,224,160
Ta 220,240,8520
Maav 60,240,0
Ta 222,240,12648
Maav 60,244,4
Maav 60,248,8
Maav 60,252,12
Maav 64,252,28
Maav 64,248,24
Maav 64,244,20
Maav 64,240,16
Maav 68,240,32
Maav 68,244,36
Maav 68,248,40
Maav 68,252,44
Maaa 72,252,172
Maaa 72,248,168
Maaa 72,244,164
Maaa 72,240,160
Wl 0
s_waitcnt vmcnt(0)
Maav 0,192,160
Maav 0,196,164
Ta 224,255,512
Maav 0,200,168
Ta 226,255,4640
Maav 0,204,172
Ta 228,255,8768
Maav 4,204,188
Ta 230,255,12896
Maav 4,200,184
Ta 232,240,512
Maav 4,196,180
Ta 234,240,4640
Maav 4,192,176
Ta 236,240,8768
Maav 8,192,192
Ta 238,240,12896
Maav 8,196,196
Ta 240,255,520
Maav 8,200,200
Ta 242,255,4648
Maav 8,204,204
Ta 244,255,8776
Maav 12,204,220
Ta 246,255,12904
Maav 12,200,216
Ta 248,240,520
Maav 12,196,212
Ta 250,240,4648
Maav 12,192,208
Ta 252,240,8776
Maav 20,208,160
Ta 254,240,12904
Maav 20,212,164
Maav 20,216,168
Maav 20,220,172
Maav 24,220,188
Maav 24,216,184
Maav 24,212,180
Maav 24,208,176
Maav 28,208,192
Maav 28,212,196
Maav 28,216,200
Maav 28,220,204
Maav 32,220,220
Ar 48,0
Ar 49,1
Maav 32,216,216
Ar 50,2
Ar 51,3
Maav 32,212,212
Pk 56,48,49
Pk 57,50,51
Maav 32,208,208
Wl 0
Ar 52,20
Ar 53,21
Maav 40,224,160
Ar 54,22
Ar 55,23
Maav 40,228,164
Pk 58,52,53
Pk 59,54,55
Maav 40,232,168
s_mov_b32 s57, 81920
buffer_store_dwordx4 v[56:59], v241, s[96:99], s57 offen offset:0 sc1
Maav 40,236,172
Ar 48,4
Ar 49,5
Maav 44,236,188
Ar 50,6
Ar 51,7
Maav 44,232,184
Pk 60,48,49
Pk 61,50,51
Maav 44,228,180
Ar 52,24
Ar 53,25
Maav 44,224,176
Ar 54,26
Ar 55,27
Maav 48,224,192
Pk 62,52,53
Pk 63,54,55
Maav 48,228,196
buffer_store_dwordx4 v[60:63], v241, s[96:99], s57 offen offset:1024 sc1
Maav 48,232,200
Ar 48,8
Ar 49,9
Maav 48,236,204
Ar 50,10
Ar 51,11
Maav 52,236,220
Ta 192,240,33792
Pk 64,48,49
Maav 52,232,216
Ta 194,240,37920
Pk 65,50,51
Maav 52,228,212
Ta 196,240,42048
Ar 52,28
Maav 52,224,208
Ta 198,240,46176
Ar 53,29
Maav 60,240,160
Ta 200,255,33792
Ar 54,30
Maav 60,244,164
Ta 202,255,37920
Ar 55,31
Maav 60,248,168
Ta 204,255,42048
Pk 66,52,53
Maav 60,252,172
Ta 206,255,46176
Pk 67,54,55
Maav 64,252,188
Ta 208,240,33800
buffer_store_dwordx4 v[64:67], v241, s[96:99], s57 offen offset:2048 sc1
Maav 64,248,184
Ta 210,240,37928
Ar 48,12
Maav 64,244,180
Ta 212,240,42056
Ar 49,13
Maav 64,240,176
Ta 214,240,46184
Ar 50,14
Maav 68,240,192
Ta 216,255,33800
Ar 51,15
Maav 68,244,196
Ta 218,255,37928
Pk 68,48,49
Maav 68,248,200
Ta 220,255,42056
Pk 69,50,51
Maav 68,252,204
Ta 222,255,46184
Ar 52,32
Maav 72,252,220
Ar 53,33
Ar 54,34
Maav 72,248,216
Ar 55,35
Pk 70,52,53
Maav 72,244,212
Pk 71,54,55
Maav 72,240,208
Wl 0
buffer_store_dwordx4 v[68:71], v241, s[96:99], s57 offen offset:3072 sc1
Mvaa 120,192,80
Ar 48,40
Ar 49,41
Mvaa 120,196,84
Ar 50,42
Ar 51,43
Mvaa 120,200,88
Pk 56,48,49
Pk 57,50,51
Mvaa 120,204,92
Ar 52,60
Ar 53,61
Mvaa 124,204,108
Ar 54,62
Ar 55,63
Mvaa 124,200,104
Pk 58,52,53
Pk 59,54,55
Mvaa 124,196,100
s_mov_b32 s57, 98304
buffer_store_dwordx4 v[56:59], v241, s[96:99], s57 offen offset:0 sc1
Mvaa 124,192,96
Ar 48,44
Ar 49,45
Mvaa 128,192,112
Ar 50,46
Ar 51,47
Mvaa 128,196,116
Pk 60,48,49
Pk 61,50,51
Mvaa 128,200,120
Ar 52,64
Ar 53,65
Mvaa 128,204,124
Ar 54,66
Ar 55,67
Mvaa 132,204,140
Ta 224,240,50304
Pk 62,52,53
Mvaa 132,200,136
Ta 226,240,54432
Pk 63,54,55
Mvaa 132,196,132
Ta 228,240,58560
buffer_store_dwordx4 v[60:63], v241, s[96:99], s57 offen offset:1024 sc1
Mvaa 132,192,128
Ta 230,240,62688
Ar 48,48
Mvaa 140,208,80
Ta 232,255,50304
Ar 49,49
Mvaa 140,212,84
Ta 234,255,54432
Ar 50,50
Mvaa 140,216,88
Ta 236,255,58560
Ar 51,51
Mvaa 140,220,92
Ta 238,255,62688
Pk 64,48,49
Mvaa 144,220,108
Ta 240,240,50312
Pk 65,50,51
Mvaa 144,216,104
Ta 242,240,54440
Ar 52,68
Mvaa 144,212,100
Ta 244,240,58568
Ar 53,69
Mvaa 144,208,96
Ta 246,240,62696
Ar 54,70
Mvaa 148,208,112
Ta 248,255,50312
Ar 55,71
Mvaa 148,212,116
Ta 250,255,54440
Pk 66,52,53
Mvaa 148,216,120
Ta 252,255,58568
Pk 67,54,55
Mvaa 148,220,124
Ta 254,255,62696
buffer_store_dwordx4 v[64:67], v241, s[96:99], s57 offen offset:2048 sc1
Mvaa 152,220,140
Ar 48,52
Ar 49,53
Mvaa 152,216,136
)A"
R"A(Ar 50,54
Ar 51,55
Mvaa 152,212,132
Pk 68,48,49
Pk 69,50,51
Mvaa 152,208,128
Wl 0
Ar 52,72
Ar 53,73
Mvav 120,224,0
Ar 54,74
Ar 55,75
Mvav 120,228,4
Ta 192,255,768
Pk 70,52,53
Mvav 120,232,8
Ta 194,255,4896
Pk 71,54,55
Mvav 120,236,12
Ta 196,255,9024
buffer_store_dwordx4 v[68:71], v241, s[96:99], s57 offen offset:3072 sc1
Mvav 124,236,28
Ta 198,255,13152
Mvav 124,232,24
Ta 200,240,768
v_bfe_u32 v76, v245, 2, 2
Mvav 124,228,20
Ta 202,240,4896
v_lshrrev_b32 v77, 4, v245
Mvav 124,224,16
Ta 204,240,9024
v_cmp_eq_u32 vcc, v76, v77
Mvav 128,224,32
Ta 206,240,13152
v_and_b32 v76, 1, v245
Mvav 128,228,36
Ta 208,255,776
v_lshlrev_b32 v76, 4, v76
Mvav 128,232,40
Ta 210,255,4904
v_mov_b32 v77, 0x3f80
Mvav 128,236,44
Ta 212,255,9032
v_lshlrev_b32 v77, v76, v77
Mvaa 132,236,172
Ta 214,255,13160
v_cndmask_b32 v77, 0, v77, vcc
Mvaa 132,232,168
Ta 216,240,776
v_and_b32 v76, 2, v245
Mvaa 132,228,164
Ta 218,240,4904
v_cmp_eq_u32 vcc, 0, v76
Mvaa 132,224,160
Ta 220,240,9032
v_mov_b32 v50, 0
Mvav 140,240,0
Ta 222,240,13160
v_mov_b32 v51, 0
Mvav 140,244,4
v_cndmask_b32 v48, 0, v77, vcc
v_sub_u32 v49, v77, v48
Mvav 140,248,8
v_mov_b32 v52, v48
v_mov_b32 v53, v49
Mvav 140,252,12
v_and_b32 v250, 15, v245
v_lshlrev_b32 v250, 11, v250
Mvav 144,252,28
v_lshrrev_b32 v251, 4, v245
v_lshl_add_u32 v250, v251, 4, v250
Mvav 144,248,24
v_lshlrev_b32 v251, 2, v245
Mvav 144,244,20
Mvav 144,240,16
Mvav 148,240,32
Mvav 148,244,36
Mvav 148,248,40
Mvav 148,252,44
Mvaa 152,252,172
Mvaa 152,248,168
Mvaa 152,244,164
Mvaa 152,240,160
Wl 0
Mvav 120,192,160
Mvav 120,196,164
Ta 224,240,33024
Mvav 120,200,168
Ta 226,240,37152
Mvav 120,204,172
Ta 228,240,41280
Mvav 124,204,188
Ta 230,240,45408
Mvav 124,200,184
Ta 232,255,33024
Mvav 124,196,180
Ta 234,255,37152
Mvav 124,192,176
Ta 236,255,41280
Mvav 128,192,192
Ta 238,255,45408
Mvav 128,196,196
Ta 240,240,33032
Mvav 128,200,200
Ta 242,240,37160
Mvav 128,204,204
Ta 244,240,41288
Mvav 132,204,220
Ta 246,240,45416
Mvav 132,200,216
Ta 248,255,33032
Mvav 132,196,212
Ta 250,255,37160
Mvav 132,192,208
Ta 252,255,41288
Mvav 140,208,160
Ta 254,255,45416
Mvav 140,212,164
Mvav 140,216,168
Mvav 140,220,172
Mvav 144,220,188
Mvav 144,216,184
s_waitcnt vmcnt(0)
s_mov_b64 exec, 0x30
global_store_dword v243, v244, s[94:95] offset:32 sc1
s_mov_b64 exec, -1
Mvav 144,212,180
Mvav 144,208,176
Mvav 148,208,192
Mvav 148,212,196
Mvav 148,216,200
Mvav 148,220,204
Mvav 152,220,220
Pk 56,120,121
Pk 57,122,123
Mvav 152,216,216
Pk 58,140,141
Pk 59,142,143
Mvav 152,212,212
s_mov_b32 s57, 114688
buffer_store_dwordx4 v[56:59], v241, s[96:99], s57 offen offset:0 sc1
Mvav 152,208,208
Wl 0
Mvaa 80,224,80
Pk 60,124,125
Pk 61,126,127
Mvaa 80,228,84
Pk 62,144,145
Pk 63,146,147
Mvaa 80,232,88
buffer_store_dwordx4 v[60:63], v241, s[96:99], s57 offen offset:1024 sc1
Mvaa 80,236,92
Mvaa 84,236,108
Pk 64,128,129
Pk 65,130,131
Mvaa 84,232,104
Pk 66,148,149
Pk 67,150,151
Mvaa 84,228,100
buffer_store_dwordx4 v[64:67], v241, s[96:99], s57 offen offset:2048 sc1
Mvaa 84,224,96
Mvaa 88,224,112
Pk 68,132,133
Pk 69,134,135
Mvaa 88,228,116
Pk 70,152,153
Pk 71,154,155
Mvaa 88,232,120
buffer_store_dwordx4 v[68:71], v241, s[96:99], s57 offen offset:3072 sc1
Mvaa 88,236,124
Mvaa 92,236,140
Ta 192,240,49536
Mvaa 92,232,136
Ta 194,240,53664
Mvaa 92,228,132
Ta 196,240,57792
Mvaa 92,224,128
Ta 198,240,61920
Mvaa 100,240,80
Ta 200,255,49536
Mvaa 100,244,84
Ta 202,255,53664
Mvaa 100,248,88
Ta 204,255,57792
Mvaa 100,252,92
Ta 206,255,61920
Mvaa 104,252,108
Ta 208,240,49544
Mvaa 104,248,104
Ta 210,240,53672
Mvaa 104,244,100
Ta 212,240,57800
v_lshlrev_b32 v242, 2, v245
global_load_dword v242, v242, s[50:51] sc1
Mvaa 104,240,96
Ta 214,240,61928
Mvaa 108,240,112
Ta 216,255,49544
Mvaa 108,244,116
Ta 218,255,53672
Mvaa 108,248,120
Ta 220,255,57800
Mvaa 108,252,124
Ta 222,255,61928
Mvaa 112,252,140
Mvaa 112,248,136
Mvaa 112,244,132
Mvaa 112,240,128
Wl 0
Mvav 80,192,0
Mvav 80,196,4
Ta 224,255,0
Mvav 80,200,8
Ta 226,255,4128
Mvav 80,204,12
Ta 228,255,8256
Mvav 84,204,28
Ta 230,255,12384
Mvav 84,200,24
Ta 232,240,0
Mvav 84,196,20
Ta 234,240,4128
Mvav 84,192,16
Ta 236,240,8256
Mvav 88,192,32
Ta 238,240,12384
Mvav 88,196,36
Ta 240,255,8
Mvav 88,200,40
Ta 242,255,4136
Mvav 88,204,44
Ta 244,255,8264
Mvaa 92,204,172
Ta 246,255,12392
Mvaa 92,200,168
Ta 248,240,8
Mvaa 92,196,164
Ta 250,240,4136
Mvaa 92,192,160
Ta 252,240,8264
Mvav 100,208,0
Ta 254,240,12392
Mvav 100,212,4
Mvav 100,216,8
s_waitcnt vmcnt(1)
s_mov_b64 exec, 0x40
global_store_dword v243, v244, s[94:95] offset:32 sc1
s_mov_b64 exec, -1
Mvav 100,220,12
Mvav 104,220,28
Mvav 104,216,24
Mvav 104,212,20
s_waitcnt vmcnt(1)
s_mov_b32 s58, 0
v_cmp_gt_i32 s[64:65], 0, v242
v_and_b32 v242, 0x7fffffff, v242
v_cmp_ne_u32 vcc, s81, v242
s_nop 0
s_and_b64 vcc, vcc, s[34:35]
s_cbranch_scc1 SPINP2e4_%=
OKP2e4_%=:
Mvav 104,208,16
s_add_u32 s53, s27, 81920
buffer_load_dwordx4 a[0:3], v241, s[40:43], s53 offen offset:0 sc1
Mvav 108,208,32
Mvav 108,212,36
Mvav 108,216,40
buffer_load_dwordx4 a[4:7], v241, s[40:43], s53 offen offset:1024 sc1
Mvav 108,220,44
Mvaa 112,220,172
Mvaa 112,216,168
buffer_load_dwordx4 a[8:11], v241, s[40:43], s53 offen offset:2048 sc1
Mvaa 112,212,164
Mvaa 112,208,160
Wl 0
Mvav 80,224,160
buffer_load_dwordx4 a[12:15], v241, s[40:43], s53 offen offset:3072 sc1
Mvav 80,228,164
Mvav 80,232,168
Mvav 80,236,172
s_add_u32 s53, s52, 98304
buffer_load_dwordx4 a[20:23], v241, s[40:43], s53 offen offset:0 sc1
Mvav 84,236,188
Mvav 84,232,184
Mvav 84,228,180
buffer_load_dwordx4 a[24:27], v241, s[40:43], s53 offen offset:1024 sc1
Mvav 84,224,176
Mvav 88,224,192
Mvav 88,228,196
buffer_load_dwordx4 a[28:31], v241, s[40:43], s53 offen offset:2048 sc1
Mvav 88,232,200
Mvav 88,236,204
Mvav 92,236,220
buffer_load_dwordx4 a[32:35], v241, s[40:43], s53 offen offset:3072 sc1
Mvav 92,232,216
Mvav 92,228,212
Mvav 92,224,208
Mvav 100,240,160
Mvav 100,244,164
Mvav 100,248,168
Mvav 100,252,172
Mvav 104,252,188
global_load_dword v242, v251, s[50:51] sc1
Mvav 104,248,184
Mvav 104,244,180
Mvav 104,240,176
Mvav 108,240,192
Mvav 108,244,196
Mvav 108,248,200
Mvav 108,252,204
Mvav 112,252,220
Mvav 112,248,216
Mvav 112,244,212
Mvav 112,240,208
s_waitcnt vmcnt(5)
Mvva 80,48,0
Mvva 100,50,0
Mvva 84,48,4
Mvva 104,50,4
Mvva 88,48,8
Mvva 108,50,8
Mvva 92,48,12
Mvva 112,50,12
s_mov_b32 s58, 0
POLLe4_%=:
s_waitcnt vmcnt(0)
v_and_b32 v242, 0x7fffffff, v242
v_cmp_ne_u32 vcc, s81, v242
s_add_u32 s58, s58, 1
s_and_b32 s57, vcc_hi, s48
s_cbranch_scc0 GOTe4_%=
global_load_dword v242, v251, s[50:51] sc1
s_cmp_lt_u32 s58, 0x100000
s_cbranch_scc1 POLLe4_%=
GOTe4_%=:
s_add_u32 s53, s55, 114688
buffer_load_dwordx4 a[40:43], v241, s[40:43], s53 offen offset:0 sc1
buffer_load_dwordx4 a[44:47], v241, s[40:43], s53 offen offset:1024 sc1
buffer_load_dwordx4 a[48:51], v241, s[40:43], s53 offen offset:2048 sc1
buffer_load_dwordx4 a[52:55], v241, s[40:43], s53 offen offset:3072 sc1
Mvva 80,48,20
Mvva 100,50,20
Mvva 84,48,24
Mvva 104,50,24
Mvva 88,48,28
Mvva 108,50,28
Mvva 92,48,32
Mvva 112,50,32
s_waitcnt vmcnt(0)
Mvva 80,48,40
Mvva 100,50,40
Mvva 84,48,44
Mvva 104,50,44
Mvva 88,48,48
Mvva 108,50,48
Mvva 92,48,52
Mvva 112,50,52
s_nop 1
Pk 160,80,81
Pk 161,82,83
Pk 162,100,101
Pk 163,102,103
buffer_store_dwordx4 v[160:163], v250, s[88:91], 0 offen sc1
v_add_u32 v250, 32768, v250
Pk 164,84,85
Pk 165,86,87
Pk 166,104,105
Pk 167,106,107
buffer_store_dwordx4 v[164:167], v250, s[88:91], 0 offen sc1
v_add_u32 v250, 32768, v250
Pk 168,88,89
Pk 169,90,91
Pk 170,108,109
Pk 171,110,111
buffer_store_dwordx4 v[168:171], v250, s[88:91], 0 offen sc1
v_add_u32 v250, 32768, v250
Pk 172,92,93
Pk 173,94,95
Pk 174,112,113
Pk 175,114,115
buffer_store_dwordx4 v[172:175], v250, s[88:91], 0 offen sc1
v_add_u32 v250, 32768, v250
s_endpgm
SPINP1e4_%=:
s_sleep 4
v_lshlrev_b32 v242, 2, v245
global_load_dword v242, v242, s[50:51] sc1
s_waitcnt vmcnt(0)
v_cmp_gt_i32 s[64:65], 0, v242
v_and_b32 v242, 0x7fffffff, v242
v_cmp_ne_u32 vcc, s81, v242
s_nop 0
s_and_b64 vcc, vcc, 0xf00
s_cbranch_scc0 OKP1e4_%=
s_add_u32 s58, s58, 1
s_cmp_lt_u32 s58, 0x80000
s_cbranch_scc1 SPINP1e4_%=
s_branch OKP1e4_%=
SPINP3e4_%=:
s_sleep 4
)A"
R"A(v_lshlrev_b32 v242, 2, v245
global_load_dword v242, v242, s[50:51] sc1
s_waitcnt vmcnt(0)
v_cmp_gt_i32 s[64:65], 0, v242
v_and_b32 v242, 0x7fffffff, v242
v_cmp_ne_u32 vcc, s81, v242
s_nop 0
s_and_b64 vcc, vcc, s[70:71]
s_cbranch_scc0 OKP3e4_%=
s_add_u32 s58, s58, 1
s_cmp_lt_u32 s58, 0x80000
s_cbranch_scc1 SPINP3e4_%=
s_branch OKP3e4_%=
SPINP2e4_%=:
s_sleep 1
v_lshlrev_b32 v242, 2, v245
global_load_dword v242, v242, s[50:51] sc1
s_waitcnt vmcnt(0)
v_cmp_gt_i32 s[64:65], 0, v242
v_and_b32 v242, 0x7fffffff, v242
v_cmp_ne_u32 vcc, s81, v242
s_nop 0
s_and_b64 vcc, vcc, s[34:35]
s_cbranch_scc0 OKP2e4_%=
s_add_u32 s58, s58, 1
s_cmp_lt_u32 s58, 0x80000
s_cbranch_scc1 SPINP2e4_%=
s_branch OKP2e4_%=
KEND_%=:
.purgem Wl
.purgem Ta
.purgem Ra
.purgem Zvaa
.purgem Mvaa
.purgem Zaaa
.purgem Maaa
.purgem Tv
.purgem Zava
.purgem Mava
.purgem Mvva
.purgem Pk
.purgem Ar
.purgem Zvav
.purgem Mvav
.purgem Cvav
.purgem Ex
.purgem Zaav
.purgem Maav
.purgem Ad
.purgem Cvaa
.purgem Mavv
)A"
    :
    : [ka] "s"(__builtin_amdgcn_kernarg_segment_ptr()), [wg] "s"(blockIdx.x), [w] "s"(w)
    : "v0","v1","v2","v3","v4","v5","v6","v7","v8","v9","v10","v11","v12","v13","v14","v15","v16","v17","v18","v19","v20","v21","v22","v23","v24","v25","v26","v27","v28","v29","v30","v31","v32","v33","v34","v35","v36","v37","v38","v39","v40","v41","v42","v43","v44","v45","v46","v47","v48","v49","v50","v51","v52","v53","v54","v55","v56","v57","v58","v59","v60","v61","v62","v63","v64","v65","v66","v67","v68","v69","v70","v71","v72","v73","v74","v75","v76","v77","v78","v79","v80","v81","v82","v83","v84","v85","v86","v87","v88","v89","v90","v91","v92","v93","v94","v95","v96","v97","v98","v99","v100","v101","v102","v103","v104","v105","v106","v107","v108","v109","v110","v111","v112","v113","v114","v115","v116","v117","v118","v119","v120","v121","v122","v123","v124","v125","v126","v127","v128","v129","v130","v131","v132","v133","v134","v135","v136","v137","v138","v139","v140","v141","v142","v143","v144","v145","v146","v147","v148","v149","v150","v151","v152","v153","v154","v155","v156","v157","v158","v159","v160","v161","v162","v163","v164","v165","v166","v167","v168","v169","v170","v171","v172","v173","v174","v175","v176","v177","v178","v179","v180","v181","v182","v183","v184","v185","v186","v187","v188","v189","v190","v191","v192","v193","v194","v195","v196","v197","v198","v199","v200","v201","v202","v203","v204","v205","v206","v207","v208","v209","v210","v211","v212","v213","v214","v215","v216","v217","v218","v219","v220","v221","v222","v223","v224","v225","v226","v227","v228","v229","v230","v231","v232","v233","v234","v235","v236","v237","v238","v239","v240","v241","v242","v243","v244","v245","v246","v247","v248","v249","v250","v251","v252","v253","v254","v255","a0","a1","a2","a3","a4","a5","a6","a7","a8","a9","a10","a11","a12","a13","a14","a15","a16","a17","a18","a19","a20","a21","a22","a23","a24","a25","a26","a27","a28","a29","a30","a31","a32","a33","a34","a35","a36","a37","a38","a39","a40","a41","a42","a43","a44","a45","a46","a47","a48","a49","a50","a51","a52","a53","a54","a55","a56","a57","a58","a59","a60","a61","a62","a63","a64","a65","a66","a67","a68","a69","a70","a71","a72","a73","a74","a75","a76","a77","a78","a79","a80","a81","a82","a83","a84","a85","a86","a87","a88","a89","a90","a91","a92","a93","a94","a95","a96","a97","a98","a99","a100","a101","a102","a103","a104","a105","a106","a107","a108","a109","a110","a111","a112","a113","a114","a115","a116","a117","a118","a119","a120","a121","a122","a123","a124","a125","a126","a127","a128","a129","a130","a131","a132","a133","a134","a135","a136","a137","a138","a139","a140","a141","a142","a143","a144","a145","a146","a147","a148","a149","a150","a151","a152","a153","a154","a155","a156","a157","a158","a159","a160","a161","a162","a163","a164","a165","a166","a167","a168","a169","a170","a171","a172","a173","a174","a175","a176","a177","a178","a179","a180","a181","a182","a183","a184","a185","a186","a187","a188","a189","a190","a191","a192","a193","a194","a195","a196","a197","a198","a199","a200","a201","a202","a203","a204","a205","a206","a207","a208","a209","a210","a211","a212","a213","a214","a215","a216","a217","a218","a219","a220","a221","a222","a223","a224","a225","a226","a227","a228","a229","a230","a231","a232","a233","a234","a235","a236","a237","a238","a239","a240","a241","a242","a243","a244","a245","a246","a247","a248","a249","a250","a251","a252","a253","a254","a255","s8","s9","s10","s11","s12","s13","s14","s15","s16","s17","s18","s19","s20","s21","s22","s23","s24","s25","s26","s27","s28","s29","s30","s31","s34","s35","s36","s37","s38","s39","s40","s41","s42","s43","s44","s45","s46","s47","s48","s49","s50","s51","s52","s53","s54","s55","s56","s57","s58","s59","s60","s61","s62","s63","s64","s65","s66","s67","s68","s69","s70","s71","s72","s73","s74","s75","s76","s77","s78","s79","s80","s81","s82","s83","s84","s85","s86","s87","s88","s89","s90","s91","s92","s93","s94","s95","s96","s97","s98","s99","vcc","scc","memory");
}

extern "C" void kernel_launch(void* const* d_in, const int* in_sizes, int n_in, void* d_out, int out_size, void* d_ws, size_t ws_size,
                              hipStream_t stream) {
  (void)hipFuncSetAttribute((const void*)fused_attn, hipFuncAttributeMaxDynamicSharedMemorySize, K_LDS);
  const u16* const* in = (const u16* const*)d_in;
  fused_attn<<<256, 256, K_LDS, stream>>>(in[0], in[1], in[2], in[3], in[4], in[5], in[6], in[7], in[8], in[9],
                                          (char*)d_ws + (ws_size - WS_USE), (u16*)d_out);
}
```
